```python
import torch

def _softmax(x, axis=-1):
    return torch.softmax(x, dim=axis)
def _gelu(x):
    return torch.nn.functional.gelu(x, approximate="tanh")
def _silu(x):
    return torch.nn.functional.silu(x)
def _sigmoid(x):
    return torch.sigmoid(x)
def _layernorm(x, axis=-1, eps=1e-5):
    m = x.mean(dim=axis, keepdim=True)
    v = x.var(dim=axis, keepdim=True, unbiased=False)
    return (x - m) / torch.sqrt(v + eps)
def _scatter_add(tgt, idx, src, axis=0):
    return tgt.clone().index_add_(axis, idx, src)
def _segment_sum(src, seg, n):
    out = torch.zeros((n,) + src.shape[1:], dtype=src.dtype)
    return out.index_add_(0, seg, src)
def _pad(x, pw):
    flat = tuple(v for pair in reversed(pw) for v in pair)
    return torch.nn.functional.pad(x, flat)

kernel_name = 'recurrent_scan__gqa_block_cached__dense_activation_relu__prompt'

def setup_inputs():

    _w = (lambda a: (lambda t: t.to(torch.bfloat16) if t.is_floating_point() else t)(torch.as_tensor(a)))
    def _A_setup():
        B, Sq, D = 1, 2048, 512
        Hq, Hkv, Sk, Dh = 32, 8, 2048, 128
        assert Hq % Hkv == 0, (Hq, Hkv)
        return {
            "x": _w((lambda *s: torch.randn(*s, dtype=torch.bfloat16) * 0.05)(B, Sq, D)),
            "k_cache": _w((lambda *s: torch.randn(*s, dtype=torch.bfloat16) * 1.6)(B, Hkv, Sk, Dh)),
            "v_cache": _w((lambda *s: torch.randn(*s, dtype=torch.bfloat16) * 0.2)(B, Hkv, Sk, Dh)),
            "wq": _w((lambda *s: torch.randn(*s, dtype=torch.bfloat16) * 1.6)(D, Hq * Dh) * (D ** -0.5)),
            "wo": _w((lambda *s: torch.randn(*s, dtype=torch.bfloat16) * 0.2)(Hq * Dh, D) * ((Hq * Dh) ** -0.5)),
        }
    _A_in = _A_setup()

    def _B_setup():
        B, S, D, Dff = 1, 2048, 512, 1792
        return {
            "x": _w((lambda *s: torch.randn(*s, dtype=torch.bfloat16) * 0.05)(B, S, D)),
            "w1": _w((lambda *s: torch.randn(*s, dtype=torch.bfloat16) * 0.1)(D, Dff)),
            "w2": _w((lambda *s: torch.randn(*s, dtype=torch.bfloat16) * 0.1)(Dff, D)),
        }
    _B_in = _B_setup()

    return {
        "A_x": _A_in["x"],
        "A_k_cache": _A_in["k_cache"],
        "A_v_cache": _A_in["v_cache"],
        "A_wq": _A_in["wq"],
        "A_wo": _A_in["wo"],
        "B_x": _B_in["x"],
        "B_w1": _B_in["w1"],
        "B_w2": _B_in["w2"],
    }

def reference(A_x, A_k_cache, A_v_cache, A_wq, A_wo, B_x, B_w1, B_w2):
    def _A_ref(x, k_cache, v_cache, wq, wo):
        B, Sq, D = x.shape
        Hq, Dh = 32, 128
        Hkv = k_cache.shape[1]
        rep = Hq // Hkv
        q = (lambda x, *d: x.permute(*d))((x @ wq).reshape(B, Sq, Hq, Dh), 0, 2, 1, 3)
        k = (lambda x, n, axis: torch.repeat_interleave(x, n, dim=axis))(k_cache, rep, axis=1)
        v = (lambda x, n, axis: torch.repeat_interleave(x, n, dim=axis))(v_cache, rep, axis=1)
        scores = torch.einsum("bhqd,bhkd->bhqk", q, k) * (Dh ** -0.5)
        p = _softmax(scores, axis=-1)
        o = torch.einsum("bhqk,bhkd->bhqd", p, v)
        return (lambda x, *d: x.permute(*d))(o, 0, 2, 1, 3).reshape(B, Sq, Hq * Dh) @ wo

    def _B_ref(x, w1, w2):
        h = torch.einsum("bsd,df->bsf", x, w1)
        zero = torch.zeros_like(h)
        h = torch.maximum(h, zero)
        return torch.einsum("bsf,fd->bsd", h, w2)

    x = A_x
    xn = _layernorm(x, axis=-1)
    s = _A_ref(xn, A_k_cache, A_v_cache, A_wq, A_wo)
    g = _silu(_B_ref(xn, B_w1, B_w2))
    return x + s * g
```

```cpp
#include <hip/hip_runtime.h>
#include <hip/hip_bf16.h>
typedef unsigned short u16;
__device__ inline float bf2f(u16 x){ return __uint_as_float(((unsigned)x)<<16); }
__device__ inline u16 f2bf(float f){ __bf16 b = (__bf16)f; return __builtin_bit_cast(u16,b); }

__global__ void k_ln(const u16* x, u16* xn){
  int r = blockIdx.x; int t = threadIdx.x;
  float v[8]; float s=0;
  for(int j=0;j<8;j++){ v[j]=bf2f(x[r*512+t*8+j]); s+=v[j]; }
  for(int o=32;o;o>>=1) s+=__shfl_xor(s,o);
  float m=s/512.f; float q=0;
  for(int j=0;j<8;j++){ float d=v[j]-m; q+=d*d; }
  for(int o=32;o;o>>=1) q+=__shfl_xor(q,o);
  float rs=1.f/sqrtf(q/512.f+1e-5f);
  for(int j=0;j<8;j++) xn[r*512+t*8+j]=f2bf((v[j]-m)*rs);
}
template<int MODE> __global__ void k_gemm(const u16* A,const u16* B,void* Cv,int M,int N,int K){
  __shared__ float As[32][33], Bs[32][33];
  int tx=threadIdx.x&31, ty=threadIdx.x>>5;
  int row=blockIdx.y*32+ty, col=blockIdx.x*32+tx; float acc=0;
  for(int k0=0;k0<K;k0+=32){
    As[ty][tx]=bf2f(A[row*K+k0+tx]); Bs[ty][tx]=bf2f(B[(k0+ty)*N+col]);
    __syncthreads();
    for(int k=0;k<32;k++) acc+=As[ty][k]*Bs[k][tx];
    __syncthreads();
  }
  if(MODE==0) ((u16*)Cv)[row*N+col]=f2bf(acc);
  if(MODE==1) ((u16*)Cv)[row*N+col]=f2bf(fmaxf(acc,0.f));
  if(MODE==2) ((float*)Cv)[row*N+col]=acc;
}
__global__ void k_attn(const u16* q,const u16* k,const u16* v,u16* o){
  __shared__ float sc[2048]; __shared__ float qs[128]; __shared__ float red[256];
  int r=blockIdx.x, h=blockIdx.y, t=threadIdx.x, kv=h>>2;
  if(t<128) qs[t]=bf2f(q[r*4096+h*128+t]);
  __syncthreads();
  float mx=-1e30f;
  for(int j=t;j<2048;j+=256){ const u16* kr=k+(kv*2048+j)*128; float s=0; for(int d=0;d<128;d++) s+=qs[d]*bf2f(kr[d]); s*=0.08838834764831845f; sc[j]=s; mx=fmaxf(mx,s);}
  red[t]=mx; __syncthreads();
  for(int o2=128;o2;o2>>=1){ if(t<o2) red[t]=fmaxf(red[t],red[t+o2]); __syncthreads(); }
  mx=red[0]; __syncthreads();
  float sm=0; for(int j=t;j<2048;j+=256){ float p=__expf(sc[j]-mx); sc[j]=p; sm+=p; }
  red[t]=sm; __syncthreads();
  for(int o2=128;o2;o2>>=1){ if(t<o2) red[t]+=red[t+o2]; __syncthreads(); }
  sm=red[0]; __syncthreads();
  int d=t&127, half=t>>7; float acc=0;
  for(int j=half*1024;j<half*1024+1024;j++) acc+=sc[j]*bf2f(v[(kv*2048+j)*128+d]);
  red[t]=acc; __syncthreads();
  if(t<128) o[r*4096+h*128+t]=f2bf((red[t]+red[t+128])/sm);
}
__global__ void k_fin(const u16* x,const float* s,const float* g,u16* out){
  int i=blockIdx.x*256+threadIdx.x; float gg=g[i]; gg=gg/(1.f+__expf(-gg)); out[i]=f2bf(bf2f(x[i])+s[i]*gg);
}
extern "C" void kernel_launch(void* const* d_in, const int* in_sizes, int n_in, void* d_out, int out_size, void* d_ws, size_t ws_size, hipStream_t stream) {
  const u16* x=(const u16*)d_in[0]; const u16* kc=(const u16*)d_in[1]; const u16* vc=(const u16*)d_in[2];
  const u16* wq=(const u16*)d_in[3]; const u16* wo=(const u16*)d_in[4]; const u16* w1=(const u16*)d_in[6]; const u16* w2=(const u16*)d_in[7];
  char* ws=(char*)d_ws;
  u16* xn=(u16*)ws; u16* q=(u16*)(ws+(4<<20)); u16* o=(u16*)(ws+(24<<20)); u16* h=(u16*)(ws+(48<<20)); float* s=(float*)(ws+(64<<20)); float* g=(float*)(ws+(72<<20));
  k_ln<<<2048,64,0,stream>>>(x,xn);
  k_gemm<0><<<dim3(4096/32,2048/32),1024,0,stream>>>(xn,wq,q,2048,4096,512);
  k_gemm<1><<<dim3(1792/32,2048/32),1024,0,stream>>>(xn,w1,h,2048,1792,512);
  k_attn<<<dim3(2048,32),256,0,stream>>>(q,kc,vc,o);
  k_gemm<2><<<dim3(512/32,2048/32),1024,0,stream>>>(o,wo,s,2048,512,4096);
  k_gemm<2><<<dim3(512/32,2048/32),1024,0,stream>>>(h,w2,g,2048,512,1792);
  k_fin<<<2048*512/256,256,0,stream>>>(x,s,g,(u16*)d_out);
}
```

```cpp
#include <hip/hip_runtime.h>

extern "C" __global__ __launch_bounds__(256, 1) __attribute__((aligned(4096))) void fused_main(const void* x, const void* k, const void* v, const void* wq, const void* wo,
                                                                const void* w1, const void* w2, void* out, void* ws) {
  __shared__ __attribute__((aligned(16))) char lds[163840];
  const unsigned w = __builtin_amdgcn_readfirstlane(threadIdx.x >> 6);
  asm volatile(
".LSTART_%=:\n"
"s_load_dwordx16 s[4:19], %0, 0x0\n"
"s_load_dwordx2 s[20:21], %0, 0x40\n"
"s_getpc_b64 s[88:89]\n"
"v_mbcnt_lo_u32_b32 v200, -1, 0\n"
"v_mbcnt_hi_u32_b32 v200, -1, v200\n"
"s_lshr_b32 s82, %1, 3\n"
"s_lshl_b32 s83, s82, 2\n"
"s_add_u32 s83, s83, %2\n"
"s_mov_b32 s90, .LEND_%=-.LSTART_%=-32\n"
"s_mov_b32 s91, 0x20000\n"
"v_lshlrev_b32_e32 v255, 4, v200\n"
"s_lshl_b32 s83, s83, 10\n"
"buffer_load_dword v192, v255, s[88:91], s83 offen\n"
"s_mov_b32 s77, %2\n"
"s_lshr_b32 s35, s82, 1\n"
"s_bfe_u32 s79, %1, 65538\n"
"s_lshl_b32 s79, s79, 4\n"
"s_add_u32 s79, s79, s35\n"
"s_and_b32 s78, %1, 3\n"
"s_lshl_b32 s78, s78, 1\n"
"s_and_b32 s82, s82, 1\n"
"s_add_u32 s78, s78, s82\n"
"s_mov_b32 s50, 0x200000\n"
"s_mov_b32 s51, 0x20000\n"
"s_lshl_b32 s82, s79, 2\n"
"s_add_u32 s82, s82, s77\n"
"s_lshl_b32 s93, s82, 14\n"
"s_lshl_b32 s83, s78, 1\n"
"s_add_u32 s83, s83, s82\n"
"s_and_b32 s83, s83, 15\n"
"s_mul_i32 s94, s83, 0x410\n"
"v_xor_b32_e32 v203, s94, v255\n"
"s_lshl_b32 s46, s77, 14\n"
"s_lshl_b32 s83, s83, 10\n"
"s_add_u32 s83, s83, s46\n"
"s_waitcnt lgkmcnt(0)\n"
"s_mov_b64 s[48:49], s[4:5]\n"
"s_xor_b32 m0, s83, 65536\n"
"v_xor_b32_e32 v204, 0, v203\n"
"buffer_load_dwordx4 v204, s[48:51], s93 offen lds\n"
"s_xor_b32 m0, s83, 66560\n"
"v_xor_b32_e32 v204, 1040, v203\n"
"buffer_load_dwordx4 v204, s[48:51], s93 offen lds\n"
"s_xor_b32 m0, s83, 67584\n"
"v_xor_b32_e32 v204, 2080, v203\n"
"buffer_load_dwordx4 v204, s[48:51], s93 offen lds\n"
"s_xor_b32 m0, s83, 68608\n"
"v_xor_b32_e32 v204, 3120, v203\n"
"buffer_load_dwordx4 v204, s[48:51], s93 offen lds\n"
"s_xor_b32 m0, s83, 69632\n"
"v_xor_b32_e32 v204, 4160, v203\n"
"buffer_load_dwordx4 v204, s[48:51], s93 offen lds\n"
"s_xor_b32 m0, s83, 70656\n"
"v_xor_b32_e32 v204, 5200, v203\n"
"buffer_load_dwordx4 v204, s[48:51], s93 offen lds\n"
"s_xor_b32 m0, s83, 71680\n"
"v_xor_b32_e32 v204, 6240, v203\n"
"buffer_load_dwordx4 v204, s[48:51], s93 offen lds\n"
"s_xor_b32 m0, s83, 72704\n"
"v_xor_b32_e32 v204, 7280, v203\n"
"buffer_load_dwordx4 v204, s[48:51], s93 offen lds\n"
"s_xor_b32 m0, s83, 73728\n"
"v_xor_b32_e32 v204, 8320, v203\n"
"buffer_load_dwordx4 v204, s[48:51], s93 offen lds\n"
"s_xor_b32 m0, s83, 74752\n"
"v_xor_b32_e32 v204, 9360, v203\n"
"buffer_load_dwordx4 v204, s[48:51], s93 offen lds\n"
"s_xor_b32 m0, s83, 75776\n"
"v_xor_b32_e32 v204, 10400, v203\n"
"buffer_load_dwordx4 v204, s[48:51], s93 offen lds\n"
"s_xor_b32 m0, s83, 76800\n"
"v_xor_b32_e32 v204, 11440, v203\n"
"buffer_load_dwordx4 v204, s[48:51], s93 offen lds\n"
"s_xor_b32 m0, s83, 77824\n"
"v_xor_b32_e32 v204, 12480, v203\n"
"buffer_load_dwordx4 v204, s[48:51], s93 offen lds\n"
"s_xor_b32 m0, s83, 78848\n"
"v_xor_b32_e32 v204, 13520, v203\n"
"buffer_load_dwordx4 v204, s[48:51], s93 offen lds\n"
"s_xor_b32 m0, s83, 79872\n"
"v_xor_b32_e32 v204, 14560, v203\n"
"buffer_load_dwordx4 v204, s[48:51], s93 offen lds\n"
"s_xor_b32 m0, s83, 80896\n"
"v_xor_b32_e32 v204, 15600, v203\n"
"buffer_load_dwordx4 v204, s[48:51], s93 offen lds\n"
"s_mov_b64 s[52:53], s[14:15]\n"
"s_mov_b32 s54, 1835008\n"
"s_mov_b32 s55, 0x20000\n"
"s_lshl_b32 s82, s35, 2\n"
"s_add_u32 s82, s82, s77\n"
"v_lshrrev_b32_e32 v222, 3, v200\n"
"v_and_b32_e32 v223, 7, v200\n"
"v_mul_u32_u24_e32 v206, 3584, v222\n"
"v_lshl_add_u32 v206, v223, 6, v206\n"
"s_mul_i32 s83, s82, 28672\n"
"s_lshl_b32 s93, s78, 2\n"
"s_lshr_b32 s93, 303043088, s93\n"
"s_and_b32 s93, s93, 3\n"
"s_mul_i32 s23, s78, 7\n"
"s_add_u32 s23, s23, s93\n"
"s_lshl_b32 s84, s23, 6\n"
"s_add_u32 s83, s83, s84\n"
"buffer_load_dword v193, v206, s[52:55], s83 offen\n"
"v_and_b32_e32 v201, 15, v200\n"
"v_lshrrev_b32_e32 v202, 4, v200\n"
"s_lshl_b32 s83, s77, 7\n"
"s_add_u32 s84, s84, s83\n"
"s_lshl_b32 s81, s77, 13\n"
"s_add_u32 s81, s81, 131072\n"
"v_lshrrev_b32_e32 v214, 3, v200\n"
"v_and_b32_e32 v205, 7, v200\n"
"v_and_b32_e32 v215, 6, v214\n"
"v_xor_b32_e32 v205, v205, v215\n"
"v_lshlrev_b32_e32 v215, 4, v205\n"
"v_lshl_add_u32 v254, v214, 10, v215\n"
"v_and_b32_e32 v207, 4, v214\n"
"v_add_u32_e32 v207, v207, v214\n"
"v_lshl_add_u32 v229, v207, 13, v215\n"
"v_mul_u32_u24_e32 v207, 3584, v207\n"
"v_add_u32_e32 v228, v207, v215\n"
"s_bfe_u32 s47, s78, 0x10001\n"
"s_xor_b32 s47, s47, 0\n"
"s_cmp_eq_u32 s77, 3\n"
"s_cselect_b32 s83, s47, 0\n"
"s_add_u32 s47, s47, s83\n"
"s_lshl_b32 s83, s83, 30\n"
"v_or_b32_e32 v228, s83, v228\n"
"s_add_u32 s83, s46, 65536\n"
"v_lshlrev_b32_e32 v205, 10, v201\n"
"v_add_u32_e32 v205, s83, v205\n"
"v_xor_b32_e32 v204, v202, v201\n"
"v_lshl_add_u32 v196, v204, 4, v205\n"
"v_xor_b32_e32 v197, 64, v196\n"
"v_xor_b32_e32 v198, 0x80, v196\n"
"v_xor_b32_e32 v199, 0xc0, v196\n"
"v_add_u32_e32 v231, s83, v255\n"
"v_add_u32_e32 v230, 65536, v255\n"
"v_mov_b32_e32 v208, 0x3f803f80\n"
"v_mov_b32_e32 v209, 0x3f803f80\n"
"v_mov_b32_e32 v210, 0x3f803f80\n"
"v_mov_b32_e32 v211, 0x3f803f80\n"
"s_mov_b64 s[56:57], s[10:11]\n"
"s_mov_b32 s58, 4194304\n"
"s_mov_b32 s59, 0x20000\n"
"s_mul_i32 s44, s82, 65536\n"
"s_mul_i32 s83, s78, 1024\n"
"s_add_u32 s44, s44, s83\n"
"v_lshlrev_b32_e32 v234, 13, v222\n"
"v_lshl_add_u32 v234, v223, 7, v234\n"
"v_lshrrev_b32_e32 v204, 2, v200\n"
"v_and_b32_e32 v205, 3, v200\n"
"v_lshlrev_b32_e32 v205, 3, v205\n"
"v_lshl_add_u32 v206, v204, 7, v205\n"
"v_bfe_u32 v207, v200, 3, 2\n"
"v_lshl_add_u32 v206, v207, 5, v206\n"
"v_add_u32_e32 v246, s46, v206\n"
"v_add_u32_e32 v224, s81, v206\n"
"v_mov_b32_e32 v216, 0x3f80\n"
"v_mov_b32_e32 v217, 0x3f800000\n"
"v_bfe_u32 v218, v200, 1, 1\n"
"v_mul_u32_u24_e32 v218, 0xaaaa, v218\n"
"v_or_b32_e32 v218, 0x4444, v218\n"
"v_mov_b32_e32 v212, 0\n"
"v_mov_b32_e32 v214, 0\n"
"v_add_u32_e32 v219, 56320, v228\n"
"v_add_u32_e32 v220, 12288, v228\n"
"v_add_u32_e32 v221, 68608, v228\n"
"v_xor_b32_e32 v247, 32, v246\n"
"v_xor_b32_e32 v225, 32, v224\n"
"v_xor_b32_e32 v248, 64, v246\n"
"v_xor_b32_e32 v226, 64, v224\n"
"v_xor_b32_e32 v249, 96, v246\n"
"v_xor_b32_e32 v227, 96, v224\n"
"s_getreg_b32 s98, hwreg(4, 8, 1)\n"
"s_lshl_b32 s98, s98, 2\n"
"s_add_u32 s98, s98, s77\n"
"s_min_u32 s96, s47, 1\n"
"s_lshl_b32 s96, s96, 3\n"
"s_add_u32 s98, s98, s96\n"
"s_lshl_b32 s96, s98, 2\n"
"s_add_u32 s96, s96, .LT1_%=-.LTP1_%=\n"
"s_getpc_b64 s[98:99]\n"
".LTP1_%=:\n"
"s_add_u32 s98, s98, s96\n"
"s_addc_u32 s99, s99, 0\n"
"s_mov_b64 exec, 0\n"
"s_swappc_b64 s[96:97], s[98:99]\n"
"s_branch .LTX1_%=\n"
".LT1_%=:\n"
"s_branch .LPG1_%=\n"
"s_branch .LOTH_%=\n"
"s_branch .LPG20_%=\n"
"s_branch .LPG21_%=\n"
"s_branch .LPG22_%=\n"
"s_branch .LPG23_%=\n"
"s_branch .LPG24_%=\n"
"s_branch .LPGR_%=\n"
"s_branch .LPG1_%=\n"
"s_branch .LPG2_%=\n"
"s_branch .LOTH_%=\n"
"s_branch .LIDLE2_%=\n"
"s_branch .LPG3_%=\n"
"s_branch .LPG4_%=\n"
"s_branch .LPG1_%=\n"
"s_branch .LIDLE2_%=\n"
".LPGR_%=:\n"
"s_setpc_b64 s[96:97]\n"
".LTX1_%=:\n"
"s_mov_b64 exec, -1\n"
"s_add_u32 m0, s46, 0\n"
"s_add_u32 s80, s84, 0\n"
"buffer_load_dwordx4 v228, s[52:55], s80 offen lds\n"
"buffer_load_dwordx4 v219, s[52:55], s80 offen offset:1024 lds\n"
"buffer_load_dwordx4 v220, s[52:55], s80 offen offset:2048 lds\n"
"buffer_load_dwordx4 v221, s[52:55], s80 offen offset:3072 lds\n"
"s_add_u32 m0, s46, 4096\n"
"s_add_u32 s80, s84, 114688\n"
"buffer_load_dwordx4 v228, s[52:55], s80 offen lds\n"
"buffer_load_dwordx4 v219, s[52:55], s80 offen offset:1024 lds\n"
"buffer_load_dwordx4 v220, s[52:55], s80 offen offset:2048 lds\n"
"buffer_load_dwordx4 v221, s[52:55], s80 offen offset:3072 lds\n"
"s_waitcnt vmcnt(9)\n"
"ds_read_b128 v[128:131], v196 offset:0\n"
"ds_read_b128 v[132:135], v197 offset:0\n"
"ds_read_b128 v[136:139], v198 offset:0\n"
"ds_read_b128 v[140:143], v199 offset:0\n"
"ds_read_b128 v[144:147], v196 offset:256\n"
"ds_read_b128 v[148:151], v197 offset:256\n"
"ds_read_b128 v[152:155], v198 offset:256\n"
"ds_read_b128 v[156:159], v199 offset:256\n"
"ds_read_b128 v[160:163], v196 offset:512\n"
"ds_read_b128 v[164:167], v197 offset:512\n"
"ds_read_b128 v[168:171], v198 offset:512\n"
"ds_read_b128 v[172:175], v199 offset:512\n"
"ds_read_b128 v[176:179], v196 offset:768\n"
"ds_read_b128 v[180:183], v197 offset:768\n"
"ds_read_b128 v[184:187], v198 offset:768\n"
"ds_read_b128 v[188:191], v199 offset:768\n"
"s_waitcnt lgkmcnt(14)\n"
"v_smfmac_f32_16x16x64_bf16 v[212:215], v[208:211], v[128:135], v218\n"
"s_waitcnt lgkmcnt(12)\n"
"v_smfmac_f32_16x16x64_bf16 v[212:215], v[208:211], v[136:143], v218\n"
"s_waitcnt lgkmcnt(10)\n"
"v_smfmac_f32_16x16x64_bf16 v[212:215], v[208:211], v[144:151], v218\n"
"s_waitcnt lgkmcnt(8)\n"
"v_smfmac_f32_16x16x64_bf16 v[212:215], v[208:211], v[152:159], v218\n"
"s_waitcnt lgkmcnt(6)\n"
"v_smfmac_f32_16x16x64_bf16 v[212:215], v[208:211], v[160:167], v218\n"
"s_waitcnt lgkmcnt(4)\n"
"v_smfmac_f32_16x16x64_bf16 v[212:215], v[208:211], v[168:175], v218\n"
"s_waitcnt lgkmcnt(2)\n"
"v_smfmac_f32_16x16x64_bf16 v[212:215], v[208:211], v[176:183], v218\n"
"s_waitcnt lgkmcnt(0)\n"
"v_smfmac_f32_16x16x64_bf16 v[212:215], v[208:211], v[184:191], v218\n"
"s_nop 11\n"
"v_add_f32_e32 v212, v212, v214\n"
"v_mul_f32_e32 v212, 0x3b000000, v212\n"
"v_cvt_pk_bf16_f32 v213, v212, v212\n"
"v_and_b32_e32 v213, 0xffff0000, v213\n"
"v_sub_f32_e32 v212, v212, v213\n"
"v_sub_f32_e32 v213, 0, v213\n"
".p2align 3\n"
"v_dot2_f32_bf16 v0, v128, v216, v213\n"
"v_dot2_f32_bf16 v1, v128, v217, v213\n"
"v_dot2_f32_bf16 v2, v129, v216, v213\n"
"v_dot2_f32_bf16 v3, v129, v217, v213\n"
"v_dot2_f32_bf16 v4, v130, v216, v213\n"
"v_dot2_f32_bf16 v5, v130, v217, v213\n"
"v_dot2_f32_bf16 v6, v131, v216, v213\n"
"v_dot2_f32_bf16 v7, v131, v217, v213\n"
"v_dot2_f32_bf16 v8, v132, v216, v213\n"
"v_dot2_f32_bf16 v9, v132, v217, v213\n"
"v_dot2_f32_bf16 v10, v133, v216, v213\n"
"v_dot2_f32_bf16 v11, v133, v217, v213\n"
"v_dot2_f32_bf16 v12, v134, v216, v213\n"
"v_dot2_f32_bf16 v13, v134, v217, v213\n"
"v_dot2_f32_bf16 v14, v135, v216, v213\n"
"v_dot2_f32_bf16 v15, v135, v217, v213\n"
"v_dot2_f32_bf16 v16, v136, v216, v213\n"
"v_dot2_f32_bf16 v17, v136, v217, v213\n"
"v_dot2_f32_bf16 v18, v137, v216, v213\n"
"v_dot2_f32_bf16 v19, v137, v217, v213\n"
"v_dot2_f32_bf16 v20, v138, v216, v213\n"
"v_dot2_f32_bf16 v21, v138, v217, v213\n"
"v_dot2_f32_bf16 v22, v139, v216, v213\n"
"s_add_u32 m0, s81, 0\n"
"s_add_u32 s80, s84, 229376\n"
"buffer_load_dwordx4 v228, s[52:55], s80 offen lds\n"
"buffer_load_dwordx4 v219, s[52:55], s80 offen offset:1024 lds\n"
"buffer_load_dwordx4 v220, s[52:55], s80 offen offset:2048 lds\n"
"buffer_load_dwordx4 v221, s[52:55], s80 offen offset:3072 lds\n"
"v_dot2_f32_bf16 v23, v139, v217, v213\n"
"v_dot2_f32_bf16 v24, v140, v216, v213\n"
"v_dot2_f32_bf16 v25, v140, v217, v213\n"
"v_dot2_f32_bf16 v26, v141, v216, v213\n"
"v_dot2_f32_bf16 v27, v141, v217, v213\n"
"v_dot2_f32_bf16 v28, v142, v216, v213\n"
"v_dot2_f32_bf16 v29, v142, v217, v213\n"
"v_dot2_f32_bf16 v30, v143, v216, v213\n"
"v_dot2_f32_bf16 v31, v143, v217, v213\n"
"v_dot2_f32_bf16 v32, v144, v216, v213\n"
"v_dot2_f32_bf16 v33, v144, v217, v213\n"
"v_dot2_f32_bf16 v34, v145, v216, v213\n"
"v_dot2_f32_bf16 v35, v145, v217, v213\n"
"v_dot2_f32_bf16 v36, v146, v216, v213\n"
"v_dot2_f32_bf16 v37, v146, v217, v213\n"
"v_dot2_f32_bf16 v38, v147, v216, v213\n"
"v_dot2_f32_bf16 v39, v147, v217, v213\n"
"v_dot2_f32_bf16 v40, v148, v216, v213\n"
"v_dot2_f32_bf16 v41, v148, v217, v213\n"
"v_dot2_f32_bf16 v42, v149, v216, v213\n"
"v_dot2_f32_bf16 v43, v149, v217, v213\n"
"v_dot2_f32_bf16 v44, v150, v216, v213\n"
"v_dot2_f32_bf16 v45, v150, v217, v213\n"
"v_dot2_f32_bf16 v46, v151, v216, v213\n"
"v_dot2_f32_bf16 v47, v151, v217, v213\n"
"v_dot2_f32_bf16 v48, v152, v216, v213\n"
"v_dot2_f32_bf16 v49, v152, v217, v213\n"
"v_dot2_f32_bf16 v50, v153, v216, v213\n"
"v_dot2_f32_bf16 v51, v153, v217, v213\n"
"v_dot2_f32_bf16 v52, v154, v216, v213\n"
"v_dot2_f32_bf16 v53, v154, v217, v213\n"
"v_dot2_f32_bf16 v54, v155, v216, v213\n"
"v_dot2_f32_bf16 v55, v155, v217, v213\n"
"v_dot2_f32_bf16 v56, v156, v216, v213\n"
"v_dot2_f32_bf16 v57, v156, v217, v213\n"
"v_dot2_f32_bf16 v58, v157, v216, v213\n"
"v_dot2_f32_bf16 v59, v157, v217, v213\n"
"v_dot2_f32_bf16 v60, v158, v216, v213\n"
"s_add_u32 m0, s81, 4096\n"
"s_add_u32 s80, s84, 344064\n"
"buffer_load_dwordx4 v228, s[52:55], s80 offen lds\n"
"buffer_load_dwordx4 v219, s[52:55], s80 offen offset:1024 lds\n"
"buffer_load_dwordx4 v220, s[52:55], s80 offen offset:2048 lds\n"
"buffer_load_dwordx4 v221, s[52:55], s80 offen offset:3072 lds\n"
"v_dot2_f32_bf16 v61, v158, v217, v213\n"
"v_dot2_f32_bf16 v62, v159, v216, v213\n"
"v_dot2_f32_bf16 v63, v159, v217, v213\n"
"v_dot2_f32_bf16 v64, v160, v216, v213\n"
"v_dot2_f32_bf16 v65, v160, v217, v213\n"
"v_dot2_f32_bf16 v66, v161, v216, v213\n"
"v_dot2_f32_bf16 v67, v161, v217, v213\n"
"v_dot2_f32_bf16 v68, v162, v216, v213\n"
"v_dot2_f32_bf16 v69, v162, v217, v213\n"
"v_dot2_f32_bf16 v70, v163, v216, v213\n"
"v_dot2_f32_bf16 v71, v163, v217, v213\n"
"v_dot2_f32_bf16 v72, v164, v216, v213\n"
"v_dot2_f32_bf16 v73, v164, v217, v213\n"
"v_dot2_f32_bf16 v74, v165, v216, v213\n"
"v_dot2_f32_bf16 v75, v165, v217, v213\n"
"v_dot2_f32_bf16 v76, v166, v216, v213\n"
"v_dot2_f32_bf16 v77, v166, v217, v213\n"
"v_dot2_f32_bf16 v78, v167, v216, v213\n"
"v_dot2_f32_bf16 v79, v167, v217, v213\n"
"v_dot2_f32_bf16 v80, v168, v216, v213\n"
"v_dot2_f32_bf16 v81, v168, v217, v213\n"
"v_dot2_f32_bf16 v82, v169, v216, v213\n"
"v_dot2_f32_bf16 v83, v169, v217, v213\n"
"v_dot2_f32_bf16 v84, v170, v216, v213\n"
"v_dot2_f32_bf16 v85, v170, v217, v213\n"
"v_dot2_f32_bf16 v86, v171, v216, v213\n"
"v_dot2_f32_bf16 v87, v171, v217, v213\n"
"v_dot2_f32_bf16 v88, v172, v216, v213\n"
"v_dot2_f32_bf16 v89, v172, v217, v213\n"
"v_dot2_f32_bf16 v90, v173, v216, v213\n"
"v_dot2_f32_bf16 v91, v173, v217, v213\n"
"v_dot2_f32_bf16 v92, v174, v216, v213\n"
"v_dot2_f32_bf16 v93, v174, v217, v213\n"
"v_dot2_f32_bf16 v94, v175, v216, v213\n"
"v_dot2_f32_bf16 v95, v175, v217, v213\n"
"v_dot2_f32_bf16 v96, v176, v216, v213\n"
"v_dot2_f32_bf16 v97, v176, v217, v213\n"
"v_dot2_f32_bf16 v98, v177, v216, v213\n"
"s_add_u32 m0, s46, 8192\n"
"s_add_u32 s80, s84, 458752\n"
"buffer_load_dwordx4 v228, s[52:55], s80 offen lds\n"
"buffer_load_dwordx4 v219, s[52:55], s80 offen offset:1024 lds\n"
"buffer_load_dwordx4 v220, s[52:55], s80 offen offset:2048 lds\n"
"buffer_load_dwordx4 v221, s[52:55], s80 offen offset:3072 lds\n"
"v_dot2_f32_bf16 v99, v177, v217, v213\n"
"v_dot2_f32_bf16 v100, v178, v216, v213\n"
"v_dot2_f32_bf16 v101, v178, v217, v213\n"
"v_dot2_f32_bf16 v102, v179, v216, v213\n"
"v_dot2_f32_bf16 v103, v179, v217, v213\n"
"v_dot2_f32_bf16 v104, v180, v216, v213\n"
"v_dot2_f32_bf16 v105, v180, v217, v213\n"
"v_dot2_f32_bf16 v106, v181, v216, v213\n"
"v_dot2_f32_bf16 v107, v181, v217, v213\n"
"v_dot2_f32_bf16 v108, v182, v216, v213\n"
"v_dot2_f32_bf16 v109, v182, v217, v213\n"
"v_dot2_f32_bf16 v110, v183, v216, v213\n"
"v_dot2_f32_bf16 v111, v183, v217, v213\n"
"v_dot2_f32_bf16 v112, v184, v216, v213\n"
"v_dot2_f32_bf16 v113, v184, v217, v213\n"
"v_dot2_f32_bf16 v114, v185, v216, v213\n"
"v_dot2_f32_bf16 v115, v185, v217, v213\n"
"v_dot2_f32_bf16 v116, v186, v216, v213\n"
"v_dot2_f32_bf16 v117, v186, v217, v213\n"
"v_dot2_f32_bf16 v118, v187, v216, v213\n"
"v_dot2_f32_bf16 v119, v187, v217, v213\n"
"v_dot2_f32_bf16 v120, v188, v216, v213\n"
"v_dot2_f32_bf16 v121, v188, v217, v213\n"
"v_dot2_f32_bf16 v122, v189, v216, v213\n"
"v_dot2_f32_bf16 v123, v189, v217, v213\n"
"v_dot2_f32_bf16 v124, v190, v216, v213\n"
"v_dot2_f32_bf16 v125, v190, v217, v213\n"
"v_dot2_f32_bf16 v126, v191, v216, v213\n"
"v_dot2_f32_bf16 v127, v191, v217, v213\n"
"v_pk_mul_f32 v[196:197], v[0:1], v[0:1]\n"
"v_pk_mul_f32 v[198:199], v[2:3], v[2:3]\n"
"v_pk_fma_f32 v[196:197], v[4:5], v[4:5], v[196:197]\n"
"v_pk_fma_f32 v[198:199], v[6:7], v[6:7], v[198:199]\n"
"v_pk_fma_f32 v[196:197], v[8:9], v[8:9], v[196:197]\n"
"v_pk_fma_f32 v[198:199], v[10:11], v[10:11], v[198:199]\n"
"v_pk_fma_f32 v[196:197], v[12:13], v[12:13], v[196:197]\n"
"v_pk_fma_f32 v[198:199], v[14:15], v[14:15], v[198:199]\n"
"v_pk_fma_f32 v[196:197], v[16:17], v[16:17], v[196:197]\n"
"s_add_u32 m0, s46, 12288\n"
"s_add_u32 s80, s84, 573440\n"
"buffer_load_dwordx4 v228, s[52:55], s80 offen lds\n"
"buffer_load_dwordx4 v219, s[52:55], s80 offen offset:1024 lds\n"
"buffer_load_dwordx4 v220, s[52:55], s80 offen offset:2048 lds\n"
"buffer_load_dwordx4 v221, s[52:55], s80 offen offset:3072 lds\n"
"v_pk_fma_f32 v[198:199], v[18:19], v[18:19], v[198:199]\n"
"v_pk_fma_f32 v[196:197], v[20:21], v[20:21], v[196:197]\n"
"v_pk_fma_f32 v[198:199], v[22:23], v[22:23], v[198:199]\n"
"v_pk_fma_f32 v[196:197], v[24:25], v[24:25], v[196:197]\n"
"v_pk_fma_f32 v[198:199], v[26:27], v[26:27], v[198:199]\n"
"v_pk_fma_f32 v[196:197], v[28:29], v[28:29], v[196:197]\n"
"v_pk_fma_f32 v[198:199], v[30:31], v[30:31], v[198:199]\n"
"v_pk_fma_f32 v[196:197], v[32:33], v[32:33], v[196:197]\n"
"v_pk_fma_f32 v[198:199], v[34:35], v[34:35], v[198:199]\n"
"v_pk_fma_f32 v[196:197], v[36:37], v[36:37], v[196:197]\n"
"v_pk_fma_f32 v[198:199], v[38:39], v[38:39], v[198:199]\n"
"v_pk_fma_f32 v[196:197], v[40:41], v[40:41], v[196:197]\n"
"v_pk_fma_f32 v[198:199], v[42:43], v[42:43], v[198:199]\n"
"v_pk_fma_f32 v[196:197], v[44:45], v[44:45], v[196:197]\n"
"v_pk_fma_f32 v[198:199], v[46:47], v[46:47], v[198:199]\n"
"v_pk_fma_f32 v[196:197], v[48:49], v[48:49], v[196:197]\n"
"v_pk_fma_f32 v[198:199], v[50:51], v[50:51], v[198:199]\n"
"v_pk_fma_f32 v[196:197], v[52:53], v[52:53], v[196:197]\n"
"v_pk_fma_f32 v[198:199], v[54:55], v[54:55], v[198:199]\n"
"v_pk_fma_f32 v[196:197], v[56:57], v[56:57], v[196:197]\n"
"v_pk_fma_f32 v[198:199], v[58:59], v[58:59], v[198:199]\n"
"v_pk_fma_f32 v[196:197], v[60:61], v[60:61], v[196:197]\n"
"v_pk_fma_f32 v[198:199], v[62:63], v[62:63], v[198:199]\n"
"v_pk_fma_f32 v[196:197], v[64:65], v[64:65], v[196:197]\n"
"v_pk_fma_f32 v[198:199], v[66:67], v[66:67], v[198:199]\n"
"v_pk_fma_f32 v[196:197], v[68:69], v[68:69], v[196:197]\n"
"v_pk_fma_f32 v[198:199], v[70:71], v[70:71], v[198:199]\n"
"v_pk_fma_f32 v[196:197], v[72:73], v[72:73], v[196:197]\n"
"v_pk_fma_f32 v[198:199], v[74:75], v[74:75], v[198:199]\n"
"v_pk_fma_f32 v[196:197], v[76:77], v[76:77], v[196:197]\n"
"v_pk_fma_f32 v[198:199], v[78:79], v[78:79], v[198:199]\n"
"v_pk_fma_f32 v[196:197], v[80:81], v[80:81], v[196:197]\n"
"v_pk_fma_f32 v[198:199], v[82:83], v[82:83], v[198:199]\n"
"v_pk_fma_f32 v[196:197], v[84:85], v[84:85], v[196:197]\n"
"v_pk_fma_f32 v[198:199], v[86:87], v[86:87], v[198:199]\n"
"v_pk_fma_f32 v[196:197], v[88:89], v[88:89], v[196:197]\n"
"v_pk_fma_f32 v[198:199], v[90:91], v[90:91], v[198:199]\n"
"v_pk_fma_f32 v[196:197], v[92:93], v[92:93], v[196:197]\n"
"buffer_load_dword v194, v234, s[56:59], s44 offen\n"
"v_pk_fma_f32 v[198:199], v[94:95], v[94:95], v[198:199]\n"
"v_pk_fma_f32 v[196:197], v[96:97], v[96:97], v[196:197]\n"
"v_pk_fma_f32 v[198:199], v[98:99], v[98:99], v[198:199]\n"
"v_pk_fma_f32 v[196:197], v[100:101], v[100:101], v[196:197]\n"
"v_pk_fma_f32 v[198:199], v[102:103], v[102:103], v[198:199]\n"
"v_pk_fma_f32 v[196:197], v[104:105], v[104:105], v[196:197]\n"
"v_pk_fma_f32 v[198:199], v[106:107], v[106:107], v[198:199]\n"
"v_pk_fma_f32 v[196:197], v[108:109], v[108:109], v[196:197]\n"
"v_pk_fma_f32 v[198:199], v[110:111], v[110:111], v[198:199]\n"
"v_pk_fma_f32 v[196:197], v[112:113], v[112:113], v[196:197]\n"
"v_pk_fma_f32 v[198:199], v[114:115], v[114:115], v[198:199]\n"
"v_pk_fma_f32 v[196:197], v[116:117], v[116:117], v[196:197]\n"
"v_pk_fma_f32 v[198:199], v[118:119], v[118:119], v[198:199]\n"
"v_pk_fma_f32 v[196:197], v[120:121], v[120:121], v[196:197]\n"
"v_pk_fma_f32 v[198:199], v[122:123], v[122:123], v[198:199]\n"
"v_pk_fma_f32 v[196:197], v[124:125], v[124:125], v[196:197]\n"
"v_pk_fma_f32 v[198:199], v[126:127], v[126:127], v[198:199]\n"
"v_pk_add_f32 v[196:197], v[196:197], v[198:199]\n"
"v_add_f32_e32 v196, v196, v197\n"
"v_mov_b32_e32 v197, v196\n"
"s_nop 1\n"
"v_permlane32_swap_b32 v196, v197\n"
"v_add_f32_e32 v196, v196, v197\n"
"v_mov_b32_e32 v197, v196\n"
"s_nop 1\n"
"v_permlane16_swap_b32 v196, v197\n"
"v_add_f32_e32 v196, v196, v197\n"
"v_mul_f32_e32 v196, 0x3b000000, v196\n"
"v_fma_f32 v196, -v212, v212, v196\n"
"v_max_f32_e32 v196, 0, v196\n"
"v_cvt_pk_bf16_f32 v196, v196, v196\n"
"v_and_b32_e32 v196, 0xffff0000, v196\n"
"v_add_f32_e32 v196, 0x3727c5ac, v196\n"
"v_cvt_pk_bf16_f32 v196, v196, v196\n"
"v_and_b32_e32 v196, 0xffff0000, v196\n"
"v_sqrt_f32_e32 v196, v196\n"
"s_nop 0\n"
"v_cvt_pk_bf16_f32 v196, v196, v196\n"
"v_and_b32_e32 v196, 0xffff0000, v196\n"
"v_rcp_f32_e32 v196, v196\n"
"s_nop 0\n"
"v_mul_f32_e32 v196, 0x3e0293ee, v196\n"
"v_mov_b32_e32 v197, v196\n"
".p2align 3\n"
"v_pk_mul_f32 v[0:1], v[0:1], v[196:197]\n"
"v_pk_mul_f32 v[2:3], v[2:3], v[196:197]\n"
"v_pk_mul_f32 v[4:5], v[4:5], v[196:197]\n"
"v_pk_mul_f32 v[6:7], v[6:7], v[196:197]\n"
"v_cvt_pk_bf16_f32 v128, v0, v1\n"
"v_cvt_pk_bf16_f32 v129, v2, v3\n"
"v_cvt_pk_bf16_f32 v130, v4, v5\n"
"v_cvt_pk_bf16_f32 v131, v6, v7\n"
"v_pk_mul_f32 v[8:9], v[8:9], v[196:197]\n"
"v_pk_mul_f32 v[10:11], v[10:11], v[196:197]\n"
"v_pk_mul_f32 v[12:13], v[12:13], v[196:197]\n"
"v_pk_mul_f32 v[14:15], v[14:15], v[196:197]\n"
"v_cvt_pk_bf16_f32 v132, v8, v9\n"
"v_cvt_pk_bf16_f32 v133, v10, v11\n"
"v_cvt_pk_bf16_f32 v134, v12, v13\n"
"v_cvt_pk_bf16_f32 v135, v14, v15\n"
"ds_write_b128 v231, v[128:131] offset:0\n"
"ds_write_b128 v231, v[132:135] offset:1024\n"
"v_pk_mul_f32 v[16:17], v[16:17], v[196:197]\n"
"v_pk_mul_f32 v[18:19], v[18:19], v[196:197]\n"
"v_pk_mul_f32 v[20:21], v[20:21], v[196:197]\n"
"v_pk_mul_f32 v[22:23], v[22:23], v[196:197]\n"
"v_cvt_pk_bf16_f32 v136, v16, v17\n"
"v_cvt_pk_bf16_f32 v137, v18, v19\n"
"v_cvt_pk_bf16_f32 v138, v20, v21\n"
"v_cvt_pk_bf16_f32 v139, v22, v23\n"
"v_pk_mul_f32 v[24:25], v[24:25], v[196:197]\n"
"v_pk_mul_f32 v[26:27], v[26:27], v[196:197]\n"
"v_pk_mul_f32 v[28:29], v[28:29], v[196:197]\n"
"v_pk_mul_f32 v[30:31], v[30:31], v[196:197]\n"
"v_cvt_pk_bf16_f32 v140, v24, v25\n"
"v_cvt_pk_bf16_f32 v141, v26, v27\n"
"v_cvt_pk_bf16_f32 v142, v28, v29\n"
"v_cvt_pk_bf16_f32 v143, v30, v31\n"
"ds_write_b128 v231, v[136:139] offset:2048\n"
"ds_write_b128 v231, v[140:143] offset:3072\n"
"v_pk_mul_f32 v[32:33], v[32:33], v[196:197]\n"
"v_pk_mul_f32 v[34:35], v[34:35], v[196:197]\n"
"v_pk_mul_f32 v[36:37], v[36:37], v[196:197]\n"
"v_pk_mul_f32 v[38:39], v[38:39], v[196:197]\n"
"v_cvt_pk_bf16_f32 v144, v32, v33\n"
"v_cvt_pk_bf16_f32 v145, v34, v35\n"
"v_cvt_pk_bf16_f32 v146, v36, v37\n"
"v_cvt_pk_bf16_f32 v147, v38, v39\n"
"v_pk_mul_f32 v[40:41], v[40:41], v[196:197]\n"
"v_pk_mul_f32 v[42:43], v[42:43], v[196:197]\n"
"v_pk_mul_f32 v[44:45], v[44:45], v[196:197]\n"
"v_pk_mul_f32 v[46:47], v[46:47], v[196:197]\n"
"v_cvt_pk_bf16_f32 v148, v40, v41\n"
"v_cvt_pk_bf16_f32 v149, v42, v43\n"
"v_cvt_pk_bf16_f32 v150, v44, v45\n"
"v_cvt_pk_bf16_f32 v151, v46, v47\n"
"ds_write_b128 v231, v[144:147] offset:4096\n"
"ds_write_b128 v231, v[148:151] offset:5120\n"
"v_pk_mul_f32 v[48:49], v[48:49], v[196:197]\n"
"v_pk_mul_f32 v[50:51], v[50:51], v[196:197]\n"
"v_pk_mul_f32 v[52:53], v[52:53], v[196:197]\n"
"v_pk_mul_f32 v[54:55], v[54:55], v[196:197]\n"
"v_cvt_pk_bf16_f32 v152, v48, v49\n"
"v_cvt_pk_bf16_f32 v153, v50, v51\n"
"v_cvt_pk_bf16_f32 v154, v52, v53\n"
"v_cvt_pk_bf16_f32 v155, v54, v55\n"
"v_pk_mul_f32 v[56:57], v[56:57], v[196:197]\n"
"v_pk_mul_f32 v[58:59], v[58:59], v[196:197]\n"
"v_pk_mul_f32 v[60:61], v[60:61], v[196:197]\n"
"v_pk_mul_f32 v[62:63], v[62:63], v[196:197]\n"
"v_cvt_pk_bf16_f32 v156, v56, v57\n"
"v_cvt_pk_bf16_f32 v157, v58, v59\n"
"v_cvt_pk_bf16_f32 v158, v60, v61\n"
"v_cvt_pk_bf16_f32 v159, v62, v63\n"
"ds_write_b128 v231, v[152:155] offset:6144\n"
"ds_write_b128 v231, v[156:159] offset:7168\n"
"v_pk_mul_f32 v[64:65], v[64:65], v[196:197]\n"
"v_pk_mul_f32 v[66:67], v[66:67], v[196:197]\n"
"v_pk_mul_f32 v[68:69], v[68:69], v[196:197]\n"
"v_pk_mul_f32 v[70:71], v[70:71], v[196:197]\n"
"v_cvt_pk_bf16_f32 v160, v64, v65\n"
"v_cvt_pk_bf16_f32 v161, v66, v67\n"
"v_cvt_pk_bf16_f32 v162, v68, v69\n"
"v_cvt_pk_bf16_f32 v163, v70, v71\n"
".LPG1_%=:\n"
"s_cbranch_execz .LPGR_%=\n"
"v_pk_mul_f32 v[72:73], v[72:73], v[196:197]\n"
"v_pk_mul_f32 v[74:75], v[74:75], v[196:197]\n"
"v_pk_mul_f32 v[76:77], v[76:77], v[196:197]\n"
"v_pk_mul_f32 v[78:79], v[78:79], v[196:197]\n"
"v_cvt_pk_bf16_f32 v164, v72, v73\n"
"v_cvt_pk_bf16_f32 v165, v74, v75\n"
"v_cvt_pk_bf16_f32 v166, v76, v77\n"
"v_cvt_pk_bf16_f32 v167, v78, v79\n"
"ds_write_b128 v231, v[160:163] offset:8192\n"
"ds_write_b128 v231, v[164:167] offset:9216\n"
"v_pk_mul_f32 v[80:81], v[80:81], v[196:197]\n"
"v_pk_mul_f32 v[82:83], v[82:83], v[196:197]\n"
"v_pk_mul_f32 v[84:85], v[84:85], v[196:197]\n"
"v_pk_mul_f32 v[86:87], v[86:87], v[196:197]\n"
"v_cvt_pk_bf16_f32 v168, v80, v81\n"
"v_cvt_pk_bf16_f32 v169, v82, v83\n"
"v_cvt_pk_bf16_f32 v170, v84, v85\n"
"v_cvt_pk_bf16_f32 v171, v86, v87\n"
"v_pk_mul_f32 v[88:89], v[88:89], v[196:197]\n"
"v_pk_mul_f32 v[90:91], v[90:91], v[196:197]\n"
"v_pk_mul_f32 v[92:93], v[92:93], v[196:197]\n"
"v_pk_mul_f32 v[94:95], v[94:95], v[196:197]\n"
"v_cvt_pk_bf16_f32 v172, v88, v89\n"
"v_cvt_pk_bf16_f32 v173, v90, v91\n"
"v_cvt_pk_bf16_f32 v174, v92, v93\n"
"v_cvt_pk_bf16_f32 v175, v94, v95\n"
"ds_write_b128 v231, v[168:171] offset:10240\n"
"ds_write_b128 v231, v[172:175] offset:11264\n"
"v_pk_mul_f32 v[96:97], v[96:97], v[196:197]\n"
"v_pk_mul_f32 v[98:99], v[98:99], v[196:197]\n"
"v_pk_mul_f32 v[100:101], v[100:101], v[196:197]\n"
"v_pk_mul_f32 v[102:103], v[102:103], v[196:197]\n"
"v_cvt_pk_bf16_f32 v176, v96, v97\n"
"v_cvt_pk_bf16_f32 v177, v98, v99\n"
"v_cvt_pk_bf16_f32 v178, v100, v101\n"
"v_cvt_pk_bf16_f32 v179, v102, v103\n"
"v_pk_mul_f32 v[104:105], v[104:105], v[196:197]\n"
"v_pk_mul_f32 v[106:107], v[106:107], v[196:197]\n"
"v_pk_mul_f32 v[108:109], v[108:109], v[196:197]\n"
"v_pk_mul_f32 v[110:111], v[110:111], v[196:197]\n"
"v_cvt_pk_bf16_f32 v180, v104, v105\n"
"v_cvt_pk_bf16_f32 v181, v106, v107\n"
"v_cvt_pk_bf16_f32 v182, v108, v109\n"
"v_cvt_pk_bf16_f32 v183, v110, v111\n"
"ds_write_b128 v231, v[176:179] offset:12288\n"
"ds_write_b128 v231, v[180:183] offset:13312\n"
"v_pk_mul_f32 v[112:113], v[112:113], v[196:197]\n"
"v_pk_mul_f32 v[114:115], v[114:115], v[196:197]\n"
"v_pk_mul_f32 v[116:117], v[116:117], v[196:197]\n"
"v_pk_mul_f32 v[118:119], v[118:119], v[196:197]\n"
"v_cvt_pk_bf16_f32 v184, v112, v113\n"
"v_cvt_pk_bf16_f32 v185, v114, v115\n"
"v_cvt_pk_bf16_f32 v186, v116, v117\n"
"v_cvt_pk_bf16_f32 v187, v118, v119\n"
"v_pk_mul_f32 v[120:121], v[120:121], v[196:197]\n"
"v_pk_mul_f32 v[122:123], v[122:123], v[196:197]\n"
"v_pk_mul_f32 v[124:125], v[124:125], v[196:197]\n"
"v_pk_mul_f32 v[126:127], v[126:127], v[196:197]\n"
"v_cvt_pk_bf16_f32 v188, v120, v121\n"
"v_cvt_pk_bf16_f32 v189, v122, v123\n"
"v_cvt_pk_bf16_f32 v190, v124, v125\n"
"v_cvt_pk_bf16_f32 v191, v126, v127\n"
"ds_write_b128 v231, v[184:187] offset:14336\n"
"ds_write_b128 v231, v[188:191] offset:15360\n"
"s_waitcnt vmcnt(17)\n"
"ds_read_b64_tr_b16 v[64:65], v246 offset:0\n"
"ds_read_b64_tr_b16 v[66:67], v246 offset:2048\n"
"ds_read_b64_tr_b16 v[68:69], v246 offset:4096\n"
"ds_read_b64_tr_b16 v[70:71], v246 offset:6144\n"
"ds_read_b64_tr_b16 v[72:73], v247 offset:0\n"
"ds_read_b64_tr_b16 v[74:75], v247 offset:2048\n"
"ds_read_b64_tr_b16 v[76:77], v247 offset:4096\n"
"ds_read_b64_tr_b16 v[78:79], v247 offset:6144\n"
"ds_read_b64_tr_b16 v[80:81], v248 offset:0\n"
"ds_read_b64_tr_b16 v[82:83], v248 offset:2048\n"
"ds_read_b64_tr_b16 v[84:85], v248 offset:4096\n"
"ds_read_b64_tr_b16 v[86:87], v248 offset:6144\n"
"s_waitcnt lgkmcnt(12)\n"
"s_barrier\n"
"ds_read_b64_tr_b16 v[88:89], v249 offset:0\n"
"ds_read_b64_tr_b16 v[90:91], v249 offset:2048\n"
"ds_read_b64_tr_b16 v[92:93], v249 offset:4096\n"
"ds_read_b64_tr_b16 v[94:95], v249 offset:6144\n"
"ds_read_b128 a[0:3], v230 offset:0\n"
"ds_read_b128 a[16:19], v230 offset:1024\n"
"ds_read_b128 a[4:7], v230 offset:16384\n"
"ds_read_b128 a[20:23], v230 offset:17408\n"
"ds_read_b128 a[8:11], v230 offset:32768\n"
"ds_read_b128 a[24:27], v230 offset:33792\n"
"ds_read_b128 a[12:15], v230 offset:49152\n"
"ds_read_b128 a[28:31], v230 offset:50176\n"
"s_mov_b32 s93, 0x3e0293ee\n"
"s_cmp_lg_u32 s47, 1\n"
"s_cbranch_scc1 .LOTH_%=\n"
".LL1_%=:\n"
"s_mov_b32 s100, 57344\n"
"s_mov_b32 s101, 43008\n"
"s_waitcnt lgkmcnt(6)\n"
"v_mfma_f32_16x16x32_bf16 v[0:3], v[64:67], a[0:3], 0\n"
"s_waitcnt vmcnt(9)\n"
"ds_read_b64_tr_b16 v[96:97], v224 offset:0\n"
"ds_read_b128 a[32:35], v230 offset:2048\n"
"v_mfma_f32_16x16x32_bf16 v[0:3], v[68:71], a[16:19], v[0:3]\n"
"s_add_u32 s80, s84, 688128\n"
"s_waitcnt lgkmcnt(6)\n"
"ds_read_b128 a[36:39], v230 offset:18432\n"
"v_mfma_f32_16x16x32_bf16 v[4:7], v[68:71], a[20:23], 0\n"
"s_add_u32 m0, s46, 0\n"
"ds_read_b64_tr_b16 v[98:99], v224 offset:2048\n"
"ds_read_b128 a[40:43], v230 offset:34816\n"
"v_mfma_f32_16x16x32_bf16 v[4:7], v[64:67], a[4:7], v[4:7]\n"
"s_waitcnt lgkmcnt(7)\n"
"buffer_load_dwordx4 v228, s[52:55], s80 offen lds\n"
"v_mfma_f32_16x16x32_bf16 v[8:11], v[64:67], a[8:11], 0\n"
"s_add_u32 s80, s80, s100\n"
"ds_read_b64_tr_b16 v[100:101], v224 offset:4096\n"
"ds_read_b128 a[44:47], v230 offset:51200\n"
"v_mfma_f32_16x16x32_bf16 v[8:11], v[68:71], a[24:27], v[8:11]\n"
"s_addk_i32 m0, 1024\n"
"s_waitcnt lgkmcnt(7)\n"
"ds_read_b128 a[48:51], v230 offset:3072\n"
"v_mfma_f32_16x16x32_bf16 v[12:15], v[68:71], a[28:31], 0\n"
"ds_read_b64_tr_b16 v[102:103], v224 offset:6144\n"
"ds_read_b128 a[52:55], v230 offset:19456\n"
"v_mfma_f32_16x16x32_bf16 v[12:15], v[64:67], a[12:15], v[12:15]\n"
"s_waitcnt lgkmcnt(14)\n"
"buffer_load_dwordx4 v228, s[52:55], s80 offen lds\n"
"v_mfma_f32_16x16x32_bf16 v[28:31], v[72:75], a[12:15], 0\n"
"s_sub_u32 s80, s80, s101\n"
"ds_read_b64_tr_b16 v[104:105], v225 offset:0\n"
"ds_read_b128 a[56:59], v230 offset:35840\n"
"v_mfma_f32_16x16x32_bf16 v[28:31], v[76:79], a[28:31], v[28:31]\n"
"s_addk_i32 m0, 1024\n"
"ds_read_b128 a[60:63], v230 offset:52224\n"
"ds_read_b128 a[64:67], v230 offset:4096\n"
"v_mfma_f32_16x16x32_bf16 v[24:27], v[76:79], a[24:27], 0\n"
"ds_read_b64_tr_b16 v[106:107], v225 offset:2048\n"
"ds_read_b128 a[68:71], v230 offset:20480\n"
"v_mfma_f32_16x16x32_bf16 v[24:27], v[72:75], a[8:11], v[24:27]\n"
"buffer_load_dwordx4 v228, s[52:55], s80 offen lds\n"
"v_mfma_f32_16x16x32_bf16 v[20:23], v[72:75], a[4:7], 0\n"
"s_add_u32 s80, s80, s100\n"
"ds_read_b64_tr_b16 v[108:109], v225 offset:4096\n"
"ds_read_b128 a[72:75], v230 offset:36864\n"
"v_mfma_f32_16x16x32_bf16 v[20:23], v[76:79], a[20:23], v[20:23]\n"
"s_addk_i32 m0, 1024\n"
"ds_read_b128 a[76:79], v230 offset:53248\n"
"ds_read_b128 a[80:83], v230 offset:5120\n"
"v_mfma_f32_16x16x32_bf16 v[16:19], v[76:79], a[16:19], 0\n"
"ds_read_b64_tr_b16 v[110:111], v225 offset:6144\n"
"ds_read_b128 a[84:87], v230 offset:21504\n"
"v_mfma_f32_16x16x32_bf16 v[16:19], v[72:75], a[0:3], v[16:19]\n"
"s_waitcnt lgkmcnt(14)\n"
"buffer_load_dwordx4 v228, s[52:55], s80 offen lds\n"
"v_mfma_f32_16x16x32_bf16 v[32:35], v[80:83], a[0:3], 0\n"
"s_add_u32 s80, s80, s101\n"
"ds_read_b64_tr_b16 v[112:113], v226 offset:0\n"
"ds_read_b128 a[88:91], v230 offset:37888\n"
"v_mfma_f32_16x16x32_bf16 v[32:35], v[84:87], a[16:19], v[32:35]\n"
"s_addk_i32 m0, 1024\n"
"ds_read_b128 a[92:95], v230 offset:54272\n"
"ds_read_b128 a[96:99], v230 offset:6144\n"
"v_mfma_f32_16x16x32_bf16 v[36:39], v[84:87], a[20:23], 0\n"
"ds_read_b64_tr_b16 v[114:115], v226 offset:2048\n"
"ds_read_b128 a[100:103], v230 offset:22528\n"
"v_mfma_f32_16x16x32_bf16 v[36:39], v[80:83], a[4:7], v[36:39]\n"
"buffer_load_dwordx4 v228, s[52:55], s80 offen lds\n"
"v_mfma_f32_16x16x32_bf16 v[40:43], v[80:83], a[8:11], 0\n"
"s_add_u32 s80, s80, s100\n"
"ds_read_b64_tr_b16 v[116:117], v226 offset:4096\n"
"ds_read_b128 a[104:107], v230 offset:38912\n"
"v_mfma_f32_16x16x32_bf16 v[40:43], v[84:87], a[24:27], v[40:43]\n"
"s_addk_i32 m0, 1024\n"
"ds_read_b128 a[108:111], v230 offset:55296\n"
"ds_read_b128 a[112:115], v230 offset:7168\n"
"v_mfma_f32_16x16x32_bf16 v[44:47], v[84:87], a[28:31], 0\n"
"ds_read_b64_tr_b16 v[118:119], v226 offset:6144\n"
"ds_read_b128 a[116:119], v230 offset:23552\n"
"v_mfma_f32_16x16x32_bf16 v[44:47], v[80:83], a[12:15], v[44:47]\n"
"s_waitcnt lgkmcnt(14)\n"
"buffer_load_dwordx4 v228, s[52:55], s80 offen lds\n"
"v_mfma_f32_16x16x32_bf16 v[60:63], v[88:91], a[12:15], 0\n"
"s_sub_u32 s80, s80, s101\n"
"ds_read_b64_tr_b16 v[120:121], v227 offset:0\n"
"ds_read_b128 a[120:123], v230 offset:39936\n"
"v_mfma_f32_16x16x32_bf16 v[60:63], v[92:95], a[28:31], v[60:63]\n"
"s_addk_i32 m0, 1024\n"
"ds_read_b128 a[124:127], v230 offset:56320\n"
"ds_read_b128 a[128:131], v230 offset:8192\n"
"v_mfma_f32_16x16x32_bf16 v[56:59], v[92:95], a[24:27], 0\n"
"ds_read_b64_tr_b16 v[122:123], v227 offset:2048\n"
"ds_read_b128 a[132:135], v230 offset:24576\n"
"v_mfma_f32_16x16x32_bf16 v[56:59], v[88:91], a[8:11], v[56:59]\n"
"buffer_load_dwordx4 v228, s[52:55], s80 offen lds\n"
"v_mfma_f32_16x16x32_bf16 v[52:55], v[88:91], a[4:7], 0\n"
"s_add_u32 s80, s80, s100\n"
"ds_read_b64_tr_b16 v[124:125], v227 offset:4096\n"
"ds_read_b128 a[136:139], v230 offset:40960\n"
"v_mfma_f32_16x16x32_bf16 v[52:55], v[92:95], a[20:23], v[52:55]\n"
"s_addk_i32 m0, 1024\n"
"ds_read_b128 a[140:143], v230 offset:57344\n"
"ds_read_b128 a[144:147], v230 offset:9216\n"
"v_mfma_f32_16x16x32_bf16 v[48:51], v[92:95], a[16:19], 0\n"
"ds_read_b64_tr_b16 v[126:127], v227 offset:6144\n"
"ds_read_b128 a[148:151], v230 offset:25600\n"
"v_mfma_f32_16x16x32_bf16 v[48:51], v[88:91], a[0:3], v[48:51]\n"
"s_waitcnt lgkmcnt(14)\n"
"buffer_load_dwordx4 v228, s[52:55], s80 offen lds\n"
"v_mfma_f32_16x16x32_bf16 v[0:3], v[96:99], a[32:35], v[0:3]\n"
"s_add_u32 s80, s84, 917504\n"
"s_waitcnt lgkmcnt(1)\n"
"ds_read_b128 a[152:155], v230 offset:41984\n"
"v_mfma_f32_16x16x32_bf16 v[0:3], v[100:103], a[48:51], v[0:3]\n"
"s_add_u32 m0, s81, 0\n"
"ds_read_b128 a[156:159], v230 offset:58368\n"
"ds_read_b128 a[160:163], v230 offset:10240\n"
"v_mfma_f32_16x16x32_bf16 v[4:7], v[100:103], a[52:55], v[4:7]\n"
"ds_read_b128 a[164:167], v230 offset:26624\n"
"ds_read_b128 a[168:171], v230 offset:43008\n"
"v_mfma_f32_16x16x32_bf16 v[4:7], v[96:99], a[36:39], v[4:7]\n"
"buffer_load_dwordx4 v228, s[52:55], s80 offen lds\n"
"v_mfma_f32_16x16x32_bf16 v[8:11], v[96:99], a[40:43], v[8:11]\n"
"s_add_u32 s80, s80, s100\n"
"ds_read_b128 a[172:175], v230 offset:59392\n"
"ds_read_b128 a[176:179], v230 offset:11264\n"
"v_mfma_f32_16x16x32_bf16 v[8:11], v[100:103], a[56:59], v[8:11]\n"
"s_addk_i32 m0, 1024\n"
"ds_read_b128 a[180:183], v230 offset:27648\n"
"ds_read_b128 a[184:187], v230 offset:44032\n"
"v_mfma_f32_16x16x32_bf16 v[12:15], v[100:103], a[60:63], v[12:15]\n"
"ds_read_b128 a[188:191], v230 offset:60416\n"
"ds_read_b128 a[192:195], v230 offset:12288\n"
"v_mfma_f32_16x16x32_bf16 v[12:15], v[96:99], a[44:47], v[12:15]\n"
"s_waitcnt lgkmcnt(14)\n"
"buffer_load_dwordx4 v228, s[52:55], s80 offen lds\n"
"v_mfma_f32_16x16x32_bf16 v[28:31], v[104:107], a[44:47], v[28:31]\n"
"s_sub_u32 s80, s80, s101\n"
"ds_read_b128 a[196:199], v230 offset:28672\n"
"ds_read_b128 a[200:203], v230 offset:45056\n"
"v_mfma_f32_16x16x32_bf16 v[28:31], v[108:111], a[60:63], v[28:31]\n"
"s_addk_i32 m0, 1024\n"
"ds_read_b128 a[204:207], v230 offset:61440\n"
"ds_read_b128 a[208:211], v230 offset:13312\n"
"v_mfma_f32_16x16x32_bf16 v[24:27], v[108:111], a[56:59], v[24:27]\n"
"ds_read_b128 a[212:215], v230 offset:29696\n"
"ds_read_b128 a[216:219], v230 offset:46080\n"
"v_mfma_f32_16x16x32_bf16 v[24:27], v[104:107], a[40:43], v[24:27]\n"
"buffer_load_dwordx4 v228, s[52:55], s80 offen lds\n"
"v_mfma_f32_16x16x32_bf16 v[20:23], v[104:107], a[36:39], v[20:23]\n"
"s_add_u32 s80, s80, s100\n"
"ds_read_b128 a[220:223], v230 offset:62464\n"
"ds_read_b128 a[224:227], v230 offset:14336\n"
"v_mfma_f32_16x16x32_bf16 v[20:23], v[108:111], a[52:55], v[20:23]\n"
"s_addk_i32 m0, 1024\n"
"ds_read_b128 a[228:231], v230 offset:30720\n"
"ds_read_b128 a[232:235], v230 offset:47104\n"
"v_mfma_f32_16x16x32_bf16 v[16:19], v[108:111], a[48:51], v[16:19]\n"
"ds_read_b128 a[236:239], v230 offset:63488\n"
"ds_read_b128 a[240:243], v230 offset:15360\n"
"v_mfma_f32_16x16x32_bf16 v[16:19], v[104:107], a[32:35], v[16:19]\n"
"s_waitcnt lgkmcnt(14)\n"
"buffer_load_dwordx4 v228, s[52:55], s80 offen lds\n"
"v_mfma_f32_16x16x32_bf16 v[32:35], v[112:115], a[32:35], v[32:35]\n"
"s_waitcnt vmcnt(13)\n"
"ds_read_b64_tr_b16 v[128:129], v246 offset:8192\n"
"ds_read_b128 a[244:247], v230 offset:31744\n"
"v_mfma_f32_16x16x32_bf16 v[32:35], v[116:119], a[48:51], v[32:35]\n"
"s_add_u32 s80, s80, s101\n"
"ds_read_b128 a[248:251], v230 offset:48128\n"
"ds_read_b128 a[252:255], v230 offset:64512\n"
"v_mfma_f32_16x16x32_bf16 v[36:39], v[116:119], a[52:55], v[36:39]\n"
"s_addk_i32 m0, 1024\n"
"ds_read_b64_tr_b16 v[130:131], v246 offset:10240\n"
"v_mfma_f32_16x16x32_bf16 v[36:39], v[112:115], a[36:39], v[36:39]\n"
"buffer_load_dwordx4 v228, s[52:55], s80 offen lds\n"
"v_mfma_f32_16x16x32_bf16 v[40:43], v[112:115], a[40:43], v[40:43]\n"
"s_add_u32 s80, s80, s100\n"
"ds_read_b64_tr_b16 v[132:133], v246 offset:12288\n"
"v_mfma_f32_16x16x32_bf16 v[40:43], v[116:119], a[56:59], v[40:43]\n"
"s_addk_i32 m0, 1024\n"
"v_mfma_f32_16x16x32_bf16 v[44:47], v[116:119], a[60:63], v[44:47]\n"
"ds_read_b64_tr_b16 v[134:135], v246 offset:14336\n"
"v_mfma_f32_16x16x32_bf16 v[44:47], v[112:115], a[44:47], v[44:47]\n"
"s_waitcnt lgkmcnt(14)\n"
"buffer_load_dwordx4 v228, s[52:55], s80 offen lds\n"
"v_mfma_f32_16x16x32_bf16 v[60:63], v[120:123], a[44:47], v[60:63]\n"
"s_sub_u32 s80, s80, s101\n"
"ds_read_b64_tr_b16 v[136:137], v247 offset:8192\n"
"v_mfma_f32_16x16x32_bf16 v[60:63], v[124:127], a[60:63], v[60:63]\n"
"s_addk_i32 m0, 1024\n"
"v_mfma_f32_16x16x32_bf16 v[56:59], v[124:127], a[56:59], v[56:59]\n"
"ds_read_b64_tr_b16 v[138:139], v247 offset:10240\n"
"v_mfma_f32_16x16x32_bf16 v[56:59], v[120:123], a[40:43], v[56:59]\n"
"buffer_load_dwordx4 v228, s[52:55], s80 offen lds\n"
"v_mfma_f32_16x16x32_bf16 v[52:55], v[120:123], a[36:39], v[52:55]\n"
"s_add_u32 s80, s80, s100\n"
"ds_read_b64_tr_b16 v[140:141], v247 offset:12288\n"
"v_mfma_f32_16x16x32_bf16 v[52:55], v[124:127], a[52:55], v[52:55]\n"
"s_addk_i32 m0, 1024\n"
"v_mfma_f32_16x16x32_bf16 v[48:51], v[124:127], a[48:51], v[48:51]\n"
"ds_read_b64_tr_b16 v[142:143], v247 offset:14336\n"
"v_mfma_f32_16x16x32_bf16 v[48:51], v[120:123], a[32:35], v[48:51]\n"
"buffer_load_dwordx4 v228, s[52:55], s80 offen lds\n"
"s_waitcnt lgkmcnt(0)\n"
"s_barrier\n"
"s_cmp_lt_u32 s77, 2\n"
"s_cselect_b64 s[72:73], s[6:7], s[8:9]\n"
"s_and_b32 s94, s77, 1\n"
"s_mul_i32 s83, s78, 524288\n"
"s_mul_i32 s95, s35, 4096\n"
"s_add_u32 s83, s83, s95\n"
"s_mul_i32 s95, s94, 2048\n"
"s_add_u32 s83, s83, s95\n"
"s_add_u32 s72, s72, s83\n"
"s_addc_u32 s73, s73, 0\n"
"s_mov_b32 s74, 460800\n"
"s_mov_b32 s75, 0x20000\n"
"s_mov_b32 s76, -65536\n"
"v_mfma_f32_16x16x32_bf16 v[0:3], v[128:131], a[64:67], v[0:3]\n"
"s_add_u32 s80, s84, 1146880\n"
"ds_read_b64_tr_b16 v[144:145], v248 offset:8192\n"
"s_mov_b64 s[60:61], s[16:17]\n"
"v_mfma_f32_16x16x32_bf16 v[0:3], v[132:135], a[80:83], v[0:3]\n"
"s_add_u32 m0, s46, 73728\n"
"s_mov_b32 s62, 1835008\n"
"v_mfma_f32_16x16x32_bf16 v[4:7], v[132:135], a[84:87], v[4:7]\n"
"ds_read_b64_tr_b16 v[146:147], v248 offset:10240\n"
"v_mfma_f32_16x16x32_bf16 v[4:7], v[128:131], a[68:71], v[4:7]\n"
"buffer_load_dwordx4 v228, s[52:55], s80 offen lds\n"
"v_mfma_f32_16x16x32_bf16 v[8:11], v[128:131], a[72:75], v[8:11]\n"
"s_add_u32 s80, s80, s100\n"
"ds_read_b64_tr_b16 v[148:149], v248 offset:12288\n"
"s_mov_b32 s63, 0x20000\n"
"v_mfma_f32_16x16x32_bf16 v[8:11], v[132:135], a[88:91], v[8:11]\n"
"s_addk_i32 m0, 1024\n"
"s_mul_i32 s34, s35, 12288\n"
"v_mfma_f32_16x16x32_bf16 v[12:15], v[132:135], a[92:95], v[12:15]\n"
"s_mul_i32 s83, s77, 3072\n"
"ds_read_b64_tr_b16 v[150:151], v248 offset:14336\n"
"v_mfma_f32_16x16x32_bf16 v[12:15], v[128:131], a[76:79], v[12:15]\n"
"buffer_load_dwordx4 v228, s[52:55], s80 offen lds\n"
"v_mfma_f32_16x16x32_bf16 v[28:31], v[136:139], a[76:79], v[28:31]\n"
"s_sub_u32 s80, s80, s101\n"
"ds_read_b64_tr_b16 v[152:153], v249 offset:8192\n"
"s_add_u32 s34, s34, s83\n"
"v_mfma_f32_16x16x32_bf16 v[28:31], v[140:143], a[92:95], v[28:31]\n"
"s_addk_i32 m0, 1024\n"
"v_mfma_f32_16x16x32_bf16 v[24:27], v[140:143], a[88:91], v[24:27]\n"
"s_mul_i32 s83, s23, 32768\n"
"ds_read_b64_tr_b16 v[154:155], v249 offset:10240\n"
"v_mfma_f32_16x16x32_bf16 v[24:27], v[136:139], a[72:75], v[24:27]\n"
"buffer_load_dwordx4 v228, s[52:55], s80 offen lds\n"
"v_mfma_f32_16x16x32_bf16 v[20:23], v[136:139], a[68:71], v[20:23]\n"
"s_add_u32 s80, s80, s100\n"
"ds_read_b64_tr_b16 v[156:157], v249 offset:12288\n"
"s_add_u32 s34, s34, s83\n"
"v_mfma_f32_16x16x32_bf16 v[20:23], v[140:143], a[84:87], v[20:23]\n"
"s_addk_i32 m0, 1024\n"
"v_lshlrev_b32_e32 v235, 2, v255\n"
"v_mfma_f32_16x16x32_bf16 v[16:19], v[140:143], a[80:83], v[16:19]\n"
"ds_read_b64_tr_b16 v[158:159], v249 offset:14336\n"
"v_mfma_f32_16x16x32_bf16 v[16:19], v[136:139], a[64:67], v[16:19]\n"
"s_waitcnt lgkmcnt(4)\n"
"buffer_load_dwordx4 v228, s[52:55], s80 offen lds\n"
"v_mfma_f32_16x16x32_bf16 v[32:35], v[144:147], a[64:67], v[32:35]\n"
"s_waitcnt vmcnt(12)\n"
"ds_read_b64_tr_b16 v[128:129], v246 offset:0\n"
"v_lshlrev_b32_e32 v234, 1, v255\n"
"v_mfma_f32_16x16x32_bf16 v[32:35], v[148:151], a[80:83], v[32:35]\n"
"s_add_u32 s80, s80, s101\n"
"v_add_u32_e32 v250, 65536, v246\n"
"v_mfma_f32_16x16x32_bf16 v[36:39], v[148:151], a[84:87], v[36:39]\n"
"s_addk_i32 m0, 1024\n"
"ds_read_b64_tr_b16 v[130:131], v246 offset:2048\n"
"v_add_u32_e32 v251, 65536, v247\n"
"v_mfma_f32_16x16x32_bf16 v[36:39], v[144:147], a[68:71], v[36:39]\n"
"buffer_load_dwordx4 v228, s[52:55], s80 offen lds\n"
"v_mfma_f32_16x16x32_bf16 v[40:43], v[144:147], a[72:75], v[40:43]\n"
"s_add_u32 s80, s80, s100\n"
"ds_read_b64_tr_b16 v[132:133], v246 offset:4096\n"
"v_add_u32_e32 v252, 65536, v248\n"
"v_mfma_f32_16x16x32_bf16 v[40:43], v[148:151], a[88:91], v[40:43]\n"
"s_addk_i32 m0, 1024\n"
"v_add_u32_e32 v253, 65536, v249\n"
"v_mfma_f32_16x16x32_bf16 v[44:47], v[148:151], a[92:95], v[44:47]\n"
"ds_read_b64_tr_b16 v[134:135], v246 offset:6144\n"
"v_mfma_f32_16x16x32_bf16 v[44:47], v[144:147], a[76:79], v[44:47]\n"
"s_waitcnt lgkmcnt(4)\n"
"buffer_load_dwordx4 v228, s[52:55], s80 offen lds\n"
"v_mfma_f32_16x16x32_bf16 v[60:63], v[152:155], a[76:79], v[60:63]\n"
"s_sub_u32 s80, s80, s101\n"
"ds_read_b64_tr_b16 v[136:137], v247 offset:0\n"
"s_and_b32 s89, s79, 1\n"
"v_mfma_f32_16x16x32_bf16 v[60:63], v[156:159], a[92:95], v[60:63]\n"
"s_addk_i32 m0, 1024\n"
"s_mul_i32 s85, s78, 1024\n"
"v_mfma_f32_16x16x32_bf16 v[56:59], v[156:159], a[88:91], v[56:59]\n"
"s_mul_i32 s83, s77, 256\n"
"ds_read_b64_tr_b16 v[138:139], v247 offset:2048\n"
"v_mfma_f32_16x16x32_bf16 v[56:59], v[152:155], a[72:75], v[56:59]\n"
"buffer_load_dwordx4 v228, s[52:55], s80 offen lds\n"
"v_mfma_f32_16x16x32_bf16 v[52:55], v[152:155], a[68:71], v[52:55]\n"
"s_add_u32 s80, s80, s100\n"
"ds_read_b64_tr_b16 v[140:141], v247 offset:4096\n"
"s_add_u32 s85, s85, s83\n"
"v_mfma_f32_16x16x32_bf16 v[52:55], v[156:159], a[84:87], v[52:55]\n"
"s_addk_i32 m0, 1024\n"
"v_mfma_f32_16x16x32_bf16 v[48:51], v[156:159], a[80:83], v[48:51]\n"
"s_mul_i32 s83, s89, 128\n"
"ds_read_b64_tr_b16 v[142:143], v247 offset:6144\n"
"v_mfma_f32_16x16x32_bf16 v[48:51], v[152:155], a[64:67], v[48:51]\n"
"s_waitcnt lgkmcnt(4)\n"
"buffer_load_dwordx4 v228, s[52:55], s80 offen lds\n"
"v_mfma_f32_16x16x32_bf16 v[0:3], v[128:131], a[96:99], v[0:3]\n"
"s_add_u32 s80, s84, 1376256\n"
"ds_read_b64_tr_b16 v[144:145], v248 offset:0\n"
"s_add_u32 s85, s85, s83\n"
"v_mfma_f32_16x16x32_bf16 v[0:3], v[132:135], a[112:115], v[0:3]\n"
"s_add_u32 m0, s46, 65536\n"
"s_xor_b32 s86, s85, 128\n"
"v_mfma_f32_16x16x32_bf16 v[4:7], v[132:135], a[116:119], v[4:7]\n"
"ds_read_b64_tr_b16 v[146:147], v248 offset:2048\n"
"v_mfma_f32_16x16x32_bf16 v[4:7], v[128:131], a[100:103], v[4:7]\n"
"buffer_load_dwordx4 v228, s[52:55], s80 offen lds\n"
"v_mfma_f32_16x16x32_bf16 v[8:11], v[128:131], a[104:107], v[8:11]\n"
"s_add_u32 s80, s80, s100\n"
"ds_read_b64_tr_b16 v[148:149], v248 offset:4096\n"
"s_mov_b64 s[24:25], s[20:21]\n"
"v_mfma_f32_16x16x32_bf16 v[8:11], v[132:135], a[120:123], v[8:11]\n"
"s_addk_i32 m0, 1024\n"
"s_mov_b32 s26, 50855936\n"
"v_mfma_f32_16x16x32_bf16 v[12:15], v[132:135], a[124:127], v[12:15]\n"
"s_mov_b32 s27, 0x20000\n"
"ds_read_b64_tr_b16 v[150:151], v248 offset:6144\n"
"v_mfma_f32_16x16x32_bf16 v[12:15], v[128:131], a[108:111], v[12:15]\n"
"s_waitcnt lgkmcnt(4)\n"
"buffer_load_dwordx4 v228, s[52:55], s80 offen lds\n"
"v_mfma_f32_16x16x32_bf16 v[28:31], v[136:139], a[108:111], v[28:31]\n"
"s_sub_u32 s80, s80, s101\n"
"ds_read_b64_tr_b16 v[152:153], v249 offset:0\n"
"s_mul_i32 s28, s79, 8192\n"
"v_mfma_f32_16x16x32_bf16 v[28:31], v[140:143], a[124:127], v[28:31]\n"
"s_addk_i32 m0, 1024\n"
"v_mfma_f32_16x16x32_bf16 v[24:27], v[140:143], a[120:123], v[24:27]\n"
"s_mul_i32 s83, s78, 1024\n"
"ds_read_b64_tr_b16 v[154:155], v249 offset:2048\n"
"v_mfma_f32_16x16x32_bf16 v[24:27], v[136:139], a[104:107], v[24:27]\n"
"buffer_load_dwordx4 v228, s[52:55], s80 offen lds\n"
"v_mfma_f32_16x16x32_bf16 v[20:23], v[136:139], a[100:103], v[20:23]\n"
"s_add_u32 s80, s80, s100\n"
"ds_read_b64_tr_b16 v[156:157], v249 offset:4096\n"
"s_add_u32 s28, s28, s83\n"
"v_mfma_f32_16x16x32_bf16 v[20:23], v[140:143], a[116:119], v[20:23]\n"
"s_addk_i32 m0, 1024\n"
"s_mul_i32 s83, s77, 256\n"
"v_mfma_f32_16x16x32_bf16 v[16:19], v[140:143], a[112:115], v[16:19]\n"
"ds_read_b64_tr_b16 v[158:159], v249 offset:6144\n"
"v_mfma_f32_16x16x32_bf16 v[16:19], v[136:139], a[96:99], v[16:19]\n"
"s_waitcnt lgkmcnt(4)\n"
"buffer_load_dwordx4 v228, s[52:55], s80 offen lds\n"
"v_mfma_f32_16x16x32_bf16 v[32:35], v[144:147], a[96:99], v[32:35]\n"
"s_waitcnt vmcnt(12)\n"
"ds_read_b64_tr_b16 v[128:129], v224 offset:0\n"
"s_add_u32 s28, s28, s83\n"
"v_mfma_f32_16x16x32_bf16 v[32:35], v[148:151], a[112:115], v[32:35]\n"
"s_add_u32 s80, s80, s101\n"
"s_add_u32 s28, s28, 50331648\n"
"v_mfma_f32_16x16x32_bf16 v[36:39], v[148:151], a[116:119], v[36:39]\n"
"s_addk_i32 m0, 1024\n"
"ds_read_b64_tr_b16 v[130:131], v224 offset:2048\n"
"s_mul_i32 s29, s79, 8192\n"
"v_mfma_f32_16x16x32_bf16 v[36:39], v[144:147], a[100:103], v[36:39]\n"
"buffer_load_dwordx4 v228, s[52:55], s80 offen lds\n"
"v_mfma_f32_16x16x32_bf16 v[40:43], v[144:147], a[104:107], v[40:43]\n"
"s_add_u32 s80, s80, s100\n"
"ds_read_b64_tr_b16 v[132:133], v224 offset:4096\n"
"s_mul_i32 s83, s77, 2048\n"
"v_mfma_f32_16x16x32_bf16 v[40:43], v[148:151], a[120:123], v[40:43]\n"
"s_addk_i32 m0, 1024\n"
"s_add_u32 s29, s29, s83\n"
"v_mfma_f32_16x16x32_bf16 v[44:47], v[148:151], a[124:127], v[44:47]\n"
"ds_read_b64_tr_b16 v[134:135], v224 offset:6144\n"
"v_mfma_f32_16x16x32_bf16 v[44:47], v[144:147], a[108:111], v[44:47]\n"
"s_waitcnt lgkmcnt(4)\n"
"buffer_load_dwordx4 v228, s[52:55], s80 offen lds\n"
"v_mfma_f32_16x16x32_bf16 v[60:63], v[152:155], a[108:111], v[60:63]\n"
"s_sub_u32 s80, s80, s101\n"
"ds_read_b64_tr_b16 v[136:137], v225 offset:0\n"
"s_mul_i32 s83, s78, 128\n"
"v_mfma_f32_16x16x32_bf16 v[60:63], v[156:159], a[124:127], v[60:63]\n"
"s_addk_i32 m0, 1024\n"
"s_add_u32 s29, s29, s83\n"
"v_mfma_f32_16x16x32_bf16 v[56:59], v[156:159], a[120:123], v[56:59]\n"
"s_add_u32 s29, s29, 50331648\n"
"ds_read_b64_tr_b16 v[138:139], v225 offset:2048\n"
"v_mfma_f32_16x16x32_bf16 v[56:59], v[152:155], a[104:107], v[56:59]\n"
"buffer_load_dwordx4 v228, s[52:55], s80 offen lds\n"
"v_mfma_f32_16x16x32_bf16 v[52:55], v[152:155], a[100:103], v[52:55]\n"
"s_add_u32 s80, s80, s100\n"
"ds_read_b64_tr_b16 v[140:141], v225 offset:4096\n"
"s_add_u32 s30, s28, 0x40000\n"
"v_mfma_f32_16x16x32_bf16 v[52:55], v[156:159], a[116:119], v[52:55]\n"
"s_addk_i32 m0, 1024\n"
"v_mfma_f32_16x16x32_bf16 v[48:51], v[156:159], a[112:115], v[48:51]\n"
"s_add_u32 s31, s29, 0x40000\n"
"ds_read_b64_tr_b16 v[142:143], v225 offset:6144\n"
"v_mfma_f32_16x16x32_bf16 v[48:51], v[152:155], a[96:99], v[48:51]\n"
"s_waitcnt lgkmcnt(4)\n"
"buffer_load_dwordx4 v228, s[52:55], s80 offen lds\n"
"v_mfma_f32_16x16x32_bf16 v[0:3], v[128:131], a[128:131], v[0:3]\n"
"s_add_u32 s80, s84, 1605632\n"
"ds_read_b64_tr_b16 v[144:145], v226 offset:0\n"
"v_mov_b32_e32 v243, 1\n"
"v_mfma_f32_16x16x32_bf16 v[0:3], v[132:135], a[144:147], v[0:3]\n"
"s_add_u32 m0, s46, 8192\n"
"v_add_u32_e32 v233, s81, v255\n"
"v_mfma_f32_16x16x32_bf16 v[4:7], v[132:135], a[148:151], v[4:7]\n"
"ds_read_b64_tr_b16 v[146:147], v226 offset:2048\n"
"v_mfma_f32_16x16x32_bf16 v[4:7], v[128:131], a[132:135], v[4:7]\n"
"buffer_load_dwordx4 v228, s[52:55], s80 offen lds\n"
"v_mfma_f32_16x16x32_bf16 v[8:11], v[128:131], a[136:139], v[8:11]\n"
"s_add_u32 s80, s80, s100\n"
"ds_read_b64_tr_b16 v[148:149], v226 offset:4096\n"
"v_add_u32_e32 v232, 131072, v255\n"
"v_mfma_f32_16x16x32_bf16 v[8:11], v[132:135], a[152:155], v[8:11]\n"
"s_addk_i32 m0, 1024\n"
"v_add_u32_e32 v204, 0, v202\n"
"v_mfma_f32_16x16x32_bf16 v[12:15], v[132:135], a[156:159], v[12:15]\n"
"ds_read_b64_tr_b16 v[150:151], v226 offset:6144\n"
"v_xor_b32_e32 v205, v204, v201\n"
"v_mfma_f32_16x16x32_bf16 v[12:15], v[128:131], a[140:143], v[12:15]\n"
"s_waitcnt lgkmcnt(4)\n"
"buffer_load_dwordx4 v228, s[52:55], s80 offen lds\n"
"v_mfma_f32_16x16x32_bf16 v[28:31], v[136:139], a[140:143], v[28:31]\n"
"s_sub_u32 s80, s80, s101\n"
"ds_read_b64_tr_b16 v[152:153], v227 offset:0\n"
"v_lshlrev_b32_e32 v205, 4, v205\n"
"v_mfma_f32_16x16x32_bf16 v[28:31], v[140:143], a[156:159], v[28:31]\n"
"s_addk_i32 m0, 1024\n"
"v_mfma_f32_16x16x32_bf16 v[24:27], v[140:143], a[152:155], v[24:27]\n"
"ds_read_b64_tr_b16 v[154:155], v227 offset:2048\n"
"v_lshl_add_u32 v236, v202, 8, v205\n"
"v_mfma_f32_16x16x32_bf16 v[24:27], v[136:139], a[136:139], v[24:27]\n"
"buffer_load_dwordx4 v228, s[52:55], s80 offen lds\n"
"v_mfma_f32_16x16x32_bf16 v[20:23], v[136:139], a[132:135], v[20:23]\n"
"s_add_u32 s80, s80, s100\n"
"ds_read_b64_tr_b16 v[156:157], v227 offset:4096\n"
"v_lshlrev_b32_e32 v205, 1, v204\n"
"v_mfma_f32_16x16x32_bf16 v[20:23], v[140:143], a[148:151], v[20:23]\n"
"s_addk_i32 m0, 1024\n"
"v_xor_b32_e32 v205, v205, v201\n"
"v_mfma_f32_16x16x32_bf16 v[16:19], v[140:143], a[144:147], v[16:19]\n"
"ds_read_b64_tr_b16 v[158:159], v227 offset:6144\n"
"v_mfma_f32_16x16x32_bf16 v[16:19], v[136:139], a[128:131], v[16:19]\n"
"s_waitcnt lgkmcnt(4)\n"
"buffer_load_dwordx4 v228, s[52:55], s80 offen lds\n"
"v_mfma_f32_16x16x32_bf16 v[32:35], v[144:147], a[128:131], v[32:35]\n"
"s_waitcnt vmcnt(12)\n"
"ds_read_b64_tr_b16 v[128:129], v250 offset:8192\n"
"v_lshlrev_b32_e32 v205, 4, v205\n"
"v_mfma_f32_16x16x32_bf16 v[32:35], v[148:151], a[144:147], v[32:35]\n"
"s_add_u32 s80, s80, s101\n"
"v_lshl_add_u32 v240, v202, 8, v205\n"
"v_mfma_f32_16x16x32_bf16 v[36:39], v[148:151], a[148:151], v[36:39]\n"
"s_addk_i32 m0, 1024\n"
"ds_read_b64_tr_b16 v[130:131], v250 offset:10240\n"
"v_add_u32_e32 v204, 4, v202\n"
"v_mfma_f32_16x16x32_bf16 v[36:39], v[144:147], a[132:135], v[36:39]\n"
"buffer_load_dwordx4 v228, s[52:55], s80 offen lds\n"
"v_mfma_f32_16x16x32_bf16 v[40:43], v[144:147], a[136:139], v[40:43]\n"
"s_add_u32 s80, s80, s100\n"
"ds_read_b64_tr_b16 v[132:133], v250 offset:12288\n"
"v_xor_b32_e32 v205, v204, v201\n"
"v_mfma_f32_16x16x32_bf16 v[40:43], v[148:151], a[152:155], v[40:43]\n"
"s_addk_i32 m0, 1024\n"
"v_lshlrev_b32_e32 v205, 4, v205\n"
"v_mfma_f32_16x16x32_bf16 v[44:47], v[148:151], a[156:159], v[44:47]\n"
"ds_read_b64_tr_b16 v[134:135], v250 offset:14336\n"
"v_mfma_f32_16x16x32_bf16 v[44:47], v[144:147], a[140:143], v[44:47]\n"
".LPG2_%=:\n"
"s_cbranch_execz .LPGR_%=\n"
"s_waitcnt lgkmcnt(4)\n"
"buffer_load_dwordx4 v228, s[52:55], s80 offen lds\n"
"v_mfma_f32_16x16x32_bf16 v[60:63], v[152:155], a[140:143], v[60:63]\n"
"s_sub_u32 s80, s80, s101\n"
"ds_read_b64_tr_b16 v[136:137], v251 offset:8192\n"
"v_lshl_add_u32 v237, v202, 8, v205\n"
"v_mfma_f32_16x16x32_bf16 v[60:63], v[156:159], a[156:159], v[60:63]\n"
"s_addk_i32 m0, 1024\n"
"v_lshlrev_b32_e32 v205, 1, v204\n"
"v_mfma_f32_16x16x32_bf16 v[56:59], v[156:159], a[152:155], v[56:59]\n"
"ds_read_b64_tr_b16 v[138:139], v251 offset:10240\n"
"v_xor_b32_e32 v205, v205, v201\n"
"v_mfma_f32_16x16x32_bf16 v[56:59], v[152:155], a[136:139], v[56:59]\n"
"buffer_load_dwordx4 v228, s[52:55], s80 offen lds\n"
"v_mfma_f32_16x16x32_bf16 v[52:55], v[152:155], a[132:135], v[52:55]\n"
"s_add_u32 s80, s80, s100\n"
"ds_read_b64_tr_b16 v[140:141], v251 offset:12288\n"
"v_lshlrev_b32_e32 v205, 4, v205\n"
"v_mfma_f32_16x16x32_bf16 v[52:55], v[156:159], a[148:151], v[52:55]\n"
"s_addk_i32 m0, 1024\n"
"v_mfma_f32_16x16x32_bf16 v[48:51], v[156:159], a[144:147], v[48:51]\n"
"ds_read_b64_tr_b16 v[142:143], v251 offset:14336\n"
"v_lshl_add_u32 v241, v202, 8, v205\n"
"v_mfma_f32_16x16x32_bf16 v[48:51], v[152:155], a[128:131], v[48:51]\n"
"s_waitcnt lgkmcnt(4)\n"
"buffer_load_dwordx4 v228, s[52:55], s80 offen lds\n"
"s_mov_b32 s100, 131072\n"
"s_mov_b32 s101, 98304\n"
"v_mfma_f32_16x16x32_bf16 v[0:3], v[128:131], a[160:163], v[0:3]\n"
"s_add_u32 s80, s85, 0\n"
"ds_read_b64_tr_b16 v[144:145], v252 offset:8192\n"
"v_add_u32_e32 v204, 8, v202\n"
"v_mfma_f32_16x16x32_bf16 v[0:3], v[132:135], a[176:179], v[0:3]\n"
"s_add_u32 m0, s81, 0\n"
"v_xor_b32_e32 v205, v204, v201\n"
"v_mfma_f32_16x16x32_bf16 v[4:7], v[132:135], a[180:183], v[4:7]\n"
"ds_read_b64_tr_b16 v[146:147], v252 offset:10240\n"
"v_mfma_f32_16x16x32_bf16 v[4:7], v[128:131], a[164:167], v[4:7]\n"
"buffer_load_dwordx4 v229, s[56:59], s80 offen lds\n"
"v_mfma_f32_16x16x32_bf16 v[8:11], v[128:131], a[168:171], v[8:11]\n"
"s_add_u32 s80, s80, s100\n"
"ds_read_b64_tr_b16 v[148:149], v252 offset:12288\n"
"v_lshlrev_b32_e32 v205, 4, v205\n"
"v_mfma_f32_16x16x32_bf16 v[8:11], v[132:135], a[184:187], v[8:11]\n"
"s_addk_i32 m0, 1024\n"
"v_lshl_add_u32 v238, v202, 8, v205\n"
"v_mfma_f32_16x16x32_bf16 v[12:15], v[132:135], a[188:191], v[12:15]\n"
"ds_read_b64_tr_b16 v[150:151], v252 offset:14336\n"
"v_add_u32_e32 v204, 12, v202\n"
"v_mfma_f32_16x16x32_bf16 v[12:15], v[128:131], a[172:175], v[12:15]\n"
"s_waitcnt lgkmcnt(4)\n"
"buffer_load_dwordx4 v229, s[56:59], s80 offen lds\n"
"v_mfma_f32_16x16x32_bf16 v[28:31], v[136:139], a[172:175], v[28:31]\n"
"s_sub_u32 s80, s80, s101\n"
"ds_read_b64_tr_b16 v[152:153], v253 offset:8192\n"
"v_xor_b32_e32 v205, v204, v201\n"
"v_mfma_f32_16x16x32_bf16 v[28:31], v[140:143], a[188:191], v[28:31]\n"
"s_addk_i32 m0, 1024\n"
"v_mfma_f32_16x16x32_bf16 v[24:27], v[140:143], a[184:187], v[24:27]\n"
"ds_read_b64_tr_b16 v[154:155], v253 offset:10240\n"
"v_lshlrev_b32_e32 v205, 4, v205\n"
"v_mfma_f32_16x16x32_bf16 v[24:27], v[136:139], a[168:171], v[24:27]\n"
"buffer_load_dwordx4 v229, s[56:59], s80 offen lds\n"
"v_mfma_f32_16x16x32_bf16 v[20:23], v[136:139], a[164:167], v[20:23]\n"
"s_add_u32 s80, s80, s100\n"
"ds_read_b64_tr_b16 v[156:157], v253 offset:12288\n"
"v_lshl_add_u32 v239, v202, 8, v205\n"
"v_mfma_f32_16x16x32_bf16 v[20:23], v[140:143], a[180:183], v[20:23]\n"
"s_addk_i32 m0, 1024\n"
"v_bfe_u32 v196, v200, 3, 2\n"
"v_mfma_f32_16x16x32_bf16 v[16:19], v[140:143], a[176:179], v[16:19]\n"
"ds_read_b64_tr_b16 v[158:159], v253 offset:14336\n"
"v_mfma_f32_16x16x32_bf16 v[16:19], v[136:139], a[160:163], v[16:19]\n"
"s_waitcnt lgkmcnt(4)\n"
"buffer_load_dwordx4 v229, s[56:59], s80 offen lds\n"
"v_mfma_f32_16x16x32_bf16 v[32:35], v[144:147], a[160:163], v[32:35]\n"
"s_waitcnt vmcnt(12)\n"
"ds_read_b64_tr_b16 v[128:129], v250 offset:0\n"
"v_lshlrev_b32_e32 v196, 1, v196\n"
"v_mfma_f32_16x16x32_bf16 v[32:35], v[148:151], a[176:179], v[32:35]\n"
"s_add_u32 s80, s80, s101\n"
"v_and_b32_e32 v197, 3, v200\n"
"v_mfma_f32_16x16x32_bf16 v[36:39], v[148:151], a[180:183], v[36:39]\n"
"s_addk_i32 m0, 1024\n"
"ds_read_b64_tr_b16 v[130:131], v250 offset:2048\n"
"v_xor_b32_e32 v196, v196, v197\n"
"v_mfma_f32_16x16x32_bf16 v[36:39], v[144:147], a[164:167], v[36:39]\n"
"buffer_load_dwordx4 v229, s[56:59], s80 offen lds\n"
"v_mfma_f32_16x16x32_bf16 v[40:43], v[144:147], a[168:171], v[40:43]\n"
"s_add_u32 s80, s80, s100\n"
"ds_read_b64_tr_b16 v[132:133], v250 offset:4096\n"
"v_lshlrev_b32_e32 v196, 4, v196\n"
"v_mfma_f32_16x16x32_bf16 v[40:43], v[148:151], a[184:187], v[40:43]\n"
"s_addk_i32 m0, 1024\n"
"v_lshrrev_b32_e32 v197, 2, v200\n"
"v_mfma_f32_16x16x32_bf16 v[44:47], v[148:151], a[188:191], v[44:47]\n"
"ds_read_b64_tr_b16 v[134:135], v250 offset:6144\n"
"v_mfma_f32_16x16x32_bf16 v[44:47], v[144:147], a[172:175], v[44:47]\n"
"s_waitcnt lgkmcnt(4)\n"
"buffer_load_dwordx4 v229, s[56:59], s80 offen lds\n"
"v_mfma_f32_16x16x32_bf16 v[60:63], v[152:155], a[172:175], v[60:63]\n"
"s_sub_u32 s80, s80, s101\n"
"ds_read_b64_tr_b16 v[136:137], v251 offset:0\n"
"v_lshl_add_u32 v197, v197, 7, v196\n"
"v_mfma_f32_16x16x32_bf16 v[60:63], v[156:159], a[188:191], v[60:63]\n"
"s_addk_i32 m0, 1024\n"
"v_add_u32_e32 v196, s46, v197\n"
"v_mfma_f32_16x16x32_bf16 v[56:59], v[156:159], a[184:187], v[56:59]\n"
"ds_read_b64_tr_b16 v[138:139], v251 offset:2048\n"
"v_add_u32_e32 v206, s81, v197\n"
"v_mfma_f32_16x16x32_bf16 v[56:59], v[152:155], a[168:171], v[56:59]\n"
"buffer_load_dwordx4 v229, s[56:59], s80 offen lds\n"
"v_mfma_f32_16x16x32_bf16 v[52:55], v[152:155], a[164:167], v[52:55]\n"
"s_add_u32 s80, s80, s100\n"
"ds_read_b64_tr_b16 v[140:141], v251 offset:4096\n"
"v_xor_b32_e32 v197, 64, v196\n"
"v_mfma_f32_16x16x32_bf16 v[52:55], v[156:159], a[180:183], v[52:55]\n"
"s_addk_i32 m0, 1024\n"
"v_mfma_f32_16x16x32_bf16 v[48:51], v[156:159], a[176:179], v[48:51]\n"
"ds_read_b64_tr_b16 v[142:143], v251 offset:6144\n"
"v_add_u32_e32 v198, 65536, v196\n"
"v_mfma_f32_16x16x32_bf16 v[48:51], v[152:155], a[160:163], v[48:51]\n"
"s_waitcnt lgkmcnt(4)\n"
"buffer_load_dwordx4 v229, s[56:59], s80 offen lds\n"
"v_mfma_f32_16x16x32_bf16 v[0:3], v[128:131], a[192:195], v[0:3]\n"
"s_add_u32 s80, s85, 524288\n"
"ds_read_b64_tr_b16 v[144:145], v252 offset:0\n"
"v_add_u32_e32 v199, 65536, v197\n"
"v_mfma_f32_16x16x32_bf16 v[0:3], v[132:135], a[208:211], v[0:3]\n"
"s_add_u32 m0, s46, 73728\n"
"v_xor_b32_e32 v207, 64, v206\n"
"v_mfma_f32_16x16x32_bf16 v[4:7], v[132:135], a[212:215], v[4:7]\n"
"ds_read_b64_tr_b16 v[146:147], v252 offset:2048\n"
"v_mfma_f32_16x16x32_bf16 v[4:7], v[128:131], a[196:199], v[4:7]\n"
"buffer_load_dwordx4 v229, s[56:59], s80 offen lds\n"
"v_mfma_f32_16x16x32_bf16 v[8:11], v[128:131], a[200:203], v[8:11]\n"
"s_add_u32 s80, s80, s100\n"
"ds_read_b64_tr_b16 v[148:149], v252 offset:4096\n"
"s_mul_i32 s87, s23, 32768\n"
"v_mfma_f32_16x16x32_bf16 v[8:11], v[132:135], a[216:219], v[8:11]\n"
"s_addk_i32 m0, 1024\n"
"s_mul_i32 s83, s77, 256\n"
"v_mfma_f32_16x16x32_bf16 v[12:15], v[132:135], a[220:223], v[12:15]\n"
"s_add_u32 s87, s87, s83\n"
"ds_read_b64_tr_b16 v[150:151], v252 offset:6144\n"
"v_mfma_f32_16x16x32_bf16 v[12:15], v[128:131], a[204:207], v[12:15]\n"
"s_waitcnt lgkmcnt(4)\n"
"buffer_load_dwordx4 v229, s[56:59], s80 offen lds\n"
"v_mfma_f32_16x16x32_bf16 v[28:31], v[136:139], a[204:207], v[28:31]\n"
"s_sub_u32 s80, s80, s101\n"
"ds_read_b64_tr_b16 v[152:153], v253 offset:0\n"
"s_mul_i32 s83, s89, 128\n"
"v_mfma_f32_16x16x32_bf16 v[28:31], v[140:143], a[220:223], v[28:31]\n"
"s_addk_i32 m0, 1024\n"
"v_mfma_f32_16x16x32_bf16 v[24:27], v[140:143], a[216:219], v[24:27]\n"
"s_add_u32 s87, s87, s83\n"
"ds_read_b64_tr_b16 v[154:155], v253 offset:2048\n"
"v_mfma_f32_16x16x32_bf16 v[24:27], v[136:139], a[200:203], v[24:27]\n"
"buffer_load_dwordx4 v229, s[56:59], s80 offen lds\n"
"v_mfma_f32_16x16x32_bf16 v[20:23], v[136:139], a[196:199], v[20:23]\n"
"s_add_u32 s80, s80, s100\n"
"ds_read_b64_tr_b16 v[156:157], v253 offset:4096\n"
"s_xor_b32 s88, s87, 128\n"
"v_mfma_f32_16x16x32_bf16 v[20:23], v[140:143], a[212:215], v[20:23]\n"
"s_addk_i32 m0, 1024\n"
"s_mov_b64 s[36:37], s[6:7]\n"
"v_mfma_f32_16x16x32_bf16 v[16:19], v[140:143], a[208:211], v[16:19]\n"
"ds_read_b64_tr_b16 v[158:159], v253 offset:6144\n"
"v_mfma_f32_16x16x32_bf16 v[16:19], v[136:139], a[192:195], v[16:19]\n"
"s_waitcnt lgkmcnt(4)\n"
"buffer_load_dwordx4 v229, s[56:59], s80 offen lds\n"
"v_mfma_f32_16x16x32_bf16 v[32:35], v[144:147], a[192:195], v[32:35]\n"
"s_waitcnt vmcnt(12)\n"
"ds_read_b64_tr_b16 v[128:129], v246 offset:8192\n"
"s_mov_b32 s38, 4194304\n"
"v_mfma_f32_16x16x32_bf16 v[32:35], v[148:151], a[208:211], v[32:35]\n"
"s_add_u32 s80, s80, s101\n"
"s_mov_b32 s39, 0x20000\n"
"v_mfma_f32_16x16x32_bf16 v[36:39], v[148:151], a[212:215], v[36:39]\n"
"s_addk_i32 m0, 1024\n"
"ds_read_b64_tr_b16 v[130:131], v246 offset:10240\n"
"s_mov_b64 s[40:41], s[8:9]\n"
"v_mfma_f32_16x16x32_bf16 v[36:39], v[144:147], a[196:199], v[36:39]\n"
"buffer_load_dwordx4 v229, s[56:59], s80 offen lds\n"
"v_mfma_f32_16x16x32_bf16 v[40:43], v[144:147], a[200:203], v[40:43]\n"
"s_add_u32 s80, s80, s100\n"
"ds_read_b64_tr_b16 v[132:133], v246 offset:12288\n"
"s_mov_b32 s42, 4194304\n"
"v_mfma_f32_16x16x32_bf16 v[40:43], v[148:151], a[216:219], v[40:43]\n"
"s_addk_i32 m0, 1024\n"
"s_mov_b32 s43, 0x20000\n"
"v_mfma_f32_16x16x32_bf16 v[44:47], v[148:151], a[220:223], v[44:47]\n"
"ds_read_b64_tr_b16 v[134:135], v246 offset:14336\n"
"v_mfma_f32_16x16x32_bf16 v[44:47], v[144:147], a[204:207], v[44:47]\n"
"s_waitcnt lgkmcnt(4)\n"
"buffer_load_dwordx4 v229, s[56:59], s80 offen lds\n"
"v_mfma_f32_16x16x32_bf16 v[60:63], v[152:155], a[204:207], v[60:63]\n"
"s_sub_u32 s80, s80, s101\n"
"ds_read_b64_tr_b16 v[136:137], v247 offset:8192\n"
"s_mul_i32 s44, s78, 524288\n"
"v_mfma_f32_16x16x32_bf16 v[60:63], v[156:159], a[220:223], v[60:63]\n"
"s_addk_i32 m0, 1024\n"
"s_mul_i32 s83, s77, 4096\n"
"v_mfma_f32_16x16x32_bf16 v[56:59], v[156:159], a[216:219], v[56:59]\n"
"s_add_u32 s44, s44, s83\n"
"ds_read_b64_tr_b16 v[138:139], v247 offset:10240\n"
"v_mfma_f32_16x16x32_bf16 v[56:59], v[152:155], a[200:203], v[56:59]\n"
"buffer_load_dwordx4 v229, s[56:59], s80 offen lds\n"
"v_mfma_f32_16x16x32_bf16 v[52:55], v[152:155], a[196:199], v[52:55]\n"
"s_add_u32 s80, s80, s100\n"
"ds_read_b64_tr_b16 v[140:141], v247 offset:12288\n"
"s_add_u32 s44, s44, -16384\n"
"v_mfma_f32_16x16x32_bf16 v[52:55], v[156:159], a[212:215], v[52:55]\n"
"s_addk_i32 m0, 1024\n"
"v_mfma_f32_16x16x32_bf16 v[48:51], v[156:159], a[208:211], v[48:51]\n"
"s_mov_b32 s45, s44\n"
"ds_read_b64_tr_b16 v[142:143], v247 offset:14336\n"
"v_mfma_f32_16x16x32_bf16 v[48:51], v[152:155], a[192:195], v[48:51]\n"
"s_waitcnt lgkmcnt(4)\n"
"buffer_load_dwordx4 v229, s[56:59], s80 offen lds\n"
"v_mfma_f32_16x16x32_bf16 v[0:3], v[128:131], a[224:227], v[0:3]\n"
"s_add_u32 s80, s85, 1048576\n"
"ds_read_b64_tr_b16 v[144:145], v248 offset:8192\n"
"s_mov_b64 s[64:65], s[12:13]\n"
"v_mfma_f32_16x16x32_bf16 v[0:3], v[132:135], a[240:243], v[0:3]\n"
"s_add_u32 m0, s46, 0\n"
"s_mov_b32 s66, 4194304\n"
"v_mfma_f32_16x16x32_bf16 v[4:7], v[132:135], a[244:247], v[4:7]\n"
"ds_read_b64_tr_b16 v[146:147], v248 offset:10240\n"
"v_mfma_f32_16x16x32_bf16 v[4:7], v[128:131], a[228:231], v[4:7]\n"
"buffer_load_dwordx4 v229, s[56:59], s80 offen lds\n"
"v_mfma_f32_16x16x32_bf16 v[8:11], v[128:131], a[232:235], v[8:11]\n"
"s_add_u32 s80, s80, s100\n"
"ds_read_b64_tr_b16 v[148:149], v248 offset:12288\n"
"s_mov_b32 s67, 0x20000\n"
"v_mfma_f32_16x16x32_bf16 v[8:11], v[132:135], a[248:251], v[8:11]\n"
"s_addk_i32 m0, 1024\n"
"s_mov_b64 s[68:69], s[20:21]\n"
"v_mfma_f32_16x16x32_bf16 v[12:15], v[132:135], a[252:255], v[12:15]\n"
"s_mov_b32 s70, 41943040\n"
"ds_read_b64_tr_b16 v[150:151], v248 offset:14336\n"
"v_mfma_f32_16x16x32_bf16 v[12:15], v[128:131], a[236:239], v[12:15]\n"
"s_waitcnt lgkmcnt(4)\n"
"buffer_load_dwordx4 v229, s[56:59], s80 offen lds\n"
"v_mfma_f32_16x16x32_bf16 v[28:31], v[136:139], a[236:239], v[28:31]\n"
"s_sub_u32 s80, s80, s101\n"
"ds_read_b64_tr_b16 v[152:153], v249 offset:8192\n"
"s_mov_b32 s71, 0x20000\n"
"v_mfma_f32_16x16x32_bf16 v[28:31], v[140:143], a[252:255], v[28:31]\n"
"s_addk_i32 m0, 1024\n"
"v_mfma_f32_16x16x32_bf16 v[24:27], v[140:143], a[248:251], v[24:27]\n"
"s_mul_i32 s90, s79, 610304\n"
"ds_read_b64_tr_b16 v[154:155], v249 offset:10240\n"
"v_mfma_f32_16x16x32_bf16 v[24:27], v[136:139], a[232:235], v[24:27]\n"
"buffer_load_dwordx4 v229, s[56:59], s80 offen lds\n"
"v_mfma_f32_16x16x32_bf16 v[20:23], v[136:139], a[228:231], v[20:23]\n"
"s_add_u32 s80, s80, s100\n"
"ds_read_b64_tr_b16 v[156:157], v249 offset:12288\n"
"s_mul_i32 s83, s77, 151552\n"
"v_mfma_f32_16x16x32_bf16 v[20:23], v[140:143], a[244:247], v[20:23]\n"
"s_addk_i32 m0, 1024\n"
"s_add_u32 s90, s90, s83\n"
"v_mfma_f32_16x16x32_bf16 v[16:19], v[140:143], a[240:243], v[16:19]\n"
"ds_read_b64_tr_b16 v[158:159], v249 offset:14336\n"
"v_mfma_f32_16x16x32_bf16 v[16:19], v[136:139], a[224:227], v[16:19]\n"
"s_waitcnt lgkmcnt(4)\n"
"buffer_load_dwordx4 v229, s[56:59], s80 offen lds\n"
"v_mfma_f32_16x16x32_bf16 v[32:35], v[144:147], a[224:227], v[32:35]\n"
"s_waitcnt vmcnt(12)\n"
"ds_read_b64_tr_b16 v[128:129], v206 offset:0\n"
"s_mul_i32 s83, s78, 2048\n"
"v_mfma_f32_16x16x32_bf16 v[32:35], v[148:151], a[240:243], v[32:35]\n"
"s_add_u32 s80, s80, s101\n"
"s_add_u32 s90, s90, s83\n"
"v_mfma_f32_16x16x32_bf16 v[36:39], v[148:151], a[244:247], v[36:39]\n"
"s_addk_i32 m0, 1024\n"
"ds_read_b64_tr_b16 v[130:131], v206 offset:2048\n"
"s_mul_i32 s83, s89, 75776\n"
"v_mfma_f32_16x16x32_bf16 v[36:39], v[144:147], a[228:231], v[36:39]\n"
"buffer_load_dwordx4 v229, s[56:59], s80 offen lds\n"
"v_mfma_f32_16x16x32_bf16 v[40:43], v[144:147], a[232:235], v[40:43]\n"
"s_add_u32 s80, s80, s100\n"
"ds_read_b64_tr_b16 v[132:133], v206 offset:4096\n"
"s_add_u32 s90, s90, s83\n"
"v_mfma_f32_16x16x32_bf16 v[40:43], v[148:151], a[248:251], v[40:43]\n"
"s_addk_i32 m0, 1024\n"
"s_add_u32 s90, s90, 20971520\n"
"v_mfma_f32_16x16x32_bf16 v[44:47], v[148:151], a[252:255], v[44:47]\n"
"ds_read_b64_tr_b16 v[134:135], v206 offset:6144\n"
"v_mfma_f32_16x16x32_bf16 v[44:47], v[144:147], a[236:239], v[44:47]\n"
"s_waitcnt lgkmcnt(4)\n"
"buffer_load_dwordx4 v229, s[56:59], s80 offen lds\n"
"v_mfma_f32_16x16x32_bf16 v[60:63], v[152:155], a[236:239], v[60:63]\n"
"s_sub_u32 s80, s80, s101\n"
"ds_read_b64_tr_b16 v[136:137], v206 offset:8\n"
"s_mul_i32 s91, s89, -151552\n"
"v_mfma_f32_16x16x32_bf16 v[60:63], v[156:159], a[252:255], v[60:63]\n"
"s_addk_i32 m0, 1024\n"
"s_mul_i32 s83, s90, 1\n"
"v_mfma_f32_16x16x32_bf16 v[56:59], v[156:159], a[248:251], v[56:59]\n"
"s_add_u32 s91, s91, s83\n"
"ds_read_b64_tr_b16 v[138:139], v206 offset:2056\n"
"v_mfma_f32_16x16x32_bf16 v[56:59], v[152:155], a[232:235], v[56:59]\n"
"buffer_load_dwordx4 v229, s[56:59], s80 offen lds\n"
"v_mfma_f32_16x16x32_bf16 v[52:55], v[152:155], a[228:231], v[52:55]\n"
"s_add_u32 s80, s80, s100\n"
"ds_read_b64_tr_b16 v[140:141], v206 offset:4104\n"
"s_add_u32 s91, s91, 75776\n"
"v_mfma_f32_16x16x32_bf16 v[52:55], v[156:159], a[244:247], v[52:55]\n"
"s_addk_i32 m0, 1024\n"
"v_mfma_f32_16x16x32_bf16 v[48:51], v[156:159], a[240:243], v[48:51]\n"
"s_mov_b32 s92, 0\n"
"ds_read_b64_tr_b16 v[142:143], v206 offset:6152\n"
"v_mfma_f32_16x16x32_bf16 v[48:51], v[152:155], a[224:227], v[48:51]\n"
"s_waitcnt lgkmcnt(4)\n"
"buffer_load_dwordx4 v229, s[56:59], s80 offen lds\n"
".LJ8_%=:\n"
"v_mfma_f32_16x16x32_bf16 v[64:67], v[128:131], a[0:3], 0\n"
"s_add_u32 s80, s85, 1572864\n"
"ds_read_b64_tr_b16 v[144:145], v207 offset:0\n"
"v_max_f32_e32 v0, 0, v0\n"
"v_mfma_f32_16x16x32_bf16 v[64:67], v[132:135], a[16:19], v[64:67]\n"
"s_add_u32 m0, s46, 65536\n"
"v_max_f32_e32 v1, 0, v1\n"
"v_mfma_f32_16x16x32_bf16 v[68:71], v[132:135], a[20:23], 0\n"
"ds_read_b64_tr_b16 v[146:147], v207 offset:2048\n"
"v_mfma_f32_16x16x32_bf16 v[68:71], v[128:131], a[4:7], v[68:71]\n"
"buffer_load_dwordx4 v229, s[56:59], s80 offen lds\n"
"v_mfma_f32_16x16x32_bf16 v[72:75], v[128:131], a[8:11], 0\n"
"s_add_u32 s80, s80, s100\n"
"ds_read_b64_tr_b16 v[148:149], v207 offset:4096\n"
"v_max_f32_e32 v2, 0, v2\n"
"v_mfma_f32_16x16x32_bf16 v[72:75], v[132:135], a[24:27], v[72:75]\n"
"s_addk_i32 m0, 1024\n"
"v_max_f32_e32 v3, 0, v3\n"
"v_mfma_f32_16x16x32_bf16 v[76:79], v[132:135], a[28:31], 0\n"
"ds_read_b64_tr_b16 v[150:151], v207 offset:6144\n"
"v_mfma_f32_16x16x32_bf16 v[76:79], v[128:131], a[12:15], v[76:79]\n"
"s_waitcnt lgkmcnt(4)\n"
"buffer_load_dwordx4 v229, s[56:59], s80 offen lds\n"
"v_mfma_f32_16x16x32_bf16 v[92:95], v[136:139], a[12:15], 0\n"
"s_sub_u32 s80, s80, s101\n"
"ds_read_b64_tr_b16 v[152:153], v207 offset:8\n"
"v_max_f32_e32 v16, 0, v16\n"
"v_mfma_f32_16x16x32_bf16 v[92:95], v[140:143], a[28:31], v[92:95]\n"
"s_addk_i32 m0, 1024\n"
"v_max_f32_e32 v17, 0, v17\n"
"v_mfma_f32_16x16x32_bf16 v[88:91], v[140:143], a[24:27], 0\n"
"ds_read_b64_tr_b16 v[154:155], v207 offset:2056\n"
"v_max_f32_e32 v18, 0, v18\n"
"v_mfma_f32_16x16x32_bf16 v[88:91], v[136:139], a[8:11], v[88:91]\n"
"buffer_load_dwordx4 v229, s[56:59], s80 offen lds\n"
"v_mfma_f32_16x16x32_bf16 v[84:87], v[136:139], a[4:7], 0\n"
"s_add_u32 s80, s80, s100\n"
"ds_read_b64_tr_b16 v[156:157], v207 offset:4104\n"
"v_max_f32_e32 v19, 0, v19\n"
"v_mfma_f32_16x16x32_bf16 v[84:87], v[140:143], a[20:23], v[84:87]\n"
"s_addk_i32 m0, 1024\n"
"v_mfma_f32_16x16x32_bf16 v[80:83], v[140:143], a[16:19], 0\n"
"ds_read_b64_tr_b16 v[158:159], v207 offset:6152\n"
"v_cvt_pk_bf16_f32 v0, v0, v1\n"
"v_mfma_f32_16x16x32_bf16 v[80:83], v[136:139], a[0:3], v[80:83]\n"
"s_waitcnt lgkmcnt(4)\n"
"buffer_load_dwordx4 v229, s[56:59], s80 offen lds\n"
"v_mfma_f32_16x16x32_bf16 v[96:99], v[144:147], a[0:3], 0\n"
"s_waitcnt vmcnt(12)\n"
"ds_read_b64_tr_b16 v[128:129], v198 offset:8192\n"
"v_cvt_pk_bf16_f32 v1, v2, v3\n"
"v_mfma_f32_16x16x32_bf16 v[96:99], v[148:151], a[16:19], v[96:99]\n"
"s_add_u32 s80, s80, s101\n"
"v_cvt_pk_bf16_f32 v2, v16, v17\n"
"v_mfma_f32_16x16x32_bf16 v[100:103], v[148:151], a[20:23], 0\n"
"s_addk_i32 m0, 1024\n"
"ds_read_b64_tr_b16 v[130:131], v198 offset:10240\n"
"v_mfma_f32_16x16x32_bf16 v[100:103], v[144:147], a[4:7], v[100:103]\n"
"buffer_load_dwordx4 v229, s[56:59], s80 offen lds\n"
"v_mfma_f32_16x16x32_bf16 v[104:107], v[144:147], a[8:11], 0\n"
"s_add_u32 s80, s80, s100\n"
"ds_read_b64_tr_b16 v[132:133], v198 offset:12288\n"
"v_cvt_pk_bf16_f32 v3, v18, v19\n"
"v_mfma_f32_16x16x32_bf16 v[104:107], v[148:151], a[24:27], v[104:107]\n"
"s_addk_i32 m0, 1024\n"
"v_max_f32_e32 v4, 0, v4\n"
"v_mfma_f32_16x16x32_bf16 v[108:111], v[148:151], a[28:31], 0\n"
"ds_read_b64_tr_b16 v[134:135], v198 offset:14336\n"
"v_mfma_f32_16x16x32_bf16 v[108:111], v[144:147], a[12:15], v[108:111]\n"
"s_waitcnt lgkmcnt(4)\n"
"buffer_load_dwordx4 v229, s[56:59], s80 offen lds\n"
"v_mfma_f32_16x16x32_bf16 v[124:127], v[152:155], a[12:15], 0\n"
"s_sub_u32 s80, s80, s101\n"
"ds_read_b64_tr_b16 v[136:137], v198 offset:8200\n"
"v_max_f32_e32 v5, 0, v5\n"
"v_mfma_f32_16x16x32_bf16 v[124:127], v[156:159], a[28:31], v[124:127]\n"
"s_addk_i32 m0, 1024\n"
"v_max_f32_e32 v6, 0, v6\n"
"v_mfma_f32_16x16x32_bf16 v[120:123], v[156:159], a[24:27], 0\n"
"ds_read_b64_tr_b16 v[138:139], v198 offset:10248\n"
"v_max_f32_e32 v7, 0, v7\n"
"v_mfma_f32_16x16x32_bf16 v[120:123], v[152:155], a[8:11], v[120:123]\n"
"buffer_load_dwordx4 v229, s[56:59], s80 offen lds\n"
"v_mfma_f32_16x16x32_bf16 v[116:119], v[152:155], a[4:7], 0\n"
"s_add_u32 s80, s80, s100\n"
"ds_read_b64_tr_b16 v[140:141], v198 offset:12296\n"
"v_max_f32_e32 v20, 0, v20\n"
"v_mfma_f32_16x16x32_bf16 v[116:119], v[156:159], a[20:23], v[116:119]\n"
"s_addk_i32 m0, 1024\n"
"v_mfma_f32_16x16x32_bf16 v[112:115], v[156:159], a[16:19], 0\n"
"ds_read_b64_tr_b16 v[142:143], v198 offset:14344\n"
"v_max_f32_e32 v21, 0, v21\n"
"v_mfma_f32_16x16x32_bf16 v[112:115], v[152:155], a[0:3], v[112:115]\n"
"s_waitcnt lgkmcnt(4)\n"
"buffer_load_dwordx4 v229, s[56:59], s80 offen lds\n"
"v_mfma_f32_16x16x32_bf16 v[64:67], v[128:131], a[32:35], v[64:67]\n"
"s_add_u32 s80, s85, 2097152\n"
"ds_read_b64_tr_b16 v[144:145], v199 offset:8192\n"
"v_max_f32_e32 v22, 0, v22\n"
"v_mfma_f32_16x16x32_bf16 v[64:67], v[132:135], a[48:51], v[64:67]\n"
"s_add_u32 m0, s46, 8192\n"
"v_max_f32_e32 v23, 0, v23\n"
"v_mfma_f32_16x16x32_bf16 v[68:71], v[132:135], a[52:55], v[68:71]\n"
"ds_read_b64_tr_b16 v[146:147], v199 offset:10240\n"
"v_mfma_f32_16x16x32_bf16 v[68:71], v[128:131], a[36:39], v[68:71]\n"
"buffer_load_dwordx4 v229, s[56:59], s80 offen lds\n"
"v_mfma_f32_16x16x32_bf16 v[72:75], v[128:131], a[40:43], v[72:75]\n"
"s_add_u32 s80, s80, s100\n"
"ds_read_b64_tr_b16 v[148:149], v199 offset:12288\n"
"v_cvt_pk_bf16_f32 v4, v4, v5\n"
"v_mfma_f32_16x16x32_bf16 v[72:75], v[132:135], a[56:59], v[72:75]\n"
"s_addk_i32 m0, 1024\n"
"v_cvt_pk_bf16_f32 v5, v6, v7\n"
"v_mfma_f32_16x16x32_bf16 v[76:79], v[132:135], a[60:63], v[76:79]\n"
"ds_read_b64_tr_b16 v[150:151], v199 offset:14336\n"
"v_mfma_f32_16x16x32_bf16 v[76:79], v[128:131], a[44:47], v[76:79]\n"
"s_waitcnt lgkmcnt(4)\n"
"buffer_load_dwordx4 v229, s[56:59], s80 offen lds\n"
"v_mfma_f32_16x16x32_bf16 v[92:95], v[136:139], a[44:47], v[92:95]\n"
"s_sub_u32 s80, s80, s101\n"
"ds_read_b64_tr_b16 v[152:153], v199 offset:8200\n"
"v_cvt_pk_bf16_f32 v6, v20, v21\n"
"v_mfma_f32_16x16x32_bf16 v[92:95], v[140:143], a[60:63], v[92:95]\n"
"s_addk_i32 m0, 1024\n"
"v_cvt_pk_bf16_f32 v7, v22, v23\n"
"v_mfma_f32_16x16x32_bf16 v[88:91], v[140:143], a[56:59], v[88:91]\n"
"ds_read_b64_tr_b16 v[154:155], v199 offset:10248\n"
"v_max_f32_e32 v8, 0, v8\n"
"v_mfma_f32_16x16x32_bf16 v[88:91], v[136:139], a[40:43], v[88:91]\n"
"buffer_load_dwordx4 v229, s[56:59], s80 offen lds\n"
"v_mfma_f32_16x16x32_bf16 v[84:87], v[136:139], a[36:39], v[84:87]\n"
"s_add_u32 s80, s80, s100\n"
"ds_read_b64_tr_b16 v[156:157], v199 offset:12296\n"
"v_max_f32_e32 v9, 0, v9\n"
"v_mfma_f32_16x16x32_bf16 v[84:87], v[140:143], a[52:55], v[84:87]\n"
"s_addk_i32 m0, 1024\n"
"v_mfma_f32_16x16x32_bf16 v[80:83], v[140:143], a[48:51], v[80:83]\n"
"ds_read_b64_tr_b16 v[158:159], v199 offset:14344\n"
"v_max_f32_e32 v10, 0, v10\n"
"v_mfma_f32_16x16x32_bf16 v[80:83], v[136:139], a[32:35], v[80:83]\n"
"s_waitcnt lgkmcnt(4)\n"
"buffer_load_dwordx4 v229, s[56:59], s80 offen lds\n"
"v_mfma_f32_16x16x32_bf16 v[96:99], v[144:147], a[32:35], v[96:99]\n"
"s_waitcnt vmcnt(12)\n"
"ds_read_b64_tr_b16 v[128:129], v196 offset:0\n"
"v_max_f32_e32 v11, 0, v11\n"
"v_mfma_f32_16x16x32_bf16 v[96:99], v[148:151], a[48:51], v[96:99]\n"
"s_add_u32 s80, s80, s101\n"
"v_max_f32_e32 v24, 0, v24\n"
"v_mfma_f32_16x16x32_bf16 v[100:103], v[148:151], a[52:55], v[100:103]\n"
"s_addk_i32 m0, 1024\n"
"ds_read_b64_tr_b16 v[130:131], v196 offset:2048\n"
"v_mfma_f32_16x16x32_bf16 v[100:103], v[144:147], a[36:39], v[100:103]\n"
"buffer_load_dwordx4 v229, s[56:59], s80 offen lds\n"
"v_mfma_f32_16x16x32_bf16 v[104:107], v[144:147], a[40:43], v[104:107]\n"
"s_add_u32 s80, s80, s100\n"
"ds_read_b64_tr_b16 v[132:133], v196 offset:4096\n"
"v_max_f32_e32 v25, 0, v25\n"
"v_mfma_f32_16x16x32_bf16 v[104:107], v[148:151], a[56:59], v[104:107]\n"
"s_addk_i32 m0, 1024\n"
"v_max_f32_e32 v26, 0, v26\n"
"v_mfma_f32_16x16x32_bf16 v[108:111], v[148:151], a[60:63], v[108:111]\n"
"ds_read_b64_tr_b16 v[134:135], v196 offset:6144\n"
"v_mfma_f32_16x16x32_bf16 v[108:111], v[144:147], a[44:47], v[108:111]\n"
"s_waitcnt lgkmcnt(4)\n"
"buffer_load_dwordx4 v229, s[56:59], s80 offen lds\n"
"v_mfma_f32_16x16x32_bf16 v[124:127], v[152:155], a[44:47], v[124:127]\n"
"s_sub_u32 s80, s80, s101\n"
"ds_read_b64_tr_b16 v[136:137], v196 offset:8\n"
"v_max_f32_e32 v27, 0, v27\n"
"v_mfma_f32_16x16x32_bf16 v[124:127], v[156:159], a[60:63], v[124:127]\n"
"s_addk_i32 m0, 1024\n"
"v_cvt_pk_bf16_f32 v8, v8, v9\n"
"v_mfma_f32_16x16x32_bf16 v[120:123], v[156:159], a[56:59], v[120:123]\n"
"ds_read_b64_tr_b16 v[138:139], v196 offset:2056\n"
"v_cvt_pk_bf16_f32 v9, v10, v11\n"
"v_mfma_f32_16x16x32_bf16 v[120:123], v[152:155], a[40:43], v[120:123]\n"
"buffer_load_dwordx4 v229, s[56:59], s80 offen lds\n"
"v_mfma_f32_16x16x32_bf16 v[116:119], v[152:155], a[36:39], v[116:119]\n"
"s_add_u32 s80, s80, s100\n"
"ds_read_b64_tr_b16 v[140:141], v196 offset:4104\n"
"v_cvt_pk_bf16_f32 v10, v24, v25\n"
"v_mfma_f32_16x16x32_bf16 v[116:119], v[156:159], a[52:55], v[116:119]\n"
"s_addk_i32 m0, 1024\n"
"v_mfma_f32_16x16x32_bf16 v[112:115], v[156:159], a[48:51], v[112:115]\n"
"ds_read_b64_tr_b16 v[142:143], v196 offset:6152\n"
"v_cvt_pk_bf16_f32 v11, v26, v27\n"
"v_mfma_f32_16x16x32_bf16 v[112:115], v[152:155], a[32:35], v[112:115]\n"
"s_waitcnt lgkmcnt(4)\n"
"buffer_load_dwordx4 v229, s[56:59], s80 offen lds\n"
"v_mfma_f32_16x16x32_bf16 v[64:67], v[128:131], a[64:67], v[64:67]\n"
"s_add_u32 s80, s85, 2621440\n"
"ds_read_b64_tr_b16 v[144:145], v197 offset:0\n"
"v_max_f32_e32 v12, 0, v12\n"
"v_mfma_f32_16x16x32_bf16 v[64:67], v[132:135], a[80:83], v[64:67]\n"
"s_add_u32 m0, s46, 73728\n"
"v_max_f32_e32 v13, 0, v13\n"
"v_mfma_f32_16x16x32_bf16 v[68:71], v[132:135], a[84:87], v[68:71]\n"
"ds_read_b64_tr_b16 v[146:147], v197 offset:2048\n"
"v_mfma_f32_16x16x32_bf16 v[68:71], v[128:131], a[68:71], v[68:71]\n"
"buffer_load_dwordx4 v229, s[56:59], s80 offen lds\n"
"v_mfma_f32_16x16x32_bf16 v[72:75], v[128:131], a[72:75], v[72:75]\n"
"s_add_u32 s80, s80, s100\n"
"ds_read_b64_tr_b16 v[148:149], v197 offset:4096\n"
"v_max_f32_e32 v14, 0, v14\n"
"v_mfma_f32_16x16x32_bf16 v[72:75], v[132:135], a[88:91], v[72:75]\n"
"s_addk_i32 m0, 1024\n"
"v_max_f32_e32 v15, 0, v15\n"
"v_mfma_f32_16x16x32_bf16 v[76:79], v[132:135], a[92:95], v[76:79]\n"
"ds_read_b64_tr_b16 v[150:151], v197 offset:6144\n"
"v_mfma_f32_16x16x32_bf16 v[76:79], v[128:131], a[76:79], v[76:79]\n"
"s_waitcnt lgkmcnt(4)\n"
"buffer_load_dwordx4 v229, s[56:59], s80 offen lds\n"
"v_mfma_f32_16x16x32_bf16 v[92:95], v[136:139], a[76:79], v[92:95]\n"
"s_sub_u32 s80, s80, s101\n"
"ds_read_b64_tr_b16 v[152:153], v197 offset:8\n"
"v_max_f32_e32 v28, 0, v28\n"
"v_mfma_f32_16x16x32_bf16 v[92:95], v[140:143], a[92:95], v[92:95]\n"
"s_addk_i32 m0, 1024\n"
"v_max_f32_e32 v29, 0, v29\n"
"v_mfma_f32_16x16x32_bf16 v[88:91], v[140:143], a[88:91], v[88:91]\n"
"ds_read_b64_tr_b16 v[154:155], v197 offset:2056\n"
"v_max_f32_e32 v30, 0, v30\n"
"v_mfma_f32_16x16x32_bf16 v[88:91], v[136:139], a[72:75], v[88:91]\n"
"buffer_load_dwordx4 v229, s[56:59], s80 offen lds\n"
"v_mfma_f32_16x16x32_bf16 v[84:87], v[136:139], a[68:71], v[84:87]\n"
"s_add_u32 s80, s80, s100\n"
"ds_read_b64_tr_b16 v[156:157], v197 offset:4104\n"
"v_max_f32_e32 v31, 0, v31\n"
"v_mfma_f32_16x16x32_bf16 v[84:87], v[140:143], a[84:87], v[84:87]\n"
"s_addk_i32 m0, 1024\n"
"v_mfma_f32_16x16x32_bf16 v[80:83], v[140:143], a[80:83], v[80:83]\n"
"ds_read_b64_tr_b16 v[158:159], v197 offset:6152\n"
"v_cvt_pk_bf16_f32 v12, v12, v13\n"
"v_mfma_f32_16x16x32_bf16 v[80:83], v[136:139], a[64:67], v[80:83]\n"
"s_waitcnt lgkmcnt(4)\n"
"buffer_load_dwordx4 v229, s[56:59], s80 offen lds\n"
"v_mfma_f32_16x16x32_bf16 v[96:99], v[144:147], a[64:67], v[96:99]\n"
"s_waitcnt vmcnt(12)\n"
"ds_read_b64_tr_b16 v[128:129], v198 offset:0\n"
"v_cvt_pk_bf16_f32 v13, v14, v15\n"
"v_mfma_f32_16x16x32_bf16 v[96:99], v[148:151], a[80:83], v[96:99]\n"
"s_add_u32 s80, s80, s101\n"
"v_cvt_pk_bf16_f32 v14, v28, v29\n"
"v_mfma_f32_16x16x32_bf16 v[100:103], v[148:151], a[84:87], v[100:103]\n"
"s_addk_i32 m0, 1024\n"
"ds_read_b64_tr_b16 v[130:131], v198 offset:2048\n"
"v_mfma_f32_16x16x32_bf16 v[100:103], v[144:147], a[68:71], v[100:103]\n"
"buffer_load_dwordx4 v229, s[56:59], s80 offen lds\n"
"v_mfma_f32_16x16x32_bf16 v[104:107], v[144:147], a[72:75], v[104:107]\n"
"s_add_u32 s80, s80, s100\n"
"ds_read_b64_tr_b16 v[132:133], v198 offset:4096\n"
"v_cvt_pk_bf16_f32 v15, v30, v31\n"
"v_mfma_f32_16x16x32_bf16 v[104:107], v[148:151], a[88:91], v[104:107]\n"
"s_addk_i32 m0, 1024\n"
"v_max_f32_e32 v32, 0, v32\n"
"v_mfma_f32_16x16x32_bf16 v[108:111], v[148:151], a[92:95], v[108:111]\n"
"ds_read_b64_tr_b16 v[134:135], v198 offset:6144\n"
"v_mfma_f32_16x16x32_bf16 v[108:111], v[144:147], a[76:79], v[108:111]\n"
"s_waitcnt lgkmcnt(4)\n"
"buffer_load_dwordx4 v229, s[56:59], s80 offen lds\n"
"v_mfma_f32_16x16x32_bf16 v[124:127], v[152:155], a[76:79], v[124:127]\n"
"s_sub_u32 s80, s80, s101\n"
"ds_read_b64_tr_b16 v[136:137], v198 offset:8\n"
"v_max_f32_e32 v33, 0, v33\n"
"v_mfma_f32_16x16x32_bf16 v[124:127], v[156:159], a[92:95], v[124:127]\n"
"s_addk_i32 m0, 1024\n"
"v_max_f32_e32 v34, 0, v34\n"
"v_mfma_f32_16x16x32_bf16 v[120:123], v[156:159], a[88:91], v[120:123]\n"
"ds_read_b64_tr_b16 v[138:139], v198 offset:2056\n"
"v_max_f32_e32 v35, 0, v35\n"
"v_mfma_f32_16x16x32_bf16 v[120:123], v[152:155], a[72:75], v[120:123]\n"
"buffer_load_dwordx4 v229, s[56:59], s80 offen lds\n"
"v_mfma_f32_16x16x32_bf16 v[116:119], v[152:155], a[68:71], v[116:119]\n"
"s_add_u32 s80, s80, s100\n"
"ds_read_b64_tr_b16 v[140:141], v198 offset:4104\n"
"v_max_f32_e32 v48, 0, v48\n"
"v_mfma_f32_16x16x32_bf16 v[116:119], v[156:159], a[84:87], v[116:119]\n"
"s_addk_i32 m0, 1024\n"
"v_mfma_f32_16x16x32_bf16 v[112:115], v[156:159], a[80:83], v[112:115]\n"
"ds_read_b64_tr_b16 v[142:143], v198 offset:6152\n"
"v_max_f32_e32 v49, 0, v49\n"
"v_mfma_f32_16x16x32_bf16 v[112:115], v[152:155], a[64:67], v[112:115]\n"
"s_waitcnt lgkmcnt(4)\n"
"buffer_load_dwordx4 v229, s[56:59], s80 offen lds\n"
"v_mfma_f32_16x16x32_bf16 v[64:67], v[128:131], a[96:99], v[64:67]\n"
"s_add_u32 s80, s85, 3145728\n"
"ds_read_b64_tr_b16 v[144:145], v199 offset:0\n"
"v_max_f32_e32 v50, 0, v50\n"
"v_mfma_f32_16x16x32_bf16 v[64:67], v[132:135], a[112:115], v[64:67]\n"
"s_add_u32 m0, s46, 0\n"
"v_max_f32_e32 v51, 0, v51\n"
"v_mfma_f32_16x16x32_bf16 v[68:71], v[132:135], a[116:119], v[68:71]\n"
".LPG3_%=:\n"
"s_cbranch_execz .LPGR_%=\n"
"ds_read_b64_tr_b16 v[146:147], v199 offset:2048\n"
"v_mfma_f32_16x16x32_bf16 v[68:71], v[128:131], a[100:103], v[68:71]\n"
"buffer_load_dwordx4 v229, s[56:59], s80 offen lds\n"
"v_mfma_f32_16x16x32_bf16 v[72:75], v[128:131], a[104:107], v[72:75]\n"
"s_add_u32 s80, s80, s100\n"
"ds_read_b64_tr_b16 v[148:149], v199 offset:4096\n"
"v_cvt_pk_bf16_f32 v32, v32, v33\n"
"v_mfma_f32_16x16x32_bf16 v[72:75], v[132:135], a[120:123], v[72:75]\n"
"s_addk_i32 m0, 1024\n"
"v_cvt_pk_bf16_f32 v33, v34, v35\n"
"v_mfma_f32_16x16x32_bf16 v[76:79], v[132:135], a[124:127], v[76:79]\n"
"ds_read_b64_tr_b16 v[150:151], v199 offset:6144\n"
"v_mfma_f32_16x16x32_bf16 v[76:79], v[128:131], a[108:111], v[76:79]\n"
"s_waitcnt lgkmcnt(4)\n"
"buffer_load_dwordx4 v229, s[56:59], s80 offen lds\n"
"v_mfma_f32_16x16x32_bf16 v[92:95], v[136:139], a[108:111], v[92:95]\n"
"s_sub_u32 s80, s80, s101\n"
"ds_read_b64_tr_b16 v[152:153], v199 offset:8\n"
"v_cvt_pk_bf16_f32 v34, v48, v49\n"
"v_mfma_f32_16x16x32_bf16 v[92:95], v[140:143], a[124:127], v[92:95]\n"
"s_addk_i32 m0, 1024\n"
"v_cvt_pk_bf16_f32 v35, v50, v51\n"
"v_mfma_f32_16x16x32_bf16 v[88:91], v[140:143], a[120:123], v[88:91]\n"
"ds_read_b64_tr_b16 v[154:155], v199 offset:2056\n"
"v_max_f32_e32 v36, 0, v36\n"
"v_mfma_f32_16x16x32_bf16 v[88:91], v[136:139], a[104:107], v[88:91]\n"
"buffer_load_dwordx4 v229, s[56:59], s80 offen lds\n"
"v_mfma_f32_16x16x32_bf16 v[84:87], v[136:139], a[100:103], v[84:87]\n"
"s_add_u32 s80, s80, s100\n"
"ds_read_b64_tr_b16 v[156:157], v199 offset:4104\n"
"v_max_f32_e32 v37, 0, v37\n"
"v_mfma_f32_16x16x32_bf16 v[84:87], v[140:143], a[116:119], v[84:87]\n"
"s_addk_i32 m0, 1024\n"
"v_mfma_f32_16x16x32_bf16 v[80:83], v[140:143], a[112:115], v[80:83]\n"
"ds_read_b64_tr_b16 v[158:159], v199 offset:6152\n"
"v_max_f32_e32 v38, 0, v38\n"
"v_mfma_f32_16x16x32_bf16 v[80:83], v[136:139], a[96:99], v[80:83]\n"
"s_waitcnt lgkmcnt(4)\n"
"buffer_load_dwordx4 v229, s[56:59], s80 offen lds\n"
"v_mfma_f32_16x16x32_bf16 v[96:99], v[144:147], a[96:99], v[96:99]\n"
"s_waitcnt vmcnt(12)\n"
"ds_read_b64_tr_b16 v[128:129], v196 offset:8192\n"
"v_max_f32_e32 v39, 0, v39\n"
"v_mfma_f32_16x16x32_bf16 v[96:99], v[148:151], a[112:115], v[96:99]\n"
"s_add_u32 s80, s80, s101\n"
"v_max_f32_e32 v52, 0, v52\n"
"v_mfma_f32_16x16x32_bf16 v[100:103], v[148:151], a[116:119], v[100:103]\n"
"s_addk_i32 m0, 1024\n"
"ds_read_b64_tr_b16 v[130:131], v196 offset:10240\n"
"v_mfma_f32_16x16x32_bf16 v[100:103], v[144:147], a[100:103], v[100:103]\n"
"buffer_load_dwordx4 v229, s[56:59], s80 offen lds\n"
"v_mfma_f32_16x16x32_bf16 v[104:107], v[144:147], a[104:107], v[104:107]\n"
"s_add_u32 s80, s80, s100\n"
"ds_read_b64_tr_b16 v[132:133], v196 offset:12288\n"
"v_max_f32_e32 v53, 0, v53\n"
"v_mfma_f32_16x16x32_bf16 v[104:107], v[148:151], a[120:123], v[104:107]\n"
"s_addk_i32 m0, 1024\n"
"v_max_f32_e32 v54, 0, v54\n"
"v_mfma_f32_16x16x32_bf16 v[108:111], v[148:151], a[124:127], v[108:111]\n"
"ds_read_b64_tr_b16 v[134:135], v196 offset:14336\n"
"v_mfma_f32_16x16x32_bf16 v[108:111], v[144:147], a[108:111], v[108:111]\n"
"s_waitcnt lgkmcnt(4)\n"
"buffer_load_dwordx4 v229, s[56:59], s80 offen lds\n"
"v_mfma_f32_16x16x32_bf16 v[124:127], v[152:155], a[108:111], v[124:127]\n"
"s_sub_u32 s80, s80, s101\n"
"ds_read_b64_tr_b16 v[136:137], v196 offset:8200\n"
"v_max_f32_e32 v55, 0, v55\n"
"v_mfma_f32_16x16x32_bf16 v[124:127], v[156:159], a[124:127], v[124:127]\n"
"s_addk_i32 m0, 1024\n"
"v_cvt_pk_bf16_f32 v36, v36, v37\n"
"v_mfma_f32_16x16x32_bf16 v[120:123], v[156:159], a[120:123], v[120:123]\n"
"ds_read_b64_tr_b16 v[138:139], v196 offset:10248\n"
"v_cvt_pk_bf16_f32 v37, v38, v39\n"
"v_mfma_f32_16x16x32_bf16 v[120:123], v[152:155], a[104:107], v[120:123]\n"
"buffer_load_dwordx4 v229, s[56:59], s80 offen lds\n"
"v_mfma_f32_16x16x32_bf16 v[116:119], v[152:155], a[100:103], v[116:119]\n"
"s_add_u32 s80, s80, s100\n"
"ds_read_b64_tr_b16 v[140:141], v196 offset:12296\n"
"v_cvt_pk_bf16_f32 v38, v52, v53\n"
"v_mfma_f32_16x16x32_bf16 v[116:119], v[156:159], a[116:119], v[116:119]\n"
"s_addk_i32 m0, 1024\n"
"v_mfma_f32_16x16x32_bf16 v[112:115], v[156:159], a[112:115], v[112:115]\n"
"ds_read_b64_tr_b16 v[142:143], v196 offset:14344\n"
"v_cvt_pk_bf16_f32 v39, v54, v55\n"
"v_mfma_f32_16x16x32_bf16 v[112:115], v[152:155], a[96:99], v[112:115]\n"
"s_waitcnt lgkmcnt(4)\n"
"buffer_load_dwordx4 v229, s[56:59], s80 offen lds\n"
"v_mfma_f32_16x16x32_bf16 v[64:67], v[128:131], a[128:131], v[64:67]\n"
"s_add_u32 s80, s85, 3670016\n"
"ds_read_b64_tr_b16 v[144:145], v197 offset:8192\n"
"v_max_f32_e32 v40, 0, v40\n"
"v_mfma_f32_16x16x32_bf16 v[64:67], v[132:135], a[144:147], v[64:67]\n"
"s_add_u32 m0, s46, 65536\n"
"v_max_f32_e32 v41, 0, v41\n"
"v_mfma_f32_16x16x32_bf16 v[68:71], v[132:135], a[148:151], v[68:71]\n"
"ds_read_b64_tr_b16 v[146:147], v197 offset:10240\n"
"v_mfma_f32_16x16x32_bf16 v[68:71], v[128:131], a[132:135], v[68:71]\n"
"buffer_load_dwordx4 v229, s[56:59], s80 offen lds\n"
"v_mfma_f32_16x16x32_bf16 v[72:75], v[128:131], a[136:139], v[72:75]\n"
"s_add_u32 s80, s80, s100\n"
"ds_read_b64_tr_b16 v[148:149], v197 offset:12288\n"
"v_max_f32_e32 v42, 0, v42\n"
"v_mfma_f32_16x16x32_bf16 v[72:75], v[132:135], a[152:155], v[72:75]\n"
"s_addk_i32 m0, 1024\n"
"v_max_f32_e32 v43, 0, v43\n"
"v_mfma_f32_16x16x32_bf16 v[76:79], v[132:135], a[156:159], v[76:79]\n"
"ds_read_b64_tr_b16 v[150:151], v197 offset:14336\n"
"v_mfma_f32_16x16x32_bf16 v[76:79], v[128:131], a[140:143], v[76:79]\n"
"s_waitcnt lgkmcnt(4)\n"
"buffer_load_dwordx4 v229, s[56:59], s80 offen lds\n"
"v_mfma_f32_16x16x32_bf16 v[92:95], v[136:139], a[140:143], v[92:95]\n"
"s_sub_u32 s80, s80, s101\n"
"ds_read_b64_tr_b16 v[152:153], v197 offset:8200\n"
"v_max_f32_e32 v56, 0, v56\n"
"v_mfma_f32_16x16x32_bf16 v[92:95], v[140:143], a[156:159], v[92:95]\n"
"s_addk_i32 m0, 1024\n"
"v_max_f32_e32 v57, 0, v57\n"
"v_mfma_f32_16x16x32_bf16 v[88:91], v[140:143], a[152:155], v[88:91]\n"
"ds_read_b64_tr_b16 v[154:155], v197 offset:10248\n"
"v_mfma_f32_16x16x32_bf16 v[88:91], v[136:139], a[136:139], v[88:91]\n"
"buffer_load_dwordx4 v229, s[56:59], s80 offen lds\n"
"v_mfma_f32_16x16x32_bf16 v[84:87], v[136:139], a[132:135], v[84:87]\n"
"s_add_u32 s80, s80, s100\n"
"ds_read_b64_tr_b16 v[156:157], v197 offset:12296\n"
"v_max_f32_e32 v58, 0, v58\n"
"v_mfma_f32_16x16x32_bf16 v[84:87], v[140:143], a[148:151], v[84:87]\n"
"s_addk_i32 m0, 1024\n"
"v_max_f32_e32 v59, 0, v59\n"
"v_mfma_f32_16x16x32_bf16 v[80:83], v[140:143], a[144:147], v[80:83]\n"
"ds_read_b64_tr_b16 v[158:159], v197 offset:14344\n"
"v_mfma_f32_16x16x32_bf16 v[80:83], v[136:139], a[128:131], v[80:83]\n"
"s_waitcnt lgkmcnt(4)\n"
"buffer_load_dwordx4 v229, s[56:59], s80 offen lds\n"
"v_mfma_f32_16x16x32_bf16 v[96:99], v[144:147], a[128:131], v[96:99]\n"
"s_waitcnt vmcnt(12)\n"
"ds_read_b64_tr_b16 v[128:129], v198 offset:8192\n"
"v_cvt_pk_bf16_f32 v40, v40, v41\n"
"v_mfma_f32_16x16x32_bf16 v[96:99], v[148:151], a[144:147], v[96:99]\n"
"s_add_u32 s80, s80, s101\n"
"v_cvt_pk_bf16_f32 v41, v42, v43\n"
"v_mfma_f32_16x16x32_bf16 v[100:103], v[148:151], a[148:151], v[100:103]\n"
"s_addk_i32 m0, 1024\n"
"ds_read_b64_tr_b16 v[130:131], v198 offset:10240\n"
"v_cvt_pk_bf16_f32 v42, v56, v57\n"
"v_mfma_f32_16x16x32_bf16 v[100:103], v[144:147], a[132:135], v[100:103]\n"
"buffer_load_dwordx4 v229, s[56:59], s80 offen lds\n"
"v_mfma_f32_16x16x32_bf16 v[104:107], v[144:147], a[136:139], v[104:107]\n"
"s_add_u32 s80, s80, s100\n"
"ds_read_b64_tr_b16 v[132:133], v198 offset:12288\n"
"v_cvt_pk_bf16_f32 v43, v58, v59\n"
"v_mfma_f32_16x16x32_bf16 v[104:107], v[148:151], a[152:155], v[104:107]\n"
"s_addk_i32 m0, 1024\n"
"v_mfma_f32_16x16x32_bf16 v[108:111], v[148:151], a[156:159], v[108:111]\n"
"ds_read_b64_tr_b16 v[134:135], v198 offset:14336\n"
"v_max_f32_e32 v44, 0, v44\n"
"v_mfma_f32_16x16x32_bf16 v[108:111], v[144:147], a[140:143], v[108:111]\n"
"s_waitcnt lgkmcnt(4)\n"
"buffer_load_dwordx4 v229, s[56:59], s80 offen lds\n"
"v_mfma_f32_16x16x32_bf16 v[124:127], v[152:155], a[140:143], v[124:127]\n"
"s_sub_u32 s80, s80, s101\n"
"ds_read_b64_tr_b16 v[136:137], v198 offset:8200\n"
"v_max_f32_e32 v45, 0, v45\n"
"v_mfma_f32_16x16x32_bf16 v[124:127], v[156:159], a[156:159], v[124:127]\n"
"s_addk_i32 m0, 1024\n"
"v_mfma_f32_16x16x32_bf16 v[120:123], v[156:159], a[152:155], v[120:123]\n"
"ds_read_b64_tr_b16 v[138:139], v198 offset:10248\n"
"v_max_f32_e32 v46, 0, v46\n"
"v_mfma_f32_16x16x32_bf16 v[120:123], v[152:155], a[136:139], v[120:123]\n"
"buffer_load_dwordx4 v229, s[56:59], s80 offen lds\n"
"v_mfma_f32_16x16x32_bf16 v[116:119], v[152:155], a[132:135], v[116:119]\n"
"s_add_u32 s80, s80, s100\n"
"ds_read_b64_tr_b16 v[140:141], v198 offset:12296\n"
"v_max_f32_e32 v47, 0, v47\n"
"v_mfma_f32_16x16x32_bf16 v[116:119], v[156:159], a[148:151], v[116:119]\n"
"s_addk_i32 m0, 1024\n"
"v_mfma_f32_16x16x32_bf16 v[112:115], v[156:159], a[144:147], v[112:115]\n"
"ds_read_b64_tr_b16 v[142:143], v198 offset:14344\n"
"v_max_f32_e32 v60, 0, v60\n"
"v_mfma_f32_16x16x32_bf16 v[112:115], v[152:155], a[128:131], v[112:115]\n"
"s_waitcnt lgkmcnt(4)\n"
"buffer_load_dwordx4 v229, s[56:59], s80 offen lds\n"
"v_mfma_f32_16x16x32_bf16 v[64:67], v[128:131], a[160:163], v[64:67]\n"
"s_add_u32 s80, s86, 0\n"
"s_add_u32 m0, s46, 8192\n"
"ds_read_b64_tr_b16 v[144:145], v199 offset:8192\n"
"v_mfma_f32_16x16x32_bf16 v[64:67], v[132:135], a[176:179], v[64:67]\n"
"s_barrier\n"
"buffer_load_dwordx4 v229, s[56:59], s80 offen lds\n"
"v_mfma_f32_16x16x32_bf16 v[68:71], v[132:135], a[180:183], v[68:71]\n"
"s_add_u32 s80, s80, s100\n"
"ds_read_b64_tr_b16 v[146:147], v199 offset:10240\n"
"v_max_f32_e32 v61, 0, v61\n"
"v_mfma_f32_16x16x32_bf16 v[68:71], v[128:131], a[164:167], v[68:71]\n"
"s_addk_i32 m0, 1024\n"
"v_max_f32_e32 v62, 0, v62\n"
"v_max_f32_e32 v63, 0, v63\n"
"v_mfma_f32_16x16x32_bf16 v[72:75], v[128:131], a[168:171], v[72:75]\n"
"ds_read_b64_tr_b16 v[148:149], v199 offset:12288\n"
"v_mfma_f32_16x16x32_bf16 v[72:75], v[132:135], a[184:187], v[72:75]\n"
"buffer_load_dwordx4 v229, s[56:59], s80 offen lds\n"
"v_mfma_f32_16x16x32_bf16 v[76:79], v[132:135], a[188:191], v[76:79]\n"
"s_sub_u32 s80, s80, s101\n"
"ds_read_b64_tr_b16 v[150:151], v199 offset:14336\n"
"v_cvt_pk_bf16_f32 v44, v44, v45\n"
"v_mfma_f32_16x16x32_bf16 v[76:79], v[128:131], a[172:175], v[76:79]\n"
"s_waitcnt lgkmcnt(4)\n"
"v_cvt_pk_bf16_f32 v45, v46, v47\n"
"v_mfma_f32_16x16x32_bf16 v[92:95], v[136:139], a[172:175], v[92:95]\n"
"s_addk_i32 m0, 1024\n"
"ds_read_b64_tr_b16 v[152:153], v199 offset:8200\n"
"v_mfma_f32_16x16x32_bf16 v[92:95], v[140:143], a[188:191], v[92:95]\n"
"buffer_load_dwordx4 v229, s[56:59], s80 offen lds\n"
"v_mfma_f32_16x16x32_bf16 v[88:91], v[140:143], a[184:187], v[88:91]\n"
"s_add_u32 s80, s80, s100\n"
"s_addk_i32 m0, 1024\n"
"ds_read_b64_tr_b16 v[154:155], v199 offset:10248\n"
"v_mfma_f32_16x16x32_bf16 v[88:91], v[136:139], a[168:171], v[88:91]\n"
"buffer_load_dwordx4 v229, s[56:59], s80 offen lds\n"
"v_mfma_f32_16x16x32_bf16 v[84:87], v[136:139], a[164:167], v[84:87]\n"
"s_add_u32 s80, s80, s101\n"
"ds_read_b64_tr_b16 v[156:157], v199 offset:12296\n"
"v_cvt_pk_bf16_f32 v46, v60, v61\n"
"v_mfma_f32_16x16x32_bf16 v[84:87], v[140:143], a[180:183], v[84:87]\n"
"s_addk_i32 m0, 1024\n"
"v_cvt_pk_bf16_f32 v47, v62, v63\n"
"v_lshrrev_b32_e32 v204, 8, v255\n"
"v_mfma_f32_16x16x32_bf16 v[80:83], v[140:143], a[176:179], v[80:83]\n"
"ds_read_b64_tr_b16 v[158:159], v199 offset:14344\n"
"v_mfma_f32_16x16x32_bf16 v[80:83], v[136:139], a[160:163], v[80:83]\n"
"s_waitcnt lgkmcnt(4)\n"
"buffer_load_dwordx4 v229, s[56:59], s80 offen lds\n"
"v_mfma_f32_16x16x32_bf16 v[96:99], v[144:147], a[160:163], v[96:99]\n"
"s_waitcnt vmcnt(13)\n"
"s_addk_i32 m0, 1024\n"
"ds_read_b64_tr_b16 v[128:129], v196 offset:0\n"
"v_mfma_f32_16x16x32_bf16 v[96:99], v[148:151], a[176:179], v[96:99]\n"
"s_add_u32 s80, s80, s100\n"
"buffer_load_dwordx4 v229, s[56:59], s80 offen lds\n"
"v_mfma_f32_16x16x32_bf16 v[100:103], v[148:151], a[180:183], v[100:103]\n"
"s_sub_u32 s80, s80, s101\n"
"ds_read_b64_tr_b16 v[130:131], v196 offset:2048\n"
"v_bfe_u32 v205, v255, 4, 4\n"
"v_mfma_f32_16x16x32_bf16 v[100:103], v[144:147], a[164:167], v[100:103]\n"
"s_addk_i32 m0, 1024\n"
"v_xor_b32_e32 v224, v204, v205\n"
"s_mul_i32 s83, s89, 128\n"
"v_mfma_f32_16x16x32_bf16 v[104:107], v[144:147], a[168:171], v[104:107]\n"
"ds_read_b64_tr_b16 v[132:133], v196 offset:4096\n"
"v_lshlrev_b32_e32 v224, 4, v224\n"
"v_mfma_f32_16x16x32_bf16 v[104:107], v[148:151], a[184:187], v[104:107]\n"
"buffer_load_dwordx4 v229, s[56:59], s80 offen lds\n"
"v_mfma_f32_16x16x32_bf16 v[108:111], v[148:151], a[188:191], v[108:111]\n"
"s_add_u32 s80, s80, s100\n"
"ds_read_b64_tr_b16 v[134:135], v196 offset:6144\n"
"v_lshl_add_u32 v224, v205, 8, v224\n"
"v_mfma_f32_16x16x32_bf16 v[108:111], v[144:147], a[172:175], v[108:111]\n"
"s_waitcnt lgkmcnt(4)\n"
"v_mfma_f32_16x16x32_bf16 v[124:127], v[152:155], a[172:175], v[124:127]\n"
"s_addk_i32 m0, 1024\n"
"ds_read_b64_tr_b16 v[136:137], v196 offset:8\n"
"v_xor_b32_e32 v224, s83, v224\n"
"v_mfma_f32_16x16x32_bf16 v[124:127], v[156:159], a[188:191], v[124:127]\n"
"buffer_load_dwordx4 v229, s[56:59], s80 offen lds\n"
"v_mfma_f32_16x16x32_bf16 v[120:123], v[156:159], a[184:187], v[120:123]\n"
"ds_read_b64_tr_b16 v[138:139], v196 offset:2056\n"
"v_xor_b32_e32 v225, 64, v224\n"
"v_mfma_f32_16x16x32_bf16 v[120:123], v[152:155], a[168:171], v[120:123]\n"
"buffer_load_dword v195, v235, s[60:63], s34 offen\n"
"v_mfma_f32_16x16x32_bf16 v[116:119], v[152:155], a[164:167], v[116:119]\n"
"s_add_u32 s76, s76, 65536\n"
"ds_read_b64_tr_b16 v[140:141], v196 offset:4104\n"
"v_xor_b32_e32 v226, 128, v224\n"
"v_mfma_f32_16x16x32_bf16 v[116:119], v[156:159], a[180:183], v[116:119]\n"
"v_mfma_f32_16x16x32_bf16 v[112:115], v[156:159], a[176:179], v[112:115]\n"
"ds_read_b64_tr_b16 v[142:143], v196 offset:6152\n"
"v_xor_b32_e32 v227, 192, v224\n"
"v_mfma_f32_16x16x32_bf16 v[112:115], v[152:155], a[160:163], v[112:115]\n"
"s_waitcnt lgkmcnt(4)\n"
"buffer_load_dword v193, v234, s[72:75], s76 offen\n"
"v_mfma_f32_16x16x32_bf16 v[64:67], v[128:131], a[192:195], v[64:67]\n"
"s_add_u32 s80, s86, 524288\n"
"ds_read_b64_tr_b16 v[144:145], v197 offset:0\n"
"ds_write_b128 v233, v[0:3] offset:0\n"
"v_mfma_f32_16x16x32_bf16 v[64:67], v[132:135], a[208:211], v[64:67]\n"
"s_add_u32 m0, s46, 73728\n"
"ds_write_b128 v233, v[4:7] offset:1024\n"
"v_mfma_f32_16x16x32_bf16 v[68:71], v[132:135], a[212:215], v[68:71]\n"
"ds_read_b64_tr_b16 v[146:147], v197 offset:2048\n"
"ds_write_b128 v233, v[8:11] offset:2048\n"
"v_mfma_f32_16x16x32_bf16 v[68:71], v[128:131], a[196:199], v[68:71]\n"
"buffer_load_dwordx4 v229, s[56:59], s80 offen lds\n"
"v_mfma_f32_16x16x32_bf16 v[72:75], v[128:131], a[200:203], v[72:75]\n"
"s_add_u32 s80, s80, s100\n"
"ds_read_b64_tr_b16 v[148:149], v197 offset:4096\n"
"ds_write_b128 v233, v[12:15] offset:3072\n"
"v_mfma_f32_16x16x32_bf16 v[72:75], v[132:135], a[216:219], v[72:75]\n"
"s_addk_i32 m0, 1024\n"
"ds_write_b128 v233, v[32:35] offset:4096\n"
"v_mfma_f32_16x16x32_bf16 v[76:79], v[132:135], a[220:223], v[76:79]\n"
"ds_read_b64_tr_b16 v[150:151], v197 offset:6144\n"
"ds_write_b128 v233, v[36:39] offset:5120\n"
"v_mfma_f32_16x16x32_bf16 v[76:79], v[128:131], a[204:207], v[76:79]\n"
"s_waitcnt lgkmcnt(10)\n"
"buffer_load_dwordx4 v229, s[56:59], s80 offen lds\n"
"v_mfma_f32_16x16x32_bf16 v[92:95], v[136:139], a[204:207], v[92:95]\n"
"s_sub_u32 s80, s80, s101\n"
"ds_read_b64_tr_b16 v[152:153], v197 offset:8\n"
"ds_write_b128 v233, v[40:43] offset:6144\n"
"v_mfma_f32_16x16x32_bf16 v[92:95], v[140:143], a[220:223], v[92:95]\n"
"s_addk_i32 m0, 1024\n"
"ds_write_b128 v233, v[44:47] offset:7168\n"
"v_mfma_f32_16x16x32_bf16 v[88:91], v[140:143], a[216:219], v[88:91]\n"
"ds_read_b64_tr_b16 v[154:155], v197 offset:2056\n"
"v_mfma_f32_16x16x32_bf16 v[88:91], v[136:139], a[200:203], v[88:91]\n"
"buffer_load_dwordx4 v229, s[56:59], s80 offen lds\n"
"v_mfma_f32_16x16x32_bf16 v[84:87], v[136:139], a[196:199], v[84:87]\n"
"s_add_u32 s80, s80, s100\n"
"ds_read_b64_tr_b16 v[156:157], v197 offset:4104\n"
"v_mfma_f32_16x16x32_bf16 v[84:87], v[140:143], a[212:215], v[84:87]\n"
"s_addk_i32 m0, 1024\n"
"v_mfma_f32_16x16x32_bf16 v[80:83], v[140:143], a[208:211], v[80:83]\n"
"ds_read_b64_tr_b16 v[158:159], v197 offset:6152\n"
"v_mfma_f32_16x16x32_bf16 v[80:83], v[136:139], a[192:195], v[80:83]\n"
"s_waitcnt lgkmcnt(7)\n"
"buffer_load_dwordx4 v229, s[56:59], s80 offen lds\n"
"v_mfma_f32_16x16x32_bf16 v[96:99], v[144:147], a[192:195], v[96:99]\n"
"s_waitcnt vmcnt(14)\n"
"ds_read_b64_tr_b16 v[128:129], v198 offset:0\n"
"v_mfma_f32_16x16x32_bf16 v[96:99], v[148:151], a[208:211], v[96:99]\n"
"s_add_u32 s80, s80, s101\n"
"v_mfma_f32_16x16x32_bf16 v[100:103], v[148:151], a[212:215], v[100:103]\n"
"s_addk_i32 m0, 1024\n"
"ds_read_b64_tr_b16 v[130:131], v198 offset:2048\n"
"v_mfma_f32_16x16x32_bf16 v[100:103], v[144:147], a[196:199], v[100:103]\n"
"buffer_load_dwordx4 v229, s[56:59], s80 offen lds\n"
"v_mfma_f32_16x16x32_bf16 v[104:107], v[144:147], a[200:203], v[104:107]\n"
"s_add_u32 s80, s80, s100\n"
"ds_read_b64_tr_b16 v[132:133], v198 offset:4096\n"
"v_mfma_f32_16x16x32_bf16 v[104:107], v[148:151], a[216:219], v[104:107]\n"
"s_addk_i32 m0, 1024\n"
"v_mfma_f32_16x16x32_bf16 v[108:111], v[148:151], a[220:223], v[108:111]\n"
"ds_read_b64_tr_b16 v[134:135], v198 offset:6144\n"
"v_mfma_f32_16x16x32_bf16 v[108:111], v[144:147], a[204:207], v[108:111]\n"
"s_waitcnt lgkmcnt(4)\n"
"buffer_load_dwordx4 v229, s[56:59], s80 offen lds\n"
"v_mfma_f32_16x16x32_bf16 v[124:127], v[152:155], a[204:207], v[124:127]\n"
"s_sub_u32 s80, s80, s101\n"
"ds_read_b64_tr_b16 v[136:137], v198 offset:8\n"
"v_mfma_f32_16x16x32_bf16 v[124:127], v[156:159], a[220:223], v[124:127]\n"
"s_addk_i32 m0, 1024\n"
"v_mfma_f32_16x16x32_bf16 v[120:123], v[156:159], a[216:219], v[120:123]\n"
"ds_read_b64_tr_b16 v[138:139], v198 offset:2056\n"
"v_mfma_f32_16x16x32_bf16 v[120:123], v[152:155], a[200:203], v[120:123]\n"
"buffer_load_dwordx4 v229, s[56:59], s80 offen lds\n"
"v_mfma_f32_16x16x32_bf16 v[116:119], v[152:155], a[196:199], v[116:119]\n"
"s_add_u32 s80, s80, s100\n"
"ds_read_b64_tr_b16 v[140:141], v198 offset:4104\n"
"v_mfma_f32_16x16x32_bf16 v[116:119], v[156:159], a[212:215], v[116:119]\n"
"s_addk_i32 m0, 1024\n"
"v_mfma_f32_16x16x32_bf16 v[112:115], v[156:159], a[208:211], v[112:115]\n"
"ds_read_b64_tr_b16 v[142:143], v198 offset:6152\n"
"v_mfma_f32_16x16x32_bf16 v[112:115], v[152:155], a[192:195], v[112:115]\n"
"s_waitcnt lgkmcnt(4)\n"
"buffer_load_dwordx4 v229, s[56:59], s80 offen lds\n"
"v_mfma_f32_16x16x32_bf16 v[64:67], v[128:131], a[224:227], v[64:67]\n"
"s_add_u32 s80, s86, 1048576\n"
"ds_read_b64_tr_b16 v[144:145], v199 offset:0\n"
"v_mfma_f32_16x16x32_bf16 v[64:67], v[132:135], a[240:243], v[64:67]\n"
"s_add_u32 m0, s46, 0\n"
"v_mfma_f32_16x16x32_bf16 v[68:71], v[132:135], a[244:247], v[68:71]\n"
"ds_read_b64_tr_b16 v[146:147], v199 offset:2048\n"
"v_mfma_f32_16x16x32_bf16 v[68:71], v[128:131], a[228:231], v[68:71]\n"
"buffer_load_dwordx4 v229, s[56:59], s80 offen lds\n"
"v_mfma_f32_16x16x32_bf16 v[72:75], v[128:131], a[232:235], v[72:75]\n"
"s_add_u32 s80, s80, s100\n"
"ds_read_b64_tr_b16 v[148:149], v199 offset:4096\n"
"v_mfma_f32_16x16x32_bf16 v[72:75], v[132:135], a[248:251], v[72:75]\n"
"s_addk_i32 m0, 1024\n"
"v_mfma_f32_16x16x32_bf16 v[76:79], v[132:135], a[252:255], v[76:79]\n"
"ds_read_b64_tr_b16 v[150:151], v199 offset:6144\n"
"v_mfma_f32_16x16x32_bf16 v[76:79], v[128:131], a[236:239], v[76:79]\n"
"s_waitcnt lgkmcnt(4)\n"
"buffer_load_dwordx4 v229, s[56:59], s80 offen lds\n"
"v_mfma_f32_16x16x32_bf16 v[92:95], v[136:139], a[236:239], v[92:95]\n"
"s_sub_u32 s80, s80, s101\n"
"ds_read_b64_tr_b16 v[152:153], v199 offset:8\n"
"v_mfma_f32_16x16x32_bf16 v[92:95], v[140:143], a[252:255], v[92:95]\n"
"s_addk_i32 m0, 1024\n"
"v_mfma_f32_16x16x32_bf16 v[88:91], v[140:143], a[248:251], v[88:91]\n"
"ds_read_b64_tr_b16 v[154:155], v199 offset:2056\n"
"v_mfma_f32_16x16x32_bf16 v[88:91], v[136:139], a[232:235], v[88:91]\n"
"buffer_load_dwordx4 v229, s[56:59], s80 offen lds\n"
"v_mfma_f32_16x16x32_bf16 v[84:87], v[136:139], a[228:231], v[84:87]\n"
"s_add_u32 s80, s80, s100\n"
"ds_read_b64_tr_b16 v[156:157], v199 offset:4104\n"
"v_mfma_f32_16x16x32_bf16 v[84:87], v[140:143], a[244:247], v[84:87]\n"
"s_addk_i32 m0, 1024\n"
"v_mfma_f32_16x16x32_bf16 v[80:83], v[140:143], a[240:243], v[80:83]\n"
"ds_read_b64_tr_b16 v[158:159], v199 offset:6152\n"
"v_mfma_f32_16x16x32_bf16 v[80:83], v[136:139], a[224:227], v[80:83]\n"
"s_waitcnt lgkmcnt(4)\n"
"buffer_load_dwordx4 v229, s[56:59], s80 offen lds\n"
"v_mfma_f32_16x16x32_bf16 v[96:99], v[144:147], a[224:227], v[96:99]\n"
"s_waitcnt vmcnt(14)\n"
"ds_read_b64_tr_b16 v[128:129], v196 offset:8192\n"
"v_mfma_f32_16x16x32_bf16 v[96:99], v[148:151], a[240:243], v[96:99]\n"
"s_add_u32 s80, s80, s101\n"
"v_mfma_f32_16x16x32_bf16 v[100:103], v[148:151], a[244:247], v[100:103]\n"
"s_addk_i32 m0, 1024\n"
"ds_read_b64_tr_b16 v[130:131], v196 offset:10240\n"
"v_mfma_f32_16x16x32_bf16 v[100:103], v[144:147], a[228:231], v[100:103]\n"
"buffer_load_dwordx4 v229, s[56:59], s80 offen lds\n"
"v_mfma_f32_16x16x32_bf16 v[104:107], v[144:147], a[232:235], v[104:107]\n"
"s_add_u32 s80, s80, s100\n"
"ds_read_b64_tr_b16 v[132:133], v196 offset:12288\n"
"v_mfma_f32_16x16x32_bf16 v[104:107], v[148:151], a[248:251], v[104:107]\n"
"s_addk_i32 m0, 1024\n"
"v_mfma_f32_16x16x32_bf16 v[108:111], v[148:151], a[252:255], v[108:111]\n"
"ds_read_b64_tr_b16 v[134:135], v196 offset:14336\n"
"v_mfma_f32_16x16x32_bf16 v[108:111], v[144:147], a[236:239], v[108:111]\n"
"s_waitcnt lgkmcnt(4)\n"
"buffer_load_dwordx4 v229, s[56:59], s80 offen lds\n"
"v_mfma_f32_16x16x32_bf16 v[124:127], v[152:155], a[236:239], v[124:127]\n"
"s_sub_u32 s80, s80, s101\n"
"ds_read_b64_tr_b16 v[136:137], v196 offset:8200\n"
"v_mfma_f32_16x16x32_bf16 v[124:127], v[156:159], a[252:255], v[124:127]\n"
"s_addk_i32 m0, 1024\n"
"v_mfma_f32_16x16x32_bf16 v[120:123], v[156:159], a[248:251], v[120:123]\n"
"ds_read_b64_tr_b16 v[138:139], v196 offset:10248\n"
"v_mfma_f32_16x16x32_bf16 v[120:123], v[152:155], a[232:235], v[120:123]\n"
"buffer_load_dwordx4 v229, s[56:59], s80 offen lds\n"
"v_mfma_f32_16x16x32_bf16 v[116:119], v[152:155], a[228:231], v[116:119]\n"
"s_add_u32 s80, s80, s100\n"
"ds_read_b64_tr_b16 v[140:141], v196 offset:12296\n"
"v_mfma_f32_16x16x32_bf16 v[116:119], v[156:159], a[244:247], v[116:119]\n"
"s_addk_i32 m0, 1024\n"
"v_mfma_f32_16x16x32_bf16 v[112:115], v[156:159], a[240:243], v[112:115]\n"
"ds_read_b64_tr_b16 v[142:143], v196 offset:14344\n"
"v_mfma_f32_16x16x32_bf16 v[112:115], v[152:155], a[224:227], v[112:115]\n"
"s_waitcnt lgkmcnt(4)\n"
"buffer_load_dwordx4 v229, s[56:59], s80 offen lds\n"
"v_mfma_f32_16x16x32_bf16 v[0:3], v[128:131], a[0:3], 0\n"
"s_add_u32 s80, s86, 1572864\n"
"ds_read_b64_tr_b16 v[144:145], v197 offset:8192\n"
"s_add_u32 s84, s78, s79\n"
"v_mfma_f32_16x16x32_bf16 v[0:3], v[132:135], a[16:19], v[0:3]\n"
"s_add_u32 m0, s46, 65536\n"
"v_mfma_f32_16x16x32_bf16 v[4:7], v[132:135], a[20:23], 0\n"
"ds_read_b64_tr_b16 v[146:147], v197 offset:10240\n"
"v_mfma_f32_16x16x32_bf16 v[4:7], v[128:131], a[4:7], v[4:7]\n"
"buffer_load_dwordx4 v229, s[56:59], s80 offen lds\n"
"v_mfma_f32_16x16x32_bf16 v[8:11], v[128:131], a[8:11], 0\n"
"s_add_u32 s80, s80, s100\n"
"ds_read_b64_tr_b16 v[148:149], v197 offset:12288\n"
"s_and_b32 s84, s84, 7\n"
"v_mfma_f32_16x16x32_bf16 v[8:11], v[132:135], a[24:27], v[8:11]\n"
"s_addk_i32 m0, 1024\n"
"v_mfma_f32_16x16x32_bf16 v[12:15], v[132:135], a[28:31], 0\n"
"s_mov_b64 s[52:53], s[20:21]\n"
"ds_read_b64_tr_b16 v[150:151], v197 offset:14336\n"
"v_mfma_f32_16x16x32_bf16 v[12:15], v[128:131], a[12:15], v[12:15]\n"
"s_waitcnt lgkmcnt(4)\n"
"buffer_load_dwordx4 v229, s[56:59], s80 offen lds\n"
"v_mfma_f32_16x16x32_bf16 v[28:31], v[136:139], a[12:15], 0\n"
"s_sub_u32 s80, s80, s101\n"
"ds_read_b64_tr_b16 v[152:153], v197 offset:8200\n"
"v_mfma_f32_16x16x32_bf16 v[28:31], v[140:143], a[28:31], v[28:31]\n"
"s_addk_i32 m0, 1024\n"
"s_mov_b32 s54, 0\n"
"v_mfma_f32_16x16x32_bf16 v[24:27], v[140:143], a[24:27], 0\n"
"ds_read_b64_tr_b16 v[154:155], v197 offset:10248\n"
"v_mfma_f32_16x16x32_bf16 v[24:27], v[136:139], a[8:11], v[24:27]\n"
"buffer_load_dwordx4 v229, s[56:59], s80 offen lds\n"
"v_mfma_f32_16x16x32_bf16 v[20:23], v[136:139], a[4:7], 0\n"
"s_add_u32 s80, s80, s100\n"
"ds_read_b64_tr_b16 v[156:157], v197 offset:12296\n"
"s_mov_b32 s55, 0x20000\n"
"v_mfma_f32_16x16x32_bf16 v[20:23], v[140:143], a[20:23], v[20:23]\n"
"s_addk_i32 m0, 1024\n"
"v_mfma_f32_16x16x32_bf16 v[16:19], v[140:143], a[16:19], 0\n"
"ds_read_b64_tr_b16 v[158:159], v197 offset:14344\n"
"v_mfma_f32_16x16x32_bf16 v[16:19], v[136:139], a[0:3], v[16:19]\n"
"s_waitcnt lgkmcnt(4)\n"
"buffer_load_dwordx4 v229, s[56:59], s80 offen lds\n"
"v_mfma_f32_16x16x32_bf16 v[32:35], v[144:147], a[0:3], 0\n"
"s_waitcnt vmcnt(12)\n"
"ds_read_b64_tr_b16 v[128:129], v198 offset:8192\n"
"s_mul_i32 s22, s79, 610304\n"
"v_mfma_f32_16x16x32_bf16 v[32:35], v[148:151], a[16:19], v[32:35]\n"
"s_add_u32 s80, s80, s101\n"
"v_mfma_f32_16x16x32_bf16 v[36:39], v[148:151], a[20:23], 0\n"
"s_addk_i32 m0, 1024\n"
"ds_read_b64_tr_b16 v[130:131], v198 offset:10240\n"
"v_mfma_f32_16x16x32_bf16 v[36:39], v[144:147], a[4:7], v[36:39]\n"
"buffer_load_dwordx4 v229, s[56:59], s80 offen lds\n"
"v_mfma_f32_16x16x32_bf16 v[40:43], v[144:147], a[8:11], 0\n"
"s_add_u32 s80, s80, s100\n"
"ds_read_b64_tr_b16 v[132:133], v198 offset:12288\n"
"s_mul_i32 s83, s84, 75776\n"
"v_mfma_f32_16x16x32_bf16 v[40:43], v[148:151], a[24:27], v[40:43]\n"
"s_addk_i32 m0, 1024\n"
"v_mfma_f32_16x16x32_bf16 v[44:47], v[148:151], a[28:31], 0\n"
"s_add_u32 s22, s22, s83\n"
"ds_read_b64_tr_b16 v[134:135], v198 offset:14336\n"
"v_mfma_f32_16x16x32_bf16 v[44:47], v[144:147], a[12:15], v[44:47]\n"
"s_waitcnt lgkmcnt(4)\n"
"buffer_load_dwordx4 v229, s[56:59], s80 offen lds\n"
"v_mfma_f32_16x16x32_bf16 v[60:63], v[152:155], a[12:15], 0\n"
"s_sub_u32 s80, s80, s101\n"
"ds_read_b64_tr_b16 v[136:137], v198 offset:8200\n"
"v_mfma_f32_16x16x32_bf16 v[60:63], v[156:159], a[28:31], v[60:63]\n"
"s_addk_i32 m0, 1024\n"
"s_mul_i32 s83, s77, 18432\n"
"v_mfma_f32_16x16x32_bf16 v[56:59], v[156:159], a[24:27], 0\n"
"ds_read_b64_tr_b16 v[138:139], v198 offset:10248\n"
"v_mfma_f32_16x16x32_bf16 v[56:59], v[152:155], a[8:11], v[56:59]\n"
"buffer_load_dwordx4 v229, s[56:59], s80 offen lds\n"
"v_mfma_f32_16x16x32_bf16 v[52:55], v[152:155], a[4:7], 0\n"
"s_add_u32 s80, s80, s100\n"
"ds_read_b64_tr_b16 v[140:141], v198 offset:12296\n"
"s_add_u32 s22, s22, s83\n"
"v_mfma_f32_16x16x32_bf16 v[52:55], v[156:159], a[20:23], v[52:55]\n"
"s_addk_i32 m0, 1024\n"
"v_mfma_f32_16x16x32_bf16 v[48:51], v[156:159], a[16:19], 0\n"
"ds_read_b64_tr_b16 v[142:143], v198 offset:14344\n"
"v_mfma_f32_16x16x32_bf16 v[48:51], v[152:155], a[0:3], v[48:51]\n"
"s_waitcnt lgkmcnt(4)\n"
"buffer_load_dwordx4 v229, s[56:59], s80 offen lds\n"
"v_mfma_f32_16x16x32_bf16 v[0:3], v[128:131], a[32:35], v[0:3]\n"
"s_add_u32 s80, s86, 2097152\n"
"ds_read_b64_tr_b16 v[144:145], v199 offset:8192\n"
"s_add_u32 s54, s22, 14336\n"
"v_mfma_f32_16x16x32_bf16 v[0:3], v[132:135], a[48:51], v[0:3]\n"
"s_add_u32 m0, s46, 8192\n"
"v_mfma_f32_16x16x32_bf16 v[4:7], v[132:135], a[52:55], v[4:7]\n"
"ds_read_b64_tr_b16 v[146:147], v199 offset:10240\n"
"v_mfma_f32_16x16x32_bf16 v[4:7], v[128:131], a[36:39], v[4:7]\n"
"buffer_load_dwordx4 v229, s[56:59], s80 offen lds\n"
"v_mfma_f32_16x16x32_bf16 v[8:11], v[128:131], a[40:43], v[8:11]\n"
"s_add_u32 s80, s80, s100\n"
"ds_read_b64_tr_b16 v[148:149], v199 offset:12288\n"
"s_sub_u32 s34, s22, 1024\n"
"v_mfma_f32_16x16x32_bf16 v[8:11], v[132:135], a[56:59], v[8:11]\n"
"s_addk_i32 m0, 1024\n"
"v_mfma_f32_16x16x32_bf16 v[12:15], v[132:135], a[60:63], v[12:15]\n"
"s_mul_i32 s85, s78, 524288\n"
"ds_read_b64_tr_b16 v[150:151], v199 offset:14336\n"
"v_mfma_f32_16x16x32_bf16 v[12:15], v[128:131], a[44:47], v[12:15]\n"
"s_waitcnt lgkmcnt(4)\n"
"buffer_load_dwordx4 v229, s[56:59], s80 offen lds\n"
"v_mfma_f32_16x16x32_bf16 v[28:31], v[136:139], a[44:47], v[28:31]\n"
"s_sub_u32 s80, s80, s101\n"
"ds_read_b64_tr_b16 v[152:153], v199 offset:8200\n"
"v_mfma_f32_16x16x32_bf16 v[28:31], v[140:143], a[60:63], v[28:31]\n"
"s_addk_i32 m0, 1024\n"
"s_mul_i32 s83, s77, 131072\n"
"v_mfma_f32_16x16x32_bf16 v[24:27], v[140:143], a[56:59], v[24:27]\n"
"ds_read_b64_tr_b16 v[154:155], v199 offset:10248\n"
"v_mfma_f32_16x16x32_bf16 v[24:27], v[136:139], a[40:43], v[24:27]\n"
"buffer_load_dwordx4 v229, s[56:59], s80 offen lds\n"
"v_mfma_f32_16x16x32_bf16 v[20:23], v[136:139], a[36:39], v[20:23]\n"
"s_add_u32 s80, s80, s100\n"
"ds_read_b64_tr_b16 v[156:157], v199 offset:12296\n"
"s_add_u32 s85, s85, s83\n"
"v_mfma_f32_16x16x32_bf16 v[20:23], v[140:143], a[52:55], v[20:23]\n"
"s_addk_i32 m0, 1024\n"
"v_mfma_f32_16x16x32_bf16 v[16:19], v[140:143], a[48:51], v[16:19]\n"
"ds_read_b64_tr_b16 v[158:159], v199 offset:14344\n"
"v_mfma_f32_16x16x32_bf16 v[16:19], v[136:139], a[32:35], v[16:19]\n"
"s_waitcnt lgkmcnt(4)\n"
"buffer_load_dwordx4 v229, s[56:59], s80 offen lds\n"
"v_mfma_f32_16x16x32_bf16 v[32:35], v[144:147], a[32:35], v[32:35]\n"
"s_waitcnt vmcnt(12)\n"
"ds_read_b64_tr_b16 v[128:129], v196 offset:0\n"
"s_mul_i32 s83, s35, 8192\n"
"v_mfma_f32_16x16x32_bf16 v[32:35], v[148:151], a[48:51], v[32:35]\n"
"s_add_u32 s80, s80, s101\n"
"v_mfma_f32_16x16x32_bf16 v[36:39], v[148:151], a[52:55], v[36:39]\n"
"s_addk_i32 m0, 1024\n"
"ds_read_b64_tr_b16 v[130:131], v196 offset:2048\n"
"v_mfma_f32_16x16x32_bf16 v[36:39], v[144:147], a[36:39], v[36:39]\n"
"buffer_load_dwordx4 v229, s[56:59], s80 offen lds\n"
"v_mfma_f32_16x16x32_bf16 v[40:43], v[144:147], a[40:43], v[40:43]\n"
"s_add_u32 s80, s80, s100\n"
"ds_read_b64_tr_b16 v[132:133], v196 offset:4096\n"
"s_add_u32 s85, s85, s83\n"
"v_mfma_f32_16x16x32_bf16 v[40:43], v[148:151], a[56:59], v[40:43]\n"
"s_addk_i32 m0, 1024\n"
"v_mfma_f32_16x16x32_bf16 v[44:47], v[148:151], a[60:63], v[44:47]\n"
"s_add_u32 s85, s85, -4096\n"
"ds_read_b64_tr_b16 v[134:135], v196 offset:6144\n"
"v_mfma_f32_16x16x32_bf16 v[44:47], v[144:147], a[44:47], v[44:47]\n"
"s_waitcnt lgkmcnt(4)\n"
"buffer_load_dwordx4 v229, s[56:59], s80 offen lds\n"
"v_mfma_f32_16x16x32_bf16 v[60:63], v[152:155], a[44:47], v[60:63]\n"
"s_sub_u32 s80, s80, s101\n"
"ds_read_b64_tr_b16 v[136:137], v196 offset:8\n"
"v_mfma_f32_16x16x32_bf16 v[60:63], v[156:159], a[60:63], v[60:63]\n"
"s_addk_i32 m0, 1024\n"
"v_lshrrev_b32_e32 v204, 6, v255\n"
"v_mfma_f32_16x16x32_bf16 v[56:59], v[156:159], a[56:59], v[56:59]\n"
".LPG4_%=:\n"
"s_cbranch_execz .LPGR_%=\n"
"ds_read_b64_tr_b16 v[138:139], v196 offset:2056\n"
"v_mfma_f32_16x16x32_bf16 v[56:59], v[152:155], a[40:43], v[56:59]\n"
"buffer_load_dwordx4 v229, s[56:59], s80 offen lds\n"
"v_mfma_f32_16x16x32_bf16 v[52:55], v[152:155], a[36:39], v[52:55]\n"
"s_add_u32 s80, s80, s100\n"
"ds_read_b64_tr_b16 v[140:141], v196 offset:4104\n"
"v_bfe_u32 v228, v255, 4, 2\n"
"v_mfma_f32_16x16x32_bf16 v[52:55], v[156:159], a[52:55], v[52:55]\n"
"s_addk_i32 m0, 1024\n"
"v_mfma_f32_16x16x32_bf16 v[48:51], v[156:159], a[48:51], v[48:51]\n"
"ds_read_b64_tr_b16 v[142:143], v196 offset:6152\n"
"v_mfma_f32_16x16x32_bf16 v[48:51], v[152:155], a[32:35], v[48:51]\n"
"s_waitcnt lgkmcnt(4)\n"
"buffer_load_dwordx4 v229, s[56:59], s80 offen lds\n"
"v_mfma_f32_16x16x32_bf16 v[0:3], v[128:131], a[64:67], v[0:3]\n"
"s_add_u32 s80, s86, 2621440\n"
"ds_read_b64_tr_b16 v[144:145], v197 offset:0\n"
"v_lshlrev_b32_e32 v228, 3, v228\n"
"v_mfma_f32_16x16x32_bf16 v[0:3], v[132:135], a[80:83], v[0:3]\n"
"s_add_u32 m0, s46, 73728\n"
"v_mfma_f32_16x16x32_bf16 v[4:7], v[132:135], a[84:87], v[4:7]\n"
"ds_read_b64_tr_b16 v[146:147], v197 offset:2048\n"
"v_mfma_f32_16x16x32_bf16 v[4:7], v[128:131], a[68:71], v[4:7]\n"
"buffer_load_dwordx4 v229, s[56:59], s80 offen lds\n"
"v_mfma_f32_16x16x32_bf16 v[8:11], v[128:131], a[72:75], v[8:11]\n"
"s_add_u32 s80, s80, s100\n"
"ds_read_b64_tr_b16 v[148:149], v197 offset:4096\n"
"v_lshl_add_u32 v228, v204, 8, v228\n"
"v_mfma_f32_16x16x32_bf16 v[8:11], v[132:135], a[88:91], v[8:11]\n"
"s_addk_i32 m0, 1024\n"
"v_mfma_f32_16x16x32_bf16 v[12:15], v[132:135], a[92:95], v[12:15]\n"
"ds_read_b64_tr_b16 v[150:151], v197 offset:6144\n"
"v_and_b32_e32 v204, 7, v204\n"
"v_mfma_f32_16x16x32_bf16 v[12:15], v[128:131], a[76:79], v[12:15]\n"
"s_waitcnt lgkmcnt(4)\n"
"buffer_load_dwordx4 v229, s[56:59], s80 offen lds\n"
"v_mfma_f32_16x16x32_bf16 v[28:31], v[136:139], a[76:79], v[28:31]\n"
"s_sub_u32 s80, s80, s101\n"
"ds_read_b64_tr_b16 v[152:153], v197 offset:8\n"
"v_mfma_f32_16x16x32_bf16 v[28:31], v[140:143], a[92:95], v[28:31]\n"
"s_addk_i32 m0, 1024\n"
"v_lshl_add_u32 v228, v204, 5, v228\n"
"v_mfma_f32_16x16x32_bf16 v[24:27], v[140:143], a[88:91], v[24:27]\n"
"ds_read_b64_tr_b16 v[154:155], v197 offset:2056\n"
"v_mfma_f32_16x16x32_bf16 v[24:27], v[136:139], a[72:75], v[24:27]\n"
"buffer_load_dwordx4 v229, s[56:59], s80 offen lds\n"
"v_mfma_f32_16x16x32_bf16 v[20:23], v[136:139], a[68:71], v[20:23]\n"
"s_add_u32 s80, s80, s100\n"
"ds_read_b64_tr_b16 v[156:157], v197 offset:4104\n"
"v_add_u32_e32 v228, 65536, v228\n"
"v_mfma_f32_16x16x32_bf16 v[20:23], v[140:143], a[84:87], v[20:23]\n"
"s_addk_i32 m0, 1024\n"
"v_mfma_f32_16x16x32_bf16 v[16:19], v[140:143], a[80:83], v[16:19]\n"
"ds_read_b64_tr_b16 v[158:159], v197 offset:6152\n"
"v_mfma_f32_16x16x32_bf16 v[16:19], v[136:139], a[64:67], v[16:19]\n"
"s_waitcnt lgkmcnt(4)\n"
"buffer_load_dwordx4 v229, s[56:59], s80 offen lds\n"
"v_mfma_f32_16x16x32_bf16 v[32:35], v[144:147], a[64:67], v[32:35]\n"
"s_waitcnt vmcnt(12)\n"
"ds_read_b64_tr_b16 v[128:129], v198 offset:0\n"
"v_cvt_pk_bf16_f32 v160, v64, v65\n"
"v_mfma_f32_16x16x32_bf16 v[32:35], v[148:151], a[80:83], v[32:35]\n"
"s_add_u32 s80, s80, s101\n"
"v_mfma_f32_16x16x32_bf16 v[36:39], v[148:151], a[84:87], v[36:39]\n"
"s_addk_i32 m0, 1024\n"
"ds_read_b64_tr_b16 v[130:131], v198 offset:2048\n"
"v_mfma_f32_16x16x32_bf16 v[36:39], v[144:147], a[68:71], v[36:39]\n"
"buffer_load_dwordx4 v229, s[56:59], s80 offen lds\n"
"v_mfma_f32_16x16x32_bf16 v[40:43], v[144:147], a[72:75], v[40:43]\n"
"s_add_u32 s80, s80, s100\n"
"ds_read_b64_tr_b16 v[132:133], v198 offset:4096\n"
"v_cvt_pk_bf16_f32 v161, v66, v67\n"
"v_mfma_f32_16x16x32_bf16 v[40:43], v[148:151], a[88:91], v[40:43]\n"
"s_addk_i32 m0, 1024\n"
"v_mfma_f32_16x16x32_bf16 v[44:47], v[148:151], a[92:95], v[44:47]\n"
"ds_read_b64_tr_b16 v[134:135], v198 offset:6144\n"
"v_cvt_pk_bf16_f32 v162, v80, v81\n"
"v_mfma_f32_16x16x32_bf16 v[44:47], v[144:147], a[76:79], v[44:47]\n"
"s_waitcnt lgkmcnt(4)\n"
"buffer_load_dwordx4 v229, s[56:59], s80 offen lds\n"
"v_mfma_f32_16x16x32_bf16 v[60:63], v[152:155], a[76:79], v[60:63]\n"
"s_sub_u32 s80, s80, s101\n"
"ds_read_b64_tr_b16 v[136:137], v198 offset:8\n"
"v_mfma_f32_16x16x32_bf16 v[60:63], v[156:159], a[92:95], v[60:63]\n"
"s_addk_i32 m0, 1024\n"
"v_cvt_pk_bf16_f32 v163, v82, v83\n"
"v_mfma_f32_16x16x32_bf16 v[56:59], v[156:159], a[88:91], v[56:59]\n"
"ds_read_b64_tr_b16 v[138:139], v198 offset:2056\n"
"v_mfma_f32_16x16x32_bf16 v[56:59], v[152:155], a[72:75], v[56:59]\n"
"buffer_load_dwordx4 v229, s[56:59], s80 offen lds\n"
"v_mfma_f32_16x16x32_bf16 v[52:55], v[152:155], a[68:71], v[52:55]\n"
"s_add_u32 s80, s80, s100\n"
"ds_read_b64_tr_b16 v[140:141], v198 offset:4104\n"
"v_cvt_pk_bf16_f32 v164, v68, v69\n"
"v_mfma_f32_16x16x32_bf16 v[52:55], v[156:159], a[84:87], v[52:55]\n"
"s_addk_i32 m0, 1024\n"
"v_mfma_f32_16x16x32_bf16 v[48:51], v[156:159], a[80:83], v[48:51]\n"
"ds_read_b64_tr_b16 v[142:143], v198 offset:6152\n"
"v_mfma_f32_16x16x32_bf16 v[48:51], v[152:155], a[64:67], v[48:51]\n"
"s_waitcnt lgkmcnt(4)\n"
"buffer_load_dwordx4 v229, s[56:59], s80 offen lds\n"
"v_mfma_f32_16x16x32_bf16 v[0:3], v[128:131], a[96:99], v[0:3]\n"
"s_add_u32 s80, s86, 3145728\n"
"ds_read_b64_tr_b16 v[144:145], v199 offset:0\n"
"v_cvt_pk_bf16_f32 v165, v70, v71\n"
"v_mfma_f32_16x16x32_bf16 v[0:3], v[132:135], a[112:115], v[0:3]\n"
"s_add_u32 m0, s46, 0\n"
"v_mfma_f32_16x16x32_bf16 v[4:7], v[132:135], a[116:119], v[4:7]\n"
"ds_read_b64_tr_b16 v[146:147], v199 offset:2048\n"
"v_mfma_f32_16x16x32_bf16 v[4:7], v[128:131], a[100:103], v[4:7]\n"
"buffer_load_dwordx4 v229, s[56:59], s80 offen lds\n"
"v_mfma_f32_16x16x32_bf16 v[8:11], v[128:131], a[104:107], v[8:11]\n"
"s_add_u32 s80, s80, s100\n"
"ds_read_b64_tr_b16 v[148:149], v199 offset:4096\n"
"v_cvt_pk_bf16_f32 v166, v84, v85\n"
"v_mfma_f32_16x16x32_bf16 v[8:11], v[132:135], a[120:123], v[8:11]\n"
"s_addk_i32 m0, 1024\n"
"v_mfma_f32_16x16x32_bf16 v[12:15], v[132:135], a[124:127], v[12:15]\n"
"ds_read_b64_tr_b16 v[150:151], v199 offset:6144\n"
"v_mfma_f32_16x16x32_bf16 v[12:15], v[128:131], a[108:111], v[12:15]\n"
"s_waitcnt lgkmcnt(4)\n"
"buffer_load_dwordx4 v229, s[56:59], s80 offen lds\n"
"v_mfma_f32_16x16x32_bf16 v[28:31], v[136:139], a[108:111], v[28:31]\n"
"s_sub_u32 s80, s80, s101\n"
"ds_read_b64_tr_b16 v[152:153], v199 offset:8\n"
"v_cvt_pk_bf16_f32 v167, v86, v87\n"
"v_mfma_f32_16x16x32_bf16 v[28:31], v[140:143], a[124:127], v[28:31]\n"
"s_addk_i32 m0, 1024\n"
"v_mfma_f32_16x16x32_bf16 v[24:27], v[140:143], a[120:123], v[24:27]\n"
"ds_read_b64_tr_b16 v[154:155], v199 offset:2056\n"
"v_cvt_pk_bf16_f32 v168, v72, v73\n"
"v_mfma_f32_16x16x32_bf16 v[24:27], v[136:139], a[104:107], v[24:27]\n"
"buffer_load_dwordx4 v229, s[56:59], s80 offen lds\n"
"v_mfma_f32_16x16x32_bf16 v[20:23], v[136:139], a[100:103], v[20:23]\n"
"s_add_u32 s80, s80, s100\n"
"ds_read_b64_tr_b16 v[156:157], v199 offset:4104\n"
"v_mfma_f32_16x16x32_bf16 v[20:23], v[140:143], a[116:119], v[20:23]\n"
"s_addk_i32 m0, 1024\n"
"v_mfma_f32_16x16x32_bf16 v[16:19], v[140:143], a[112:115], v[16:19]\n"
"ds_read_b64_tr_b16 v[158:159], v199 offset:6152\n"
"v_cvt_pk_bf16_f32 v169, v74, v75\n"
"v_mfma_f32_16x16x32_bf16 v[16:19], v[136:139], a[96:99], v[16:19]\n"
"s_waitcnt lgkmcnt(4)\n"
"buffer_load_dwordx4 v229, s[56:59], s80 offen lds\n"
"v_mfma_f32_16x16x32_bf16 v[32:35], v[144:147], a[96:99], v[32:35]\n"
"s_waitcnt vmcnt(12)\n"
"ds_read_b64_tr_b16 v[128:129], v196 offset:8192\n"
"v_mfma_f32_16x16x32_bf16 v[32:35], v[148:151], a[112:115], v[32:35]\n"
"s_add_u32 s80, s80, s101\n"
"v_cvt_pk_bf16_f32 v170, v88, v89\n"
"v_mfma_f32_16x16x32_bf16 v[36:39], v[148:151], a[116:119], v[36:39]\n"
"s_addk_i32 m0, 1024\n"
"ds_read_b64_tr_b16 v[130:131], v196 offset:10240\n"
"v_mfma_f32_16x16x32_bf16 v[36:39], v[144:147], a[100:103], v[36:39]\n"
"buffer_load_dwordx4 v229, s[56:59], s80 offen lds\n"
"v_mfma_f32_16x16x32_bf16 v[40:43], v[144:147], a[104:107], v[40:43]\n"
"s_add_u32 s80, s80, s100\n"
"ds_read_b64_tr_b16 v[132:133], v196 offset:12288\n"
"v_mfma_f32_16x16x32_bf16 v[40:43], v[148:151], a[120:123], v[40:43]\n"
"s_addk_i32 m0, 1024\n"
"v_cvt_pk_bf16_f32 v171, v90, v91\n"
"v_mfma_f32_16x16x32_bf16 v[44:47], v[148:151], a[124:127], v[44:47]\n"
"ds_read_b64_tr_b16 v[134:135], v196 offset:14336\n"
"v_mfma_f32_16x16x32_bf16 v[44:47], v[144:147], a[108:111], v[44:47]\n"
"s_waitcnt lgkmcnt(4)\n"
"buffer_load_dwordx4 v229, s[56:59], s80 offen lds\n"
"v_mfma_f32_16x16x32_bf16 v[60:63], v[152:155], a[108:111], v[60:63]\n"
"s_sub_u32 s80, s80, s101\n"
"ds_read_b64_tr_b16 v[136:137], v196 offset:8200\n"
"v_cvt_pk_bf16_f32 v172, v76, v77\n"
"v_mfma_f32_16x16x32_bf16 v[60:63], v[156:159], a[124:127], v[60:63]\n"
"s_addk_i32 m0, 1024\n"
"v_mfma_f32_16x16x32_bf16 v[56:59], v[156:159], a[120:123], v[56:59]\n"
"ds_read_b64_tr_b16 v[138:139], v196 offset:10248\n"
"v_mfma_f32_16x16x32_bf16 v[56:59], v[152:155], a[104:107], v[56:59]\n"
"buffer_load_dwordx4 v229, s[56:59], s80 offen lds\n"
"v_mfma_f32_16x16x32_bf16 v[52:55], v[152:155], a[100:103], v[52:55]\n"
"s_add_u32 s80, s80, s100\n"
"ds_read_b64_tr_b16 v[140:141], v196 offset:12296\n"
"v_cvt_pk_bf16_f32 v173, v78, v79\n"
"v_mfma_f32_16x16x32_bf16 v[52:55], v[156:159], a[116:119], v[52:55]\n"
"s_addk_i32 m0, 1024\n"
"v_mfma_f32_16x16x32_bf16 v[48:51], v[156:159], a[112:115], v[48:51]\n"
"ds_read_b64_tr_b16 v[142:143], v196 offset:14344\n"
"v_mfma_f32_16x16x32_bf16 v[48:51], v[152:155], a[96:99], v[48:51]\n"
"buffer_load_dwordx4 v229, s[56:59], s80 offen lds\n"
"s_waitcnt lgkmcnt(0)\n"
"s_barrier\n"
"v_mfma_f32_16x16x32_bf16 v[0:3], v[128:131], a[128:131], v[0:3]\n"
"s_add_u32 s80, s86, 3670016\n"
"ds_read_b64_tr_b16 v[144:145], v197 offset:8192\n"
"ds_read_b128 a[0:3], v232 offset:0\n"
"v_mfma_f32_16x16x32_bf16 v[0:3], v[132:135], a[144:147], v[0:3]\n"
"s_add_u32 m0, s46, 65536\n"
"v_cvt_pk_bf16_f32 v174, v92, v93\n"
"v_mfma_f32_16x16x32_bf16 v[4:7], v[132:135], a[148:151], v[4:7]\n"
"ds_read_b64_tr_b16 v[146:147], v197 offset:10240\n"
"ds_read_b128 a[4:7], v232 offset:1024\n"
"v_mfma_f32_16x16x32_bf16 v[4:7], v[128:131], a[132:135], v[4:7]\n"
"buffer_load_dwordx4 v229, s[56:59], s80 offen lds\n"
"v_mfma_f32_16x16x32_bf16 v[8:11], v[128:131], a[136:139], v[8:11]\n"
"s_add_u32 s80, s80, s100\n"
"ds_read_b64_tr_b16 v[148:149], v197 offset:12288\n"
"ds_read_b128 a[8:11], v232 offset:2048\n"
"v_mfma_f32_16x16x32_bf16 v[8:11], v[132:135], a[152:155], v[8:11]\n"
"s_addk_i32 m0, 1024\n"
"v_cvt_pk_bf16_f32 v175, v94, v95\n"
"v_mfma_f32_16x16x32_bf16 v[12:15], v[132:135], a[156:159], v[12:15]\n"
"ds_read_b64_tr_b16 v[150:151], v197 offset:14336\n"
"ds_read_b128 a[12:15], v232 offset:3072\n"
"v_mfma_f32_16x16x32_bf16 v[12:15], v[128:131], a[140:143], v[12:15]\n"
"buffer_load_dwordx4 v229, s[56:59], s80 offen lds\n"
"v_mfma_f32_16x16x32_bf16 v[28:31], v[136:139], a[140:143], v[28:31]\n"
"s_sub_u32 s80, s80, s101\n"
"ds_read_b64_tr_b16 v[152:153], v197 offset:8200\n"
"ds_read_b128 a[16:19], v232 offset:4096\n"
"v_mfma_f32_16x16x32_bf16 v[28:31], v[140:143], a[156:159], v[28:31]\n"
"s_addk_i32 m0, 1024\n"
"v_cvt_pk_bf16_f32 v176, v96, v97\n"
"v_mfma_f32_16x16x32_bf16 v[24:27], v[140:143], a[152:155], v[24:27]\n"
"ds_read_b64_tr_b16 v[154:155], v197 offset:10248\n"
"ds_read_b128 a[20:23], v232 offset:5120\n"
"v_mfma_f32_16x16x32_bf16 v[24:27], v[136:139], a[136:139], v[24:27]\n"
"buffer_load_dwordx4 v229, s[56:59], s80 offen lds\n"
"v_mfma_f32_16x16x32_bf16 v[20:23], v[136:139], a[132:135], v[20:23]\n"
"s_add_u32 s80, s80, s100\n"
"ds_read_b64_tr_b16 v[156:157], v197 offset:12296\n"
"ds_read_b128 a[24:27], v232 offset:6144\n"
"v_mfma_f32_16x16x32_bf16 v[20:23], v[140:143], a[148:151], v[20:23]\n"
"s_addk_i32 m0, 1024\n"
"v_cvt_pk_bf16_f32 v177, v98, v99\n"
"v_mfma_f32_16x16x32_bf16 v[16:19], v[140:143], a[144:147], v[16:19]\n"
"ds_read_b64_tr_b16 v[158:159], v197 offset:14344\n"
"ds_read_b128 a[28:31], v232 offset:7168\n"
"v_mfma_f32_16x16x32_bf16 v[16:19], v[136:139], a[128:131], v[16:19]\n"
"s_waitcnt lgkmcnt(9)\n"
"buffer_load_dwordx4 v229, s[56:59], s80 offen lds\n"
"v_mfma_f32_16x16x32_bf16 v[32:35], v[144:147], a[128:131], v[32:35]\n"
"s_waitcnt vmcnt(12)\n"
"ds_read_b64_tr_b16 v[128:129], v198 offset:8192\n"
"v_cvt_pk_bf16_f32 v178, v112, v113\n"
"v_mfma_f32_16x16x32_bf16 v[32:35], v[148:151], a[144:147], v[32:35]\n"
"s_add_u32 s80, s80, s101\n"
"v_cvt_pk_bf16_f32 v179, v114, v115\n"
"v_mfma_f32_16x16x32_bf16 v[36:39], v[148:151], a[148:151], v[36:39]\n"
"s_addk_i32 m0, 1024\n"
"ds_read_b64_tr_b16 v[130:131], v198 offset:10240\n"
"v_mfma_f32_16x16x32_bf16 v[36:39], v[144:147], a[132:135], v[36:39]\n"
"buffer_load_dwordx4 v229, s[56:59], s80 offen lds\n"
"v_mfma_f32_16x16x32_bf16 v[40:43], v[144:147], a[136:139], v[40:43]\n"
"s_add_u32 s80, s80, s100\n"
"ds_read_b64_tr_b16 v[132:133], v198 offset:12288\n"
"v_mfma_f32_16x16x32_bf16 v[40:43], v[148:151], a[152:155], v[40:43]\n"
"s_addk_i32 m0, 1024\n"
"v_cvt_pk_bf16_f32 v180, v100, v101\n"
"v_mfma_f32_16x16x32_bf16 v[44:47], v[148:151], a[156:159], v[44:47]\n"
"ds_read_b64_tr_b16 v[134:135], v198 offset:14336\n"
"v_mfma_f32_16x16x32_bf16 v[44:47], v[144:147], a[140:143], v[44:47]\n"
"s_waitcnt lgkmcnt(5)\n"
"buffer_load_dwordx4 v229, s[56:59], s80 offen lds\n"
"v_mfma_f32_16x16x32_bf16 v[60:63], v[152:155], a[140:143], v[60:63]\n"
"s_sub_u32 s80, s80, s101\n"
"ds_read_b64_tr_b16 v[136:137], v198 offset:8200\n"
"v_cvt_pk_bf16_f32 v181, v102, v103\n"
"v_mfma_f32_16x16x32_bf16 v[60:63], v[156:159], a[156:159], v[60:63]\n"
"s_addk_i32 m0, 1024\n"
"v_mfma_f32_16x16x32_bf16 v[56:59], v[156:159], a[152:155], v[56:59]\n"
"ds_read_b64_tr_b16 v[138:139], v198 offset:10248\n"
"v_mfma_f32_16x16x32_bf16 v[56:59], v[152:155], a[136:139], v[56:59]\n"
"buffer_load_dwordx4 v229, s[56:59], s80 offen lds\n"
"v_mfma_f32_16x16x32_bf16 v[52:55], v[152:155], a[132:135], v[52:55]\n"
"s_add_u32 s80, s80, s100\n"
"ds_read_b64_tr_b16 v[140:141], v198 offset:12296\n"
"v_cvt_pk_bf16_f32 v182, v116, v117\n"
"v_mfma_f32_16x16x32_bf16 v[52:55], v[156:159], a[148:151], v[52:55]\n"
"s_addk_i32 m0, 1024\n"
"v_mfma_f32_16x16x32_bf16 v[48:51], v[156:159], a[144:147], v[48:51]\n"
"ds_read_b64_tr_b16 v[142:143], v198 offset:14344\n"
"v_mfma_f32_16x16x32_bf16 v[48:51], v[152:155], a[128:131], v[48:51]\n"
"s_waitcnt lgkmcnt(4)\n"
"buffer_load_dwordx4 v229, s[56:59], s80 offen lds\n"
"v_mfma_f32_16x16x32_bf16 v[0:3], v[128:131], a[160:163], v[0:3]\n"
"s_add_u32 s80, s87, 0\n"
"ds_read_b64_tr_b16 v[144:145], v199 offset:8192\n"
"ds_read_b128 a[32:35], v232 offset:8192\n"
"v_mfma_f32_16x16x32_bf16 v[0:3], v[132:135], a[176:179], v[0:3]\n"
"s_add_u32 m0, s46, 8192\n"
"v_cvt_pk_bf16_f32 v183, v118, v119\n"
"v_mfma_f32_16x16x32_bf16 v[4:7], v[132:135], a[180:183], v[4:7]\n"
"ds_read_b64_tr_b16 v[146:147], v199 offset:10240\n"
"ds_read_b128 a[36:39], v232 offset:9216\n"
"v_mfma_f32_16x16x32_bf16 v[4:7], v[128:131], a[164:167], v[4:7]\n"
"buffer_load_dwordx4 v254, s[60:63], s80 offen lds\n"
"v_mfma_f32_16x16x32_bf16 v[8:11], v[128:131], a[168:171], v[8:11]\n"
"s_addk_i32 s80, 8192\n"
"ds_read_b64_tr_b16 v[148:149], v199 offset:12288\n"
"ds_read_b128 a[40:43], v232 offset:10240\n"
"v_mfma_f32_16x16x32_bf16 v[8:11], v[132:135], a[184:187], v[8:11]\n"
"s_addk_i32 m0, 1024\n"
"v_cvt_pk_bf16_f32 v184, v104, v105\n"
"v_mfma_f32_16x16x32_bf16 v[12:15], v[132:135], a[188:191], v[12:15]\n"
"ds_read_b64_tr_b16 v[150:151], v199 offset:14336\n"
"ds_read_b128 a[44:47], v232 offset:11264\n"
"v_mfma_f32_16x16x32_bf16 v[12:15], v[128:131], a[172:175], v[12:15]\n"
"s_waitcnt lgkmcnt(8)\n"
"buffer_load_dwordx4 v254, s[60:63], s80 offen lds\n"
"v_mfma_f32_16x16x32_bf16 v[28:31], v[136:139], a[172:175], v[28:31]\n"
"s_addk_i32 s80, 8192\n"
"ds_read_b64_tr_b16 v[152:153], v199 offset:8200\n"
"ds_read_b128 a[48:51], v232 offset:12288\n"
"v_mfma_f32_16x16x32_bf16 v[28:31], v[140:143], a[188:191], v[28:31]\n"
"s_addk_i32 m0, 1024\n"
"v_cvt_pk_bf16_f32 v185, v106, v107\n"
"v_mfma_f32_16x16x32_bf16 v[24:27], v[140:143], a[184:187], v[24:27]\n"
"ds_read_b64_tr_b16 v[154:155], v199 offset:10248\n"
"ds_read_b128 a[52:55], v232 offset:13312\n"
"v_mfma_f32_16x16x32_bf16 v[24:27], v[136:139], a[168:171], v[24:27]\n"
"buffer_load_dwordx4 v254, s[60:63], s80 offen lds\n"
"v_mfma_f32_16x16x32_bf16 v[20:23], v[136:139], a[164:167], v[20:23]\n"
"s_addk_i32 s80, 8192\n"
"ds_read_b64_tr_b16 v[156:157], v199 offset:12296\n"
"ds_read_b128 a[56:59], v232 offset:14336\n"
"v_mfma_f32_16x16x32_bf16 v[20:23], v[140:143], a[180:183], v[20:23]\n"
"s_addk_i32 m0, 1024\n"
"v_cvt_pk_bf16_f32 v186, v120, v121\n"
"v_mfma_f32_16x16x32_bf16 v[16:19], v[140:143], a[176:179], v[16:19]\n"
"ds_read_b64_tr_b16 v[158:159], v199 offset:14344\n"
"ds_read_b128 a[60:63], v232 offset:15360\n"
"v_mfma_f32_16x16x32_bf16 v[16:19], v[136:139], a[160:163], v[16:19]\n"
"s_waitcnt lgkmcnt(9)\n"
"buffer_load_dwordx4 v254, s[60:63], s80 offen lds\n"
"v_mfma_f32_16x16x32_bf16 v[32:35], v[144:147], a[160:163], v[32:35]\n"
"s_waitcnt vmcnt(12)\n"
"ds_read_b64_tr_b16 v[128:129], v196 offset:0\n"
"v_cvt_pk_bf16_f32 v187, v122, v123\n"
"v_mfma_f32_16x16x32_bf16 v[32:35], v[148:151], a[176:179], v[32:35]\n"
"s_addk_i32 s80, 8192\n"
"v_cvt_pk_bf16_f32 v188, v108, v109\n"
"v_mfma_f32_16x16x32_bf16 v[36:39], v[148:151], a[180:183], v[36:39]\n"
"s_addk_i32 m0, 1024\n"
"ds_read_b64_tr_b16 v[130:131], v196 offset:2048\n"
"v_mfma_f32_16x16x32_bf16 v[36:39], v[144:147], a[164:167], v[36:39]\n"
"buffer_load_dwordx4 v254, s[60:63], s80 offen lds\n"
"v_mfma_f32_16x16x32_bf16 v[40:43], v[144:147], a[168:171], v[40:43]\n"
"s_addk_i32 s80, 8192\n"
"ds_read_b64_tr_b16 v[132:133], v196 offset:4096\n"
"v_mfma_f32_16x16x32_bf16 v[40:43], v[148:151], a[184:187], v[40:43]\n"
"s_addk_i32 m0, 1024\n"
"v_cvt_pk_bf16_f32 v189, v110, v111\n"
"v_mfma_f32_16x16x32_bf16 v[44:47], v[148:151], a[188:191], v[44:47]\n"
"ds_read_b64_tr_b16 v[134:135], v196 offset:6144\n"
"v_mfma_f32_16x16x32_bf16 v[44:47], v[144:147], a[172:175], v[44:47]\n"
"s_waitcnt lgkmcnt(5)\n"
"buffer_load_dwordx4 v254, s[60:63], s80 offen lds\n"
"v_mfma_f32_16x16x32_bf16 v[60:63], v[152:155], a[172:175], v[60:63]\n"
"s_addk_i32 s80, 8192\n"
"ds_read_b64_tr_b16 v[136:137], v196 offset:8\n"
"v_cvt_pk_bf16_f32 v190, v124, v125\n"
"v_mfma_f32_16x16x32_bf16 v[60:63], v[156:159], a[188:191], v[60:63]\n"
"s_addk_i32 m0, 1024\n"
"v_mfma_f32_16x16x32_bf16 v[56:59], v[156:159], a[184:187], v[56:59]\n"
"ds_read_b64_tr_b16 v[138:139], v196 offset:2056\n"
"v_mfma_f32_16x16x32_bf16 v[56:59], v[152:155], a[168:171], v[56:59]\n"
"buffer_load_dwordx4 v254, s[60:63], s80 offen lds\n"
"v_mfma_f32_16x16x32_bf16 v[52:55], v[152:155], a[164:167], v[52:55]\n"
"s_addk_i32 s80, 8192\n"
"ds_read_b64_tr_b16 v[140:141], v196 offset:4104\n"
"v_cvt_pk_bf16_f32 v191, v126, v127\n"
"v_mfma_f32_16x16x32_bf16 v[52:55], v[156:159], a[180:183], v[52:55]\n"
"s_addk_i32 m0, 1024\n"
"v_mfma_f32_16x16x32_bf16 v[48:51], v[156:159], a[176:179], v[48:51]\n"
"ds_read_b64_tr_b16 v[142:143], v196 offset:6152\n"
"v_mfma_f32_16x16x32_bf16 v[48:51], v[152:155], a[160:163], v[48:51]\n"
"s_waitcnt lgkmcnt(4)\n"
"buffer_load_dwordx4 v254, s[60:63], s80 offen lds\n"
"v_mfma_f32_16x16x32_bf16 v[0:3], v[128:131], a[192:195], v[0:3]\n"
"s_add_u32 s80, s87, 65536\n"
"ds_read_b64_tr_b16 v[144:145], v197 offset:0\n"
"ds_read_b128 a[64:67], v232 offset:16384\n"
"v_mfma_f32_16x16x32_bf16 v[0:3], v[132:135], a[208:211], v[0:3]\n"
"s_add_u32 m0, s46, 73728\n"
"s_mov_b64 s[56:57], s[24:25]\n"
"s_mov_b32 s59, 0x20000\n"
"v_mfma_f32_16x16x32_bf16 v[4:7], v[132:135], a[212:215], v[4:7]\n"
"ds_read_b64_tr_b16 v[146:147], v197 offset:2048\n"
"ds_read_b128 a[68:71], v232 offset:17408\n"
"v_mfma_f32_16x16x32_bf16 v[4:7], v[128:131], a[196:199], v[4:7]\n"
"buffer_load_dwordx4 v254, s[60:63], s80 offen lds\n"
"v_mfma_f32_16x16x32_bf16 v[8:11], v[128:131], a[200:203], v[8:11]\n"
"s_addk_i32 s80, 8192\n"
"ds_read_b64_tr_b16 v[148:149], v197 offset:4096\n"
"ds_read_b128 a[72:75], v232 offset:18432\n"
"v_mfma_f32_16x16x32_bf16 v[8:11], v[132:135], a[216:219], v[8:11]\n"
"s_addk_i32 m0, 1024\n"
"v_accvgpr_write_b32 a128, v160\n"
"v_accvgpr_write_b32 a129, v161\n"
"v_mfma_f32_16x16x32_bf16 v[12:15], v[132:135], a[220:223], v[12:15]\n"
"ds_read_b64_tr_b16 v[150:151], v197 offset:6144\n"
"ds_read_b128 a[76:79], v232 offset:19456\n"
"v_mfma_f32_16x16x32_bf16 v[12:15], v[128:131], a[204:207], v[12:15]\n"
"s_waitcnt lgkmcnt(8)\n"
"buffer_load_dwordx4 v254, s[60:63], s80 offen lds\n"
"v_mfma_f32_16x16x32_bf16 v[28:31], v[136:139], a[204:207], v[28:31]\n"
"s_addk_i32 s80, 8192\n"
"ds_read_b64_tr_b16 v[152:153], v197 offset:8\n"
"ds_read_b128 a[80:83], v232 offset:20480\n"
"v_mfma_f32_16x16x32_bf16 v[28:31], v[140:143], a[220:223], v[28:31]\n"
"s_addk_i32 m0, 1024\n"
"v_accvgpr_write_b32 a130, v162\n"
"v_accvgpr_write_b32 a131, v163\n"
"v_mfma_f32_16x16x32_bf16 v[24:27], v[140:143], a[216:219], v[24:27]\n"
"ds_read_b64_tr_b16 v[154:155], v197 offset:2056\n"
"ds_read_b128 a[84:87], v232 offset:21504\n"
"v_mfma_f32_16x16x32_bf16 v[24:27], v[136:139], a[200:203], v[24:27]\n"
"buffer_load_dwordx4 v254, s[60:63], s80 offen lds\n"
"v_mfma_f32_16x16x32_bf16 v[20:23], v[136:139], a[196:199], v[20:23]\n"
"s_addk_i32 s80, 8192\n"
"ds_read_b64_tr_b16 v[156:157], v197 offset:4104\n"
"ds_read_b128 a[88:91], v232 offset:22528\n"
"v_mfma_f32_16x16x32_bf16 v[20:23], v[140:143], a[212:215], v[20:23]\n"
"s_addk_i32 m0, 1024\n"
"v_accvgpr_write_b32 a132, v176\n"
"v_accvgpr_write_b32 a133, v177\n"
"v_mfma_f32_16x16x32_bf16 v[16:19], v[140:143], a[208:211], v[16:19]\n"
"ds_read_b64_tr_b16 v[158:159], v197 offset:6152\n"
"ds_read_b128 a[92:95], v232 offset:23552\n"
"v_mfma_f32_16x16x32_bf16 v[16:19], v[136:139], a[192:195], v[16:19]\n"
"s_waitcnt lgkmcnt(9)\n"
"buffer_load_dwordx4 v254, s[60:63], s80 offen lds\n"
"v_mfma_f32_16x16x32_bf16 v[32:35], v[144:147], a[192:195], v[32:35]\n"
"s_waitcnt vmcnt(12)\n"
"ds_read_b64_tr_b16 v[128:129], v198 offset:0\n"
"v_accvgpr_write_b32 a134, v178\n"
"v_mfma_f32_16x16x32_bf16 v[32:35], v[148:151], a[208:211], v[32:35]\n"
"s_addk_i32 s80, 8192\n"
"v_accvgpr_write_b32 a135, v179\n"
"v_accvgpr_write_b32 a144, v164\n"
"v_mfma_f32_16x16x32_bf16 v[36:39], v[148:151], a[212:215], v[36:39]\n"
"s_addk_i32 m0, 1024\n"
"ds_read_b64_tr_b16 v[130:131], v198 offset:2048\n"
"v_accvgpr_write_b32 a145, v165\n"
"v_mfma_f32_16x16x32_bf16 v[36:39], v[144:147], a[196:199], v[36:39]\n"
"buffer_load_dwordx4 v254, s[60:63], s80 offen lds\n"
"v_mfma_f32_16x16x32_bf16 v[40:43], v[144:147], a[200:203], v[40:43]\n"
"s_addk_i32 s80, 8192\n"
"ds_read_b64_tr_b16 v[132:133], v198 offset:4096\n"
"v_accvgpr_write_b32 a146, v166\n"
"v_mfma_f32_16x16x32_bf16 v[40:43], v[148:151], a[216:219], v[40:43]\n"
"s_addk_i32 m0, 1024\n"
"v_accvgpr_write_b32 a147, v167\n"
"v_accvgpr_write_b32 a148, v180\n"
"v_mfma_f32_16x16x32_bf16 v[44:47], v[148:151], a[220:223], v[44:47]\n"
"ds_read_b64_tr_b16 v[134:135], v198 offset:6144\n"
"v_accvgpr_write_b32 a149, v181\n"
"v_mfma_f32_16x16x32_bf16 v[44:47], v[144:147], a[204:207], v[44:47]\n"
"s_waitcnt lgkmcnt(5)\n"
"buffer_load_dwordx4 v254, s[60:63], s80 offen lds\n"
"v_mfma_f32_16x16x32_bf16 v[60:63], v[152:155], a[204:207], v[60:63]\n"
"s_addk_i32 s80, 8192\n"
"ds_read_b64_tr_b16 v[136:137], v198 offset:8\n"
"v_accvgpr_write_b32 a150, v182\n"
"v_mfma_f32_16x16x32_bf16 v[60:63], v[156:159], a[220:223], v[60:63]\n"
"s_addk_i32 m0, 1024\n"
"v_accvgpr_write_b32 a151, v183\n"
"v_accvgpr_write_b32 a160, v168\n"
"v_mfma_f32_16x16x32_bf16 v[56:59], v[156:159], a[216:219], v[56:59]\n"
"ds_read_b64_tr_b16 v[138:139], v198 offset:2056\n"
"v_accvgpr_write_b32 a161, v169\n"
"v_mfma_f32_16x16x32_bf16 v[56:59], v[152:155], a[200:203], v[56:59]\n"
"buffer_load_dwordx4 v254, s[60:63], s80 offen lds\n"
"v_mfma_f32_16x16x32_bf16 v[52:55], v[152:155], a[196:199], v[52:55]\n"
"s_addk_i32 s80, 8192\n"
"ds_read_b64_tr_b16 v[140:141], v198 offset:4104\n"
"v_accvgpr_write_b32 a162, v170\n"
"v_mfma_f32_16x16x32_bf16 v[52:55], v[156:159], a[212:215], v[52:55]\n"
"s_addk_i32 m0, 1024\n"
"v_accvgpr_write_b32 a163, v171\n"
"v_accvgpr_write_b32 a164, v184\n"
"v_mfma_f32_16x16x32_bf16 v[48:51], v[156:159], a[208:211], v[48:51]\n"
"ds_read_b64_tr_b16 v[142:143], v198 offset:6152\n"
"v_accvgpr_write_b32 a165, v185\n"
"v_mfma_f32_16x16x32_bf16 v[48:51], v[152:155], a[192:195], v[48:51]\n"
"s_waitcnt lgkmcnt(4)\n"
"buffer_load_dwordx4 v254, s[60:63], s80 offen lds\n"
"v_mfma_f32_16x16x32_bf16 v[0:3], v[128:131], a[224:227], v[0:3]\n"
"s_add_u32 s80, s87, 131072\n"
"ds_read_b64_tr_b16 v[144:145], v199 offset:0\n"
"v_accvgpr_write_b32 a166, v186\n"
"v_mfma_f32_16x16x32_bf16 v[0:3], v[132:135], a[240:243], v[0:3]\n"
"s_add_u32 m0, s46, 0\n"
"v_accvgpr_write_b32 a167, v187\n"
"v_accvgpr_write_b32 a176, v172\n"
"v_mfma_f32_16x16x32_bf16 v[4:7], v[132:135], a[244:247], v[4:7]\n"
"ds_read_b64_tr_b16 v[146:147], v199 offset:2048\n"
"v_accvgpr_write_b32 a177, v173\n"
"v_mfma_f32_16x16x32_bf16 v[4:7], v[128:131], a[228:231], v[4:7]\n"
"buffer_load_dwordx4 v254, s[60:63], s80 offen lds\n"
"v_mfma_f32_16x16x32_bf16 v[8:11], v[128:131], a[232:235], v[8:11]\n"
"s_addk_i32 s80, 8192\n"
"ds_read_b64_tr_b16 v[148:149], v199 offset:4096\n"
"v_accvgpr_write_b32 a178, v174\n"
"v_mfma_f32_16x16x32_bf16 v[8:11], v[132:135], a[248:251], v[8:11]\n"
"s_addk_i32 m0, 1024\n"
"v_accvgpr_write_b32 a179, v175\n"
"v_accvgpr_write_b32 a180, v188\n"
"v_mfma_f32_16x16x32_bf16 v[12:15], v[132:135], a[252:255], v[12:15]\n"
"ds_read_b64_tr_b16 v[150:151], v199 offset:6144\n"
"v_accvgpr_write_b32 a181, v189\n"
"v_mfma_f32_16x16x32_bf16 v[12:15], v[128:131], a[236:239], v[12:15]\n"
"s_waitcnt lgkmcnt(4)\n"
"buffer_load_dwordx4 v254, s[60:63], s80 offen lds\n"
"v_mfma_f32_16x16x32_bf16 v[28:31], v[136:139], a[236:239], v[28:31]\n"
"s_addk_i32 s80, 8192\n"
"ds_read_b64_tr_b16 v[152:153], v199 offset:8\n"
"v_accvgpr_write_b32 a182, v190\n"
"v_mfma_f32_16x16x32_bf16 v[28:31], v[140:143], a[252:255], v[28:31]\n"
"s_addk_i32 m0, 1024\n"
"v_accvgpr_write_b32 a183, v191\n"
"v_mfma_f32_16x16x32_bf16 v[24:27], v[140:143], a[248:251], v[24:27]\n"
"ds_read_b64_tr_b16 v[154:155], v199 offset:2056\n"
"v_mfma_f32_16x16x32_bf16 v[24:27], v[136:139], a[232:235], v[24:27]\n"
"buffer_load_dwordx4 v254, s[60:63], s80 offen lds\n"
"v_mfma_f32_16x16x32_bf16 v[20:23], v[136:139], a[228:231], v[20:23]\n"
"s_addk_i32 s80, 8192\n"
"ds_read_b64_tr_b16 v[156:157], v199 offset:4104\n"
"v_mfma_f32_16x16x32_bf16 v[20:23], v[140:143], a[244:247], v[20:23]\n"
"s_addk_i32 m0, 1024\n"
"v_mfma_f32_16x16x32_bf16 v[16:19], v[140:143], a[240:243], v[16:19]\n"
"ds_read_b64_tr_b16 v[158:159], v199 offset:6152\n"
"v_mfma_f32_16x16x32_bf16 v[16:19], v[136:139], a[224:227], v[16:19]\n"
"s_waitcnt lgkmcnt(4)\n"
"buffer_load_dwordx4 v254, s[60:63], s80 offen lds\n"
"v_mfma_f32_16x16x32_bf16 v[32:35], v[144:147], a[224:227], v[32:35]\n"
"s_waitcnt vmcnt(12)\n"
"ds_read_b64_tr_b16 v[128:129], v246 offset:8192\n"
"v_mfma_f32_16x16x32_bf16 v[32:35], v[148:151], a[240:243], v[32:35]\n"
"s_addk_i32 s80, 8192\n"
"v_mfma_f32_16x16x32_bf16 v[36:39], v[148:151], a[244:247], v[36:39]\n"
"s_addk_i32 m0, 1024\n"
"ds_read_b64_tr_b16 v[130:131], v246 offset:10240\n"
"v_mfma_f32_16x16x32_bf16 v[36:39], v[144:147], a[228:231], v[36:39]\n"
"buffer_load_dwordx4 v254, s[60:63], s80 offen lds\n"
"v_mfma_f32_16x16x32_bf16 v[40:43], v[144:147], a[232:235], v[40:43]\n"
"s_addk_i32 s80, 8192\n"
"ds_read_b64_tr_b16 v[132:133], v246 offset:12288\n"
"v_mfma_f32_16x16x32_bf16 v[40:43], v[148:151], a[248:251], v[40:43]\n"
"s_addk_i32 m0, 1024\n"
"v_mfma_f32_16x16x32_bf16 v[44:47], v[148:151], a[252:255], v[44:47]\n"
"ds_read_b64_tr_b16 v[134:135], v246 offset:14336\n"
"v_mfma_f32_16x16x32_bf16 v[44:47], v[144:147], a[236:239], v[44:47]\n"
"s_waitcnt lgkmcnt(4)\n"
"buffer_load_dwordx4 v254, s[60:63], s80 offen lds\n"
"v_mfma_f32_16x16x32_bf16 v[60:63], v[152:155], a[236:239], v[60:63]\n"
"s_addk_i32 s80, 8192\n"
"ds_read_b64_tr_b16 v[136:137], v247 offset:8192\n"
"v_mfma_f32_16x16x32_bf16 v[60:63], v[156:159], a[252:255], v[60:63]\n"
"s_addk_i32 m0, 1024\n"
"v_mfma_f32_16x16x32_bf16 v[56:59], v[156:159], a[248:251], v[56:59]\n"
"ds_read_b64_tr_b16 v[138:139], v247 offset:10240\n"
"v_mfma_f32_16x16x32_bf16 v[56:59], v[152:155], a[232:235], v[56:59]\n"
"buffer_load_dwordx4 v254, s[60:63], s80 offen lds\n"
"v_mfma_f32_16x16x32_bf16 v[52:55], v[152:155], a[228:231], v[52:55]\n"
"s_addk_i32 s80, 8192\n"
"ds_read_b64_tr_b16 v[140:141], v247 offset:12288\n"
"v_mfma_f32_16x16x32_bf16 v[52:55], v[156:159], a[244:247], v[52:55]\n"
"s_addk_i32 m0, 1024\n"
"v_mfma_f32_16x16x32_bf16 v[48:51], v[156:159], a[240:243], v[48:51]\n"
"ds_read_b64_tr_b16 v[142:143], v247 offset:14336\n"
"v_mfma_f32_16x16x32_bf16 v[48:51], v[152:155], a[224:227], v[48:51]\n"
".LPG5_%=:\n"
"s_cbranch_execz .LPGR_%=\n"
"s_waitcnt lgkmcnt(4)\n"
"buffer_load_dwordx4 v254, s[60:63], s80 offen lds\n"
"v_mfma_f32_16x16x32_bf16 v[64:67], v[128:131], a[0:3], 0\n"
"s_add_u32 s80, s88, 0\n"
"ds_read_b64_tr_b16 v[144:145], v248 offset:8192\n"
"v_cvt_pk_bf16_f32 v192, v0, v1\n"
"v_mfma_f32_16x16x32_bf16 v[64:67], v[132:135], a[16:19], v[64:67]\n"
"s_add_u32 m0, s46, 65536\n"
"v_cvt_pk_bf16_f32 v193, v2, v3\n"
"v_cvt_pk_bf16_f32 v194, v16, v17\n"
"v_mfma_f32_16x16x32_bf16 v[68:71], v[132:135], a[20:23], 0\n"
"ds_read_b64_tr_b16 v[146:147], v248 offset:10240\n"
"v_cvt_pk_bf16_f32 v195, v18, v19\n"
"v_mfma_f32_16x16x32_bf16 v[68:71], v[128:131], a[4:7], v[68:71]\n"
"buffer_load_dwordx4 v254, s[60:63], s80 offen lds\n"
"v_mfma_f32_16x16x32_bf16 v[72:75], v[128:131], a[8:11], 0\n"
"s_addk_i32 s80, 8192\n"
"ds_read_b64_tr_b16 v[148:149], v248 offset:12288\n"
"v_cvt_pk_bf16_f32 v196, v4, v5\n"
"v_mfma_f32_16x16x32_bf16 v[72:75], v[132:135], a[24:27], v[72:75]\n"
"s_addk_i32 m0, 1024\n"
"v_cvt_pk_bf16_f32 v197, v6, v7\n"
"v_cvt_pk_bf16_f32 v198, v20, v21\n"
"v_mfma_f32_16x16x32_bf16 v[76:79], v[132:135], a[28:31], 0\n"
"ds_read_b64_tr_b16 v[150:151], v248 offset:14336\n"
"v_cvt_pk_bf16_f32 v199, v22, v23\n"
"v_mfma_f32_16x16x32_bf16 v[76:79], v[128:131], a[12:15], v[76:79]\n"
"s_waitcnt lgkmcnt(4)\n"
"buffer_load_dwordx4 v254, s[60:63], s80 offen lds\n"
"v_mfma_f32_16x16x32_bf16 v[92:95], v[136:139], a[12:15], 0\n"
"s_addk_i32 s80, 8192\n"
"ds_read_b64_tr_b16 v[152:153], v249 offset:8192\n"
"v_cvt_pk_bf16_f32 v200, v8, v9\n"
"v_mfma_f32_16x16x32_bf16 v[92:95], v[140:143], a[28:31], v[92:95]\n"
"s_addk_i32 m0, 1024\n"
"v_cvt_pk_bf16_f32 v201, v10, v11\n"
"v_cvt_pk_bf16_f32 v202, v24, v25\n"
"v_mfma_f32_16x16x32_bf16 v[88:91], v[140:143], a[24:27], 0\n"
"ds_read_b64_tr_b16 v[154:155], v249 offset:10240\n"
"v_cvt_pk_bf16_f32 v203, v26, v27\n"
"v_mfma_f32_16x16x32_bf16 v[88:91], v[136:139], a[8:11], v[88:91]\n"
"buffer_load_dwordx4 v254, s[60:63], s80 offen lds\n"
"v_mfma_f32_16x16x32_bf16 v[84:87], v[136:139], a[4:7], 0\n"
"s_addk_i32 s80, 8192\n"
"ds_read_b64_tr_b16 v[156:157], v249 offset:12288\n"
"v_cvt_pk_bf16_f32 v204, v12, v13\n"
"v_mfma_f32_16x16x32_bf16 v[84:87], v[140:143], a[20:23], v[84:87]\n"
"s_addk_i32 m0, 1024\n"
"v_cvt_pk_bf16_f32 v205, v14, v15\n"
"v_cvt_pk_bf16_f32 v206, v28, v29\n"
"v_mfma_f32_16x16x32_bf16 v[80:83], v[140:143], a[16:19], 0\n"
"ds_read_b64_tr_b16 v[158:159], v249 offset:14336\n"
"v_cvt_pk_bf16_f32 v207, v30, v31\n"
"v_mfma_f32_16x16x32_bf16 v[80:83], v[136:139], a[0:3], v[80:83]\n"
"s_waitcnt lgkmcnt(4)\n"
"buffer_load_dwordx4 v254, s[60:63], s80 offen lds\n"
"v_mfma_f32_16x16x32_bf16 v[96:99], v[144:147], a[0:3], 0\n"
"s_waitcnt vmcnt(12)\n"
"ds_read_b64_tr_b16 v[128:129], v250 offset:8192\n"
"v_cvt_pk_bf16_f32 v208, v32, v33\n"
"v_mfma_f32_16x16x32_bf16 v[96:99], v[148:151], a[16:19], v[96:99]\n"
"s_addk_i32 s80, 8192\n"
"v_cvt_pk_bf16_f32 v209, v34, v35\n"
"v_cvt_pk_bf16_f32 v210, v48, v49\n"
"v_mfma_f32_16x16x32_bf16 v[100:103], v[148:151], a[20:23], 0\n"
"s_addk_i32 m0, 1024\n"
"ds_read_b64_tr_b16 v[130:131], v250 offset:10240\n"
"v_cvt_pk_bf16_f32 v211, v50, v51\n"
"v_mfma_f32_16x16x32_bf16 v[100:103], v[144:147], a[4:7], v[100:103]\n"
"buffer_load_dwordx4 v254, s[60:63], s80 offen lds\n"
"v_mfma_f32_16x16x32_bf16 v[104:107], v[144:147], a[8:11], 0\n"
"s_addk_i32 s80, 8192\n"
"ds_read_b64_tr_b16 v[132:133], v250 offset:12288\n"
"v_cvt_pk_bf16_f32 v212, v36, v37\n"
"v_mfma_f32_16x16x32_bf16 v[104:107], v[148:151], a[24:27], v[104:107]\n"
"s_addk_i32 m0, 1024\n"
"v_cvt_pk_bf16_f32 v213, v38, v39\n"
"v_cvt_pk_bf16_f32 v214, v52, v53\n"
"v_mfma_f32_16x16x32_bf16 v[108:111], v[148:151], a[28:31], 0\n"
"ds_read_b64_tr_b16 v[134:135], v250 offset:14336\n"
"v_cvt_pk_bf16_f32 v215, v54, v55\n"
"v_mfma_f32_16x16x32_bf16 v[108:111], v[144:147], a[12:15], v[108:111]\n"
"s_waitcnt lgkmcnt(4)\n"
"buffer_load_dwordx4 v254, s[60:63], s80 offen lds\n"
"v_mfma_f32_16x16x32_bf16 v[124:127], v[152:155], a[12:15], 0\n"
"s_addk_i32 s80, 8192\n"
"ds_read_b64_tr_b16 v[136:137], v251 offset:8192\n"
"v_cvt_pk_bf16_f32 v216, v40, v41\n"
"v_mfma_f32_16x16x32_bf16 v[124:127], v[156:159], a[28:31], v[124:127]\n"
"s_addk_i32 m0, 1024\n"
"v_cvt_pk_bf16_f32 v217, v42, v43\n"
"v_cvt_pk_bf16_f32 v218, v56, v57\n"
"v_mfma_f32_16x16x32_bf16 v[120:123], v[156:159], a[24:27], 0\n"
"ds_read_b64_tr_b16 v[138:139], v251 offset:10240\n"
"v_cvt_pk_bf16_f32 v219, v58, v59\n"
"v_mfma_f32_16x16x32_bf16 v[120:123], v[152:155], a[8:11], v[120:123]\n"
"buffer_load_dwordx4 v254, s[60:63], s80 offen lds\n"
"v_mfma_f32_16x16x32_bf16 v[116:119], v[152:155], a[4:7], 0\n"
"s_addk_i32 s80, 8192\n"
"ds_read_b64_tr_b16 v[140:141], v251 offset:12288\n"
"v_cvt_pk_bf16_f32 v220, v44, v45\n"
"v_mfma_f32_16x16x32_bf16 v[116:119], v[156:159], a[20:23], v[116:119]\n"
"s_addk_i32 m0, 1024\n"
"v_cvt_pk_bf16_f32 v221, v46, v47\n"
"v_cvt_pk_bf16_f32 v222, v60, v61\n"
"v_mfma_f32_16x16x32_bf16 v[112:115], v[156:159], a[16:19], 0\n"
"ds_read_b64_tr_b16 v[142:143], v251 offset:14336\n"
"v_cvt_pk_bf16_f32 v223, v62, v63\n"
"v_mfma_f32_16x16x32_bf16 v[112:115], v[152:155], a[0:3], v[112:115]\n"
"s_waitcnt lgkmcnt(4)\n"
"buffer_load_dwordx4 v254, s[60:63], s80 offen lds\n"
"v_mfma_f32_16x16x32_bf16 v[64:67], v[128:131], a[32:35], v[64:67]\n"
"s_add_u32 s80, s88, 65536\n"
"ds_read_b64_tr_b16 v[144:145], v252 offset:8192\n"
"v_accvgpr_write_b32 a136, v192\n"
"v_mfma_f32_16x16x32_bf16 v[64:67], v[132:135], a[48:51], v[64:67]\n"
"s_add_u32 m0, s46, 8192\n"
"v_accvgpr_write_b32 a137, v193\n"
"v_accvgpr_write_b32 a138, v194\n"
"v_mfma_f32_16x16x32_bf16 v[68:71], v[132:135], a[52:55], v[68:71]\n"
"ds_read_b64_tr_b16 v[146:147], v252 offset:10240\n"
"v_accvgpr_write_b32 a139, v195\n"
"v_mfma_f32_16x16x32_bf16 v[68:71], v[128:131], a[36:39], v[68:71]\n"
"buffer_load_dwordx4 v254, s[60:63], s80 offen lds\n"
"v_mfma_f32_16x16x32_bf16 v[72:75], v[128:131], a[40:43], v[72:75]\n"
"s_addk_i32 s80, 8192\n"
"ds_read_b64_tr_b16 v[148:149], v252 offset:12288\n"
"v_accvgpr_write_b32 a140, v208\n"
"v_mfma_f32_16x16x32_bf16 v[72:75], v[132:135], a[56:59], v[72:75]\n"
"s_addk_i32 m0, 1024\n"
"v_accvgpr_write_b32 a141, v209\n"
"v_accvgpr_write_b32 a142, v210\n"
"v_mfma_f32_16x16x32_bf16 v[76:79], v[132:135], a[60:63], v[76:79]\n"
"ds_read_b64_tr_b16 v[150:151], v252 offset:14336\n"
"v_accvgpr_write_b32 a143, v211\n"
"v_mfma_f32_16x16x32_bf16 v[76:79], v[128:131], a[44:47], v[76:79]\n"
"s_waitcnt lgkmcnt(4)\n"
"buffer_load_dwordx4 v254, s[60:63], s80 offen lds\n"
"v_mfma_f32_16x16x32_bf16 v[92:95], v[136:139], a[44:47], v[92:95]\n"
"s_addk_i32 s80, 8192\n"
"ds_read_b64_tr_b16 v[152:153], v253 offset:8192\n"
"v_accvgpr_write_b32 a152, v196\n"
"v_mfma_f32_16x16x32_bf16 v[92:95], v[140:143], a[60:63], v[92:95]\n"
"s_addk_i32 m0, 1024\n"
"v_accvgpr_write_b32 a153, v197\n"
"v_accvgpr_write_b32 a154, v198\n"
"v_mfma_f32_16x16x32_bf16 v[88:91], v[140:143], a[56:59], v[88:91]\n"
"ds_read_b64_tr_b16 v[154:155], v253 offset:10240\n"
"v_accvgpr_write_b32 a155, v199\n"
"v_mfma_f32_16x16x32_bf16 v[88:91], v[136:139], a[40:43], v[88:91]\n"
"buffer_load_dwordx4 v254, s[60:63], s80 offen lds\n"
"v_mfma_f32_16x16x32_bf16 v[84:87], v[136:139], a[36:39], v[84:87]\n"
"s_addk_i32 s80, 8192\n"
"ds_read_b64_tr_b16 v[156:157], v253 offset:12288\n"
"v_accvgpr_write_b32 a156, v212\n"
"v_mfma_f32_16x16x32_bf16 v[84:87], v[140:143], a[52:55], v[84:87]\n"
"s_addk_i32 m0, 1024\n"
"v_accvgpr_write_b32 a157, v213\n"
"v_accvgpr_write_b32 a158, v214\n"
"v_mfma_f32_16x16x32_bf16 v[80:83], v[140:143], a[48:51], v[80:83]\n"
"ds_read_b64_tr_b16 v[158:159], v253 offset:14336\n"
"v_accvgpr_write_b32 a159, v215\n"
"v_mfma_f32_16x16x32_bf16 v[80:83], v[136:139], a[32:35], v[80:83]\n"
"s_waitcnt lgkmcnt(4)\n"
"buffer_load_dwordx4 v254, s[60:63], s80 offen lds\n"
"v_mfma_f32_16x16x32_bf16 v[96:99], v[144:147], a[32:35], v[96:99]\n"
"s_waitcnt vmcnt(12)\n"
"ds_read_b64_tr_b16 v[128:129], v246 offset:0\n"
"v_accvgpr_write_b32 a168, v200\n"
"v_mfma_f32_16x16x32_bf16 v[96:99], v[148:151], a[48:51], v[96:99]\n"
"s_addk_i32 s80, 8192\n"
"v_accvgpr_write_b32 a169, v201\n"
"v_accvgpr_write_b32 a170, v202\n"
"v_mfma_f32_16x16x32_bf16 v[100:103], v[148:151], a[52:55], v[100:103]\n"
"s_addk_i32 m0, 1024\n"
"ds_read_b64_tr_b16 v[130:131], v246 offset:2048\n"
"v_accvgpr_write_b32 a171, v203\n"
"v_mfma_f32_16x16x32_bf16 v[100:103], v[144:147], a[36:39], v[100:103]\n"
"buffer_load_dwordx4 v254, s[60:63], s80 offen lds\n"
"v_mfma_f32_16x16x32_bf16 v[104:107], v[144:147], a[40:43], v[104:107]\n"
"s_addk_i32 s80, 8192\n"
"ds_read_b64_tr_b16 v[132:133], v246 offset:4096\n"
"v_accvgpr_write_b32 a172, v216\n"
"v_mfma_f32_16x16x32_bf16 v[104:107], v[148:151], a[56:59], v[104:107]\n"
"s_addk_i32 m0, 1024\n"
"v_accvgpr_write_b32 a173, v217\n"
"v_accvgpr_write_b32 a174, v218\n"
"v_mfma_f32_16x16x32_bf16 v[108:111], v[148:151], a[60:63], v[108:111]\n"
"ds_read_b64_tr_b16 v[134:135], v246 offset:6144\n"
"v_accvgpr_write_b32 a175, v219\n"
"v_mfma_f32_16x16x32_bf16 v[108:111], v[144:147], a[44:47], v[108:111]\n"
"s_waitcnt lgkmcnt(4)\n"
"buffer_load_dwordx4 v254, s[60:63], s80 offen lds\n"
"v_mfma_f32_16x16x32_bf16 v[124:127], v[152:155], a[44:47], v[124:127]\n"
"s_addk_i32 s80, 8192\n"
"ds_read_b64_tr_b16 v[136:137], v247 offset:0\n"
"v_accvgpr_write_b32 a184, v204\n"
"v_mfma_f32_16x16x32_bf16 v[124:127], v[156:159], a[60:63], v[124:127]\n"
"s_addk_i32 m0, 1024\n"
"v_accvgpr_write_b32 a185, v205\n"
"v_accvgpr_write_b32 a186, v206\n"
"v_mfma_f32_16x16x32_bf16 v[120:123], v[156:159], a[56:59], v[120:123]\n"
"ds_read_b64_tr_b16 v[138:139], v247 offset:2048\n"
"v_accvgpr_write_b32 a187, v207\n"
"v_mfma_f32_16x16x32_bf16 v[120:123], v[152:155], a[40:43], v[120:123]\n"
"buffer_load_dwordx4 v254, s[60:63], s80 offen lds\n"
"v_mfma_f32_16x16x32_bf16 v[116:119], v[152:155], a[36:39], v[116:119]\n"
"s_addk_i32 s80, 8192\n"
"ds_read_b64_tr_b16 v[140:141], v247 offset:4096\n"
"v_accvgpr_write_b32 a188, v220\n"
"v_mfma_f32_16x16x32_bf16 v[116:119], v[156:159], a[52:55], v[116:119]\n"
"s_addk_i32 m0, 1024\n"
"v_accvgpr_write_b32 a189, v221\n"
"v_accvgpr_write_b32 a190, v222\n"
"v_mfma_f32_16x16x32_bf16 v[112:115], v[156:159], a[48:51], v[112:115]\n"
"ds_read_b64_tr_b16 v[142:143], v247 offset:6144\n"
"v_accvgpr_write_b32 a191, v223\n"
"v_mfma_f32_16x16x32_bf16 v[112:115], v[152:155], a[32:35], v[112:115]\n"
"s_waitcnt lgkmcnt(4)\n"
"buffer_load_dwordx4 v254, s[60:63], s80 offen lds\n"
"v_mfma_f32_16x16x32_bf16 v[64:67], v[128:131], a[64:67], v[64:67]\n"
"s_add_u32 s80, s88, 131072\n"
"ds_read_b64_tr_b16 v[144:145], v248 offset:0\n"
"v_xor_b32_e32 v229, 32, v228\n"
"v_mfma_f32_16x16x32_bf16 v[64:67], v[132:135], a[80:83], v[64:67]\n"
"s_add_u32 m0, s46, 73728\n"
"v_xor_b32_e32 v230, 64, v228\n"
"v_xor_b32_e32 v231, 96, v228\n"
"v_mfma_f32_16x16x32_bf16 v[68:71], v[132:135], a[84:87], v[68:71]\n"
"ds_read_b64_tr_b16 v[146:147], v248 offset:2048\n"
"v_xor_b32_e32 v232, 128, v228\n"
"v_mfma_f32_16x16x32_bf16 v[68:71], v[128:131], a[68:71], v[68:71]\n"
"buffer_load_dwordx4 v254, s[60:63], s80 offen lds\n"
"v_mfma_f32_16x16x32_bf16 v[72:75], v[128:131], a[72:75], v[72:75]\n"
"s_addk_i32 s80, 8192\n"
"ds_read_b64_tr_b16 v[148:149], v248 offset:4096\n"
"v_xor_b32_e32 v233, 160, v228\n"
"v_mfma_f32_16x16x32_bf16 v[72:75], v[132:135], a[88:91], v[72:75]\n"
"s_addk_i32 m0, 1024\n"
"v_xor_b32_e32 v234, 192, v228\n"
"v_xor_b32_e32 v235, 224, v228\n"
"v_mfma_f32_16x16x32_bf16 v[76:79], v[132:135], a[92:95], v[76:79]\n"
"ds_read_b64_tr_b16 v[150:151], v248 offset:6144\n"
"v_mfma_f32_16x16x32_bf16 v[76:79], v[128:131], a[76:79], v[76:79]\n"
"s_waitcnt lgkmcnt(4)\n"
"buffer_load_dwordx4 v254, s[60:63], s80 offen lds\n"
"v_mfma_f32_16x16x32_bf16 v[92:95], v[136:139], a[76:79], v[92:95]\n"
"s_addk_i32 s80, 8192\n"
"ds_read_b64_tr_b16 v[152:153], v249 offset:0\n"
"v_mfma_f32_16x16x32_bf16 v[92:95], v[140:143], a[92:95], v[92:95]\n"
"s_addk_i32 m0, 1024\n"
"v_mfma_f32_16x16x32_bf16 v[88:91], v[140:143], a[88:91], v[88:91]\n"
"ds_read_b64_tr_b16 v[154:155], v249 offset:2048\n"
"v_mfma_f32_16x16x32_bf16 v[88:91], v[136:139], a[72:75], v[88:91]\n"
"buffer_load_dwordx4 v254, s[60:63], s80 offen lds\n"
"v_mfma_f32_16x16x32_bf16 v[84:87], v[136:139], a[68:71], v[84:87]\n"
"s_addk_i32 s80, 8192\n"
"ds_read_b64_tr_b16 v[156:157], v249 offset:4096\n"
"v_mfma_f32_16x16x32_bf16 v[84:87], v[140:143], a[84:87], v[84:87]\n"
"s_addk_i32 m0, 1024\n"
"v_mfma_f32_16x16x32_bf16 v[80:83], v[140:143], a[80:83], v[80:83]\n"
"ds_read_b64_tr_b16 v[158:159], v249 offset:6144\n"
"v_mfma_f32_16x16x32_bf16 v[80:83], v[136:139], a[64:67], v[80:83]\n"
"s_waitcnt lgkmcnt(4)\n"
"buffer_load_dwordx4 v254, s[60:63], s80 offen lds\n"
"v_mfma_f32_16x16x32_bf16 v[96:99], v[144:147], a[64:67], v[96:99]\n"
"s_waitcnt vmcnt(12)\n"
"ds_read_b64_tr_b16 v[128:129], v250 offset:0\n"
"v_mfma_f32_16x16x32_bf16 v[96:99], v[148:151], a[80:83], v[96:99]\n"
"s_addk_i32 s80, 8192\n"
"v_mfma_f32_16x16x32_bf16 v[100:103], v[148:151], a[84:87], v[100:103]\n"
"s_addk_i32 m0, 1024\n"
"ds_read_b64_tr_b16 v[130:131], v250 offset:2048\n"
"v_mfma_f32_16x16x32_bf16 v[100:103], v[144:147], a[68:71], v[100:103]\n"
"buffer_load_dwordx4 v254, s[60:63], s80 offen lds\n"
"v_mfma_f32_16x16x32_bf16 v[104:107], v[144:147], a[72:75], v[104:107]\n"
"s_addk_i32 s80, 8192\n"
"ds_read_b64_tr_b16 v[132:133], v250 offset:4096\n"
"v_mfma_f32_16x16x32_bf16 v[104:107], v[148:151], a[88:91], v[104:107]\n"
"s_addk_i32 m0, 1024\n"
"v_mfma_f32_16x16x32_bf16 v[108:111], v[148:151], a[92:95], v[108:111]\n"
"ds_read_b64_tr_b16 v[134:135], v250 offset:6144\n"
"v_mfma_f32_16x16x32_bf16 v[108:111], v[144:147], a[76:79], v[108:111]\n"
"s_waitcnt lgkmcnt(4)\n"
"buffer_load_dwordx4 v254, s[60:63], s80 offen lds\n"
"v_mfma_f32_16x16x32_bf16 v[124:127], v[152:155], a[76:79], v[124:127]\n"
"s_addk_i32 s80, 8192\n"
"ds_read_b64_tr_b16 v[136:137], v251 offset:0\n"
"v_mfma_f32_16x16x32_bf16 v[124:127], v[156:159], a[92:95], v[124:127]\n"
"s_addk_i32 m0, 1024\n"
"v_mfma_f32_16x16x32_bf16 v[120:123], v[156:159], a[88:91], v[120:123]\n"
"ds_read_b64_tr_b16 v[138:139], v251 offset:2048\n"
"v_mfma_f32_16x16x32_bf16 v[120:123], v[152:155], a[72:75], v[120:123]\n"
"buffer_load_dwordx4 v254, s[60:63], s80 offen lds\n"
"v_mfma_f32_16x16x32_bf16 v[116:119], v[152:155], a[68:71], v[116:119]\n"
"s_addk_i32 s80, 8192\n"
"ds_read_b64_tr_b16 v[140:141], v251 offset:4096\n"
"v_mfma_f32_16x16x32_bf16 v[116:119], v[156:159], a[84:87], v[116:119]\n"
"s_addk_i32 m0, 1024\n"
"v_mfma_f32_16x16x32_bf16 v[112:115], v[156:159], a[80:83], v[112:115]\n"
"ds_read_b64_tr_b16 v[142:143], v251 offset:6144\n"
"v_mfma_f32_16x16x32_bf16 v[112:115], v[152:155], a[64:67], v[112:115]\n"
"s_waitcnt lgkmcnt(4)\n"
"buffer_load_dwordx4 v254, s[60:63], s80 offen lds\n"
"v_mfma_f32_16x16x32_bf16 v[0:3], v[128:131], a[0:3], 0\n"
"s_add_u32 s44, s44, 16384\n"
"ds_read_b64_tr_b16 v[144:145], v252 offset:0\n"
"v_cvt_pk_bf16_f32 v64, v64, v65\n"
"v_mfma_f32_16x16x32_bf16 v[0:3], v[132:135], a[16:19], v[0:3]\n"
"s_add_u32 m0, s46, 0\n"
"v_cvt_pk_bf16_f32 v65, v66, v67\n"
"v_cvt_pk_bf16_f32 v66, v68, v69\n"
"v_mfma_f32_16x16x32_bf16 v[4:7], v[132:135], a[20:23], 0\n"
"ds_read_b64_tr_b16 v[146:147], v252 offset:2048\n"
"v_cvt_pk_bf16_f32 v67, v70, v71\n"
"v_mfma_f32_16x16x32_bf16 v[4:7], v[128:131], a[4:7], v[4:7]\n"
"buffer_load_dwordx4 v236, s[36:39], s44 offen offset:0 lds\n"
"v_mfma_f32_16x16x32_bf16 v[8:11], v[128:131], a[8:11], 0\n"
"ds_read_b64_tr_b16 v[148:149], v252 offset:4096\n"
"v_cvt_pk_bf16_f32 v72, v72, v73\n"
"v_mfma_f32_16x16x32_bf16 v[8:11], v[132:135], a[24:27], v[8:11]\n"
"v_cvt_pk_bf16_f32 v73, v74, v75\n"
"v_cvt_pk_bf16_f32 v74, v76, v77\n"
"v_mfma_f32_16x16x32_bf16 v[12:15], v[132:135], a[28:31], 0\n"
"ds_read_b64_tr_b16 v[150:151], v252 offset:6144\n"
"v_cvt_pk_bf16_f32 v75, v78, v79\n"
"v_mfma_f32_16x16x32_bf16 v[12:15], v[128:131], a[12:15], v[12:15]\n"
"s_waitcnt lgkmcnt(4)\n"
"buffer_load_dwordx4 v237, s[36:39], s44 offen offset:1024 lds\n"
"v_mfma_f32_16x16x32_bf16 v[28:31], v[136:139], a[12:15], 0\n"
"ds_read_b64_tr_b16 v[152:153], v253 offset:0\n"
"v_cvt_pk_bf16_f32 v80, v80, v81\n"
"v_mfma_f32_16x16x32_bf16 v[28:31], v[140:143], a[28:31], v[28:31]\n"
"v_cvt_pk_bf16_f32 v81, v82, v83\n"
"v_cvt_pk_bf16_f32 v82, v84, v85\n"
"v_mfma_f32_16x16x32_bf16 v[24:27], v[140:143], a[24:27], 0\n"
"ds_read_b64_tr_b16 v[154:155], v253 offset:2048\n"
"v_cvt_pk_bf16_f32 v83, v86, v87\n"
"v_mfma_f32_16x16x32_bf16 v[24:27], v[136:139], a[8:11], v[24:27]\n"
"buffer_load_dwordx4 v238, s[36:39], s44 offen offset:2048 lds\n"
"v_mfma_f32_16x16x32_bf16 v[20:23], v[136:139], a[4:7], 0\n"
"ds_read_b64_tr_b16 v[156:157], v253 offset:4096\n"
"v_cvt_pk_bf16_f32 v88, v88, v89\n"
"v_mfma_f32_16x16x32_bf16 v[20:23], v[140:143], a[20:23], v[20:23]\n"
"v_cvt_pk_bf16_f32 v89, v90, v91\n"
"v_cvt_pk_bf16_f32 v90, v92, v93\n"
"v_mfma_f32_16x16x32_bf16 v[16:19], v[140:143], a[16:19], 0\n"
"ds_read_b64_tr_b16 v[158:159], v253 offset:6144\n"
"v_cvt_pk_bf16_f32 v91, v94, v95\n"
"v_mfma_f32_16x16x32_bf16 v[16:19], v[136:139], a[0:3], v[16:19]\n"
"s_waitcnt lgkmcnt(4)\n"
"buffer_load_dwordx4 v239, s[36:39], s44 offen offset:3072 lds\n"
"v_mfma_f32_16x16x32_bf16 v[32:35], v[144:147], a[0:3], 0\n"
"s_waitcnt vmcnt(12)\n"
"ds_read_b64_tr_b16 v[128:129], v246 offset:8192\n"
"v_cvt_pk_bf16_f32 v96, v96, v97\n"
"v_mfma_f32_16x16x32_bf16 v[32:35], v[148:151], a[16:19], v[32:35]\n"
"s_add_u32 s44, s44, 16384\n"
"v_cvt_pk_bf16_f32 v97, v98, v99\n"
"v_cvt_pk_bf16_f32 v98, v100, v101\n"
"v_mfma_f32_16x16x32_bf16 v[36:39], v[148:151], a[20:23], 0\n"
"s_add_u32 m0, s46, 4096\n"
"ds_read_b64_tr_b16 v[130:131], v246 offset:10240\n"
"v_cvt_pk_bf16_f32 v99, v102, v103\n"
"v_mfma_f32_16x16x32_bf16 v[36:39], v[144:147], a[4:7], v[36:39]\n"
"buffer_load_dwordx4 v236, s[36:39], s44 offen offset:0 lds\n"
"v_mfma_f32_16x16x32_bf16 v[40:43], v[144:147], a[8:11], 0\n"
"ds_read_b64_tr_b16 v[132:133], v246 offset:12288\n"
"v_cvt_pk_bf16_f32 v104, v104, v105\n"
"v_mfma_f32_16x16x32_bf16 v[40:43], v[148:151], a[24:27], v[40:43]\n"
"v_cvt_pk_bf16_f32 v105, v106, v107\n"
"v_cvt_pk_bf16_f32 v106, v108, v109\n"
"v_mfma_f32_16x16x32_bf16 v[44:47], v[148:151], a[28:31], 0\n"
"ds_read_b64_tr_b16 v[134:135], v246 offset:14336\n"
"v_cvt_pk_bf16_f32 v107, v110, v111\n"
"v_mfma_f32_16x16x32_bf16 v[44:47], v[144:147], a[12:15], v[44:47]\n"
"s_waitcnt lgkmcnt(4)\n"
"buffer_load_dwordx4 v237, s[36:39], s44 offen offset:1024 lds\n"
"v_mfma_f32_16x16x32_bf16 v[60:63], v[152:155], a[12:15], 0\n"
"ds_read_b64_tr_b16 v[136:137], v247 offset:8192\n"
"v_cvt_pk_bf16_f32 v112, v112, v113\n"
"v_mfma_f32_16x16x32_bf16 v[60:63], v[156:159], a[28:31], v[60:63]\n"
"v_cvt_pk_bf16_f32 v113, v114, v115\n"
"v_cvt_pk_bf16_f32 v114, v116, v117\n"
"v_mfma_f32_16x16x32_bf16 v[56:59], v[156:159], a[24:27], 0\n"
"ds_read_b64_tr_b16 v[138:139], v247 offset:10240\n"
"v_cvt_pk_bf16_f32 v115, v118, v119\n"
"v_mfma_f32_16x16x32_bf16 v[56:59], v[152:155], a[8:11], v[56:59]\n"
"buffer_load_dwordx4 v238, s[36:39], s44 offen offset:2048 lds\n"
"v_mfma_f32_16x16x32_bf16 v[52:55], v[152:155], a[4:7], 0\n"
"ds_read_b64_tr_b16 v[140:141], v247 offset:12288\n"
"v_cvt_pk_bf16_f32 v120, v120, v121\n"
"v_mfma_f32_16x16x32_bf16 v[52:55], v[156:159], a[20:23], v[52:55]\n"
"v_cvt_pk_bf16_f32 v121, v122, v123\n"
"v_cvt_pk_bf16_f32 v122, v124, v125\n"
"v_mfma_f32_16x16x32_bf16 v[48:51], v[156:159], a[16:19], 0\n"
"ds_read_b64_tr_b16 v[142:143], v247 offset:14336\n"
"v_cvt_pk_bf16_f32 v123, v126, v127\n"
"v_mfma_f32_16x16x32_bf16 v[48:51], v[152:155], a[0:3], v[48:51]\n"
"s_waitcnt lgkmcnt(4)\n"
"buffer_load_dwordx4 v239, s[36:39], s44 offen offset:3072 lds\n"
"v_mfma_f32_16x16x32_bf16 v[0:3], v[128:131], a[32:35], v[0:3]\n"
"ds_read_b64_tr_b16 v[144:145], v248 offset:8192\n"
"v_mfma_f32_16x16x32_bf16 v[0:3], v[132:135], a[48:51], v[0:3]\n"
"v_mfma_f32_16x16x32_bf16 v[4:7], v[132:135], a[52:55], v[4:7]\n"
"s_add_u32 s45, s45, 16384\n"
"ds_read_b64_tr_b16 v[146:147], v248 offset:10240\n"
"v_mfma_f32_16x16x32_bf16 v[4:7], v[128:131], a[36:39], v[4:7]\n"
"s_add_u32 m0, s46, 69632\n"
"v_mfma_f32_16x16x32_bf16 v[8:11], v[128:131], a[40:43], v[8:11]\n"
"ds_read_b64_tr_b16 v[148:149], v248 offset:12288\n"
"v_mfma_f32_16x16x32_bf16 v[8:11], v[132:135], a[56:59], v[8:11]\n"
"buffer_load_dwordx4 v240, s[40:43], s45 offen offset:0 lds\n"
"v_mfma_f32_16x16x32_bf16 v[12:15], v[132:135], a[60:63], v[12:15]\n"
"ds_read_b64_tr_b16 v[150:151], v248 offset:14336\n"
"v_mfma_f32_16x16x32_bf16 v[12:15], v[128:131], a[44:47], v[12:15]\n"
"s_waitcnt lgkmcnt(4)\n"
"v_mfma_f32_16x16x32_bf16 v[28:31], v[136:139], a[44:47], v[28:31]\n"
"ds_read_b64_tr_b16 v[152:153], v249 offset:8192\n"
"v_mfma_f32_16x16x32_bf16 v[28:31], v[140:143], a[60:63], v[28:31]\n"
"v_mfma_f32_16x16x32_bf16 v[24:27], v[140:143], a[56:59], v[24:27]\n"
"ds_read_b64_tr_b16 v[154:155], v249 offset:10240\n"
"v_mfma_f32_16x16x32_bf16 v[24:27], v[136:139], a[40:43], v[24:27]\n"
"v_mfma_f32_16x16x32_bf16 v[20:23], v[136:139], a[36:39], v[20:23]\n"
"ds_read_b64_tr_b16 v[156:157], v249 offset:12288\n"
"v_mfma_f32_16x16x32_bf16 v[20:23], v[140:143], a[52:55], v[20:23]\n"
"buffer_load_dwordx4 v241, s[40:43], s45 offen offset:1024 lds\n"
"v_mfma_f32_16x16x32_bf16 v[16:19], v[140:143], a[48:51], v[16:19]\n"
"ds_read_b64_tr_b16 v[158:159], v249 offset:14336\n"
"v_mfma_f32_16x16x32_bf16 v[16:19], v[136:139], a[32:35], v[16:19]\n"
"s_waitcnt lgkmcnt(4)\n"
"v_mfma_f32_16x16x32_bf16 v[32:35], v[144:147], a[32:35], v[32:35]\n"
"s_waitcnt vmcnt(10)\n"
"ds_read_b64_tr_b16 v[128:129], v250 offset:8192\n"
"v_mfma_f32_16x16x32_bf16 v[32:35], v[148:151], a[48:51], v[32:35]\n"
"v_mfma_f32_16x16x32_bf16 v[36:39], v[148:151], a[52:55], v[36:39]\n"
"ds_read_b64_tr_b16 v[130:131], v250 offset:10240\n"
"v_mfma_f32_16x16x32_bf16 v[36:39], v[144:147], a[36:39], v[36:39]\n"
"v_mfma_f32_16x16x32_bf16 v[40:43], v[144:147], a[40:43], v[40:43]\n"
"ds_read_b64_tr_b16 v[132:133], v250 offset:12288\n"
"v_mfma_f32_16x16x32_bf16 v[40:43], v[148:151], a[56:59], v[40:43]\n"
"buffer_load_dwordx4 v240, s[40:43], s45 offen offset:2048 lds\n"
"v_mfma_f32_16x16x32_bf16 v[44:47], v[148:151], a[60:63], v[44:47]\n"
"ds_read_b64_tr_b16 v[134:135], v250 offset:14336\n"
"v_mfma_f32_16x16x32_bf16 v[44:47], v[144:147], a[44:47], v[44:47]\n"
"s_waitcnt lgkmcnt(4)\n"
"v_mfma_f32_16x16x32_bf16 v[60:63], v[152:155], a[44:47], v[60:63]\n"
"ds_read_b64_tr_b16 v[136:137], v251 offset:8192\n"
"v_mfma_f32_16x16x32_bf16 v[60:63], v[156:159], a[60:63], v[60:63]\n"
"v_mfma_f32_16x16x32_bf16 v[56:59], v[156:159], a[56:59], v[56:59]\n"
"ds_read_b64_tr_b16 v[138:139], v251 offset:10240\n"
"v_mfma_f32_16x16x32_bf16 v[56:59], v[152:155], a[40:43], v[56:59]\n"
"v_mfma_f32_16x16x32_bf16 v[52:55], v[152:155], a[36:39], v[52:55]\n"
"ds_read_b64_tr_b16 v[140:141], v251 offset:12288\n"
"v_mfma_f32_16x16x32_bf16 v[52:55], v[156:159], a[52:55], v[52:55]\n"
"buffer_load_dwordx4 v241, s[40:43], s45 offen offset:3072 lds\n"
"v_mfma_f32_16x16x32_bf16 v[48:51], v[156:159], a[48:51], v[48:51]\n"
"ds_read_b64_tr_b16 v[142:143], v251 offset:14336\n"
"v_mfma_f32_16x16x32_bf16 v[48:51], v[152:155], a[32:35], v[48:51]\n"
"s_waitcnt lgkmcnt(4)\n"
"v_mfma_f32_16x16x32_bf16 v[0:3], v[128:131], a[64:67], v[0:3]\n"
"s_add_u32 s44, s44, 16384\n"
"ds_read_b64_tr_b16 v[144:145], v252 offset:8192\n"
"v_mfma_f32_16x16x32_bf16 v[0:3], v[132:135], a[80:83], v[0:3]\n"
"s_add_u32 m0, s46, 8192\n"
"v_mfma_f32_16x16x32_bf16 v[4:7], v[132:135], a[84:87], v[4:7]\n"
"ds_read_b64_tr_b16 v[146:147], v252 offset:10240\n"
"v_mfma_f32_16x16x32_bf16 v[4:7], v[128:131], a[68:71], v[4:7]\n"
"buffer_load_dwordx4 v236, s[36:39], s44 offen offset:0 lds\n"
"v_mfma_f32_16x16x32_bf16 v[8:11], v[128:131], a[72:75], v[8:11]\n"
"ds_read_b64_tr_b16 v[148:149], v252 offset:12288\n"
"v_mfma_f32_16x16x32_bf16 v[8:11], v[132:135], a[88:91], v[8:11]\n"
"v_mfma_f32_16x16x32_bf16 v[12:15], v[132:135], a[92:95], v[12:15]\n"
"ds_read_b64_tr_b16 v[150:151], v252 offset:14336\n"
"v_mfma_f32_16x16x32_bf16 v[12:15], v[128:131], a[76:79], v[12:15]\n"
"s_waitcnt lgkmcnt(4)\n"
"buffer_load_dwordx4 v237, s[36:39], s44 offen offset:1024 lds\n"
"v_mfma_f32_16x16x32_bf16 v[28:31], v[136:139], a[76:79], v[28:31]\n"
"ds_read_b64_tr_b16 v[152:153], v253 offset:8192\n"
"v_mfma_f32_16x16x32_bf16 v[28:31], v[140:143], a[92:95], v[28:31]\n"
"v_mfma_f32_16x16x32_bf16 v[24:27], v[140:143], a[88:91], v[24:27]\n"
"ds_read_b64_tr_b16 v[154:155], v253 offset:10240\n"
"v_mfma_f32_16x16x32_bf16 v[24:27], v[136:139], a[72:75], v[24:27]\n"
"buffer_load_dwordx4 v238, s[36:39], s44 offen offset:2048 lds\n"
"v_mfma_f32_16x16x32_bf16 v[20:23], v[136:139], a[68:71], v[20:23]\n"
"ds_read_b64_tr_b16 v[156:157], v253 offset:12288\n"
"v_mfma_f32_16x16x32_bf16 v[20:23], v[140:143], a[84:87], v[20:23]\n"
"v_mfma_f32_16x16x32_bf16 v[16:19], v[140:143], a[80:83], v[16:19]\n"
"ds_read_b64_tr_b16 v[158:159], v253 offset:14336\n"
"v_mfma_f32_16x16x32_bf16 v[16:19], v[136:139], a[64:67], v[16:19]\n"
"s_waitcnt lgkmcnt(4)\n"
"buffer_load_dwordx4 v239, s[36:39], s44 offen offset:3072 lds\n"
"v_mfma_f32_16x16x32_bf16 v[32:35], v[144:147], a[64:67], v[32:35]\n"
"s_add_u32 s44, s44, 16384\n"
"v_mfma_f32_16x16x32_bf16 v[32:35], v[148:151], a[80:83], v[32:35]\n"
"s_add_u32 m0, s46, 12288\n"
"v_mfma_f32_16x16x32_bf16 v[36:39], v[148:151], a[84:87], v[36:39]\n"
"v_mfma_f32_16x16x32_bf16 v[36:39], v[144:147], a[68:71], v[36:39]\n"
"buffer_load_dwordx4 v236, s[36:39], s44 offen offset:0 lds\n"
"v_mfma_f32_16x16x32_bf16 v[40:43], v[144:147], a[72:75], v[40:43]\n"
"v_mfma_f32_16x16x32_bf16 v[40:43], v[148:151], a[88:91], v[40:43]\n"
"v_mfma_f32_16x16x32_bf16 v[44:47], v[148:151], a[92:95], v[44:47]\n"
"v_mfma_f32_16x16x32_bf16 v[44:47], v[144:147], a[76:79], v[44:47]\n"
"s_waitcnt lgkmcnt(0)\n"
"buffer_load_dwordx4 v237, s[36:39], s44 offen offset:1024 lds\n"
"v_mfma_f32_16x16x32_bf16 v[60:63], v[152:155], a[76:79], v[60:63]\n"
"v_mfma_f32_16x16x32_bf16 v[60:63], v[156:159], a[92:95], v[60:63]\n"
"v_mfma_f32_16x16x32_bf16 v[56:59], v[156:159], a[88:91], v[56:59]\n"
"v_mfma_f32_16x16x32_bf16 v[56:59], v[152:155], a[72:75], v[56:59]\n"
"buffer_load_dwordx4 v238, s[36:39], s44 offen offset:2048 lds\n"
"v_mfma_f32_16x16x32_bf16 v[52:55], v[152:155], a[68:71], v[52:55]\n"
"v_mfma_f32_16x16x32_bf16 v[52:55], v[156:159], a[84:87], v[52:55]\n"
"v_mfma_f32_16x16x32_bf16 v[48:51], v[156:159], a[80:83], v[48:51]\n"
"v_mfma_f32_16x16x32_bf16 v[48:51], v[152:155], a[64:67], v[48:51]\n"
"buffer_load_dwordx4 v239, s[36:39], s44 offen offset:3072 lds\n"
"s_waitcnt vmcnt(8)\n"
"s_barrier\n"
"ds_read_b128 a[224:227], v224 offset:0\n"
"ds_read_b128 a[228:231], v225 offset:0\n"
"ds_read_b128 a[232:235], v226 offset:0\n"
"ds_read_b128 a[236:239], v227 offset:0\n"
"s_waitcnt lgkmcnt(0)\n"
"v_mfma_f32_16x16x32_bf16 v[192:195], a[224:227], a[128:131], 0\n"
"s_add_u32 m0, s46, 73728\n"
"s_add_u32 s45, s45, 16384\n"
"v_cvt_pk_bf16_f32 v0, v0, v1\n"
"v_mfma_f32_16x16x32_bf16 v[192:195], a[228:231], a[132:135], v[192:195]\n"
"buffer_load_dwordx4 v240, s[40:43], s45 offen offset:0 lds\n"
"v_cvt_pk_bf16_f32 v1, v2, v3\n"
"v_mfma_f32_16x16x32_bf16 v[192:195], a[232:235], a[136:139], v[192:195]\n"
"v_cvt_pk_bf16_f32 v2, v4, v5\n"
"v_cvt_pk_bf16_f32 v3, v6, v7\n"
"v_mfma_f32_16x16x32_bf16 v[192:195], a[236:239], a[140:143], v[192:195]\n"
"buffer_load_dwordx4 v241, s[40:43], s45 offen offset:1024 lds\n"
"v_cvt_pk_bf16_f32 v8, v8, v9\n"
"v_mfma_f32_16x16x32_bf16 v[196:199], a[224:227], a[144:147], 0\n"
"v_cvt_pk_bf16_f32 v9, v10, v11\n"
"v_cvt_pk_bf16_f32 v10, v12, v13\n"
"v_mfma_f32_16x16x32_bf16 v[196:199], a[228:231], a[148:151], v[196:199]\n"
"buffer_load_dwordx4 v240, s[40:43], s45 offen offset:2048 lds\n"
"v_cvt_pk_bf16_f32 v11, v14, v15\n"
"v_mfma_f32_16x16x32_bf16 v[196:199], a[232:235], a[152:155], v[196:199]\n"
"v_cvt_pk_bf16_f32 v16, v16, v17\n"
"v_cvt_pk_bf16_f32 v17, v18, v19\n"
"v_mfma_f32_16x16x32_bf16 v[196:199], a[236:239], a[156:159], v[196:199]\n"
"buffer_load_dwordx4 v241, s[40:43], s45 offen offset:3072 lds\n"
"v_cvt_pk_bf16_f32 v18, v20, v21\n"
"v_mfma_f32_16x16x32_bf16 v[200:203], a[224:227], a[160:163], 0\n"
"s_add_u32 m0, s46, 77824\n"
"s_add_u32 s45, s45, 16384\n"
"v_cvt_pk_bf16_f32 v19, v22, v23\n"
"v_mfma_f32_16x16x32_bf16 v[200:203], a[228:231], a[164:167], v[200:203]\n"
".LPG6_%=:\n"
"s_cbranch_execz .LPGR_%=\n"
"buffer_load_dwordx4 v240, s[40:43], s45 offen offset:0 lds\n"
"v_cvt_pk_bf16_f32 v24, v24, v25\n"
"v_mfma_f32_16x16x32_bf16 v[200:203], a[232:235], a[168:171], v[200:203]\n"
"v_cvt_pk_bf16_f32 v25, v26, v27\n"
"v_cvt_pk_bf16_f32 v26, v28, v29\n"
"v_mfma_f32_16x16x32_bf16 v[200:203], a[236:239], a[172:175], v[200:203]\n"
"buffer_load_dwordx4 v241, s[40:43], s45 offen offset:1024 lds\n"
"v_cvt_pk_bf16_f32 v27, v30, v31\n"
"v_mfma_f32_16x16x32_bf16 v[204:207], a[224:227], a[176:179], 0\n"
"v_cvt_pk_bf16_f32 v32, v32, v33\n"
"v_cvt_pk_bf16_f32 v33, v34, v35\n"
"v_mfma_f32_16x16x32_bf16 v[204:207], a[228:231], a[180:183], v[204:207]\n"
"buffer_load_dwordx4 v240, s[40:43], s45 offen offset:2048 lds\n"
"v_cvt_pk_bf16_f32 v34, v36, v37\n"
"v_mfma_f32_16x16x32_bf16 v[204:207], a[232:235], a[184:187], v[204:207]\n"
"v_cvt_pk_bf16_f32 v35, v38, v39\n"
"v_cvt_pk_bf16_f32 v40, v40, v41\n"
"v_mfma_f32_16x16x32_bf16 v[204:207], a[236:239], a[188:191], v[204:207]\n"
"buffer_load_dwordx4 v241, s[40:43], s45 offen offset:3072 lds\n"
"v_cvt_pk_bf16_f32 v41, v42, v43\n"
"v_cvt_pk_bf16_f32 v42, v44, v45\n"
"v_cvt_pk_bf16_f32 v43, v46, v47\n"
"v_cvt_pk_bf16_f32 v48, v48, v49\n"
"v_cvt_pk_bf16_f32 v49, v50, v51\n"
"v_cvt_pk_bf16_f32 v50, v52, v53\n"
"v_cvt_pk_bf16_f32 v51, v54, v55\n"
"v_cvt_pk_bf16_f32 v56, v56, v57\n"
"v_cvt_pk_bf16_f32 v57, v58, v59\n"
"v_cvt_pk_bf16_f32 v58, v60, v61\n"
"v_cvt_pk_bf16_f32 v59, v62, v63\n"
".LJOIN_%=:\n"
"s_cbranch_execz .LPGR_%=\n"
"v_max3_f32 v128, v192, v193, v194\n"
"v_max3_f32 v130, v196, v197, v198\n"
"v_max3_f32 v132, v200, v201, v202\n"
"v_max3_f32 v134, v204, v205, v206\n"
"v_max_f32_e32 v128, v128, v195\n"
"v_max_f32_e32 v130, v130, v199\n"
"v_max_f32_e32 v132, v132, v203\n"
"v_max_f32_e32 v134, v134, v207\n"
"v_mov_b32_e32 v129, v128\n"
"v_mov_b32_e32 v131, v130\n"
"v_mov_b32_e32 v133, v132\n"
"v_mov_b32_e32 v135, v134\n"
"v_permlane32_swap_b32 v128, v129\n"
"v_permlane32_swap_b32 v130, v131\n"
"v_permlane32_swap_b32 v132, v133\n"
"v_permlane32_swap_b32 v134, v135\n"
"v_max_f32_e32 v128, v128, v129\n"
"v_max_f32_e32 v130, v130, v131\n"
"v_max_f32_e32 v132, v132, v133\n"
"v_max_f32_e32 v134, v134, v135\n"
"v_mov_b32_e32 v129, v128\n"
"v_mov_b32_e32 v131, v130\n"
"v_mov_b32_e32 v133, v132\n"
"v_mov_b32_e32 v135, v134\n"
"v_permlane16_swap_b32 v128, v129\n"
"v_permlane16_swap_b32 v130, v131\n"
"v_permlane16_swap_b32 v132, v133\n"
"v_permlane16_swap_b32 v134, v135\n"
"v_max_f32_e32 v128, v128, v129\n"
"v_max_f32_e32 v130, v130, v131\n"
"v_max_f32_e32 v132, v132, v133\n"
"v_max_f32_e32 v134, v134, v135\n"
"v_add_f32_e32 v128, 0x41700000, v128\n"
"v_add_f32_e32 v130, 0x41700000, v130\n"
"v_add_f32_e32 v132, 0x41700000, v132\n"
"v_add_f32_e32 v134, 0x41700000, v134\n"
"v_sub_f32_e32 v208, 0, v128\n"
"v_sub_f32_e32 v212, 0, v130\n"
"v_sub_f32_e32 v216, 0, v132\n"
"v_sub_f32_e32 v220, 0, v134\n"
"v_sub_f32_e32 v209, 0, v128\n"
"v_sub_f32_e32 v213, 0, v130\n"
"v_sub_f32_e32 v217, 0, v132\n"
"v_sub_f32_e32 v221, 0, v134\n"
"v_sub_f32_e32 v210, 0, v128\n"
"v_sub_f32_e32 v214, 0, v130\n"
"v_sub_f32_e32 v218, 0, v132\n"
"v_sub_f32_e32 v222, 0, v134\n"
"v_sub_f32_e32 v211, 0, v128\n"
"v_sub_f32_e32 v215, 0, v130\n"
"v_sub_f32_e32 v219, 0, v132\n"
"v_sub_f32_e32 v223, 0, v134\n"
"v_sub_f32_e32 v192, v192, v128\n"
"v_sub_f32_e32 v196, v196, v130\n"
"v_sub_f32_e32 v200, v200, v132\n"
"v_sub_f32_e32 v204, v204, v134\n"
"v_sub_f32_e32 v193, v193, v128\n"
"v_sub_f32_e32 v197, v197, v130\n"
"v_sub_f32_e32 v201, v201, v132\n"
"v_sub_f32_e32 v205, v205, v134\n"
"v_sub_f32_e32 v194, v194, v128\n"
"v_sub_f32_e32 v198, v198, v130\n"
"v_sub_f32_e32 v202, v202, v132\n"
"v_sub_f32_e32 v206, v206, v134\n"
"v_sub_f32_e32 v195, v195, v128\n"
"v_sub_f32_e32 v199, v199, v130\n"
"v_sub_f32_e32 v203, v203, v132\n"
"v_sub_f32_e32 v207, v207, v134\n"
".LATT_%=:\n"
"s_getpc_b64 s[60:61]\n"
".LPC_%=:\n"
"s_mov_b32 s93, 0x07060302\n"
"ds_read_b64 v[160:161], v228 offset:4096\n"
"ds_read_b128 a[240:243], v224 offset:16384\n"
"ds_read_b128 a[244:247], v225 offset:16384\n"
"s_add_u32 s94, s91, 0\n"
"ds_read_b128 a[248:251], v226 offset:16384\n"
"buffer_store_dwordx4 v[0:3], v255, s[68:71], s94 offen offset:0 sc1\n"
"v_exp_f32_e32 v192, v192\n"
"ds_read_b128 a[252:255], v227 offset:16384\n"
"v_exp_f32_e32 v193, v193\n"
"s_nop 0\n"
"v_perm_b32 v128, v193, v192, s93\n"
"v_exp_f32_e32 v194, v194\n"
"v_exp_f32_e32 v195, v195\n"
"s_nop 0\n"
"v_perm_b32 v129, v195, v194, s93\n"
"v_exp_f32_e32 v196, v196\n"
"v_exp_f32_e32 v197, v197\n"
"s_nop 0\n"
"v_perm_b32 v136, v197, v196, s93\n"
"v_exp_f32_e32 v198, v198\n"
"v_exp_f32_e32 v199, v199\n"
"buffer_store_dwordx4 v[8:11], v255, s[68:71], s94 offen offset:1024 sc1\n"
"v_exp_f32_e32 v200, v200\n"
"v_perm_b32 v137, v199, v198, s93\n"
"v_exp_f32_e32 v201, v201\n"
"s_nop 0\n"
"v_perm_b32 v144, v201, v200, s93\n"
"v_exp_f32_e32 v202, v202\n"
"v_exp_f32_e32 v203, v203\n"
"s_nop 0\n"
"v_perm_b32 v145, v203, v202, s93\n"
"v_exp_f32_e32 v204, v204\n"
"s_waitcnt lgkmcnt(0)\n"
"v_mfma_f32_16x16x32_bf16 v[192:195], a[240:243], a[128:131], v[208:211]\n"
"s_mov_b32 s47, 5\n"
"ds_read_b128 a[224:227], v224 offset:32768\n"
"v_mfma_f32_16x16x32_bf16 v[192:195], a[244:247], a[132:135], v[192:195]\n"
"s_andn2_b32 s58, s26, s92\n"
"v_exp_f32_e32 v205, v205\n"
"v_mfma_f32_16x16x32_bf16 v[192:195], a[248:251], a[136:139], v[192:195]\n"
"s_mov_b32 s62, .LLATEEND_%=-.LPC_%=\n"
"ds_read_b128 a[228:231], v225 offset:32768\n"
"v_perm_b32 v152, v205, v204, s93\n"
"v_mfma_f32_16x16x32_bf16 v[192:195], a[252:255], a[140:143], v[192:195]\n"
"s_add_u32 s94, s91, 18432\n"
"v_exp_f32_e32 v206, v206\n"
"v_mfma_f32_16x16x32_bf16 v[196:199], a[252:255], a[156:159], v[212:215]\n"
"s_mov_b32 s63, 0x20000\n"
"ds_read_b128 a[232:235], v226 offset:32768\n"
"v_mfma_f32_16x16x32_bf16 v[196:199], a[248:251], a[152:155], v[196:199]\n"
"s_lshr_b32 s86, %1, 3\n"
"v_exp_f32_e32 v207, v207\n"
"v_mfma_f32_16x16x32_bf16 v[196:199], a[244:247], a[148:151], v[196:199]\n"
"s_lshl_b32 s86, s86, 2\n"
"buffer_store_dwordx4 v[16:19], v255, s[68:71], s94 offen offset:0 sc1\n"
"v_mfma_f32_16x16x32_bf16 v[196:199], a[240:243], a[144:147], v[196:199]\n"
"s_add_u32 s86, s86, s77\n"
"v_exp_f32_e32 v192, v192\n"
"v_mfma_f32_16x16x32_bf16 v[200:203], a[240:243], a[160:163], v[216:219]\n"
"s_lshl_b32 s86, s86, 9\n"
"ds_read_b128 a[236:239], v227 offset:32768\n"
"v_perm_b32 v153, v207, v206, s93\n"
"v_mfma_f32_16x16x32_bf16 v[200:203], a[244:247], a[164:167], v[200:203]\n"
"s_add_u32 s86, s86, .LLATE_%=-.LPC_%=\n"
"v_exp_f32_e32 v193, v193\n"
"v_mfma_f32_16x16x32_bf16 v[200:203], a[248:251], a[168:171], v[200:203]\n"
"v_lshlrev_b32_e32 v162, 16, v160\n"
"v_max3_f32 v160, abs(v160), abs(v161), abs(v162)\n"
"v_mfma_f32_16x16x32_bf16 v[200:203], a[252:255], a[172:175], v[200:203]\n"
"v_exp_f32_e32 v194, v194\n"
"v_mfma_f32_16x16x32_bf16 v[204:207], a[252:255], a[188:191], v[220:223]\n"
"v_perm_b32 v130, v193, v192, s93\n"
"v_readlane_b32 s23, v160, 1\n"
"v_mfma_f32_16x16x32_bf16 v[204:207], a[248:251], a[184:187], v[204:207]\n"
"v_exp_f32_e32 v195, v195\n"
"v_mfma_f32_16x16x32_bf16 v[204:207], a[244:247], a[180:183], v[204:207]\n"
"v_readlane_b32 s82, v160, 22\n"
"v_readlane_b32 s83, v160, 43\n"
"v_mfma_f32_16x16x32_bf16 v[204:207], a[240:243], a[176:179], v[204:207]\n"
"v_exp_f32_e32 v196, v196\n"
"v_perm_b32 v131, v195, v194, s93\n"
"v_exp_f32_e32 v197, v197\n"
"s_nop 0\n"
"v_perm_b32 v138, v197, v196, s93\n"
"v_exp_f32_e32 v198, v198\n"
"v_exp_f32_e32 v199, v199\n"
"s_nop 0\n"
"v_perm_b32 v139, v199, v198, s93\n"
"v_exp_f32_e32 v200, v200\n"
"s_min_u32 s23, s23, s82\n"
"v_exp_f32_e32 v201, v201\n"
"s_min_u32 s23, s23, s83\n"
"v_perm_b32 v146, v201, v200, s93\n"
"v_exp_f32_e32 v202, v202\n"
"s_and_b32 s23, s23, 0x7f800000\n"
"v_exp_f32_e32 v203, v203\n"
"s_max_u32 s23, s23, 0x10000000\n"
"v_perm_b32 v147, v203, v202, s93\n"
"v_exp_f32_e32 v204, v204\n"
"s_waitcnt lgkmcnt(0)\n"
"s_min_u32 s23, s23, 0x6e000000\n"
"v_mfma_f32_16x16x32_bf16 v[192:195], a[224:227], a[128:131], v[208:211]\n"
"s_add_u32 s23, s23, 0xfdc00000\n"
"ds_read_b128 a[240:243], v224 offset:49152\n"
"v_mfma_f32_16x16x32_bf16 v[192:195], a[228:231], a[132:135], v[192:195]\n"
"v_exp_f32_e32 v205, v205\n"
"v_mfma_f32_16x16x32_bf16 v[192:195], a[232:235], a[136:139], v[192:195]\n"
"ds_read_b128 a[244:247], v225 offset:49152\n"
"v_perm_b32 v154, v205, v204, s93\n"
"v_mfma_f32_16x16x32_bf16 v[192:195], a[236:239], a[140:143], v[192:195]\n"
"v_exp_f32_e32 v206, v206\n"
"v_mfma_f32_16x16x32_bf16 v[196:199], a[236:239], a[156:159], v[212:215]\n"
"ds_read_b128 a[248:251], v226 offset:49152\n"
"v_mfma_f32_16x16x32_bf16 v[196:199], a[232:235], a[152:155], v[196:199]\n"
"v_exp_f32_e32 v207, v207\n"
"v_mfma_f32_16x16x32_bf16 v[196:199], a[228:231], a[148:151], v[196:199]\n"
"buffer_store_dwordx4 v[24:27], v255, s[68:71], s94 offen offset:1024 sc1\n"
"v_mfma_f32_16x16x32_bf16 v[196:199], a[224:227], a[144:147], v[196:199]\n"
"v_exp_f32_e32 v192, v192\n"
"v_mfma_f32_16x16x32_bf16 v[200:203], a[224:227], a[160:163], v[216:219]\n"
"ds_read_b128 a[252:255], v227 offset:49152\n"
"v_perm_b32 v155, v207, v206, s93\n"
"v_mfma_f32_16x16x32_bf16 v[200:203], a[228:231], a[164:167], v[200:203]\n"
"v_exp_f32_e32 v193, v193\n"
"v_mfma_f32_16x16x32_bf16 v[200:203], a[232:235], a[168:171], v[200:203]\n"
"v_perm_b32 v132, v193, v192, s93\n"
"v_mfma_f32_16x16x32_bf16 v[200:203], a[236:239], a[172:175], v[200:203]\n"
"v_exp_f32_e32 v194, v194\n"
"v_mfma_f32_16x16x32_bf16 v[204:207], a[236:239], a[188:191], v[220:223]\n"
"v_mfma_f32_16x16x32_bf16 v[204:207], a[232:235], a[184:187], v[204:207]\n"
"v_exp_f32_e32 v195, v195\n"
"v_mfma_f32_16x16x32_bf16 v[204:207], a[228:231], a[180:183], v[204:207]\n"
"v_perm_b32 v133, v195, v194, s93\n"
"v_mov_b32_e32 v250, s23\n"
"v_mfma_f32_16x16x32_bf16 v[204:207], a[224:227], a[176:179], v[204:207]\n"
"v_exp_f32_e32 v196, v196\n"
"v_mov_b32_e32 v251, s23\n"
"v_mov_b32_e32 v252, s23\n"
"v_exp_f32_e32 v197, v197\n"
"v_mov_b32_e32 v253, s23\n"
"v_perm_b32 v140, v197, v196, s93\n"
"s_add_u32 s94, s91, 36864\n"
"v_exp_f32_e32 v198, v198\n"
"v_exp_f32_e32 v199, v199\n"
"buffer_store_dwordx4 v[32:35], v255, s[68:71], s94 offen offset:0 sc1\n"
"v_exp_f32_e32 v200, v200\n"
"v_perm_b32 v141, v199, v198, s93\n"
"v_exp_f32_e32 v201, v201\n"
"s_nop 0\n"
"v_perm_b32 v148, v201, v200, s93\n"
"v_exp_f32_e32 v202, v202\n"
"v_exp_f32_e32 v203, v203\n"
"s_nop 0\n"
"v_perm_b32 v149, v203, v202, s93\n"
"v_exp_f32_e32 v204, v204\n"
"s_waitcnt lgkmcnt(0)\n"
"v_mfma_f32_16x16x32_bf16 v[192:195], a[240:243], a[128:131], v[208:211]\n"
"ds_read_b128 a[224:227], v224 offset:4096\n"
"v_mfma_f32_16x16x32_bf16 v[192:195], a[244:247], a[132:135], v[192:195]\n"
"v_exp_f32_e32 v205, v205\n"
"v_mfma_f32_16x16x32_bf16 v[192:195], a[248:251], a[136:139], v[192:195]\n"
"ds_read_b128 a[228:231], v225 offset:4096\n"
"v_perm_b32 v156, v205, v204, s93\n"
"v_mfma_f32_16x16x32_bf16 v[192:195], a[252:255], a[140:143], v[192:195]\n"
"v_exp_f32_e32 v206, v206\n"
"v_mfma_f32_16x16x32_bf16 v[196:199], a[252:255], a[156:159], v[212:215]\n"
"ds_read_b128 a[232:235], v226 offset:4096\n"
"v_mfma_f32_16x16x32_bf16 v[196:199], a[248:251], a[152:155], v[196:199]\n"
"v_exp_f32_e32 v207, v207\n"
"v_mfma_f32_16x16x32_bf16 v[196:199], a[244:247], a[148:151], v[196:199]\n"
"buffer_store_dwordx4 v[40:43], v255, s[68:71], s94 offen offset:1024 sc1\n"
"v_mfma_f32_16x16x32_bf16 v[196:199], a[240:243], a[144:147], v[196:199]\n"
"v_exp_f32_e32 v192, v192\n"
"v_mfma_f32_16x16x32_bf16 v[200:203], a[240:243], a[160:163], v[216:219]\n"
"ds_read_b128 a[236:239], v227 offset:4096\n"
"v_perm_b32 v157, v207, v206, s93\n"
"v_mfma_f32_16x16x32_bf16 v[200:203], a[244:247], a[164:167], v[200:203]\n"
"v_exp_f32_e32 v193, v193\n"
"v_mfma_f32_16x16x32_bf16 v[200:203], a[248:251], a[168:171], v[200:203]\n"
"ds_read_b64_tr_b16 a[192:193], v228 offset:4096\n"
"v_perm_b32 v134, v193, v192, s93\n"
"v_mfma_f32_16x16x32_bf16 v[200:203], a[252:255], a[172:175], v[200:203]\n"
"v_exp_f32_e32 v194, v194\n"
"v_mfma_f32_16x16x32_bf16 v[204:207], a[252:255], a[188:191], v[220:223]\n"
"ds_read_b64_tr_b16 a[194:195], v228 offset:20480\n"
"v_mfma_f32_16x16x32_bf16 v[204:207], a[248:251], a[184:187], v[204:207]\n"
"v_exp_f32_e32 v195, v195\n"
"v_mfma_f32_16x16x32_bf16 v[204:207], a[244:247], a[180:183], v[204:207]\n"
"v_perm_b32 v135, v195, v194, s93\n"
"v_mfma_f32_16x16x32_bf16 v[204:207], a[240:243], a[176:179], v[204:207]\n"
"v_exp_f32_e32 v196, v196\n"
"ds_read_b64_tr_b16 a[196:197], v228 offset:36864\n"
"v_exp_f32_e32 v197, v197\n"
"ds_read_b64_tr_b16 a[198:199], v228 offset:53248\n"
"v_perm_b32 v142, v197, v196, s93\n"
"v_exp_f32_e32 v198, v198\n"
"ds_read_b64_tr_b16 a[200:201], v229 offset:4096\n"
"v_exp_f32_e32 v199, v199\n"
"s_nop 0\n"
"v_perm_b32 v143, v199, v198, s93\n"
"v_exp_f32_e32 v200, v200\n"
"ds_read_b64_tr_b16 a[202:203], v229 offset:20480\n"
"v_exp_f32_e32 v201, v201\n"
"ds_read_b64_tr_b16 a[204:205], v229 offset:36864\n"
"v_perm_b32 v150, v201, v200, s93\n"
"v_exp_f32_e32 v202, v202\n"
"ds_read_b64_tr_b16 a[206:207], v229 offset:53248\n"
"v_exp_f32_e32 v203, v203\n"
"s_nop 0\n"
"v_perm_b32 v151, v203, v202, s93\n"
"v_exp_f32_e32 v204, v204\n"
"s_waitcnt lgkmcnt(8)\n"
"v_mfma_f32_16x16x32_bf16 v[192:195], a[224:227], a[128:131], v[208:211]\n"
"ds_read_b128 a[240:243], v224 offset:20480\n"
"v_bfe_u32 v248, v255, 4, 1\n"
"v_mfma_f32_16x16x32_bf16 v[192:195], a[228:231], a[132:135], v[192:195]\n"
"v_exp_f32_e32 v205, v205\n"
"v_mfma_f32_16x16x32_bf16 v[192:195], a[232:235], a[136:139], v[192:195]\n"
"ds_read_b128 a[244:247], v225 offset:20480\n"
"v_add_u32_e32 v248, -1, v248\n"
"v_mfma_f32_16x16x32_bf16 v[192:195], a[236:239], a[140:143], v[192:195]\n"
"s_add_u32 s94, s91, 55296\n"
"v_exp_f32_e32 v206, v206\n"
"v_mfma_f32_16x16x32_bf16 v[196:199], a[236:239], a[156:159], v[212:215]\n"
"ds_read_b128 a[248:251], v226 offset:20480\n"
"v_perm_b32 v158, v205, v204, s93\n"
"v_mfma_f32_16x16x32_bf16 v[196:199], a[232:235], a[152:155], v[196:199]\n"
"v_exp_f32_e32 v207, v207\n"
"v_mfma_f32_16x16x32_bf16 v[196:199], a[228:231], a[148:151], v[196:199]\n"
"buffer_store_dwordx4 v[48:51], v255, s[68:71], s94 offen offset:0 sc1\n"
"v_mfma_f32_16x16x32_bf16 v[196:199], a[224:227], a[144:147], v[196:199]\n"
"v_exp_f32_e32 v192, v192\n"
"v_mfma_f32_16x16x32_bf16 v[200:203], a[224:227], a[160:163], v[216:219]\n"
"ds_read_b128 a[252:255], v227 offset:20480\n"
"v_and_b32_e32 v248, 0x3f803f80, v248\n"
"v_mfma_f32_16x16x32_bf16 v[200:203], a[228:231], a[164:167], v[200:203]\n"
"v_exp_f32_e32 v193, v193\n"
"v_mfma_f32_16x16x32_bf16 v[200:203], a[232:235], a[168:171], v[200:203]\n"
"ds_read_b64_tr_b16 a[208:209], v230 offset:4096\n"
"v_accvgpr_write_b32 a92, v248\n"
"v_accvgpr_write_b32 a112, 0\n"
"v_accvgpr_write_b32 a93, v248\n"
"v_accvgpr_write_b32 a114, 0\n"
"v_accvgpr_write_b32 a94, v248\n"
"v_perm_b32 v159, v207, v206, s93\n"
"v_mfma_f32_16x16x32_bf16 v[200:203], a[236:239], a[172:175], v[200:203]\n"
"v_exp_f32_e32 v194, v194\n"
"v_mfma_f32_16x16x32_bf16 v[204:207], a[236:239], a[188:191], v[220:223]\n"
"ds_read_b64_tr_b16 a[210:211], v230 offset:20480\n"
"v_accvgpr_write_b32 a95, v248\n"
"v_mfma_f32_16x16x32_bf16 v[204:207], a[232:235], a[184:187], v[204:207]\n"
"v_exp_f32_e32 v195, v195\n"
"v_bfe_u32 v244, v255, 5, 1\n"
"v_mul_u32_u24_e32 v244, 0xaaaa, v244\n"
"v_or_b32_e32 v244, 0x4444, v244\n"
"v_mfma_f32_16x16x32_bf16 v[204:207], a[228:231], a[180:183], v[204:207]\n"
"v_perm_b32 v160, v193, v192, s93\n"
"v_perm_b32 v161, v195, v194, s93\n"
"v_mfma_f32_16x16x32_bf16 v[204:207], a[224:227], a[176:179], v[204:207]\n"
"s_waitcnt lgkmcnt(6)\n"
"v_exp_f32_e32 v196, v196\n"
"v_mfma_f32_16x16x32_bf16 v[0:3], a[192:195], v[128:131], v[250:253]\n"
"ds_read_b64_tr_b16 a[212:213], v230 offset:36864\n"
"v_accvgpr_write_b32 a116, 0\n"
"v_mfma_f32_16x16x32_bf16 v[0:3], a[196:199], v[132:135], v[0:3]\n"
"v_exp_f32_e32 v197, v197\n"
"v_mfma_f32_16x16x32_bf16 v[4:7], a[196:199], v[140:143], v[250:253]\n"
"ds_read_b64_tr_b16 a[214:215], v230 offset:53248\n"
"v_accvgpr_write_b32 a12, v64\n"
"v_mfma_f32_16x16x32_bf16 v[4:7], a[192:195], v[136:139], v[4:7]\n"
"v_exp_f32_e32 v198, v198\n"
"v_mfma_f32_16x16x32_bf16 v[8:11], a[192:195], v[144:147], v[250:253]\n"
"ds_read_b64_tr_b16 a[216:217], v231 offset:4096\n"
"v_accvgpr_write_b32 a118, 0\n"
"v_mfma_f32_16x16x32_bf16 v[8:11], a[196:199], v[148:151], v[8:11]\n"
"v_exp_f32_e32 v199, v199\n"
"v_mfma_f32_16x16x32_bf16 v[12:15], a[196:199], v[156:159], v[250:253]\n"
"buffer_store_dwordx4 v[56:59], v255, s[68:71], s94 offen offset:1024 sc1\n"
"v_mfma_f32_16x16x32_bf16 v[12:15], a[192:195], v[152:155], v[12:15]\n"
"v_exp_f32_e32 v200, v200\n"
"v_mfma_f32_16x16x32_bf16 v[28:31], a[200:203], v[152:155], v[250:253]\n"
"ds_read_b64_tr_b16 a[218:219], v231 offset:20480\n"
"v_perm_b32 v168, v197, v196, s93\n"
"v_mfma_f32_16x16x32_bf16 v[28:31], a[204:207], v[156:159], v[28:31]\n"
"v_exp_f32_e32 v201, v201\n"
"v_mfma_f32_16x16x32_bf16 v[24:27], a[204:207], v[148:151], v[250:253]\n"
"ds_read_b64_tr_b16 a[220:221], v231 offset:36864\n"
"v_accvgpr_write_b32 a13, v65\n"
"v_mfma_f32_16x16x32_bf16 v[24:27], a[200:203], v[144:147], v[24:27]\n"
"v_exp_f32_e32 v202, v202\n"
"v_mfma_f32_16x16x32_bf16 v[20:23], a[200:203], v[136:139], v[250:253]\n"
"ds_read_b64_tr_b16 a[222:223], v231 offset:53248\n"
"v_lshlrev_b32_e32 v248, 1, v255\n"
"v_mfma_f32_16x16x32_bf16 v[20:23], a[204:207], v[140:143], v[20:23]\n"
"v_exp_f32_e32 v203, v203\n"
"v_mfma_f32_16x16x32_bf16 v[16:19], a[204:207], v[132:135], v[250:253]\n"
"v_accvgpr_write_b32 a14, v66\n"
"v_perm_b32 v169, v199, v198, s93\n"
"v_mfma_f32_16x16x32_bf16 v[16:19], a[200:203], v[128:131], v[16:19]\n"
"v_exp_f32_e32 v204, v204\n"
"v_smfmac_f32_16x16x64_bf16 a[112:115], a[92:95], v[128:135], v244\n"
"s_waitcnt lgkmcnt(8)\n"
"v_accvgpr_write_b32 a15, v67\n"
"v_mfma_f32_16x16x32_bf16 v[192:195], a[240:243], a[128:131], v[208:211]\n"
"ds_read_b128 a[224:227], v224 offset:36864\n"
"v_perm_b32 v176, v201, v200, s93\n"
"v_mfma_f32_16x16x32_bf16 v[192:195], a[244:247], a[132:135], v[192:195]\n"
"v_exp_f32_e32 v205, v205\n"
"v_mfma_f32_16x16x32_bf16 v[192:195], a[248:251], a[136:139], v[192:195]\n"
"ds_read_b128 a[228:231], v225 offset:36864\n"
"v_accvgpr_write_b32 a16, v72\n"
"v_mfma_f32_16x16x32_bf16 v[192:195], a[252:255], a[140:143], v[192:195]\n"
"s_add_u32 s76, s76, 65536\n"
"v_exp_f32_e32 v206, v206\n"
"v_mfma_f32_16x16x32_bf16 v[196:199], a[252:255], a[156:159], v[212:215]\n"
"ds_read_b128 a[232:235], v226 offset:36864\n"
"v_perm_b32 v177, v203, v202, s93\n"
"v_mfma_f32_16x16x32_bf16 v[196:199], a[248:251], a[152:155], v[196:199]\n"
"v_exp_f32_e32 v207, v207\n"
"v_mfma_f32_16x16x32_bf16 v[196:199], a[244:247], a[148:151], v[196:199]\n"
"buffer_load_dword a8, v248, s[72:75], s76 offen\n"
"v_mfma_f32_16x16x32_bf16 v[196:199], a[240:243], a[144:147], v[196:199]\n"
"v_exp_f32_e32 v192, v192\n"
"v_mfma_f32_16x16x32_bf16 v[200:203], a[240:243], a[160:163], v[216:219]\n"
"ds_read_b128 a[236:239], v227 offset:36864\n"
"v_accvgpr_write_b32 a17, v73\n"
"v_accvgpr_write_b32 a18, v74\n"
"v_mfma_f32_16x16x32_bf16 v[200:203], a[244:247], a[164:167], v[200:203]\n"
"v_exp_f32_e32 v193, v193\n"
"v_mfma_f32_16x16x32_bf16 v[200:203], a[248:251], a[168:171], v[200:203]\n"
"ds_read_b64_tr_b16 a[192:193], v232 offset:4096\n"
"v_perm_b32 v184, v205, v204, s93\n"
"v_accvgpr_write_b32 a19, v75\n"
"v_accvgpr_write_b32 a20, v80\n"
"v_perm_b32 v185, v207, v206, s93\n"
"v_mfma_f32_16x16x32_bf16 v[200:203], a[252:255], a[172:175], v[200:203]\n"
"v_exp_f32_e32 v194, v194\n"
"v_mfma_f32_16x16x32_bf16 v[204:207], a[252:255], a[188:191], v[220:223]\n"
"ds_read_b64_tr_b16 a[194:195], v232 offset:20480\n"
"v_accvgpr_write_b32 a21, v81\n"
"v_mfma_f32_16x16x32_bf16 v[204:207], a[248:251], a[184:187], v[204:207]\n"
"v_exp_f32_e32 v195, v195\n"
"v_mfma_f32_16x16x32_bf16 v[204:207], a[244:247], a[180:183], v[204:207]\n"
"v_perm_b32 v162, v193, v192, s93\n"
"v_accvgpr_write_b32 a22, v82\n"
"v_mfma_f32_16x16x32_bf16 v[204:207], a[240:243], a[176:179], v[204:207]\n"
"s_waitcnt lgkmcnt(6)\n"
"v_exp_f32_e32 v196, v196\n"
"v_mfma_f32_16x16x32_bf16 v[32:35], a[208:211], v[128:131], v[250:253]\n"
"ds_read_b64_tr_b16 a[196:197], v232 offset:36864\n"
"v_accvgpr_write_b32 a23, v83\n"
"v_mfma_f32_16x16x32_bf16 v[32:35], a[212:215], v[132:135], v[32:35]\n"
"v_exp_f32_e32 v197, v197\n"
"v_mfma_f32_16x16x32_bf16 v[36:39], a[212:215], v[140:143], v[250:253]\n"
"ds_read_b64_tr_b16 a[198:199], v232 offset:53248\n"
"v_perm_b32 v163, v195, v194, s93\n"
"v_mfma_f32_16x16x32_bf16 v[36:39], a[208:211], v[136:139], v[36:39]\n"
"v_exp_f32_e32 v198, v198\n"
"v_mfma_f32_16x16x32_bf16 v[40:43], a[208:211], v[144:147], v[250:253]\n"
"ds_read_b64_tr_b16 a[200:201], v233 offset:4096\n"
"v_accvgpr_write_b32 a120, 0\n"
"v_mfma_f32_16x16x32_bf16 v[40:43], a[212:215], v[148:151], v[40:43]\n"
"v_exp_f32_e32 v199, v199\n"
"v_mfma_f32_16x16x32_bf16 v[44:47], a[212:215], v[156:159], v[250:253]\n"
"v_accvgpr_write_b32 a24, v88\n"
"v_accvgpr_write_b32 a122, 0\n"
"v_mfma_f32_16x16x32_bf16 v[44:47], a[208:211], v[152:155], v[44:47]\n"
"v_exp_f32_e32 v200, v200\n"
"v_mfma_f32_16x16x32_bf16 v[60:63], a[216:219], v[152:155], v[250:253]\n"
"ds_read_b64_tr_b16 a[202:203], v233 offset:20480\n"
"v_perm_b32 v170, v197, v196, s93\n"
"v_mfma_f32_16x16x32_bf16 v[60:63], a[220:223], v[156:159], v[60:63]\n"
"v_exp_f32_e32 v201, v201\n"
"v_mfma_f32_16x16x32_bf16 v[56:59], a[220:223], v[148:151], v[250:253]\n"
"ds_read_b64_tr_b16 a[204:205], v233 offset:36864\n"
"v_accvgpr_write_b32 a25, v89\n"
"v_mfma_f32_16x16x32_bf16 v[56:59], a[216:219], v[144:147], v[56:59]\n"
"v_exp_f32_e32 v202, v202\n"
"v_mfma_f32_16x16x32_bf16 v[52:55], a[216:219], v[136:139], v[250:253]\n"
"ds_read_b64_tr_b16 a[206:207], v233 offset:53248\n"
"v_perm_b32 v171, v199, v198, s93\n"
"v_mfma_f32_16x16x32_bf16 v[52:55], a[220:223], v[140:143], v[52:55]\n"
"v_exp_f32_e32 v203, v203\n"
"v_mfma_f32_16x16x32_bf16 v[48:51], a[220:223], v[132:135], v[250:253]\n"
"v_accvgpr_write_b32 a26, v90\n"
"v_accvgpr_write_b32 a27, v91\n"
"v_mfma_f32_16x16x32_bf16 v[48:51], a[216:219], v[128:131], v[48:51]\n"
"v_exp_f32_e32 v204, v204\n"
"v_smfmac_f32_16x16x64_bf16 a[116:119], a[92:95], v[136:143], v244\n"
"s_waitcnt lgkmcnt(8)\n"
"v_perm_b32 v178, v201, v200, s93\n"
"v_mfma_f32_16x16x32_bf16 v[192:195], a[224:227], a[128:131], v[208:211]\n"
"ds_read_b128 a[240:243], v224 offset:53248\n"
"v_accvgpr_write_b32 a28, v96\n"
"v_mfma_f32_16x16x32_bf16 v[192:195], a[228:231], a[132:135], v[192:195]\n"
"v_exp_f32_e32 v205, v205\n"
"v_mfma_f32_16x16x32_bf16 v[192:195], a[232:235], a[136:139], v[192:195]\n"
"ds_read_b128 a[244:247], v225 offset:53248\n"
"v_perm_b32 v179, v203, v202, s93\n"
"v_mfma_f32_16x16x32_bf16 v[192:195], a[236:239], a[140:143], v[192:195]\n"
"v_exp_f32_e32 v206, v206\n"
"v_mfma_f32_16x16x32_bf16 v[196:199], a[236:239], a[156:159], v[212:215]\n"
"ds_read_b128 a[248:251], v226 offset:53248\n"
"v_accvgpr_write_b32 a29, v97\n"
"v_mfma_f32_16x16x32_bf16 v[196:199], a[232:235], a[152:155], v[196:199]\n"
"v_exp_f32_e32 v207, v207\n"
"v_mfma_f32_16x16x32_bf16 v[196:199], a[228:231], a[148:151], v[196:199]\n"
"v_accvgpr_write_b32 a30, v98\n"
"v_perm_b32 v186, v205, v204, s93\n"
"v_mfma_f32_16x16x32_bf16 v[196:199], a[224:227], a[144:147], v[196:199]\n"
"v_exp_f32_e32 v192, v192\n"
"v_mfma_f32_16x16x32_bf16 v[200:203], a[224:227], a[160:163], v[216:219]\n"
"ds_read_b128 a[252:255], v227 offset:53248\n"
"v_accvgpr_write_b32 a31, v99\n"
"v_mfma_f32_16x16x32_bf16 v[200:203], a[228:231], a[164:167], v[200:203]\n"
"v_exp_f32_e32 v193, v193\n"
"v_mfma_f32_16x16x32_bf16 v[200:203], a[232:235], a[168:171], v[200:203]\n"
"ds_read_b64_tr_b16 a[208:209], v234 offset:4096\n"
"v_perm_b32 v187, v207, v206, s93\n"
"v_mfma_f32_16x16x32_bf16 v[200:203], a[236:239], a[172:175], v[200:203]\n"
"v_exp_f32_e32 v194, v194\n"
"v_mfma_f32_16x16x32_bf16 v[204:207], a[236:239], a[188:191], v[220:223]\n"
"ds_read_b64_tr_b16 a[210:211], v234 offset:20480\n"
"v_accvgpr_write_b32 a32, v104\n"
"v_mfma_f32_16x16x32_bf16 v[204:207], a[232:235], a[184:187], v[204:207]\n"
"v_exp_f32_e32 v195, v195\n"
"v_mfma_f32_16x16x32_bf16 v[204:207], a[228:231], a[180:183], v[204:207]\n"
"v_accvgpr_write_b32 a33, v105\n"
"v_perm_b32 v164, v193, v192, s93\n"
"v_mfma_f32_16x16x32_bf16 v[204:207], a[224:227], a[176:179], v[204:207]\n"
"s_waitcnt lgkmcnt(6)\n"
"v_exp_f32_e32 v196, v196\n"
"v_mfma_f32_16x16x32_bf16 v[64:67], a[192:195], v[128:131], v[250:253]\n"
"ds_read_b64_tr_b16 a[212:213], v234 offset:36864\n"
"v_accvgpr_write_b32 a34, v106\n"
"v_mfma_f32_16x16x32_bf16 v[64:67], a[196:199], v[132:135], v[64:67]\n"
"v_exp_f32_e32 v197, v197\n"
"v_mfma_f32_16x16x32_bf16 v[68:71], a[196:199], v[140:143], v[250:253]\n"
"ds_read_b64_tr_b16 a[214:215], v234 offset:53248\n"
"v_perm_b32 v165, v195, v194, s93\n"
"v_mfma_f32_16x16x32_bf16 v[68:71], a[192:195], v[136:139], v[68:71]\n"
"v_exp_f32_e32 v198, v198\n"
"v_mfma_f32_16x16x32_bf16 v[72:75], a[192:195], v[144:147], v[250:253]\n"
"ds_read_b64_tr_b16 a[216:217], v235 offset:4096\n"
"v_accvgpr_write_b32 a35, v107\n"
"v_mfma_f32_16x16x32_bf16 v[72:75], a[196:199], v[148:151], v[72:75]\n"
"v_exp_f32_e32 v199, v199\n"
"v_mfma_f32_16x16x32_bf16 v[76:79], a[196:199], v[156:159], v[250:253]\n"
"v_accvgpr_write_b32 a124, 0\n"
"v_accvgpr_write_b32 a126, 0\n"
"v_mfma_f32_16x16x32_bf16 v[76:79], a[192:195], v[152:155], v[76:79]\n"
"v_exp_f32_e32 v200, v200\n"
"v_mfma_f32_16x16x32_bf16 v[92:95], a[200:203], v[152:155], v[250:253]\n"
"ds_read_b64_tr_b16 a[218:219], v235 offset:20480\n"
"v_accvgpr_write_b32 a36, v112\n"
"v_mfma_f32_16x16x32_bf16 v[92:95], a[204:207], v[156:159], v[92:95]\n"
"v_exp_f32_e32 v201, v201\n"
"v_mfma_f32_16x16x32_bf16 v[88:91], a[204:207], v[148:151], v[250:253]\n"
"ds_read_b64_tr_b16 a[220:221], v235 offset:36864\n"
"v_perm_b32 v172, v197, v196, s93\n"
"v_and_b32_e32 v246, 0x3e0, v255\n"
"v_mfma_f32_16x16x32_bf16 v[88:91], a[200:203], v[144:147], v[88:91]\n"
"v_exp_f32_e32 v202, v202\n"
"v_mfma_f32_16x16x32_bf16 v[84:87], a[200:203], v[136:139], v[250:253]\n"
"ds_read_b64_tr_b16 a[222:223], v235 offset:53248\n"
"v_accvgpr_write_b32 a37, v113\n"
"v_mfma_f32_16x16x32_bf16 v[84:87], a[204:207], v[140:143], v[84:87]\n"
"v_exp_f32_e32 v203, v203\n"
"v_mfma_f32_16x16x32_bf16 v[80:83], a[204:207], v[132:135], v[250:253]\n"
"v_lshlrev_b32_e32 v246, 20, v246\n"
"v_perm_b32 v173, v199, v198, s93\n"
"v_mfma_f32_16x16x32_bf16 v[80:83], a[200:203], v[128:131], v[80:83]\n"
"s_waitcnt vmcnt(9) lgkmcnt(0)\n"
"v_exp_f32_e32 v204, v204\n"
"v_smfmac_f32_16x16x64_bf16 a[120:123], a[92:95], v[144:151], v244\n"
"s_waitcnt lgkmcnt(8)\n"
"v_accvgpr_write_b32 a38, v114\n"
"v_lshl_or_b32 v246, v255, 3, v246\n"
"v_mfma_f32_16x16x32_bf16 v[192:195], a[240:243], a[128:131], v[208:211]\n"
"s_barrier\n"
"ds_read_b128 a[224:227], v224 offset:8192\n"
"v_accvgpr_write_b32 a39, v115\n"
"v_mfma_f32_16x16x32_bf16 v[192:195], a[244:247], a[132:135], v[192:195]\n"
"v_exp_f32_e32 v205, v205\n"
"v_mfma_f32_16x16x32_bf16 v[192:195], a[248:251], a[136:139], v[192:195]\n"
"ds_read_b128 a[228:231], v225 offset:8192\n"
"v_perm_b32 v180, v201, v200, s93\n"
"v_mfma_f32_16x16x32_bf16 v[192:195], a[252:255], a[140:143], v[192:195]\n"
"v_exp_f32_e32 v206, v206\n"
"v_mfma_f32_16x16x32_bf16 v[196:199], a[252:255], a[156:159], v[212:215]\n"
"ds_read_b128 a[232:235], v226 offset:8192\n"
"v_accvgpr_write_b32 a40, v120\n"
"v_mfma_f32_16x16x32_bf16 v[196:199], a[248:251], a[152:155], v[196:199]\n"
"v_exp_f32_e32 v207, v207\n"
"v_mfma_f32_16x16x32_bf16 v[196:199], a[244:247], a[148:151], v[196:199]\n"
"buffer_load_dword v242, v246, s[56:59], s28 offen sc1\n"
"v_perm_b32 v181, v203, v202, s93\n"
"v_mfma_f32_16x16x32_bf16 v[196:199], a[240:243], a[144:147], v[196:199]\n"
"v_exp_f32_e32 v192, v192\n"
"v_mfma_f32_16x16x32_bf16 v[200:203], a[240:243], a[160:163], v[216:219]\n"
"ds_read_b128 a[236:239], v227 offset:8192\n"
"v_accvgpr_write_b32 a41, v121\n"
"v_mfma_f32_16x16x32_bf16 v[200:203], a[244:247], a[164:167], v[200:203]\n"
"v_exp_f32_e32 v193, v193\n"
"v_mfma_f32_16x16x32_bf16 v[200:203], a[248:251], a[168:171], v[200:203]\n"
"ds_read_b64_tr_b16 a[192:193], v228 offset:8192\n"
"v_accvgpr_write_b32 a42, v122\n"
"v_perm_b32 v188, v205, v204, s93\n"
"v_accvgpr_write_b32 a43, v123\n"
"v_perm_b32 v189, v207, v206, s93\n"
"v_mfma_f32_16x16x32_bf16 v[200:203], a[252:255], a[172:175], v[200:203]\n"
"v_exp_f32_e32 v194, v194\n"
"v_mfma_f32_16x16x32_bf16 v[204:207], a[252:255], a[188:191], v[220:223]\n"
"ds_read_b64_tr_b16 a[194:195], v228 offset:24576\n"
"v_perm_b32 v166, v193, v192, s93\n"
"v_mfma_f32_16x16x32_bf16 v[204:207], a[248:251], a[184:187], v[204:207]\n"
"v_exp_f32_e32 v195, v195\n"
"v_mfma_f32_16x16x32_bf16 v[204:207], a[244:247], a[180:183], v[204:207]\n"
".LPG7_%=:\n"
"s_cbranch_execz .LPGR_%=\n"
"v_perm_b32 v167, v195, v194, s93\n"
"v_mfma_f32_16x16x32_bf16 v[204:207], a[240:243], a[176:179], v[204:207]\n"
"s_waitcnt lgkmcnt(6)\n"
"v_exp_f32_e32 v196, v196\n"
"v_mfma_f32_16x16x32_bf16 v[96:99], a[208:211], v[128:131], v[250:253]\n"
"ds_read_b64_tr_b16 a[196:197], v228 offset:40960\n"
"v_mfma_f32_16x16x32_bf16 v[96:99], a[212:215], v[132:135], v[96:99]\n"
"v_exp_f32_e32 v197, v197\n"
"v_mfma_f32_16x16x32_bf16 v[100:103], a[212:215], v[140:143], v[250:253]\n"
"ds_read_b64_tr_b16 a[198:199], v228 offset:57344\n"
"v_perm_b32 v174, v197, v196, s93\n"
"v_mfma_f32_16x16x32_bf16 v[100:103], a[208:211], v[136:139], v[100:103]\n"
"s_add_u32 s94, s90, 0\n"
"v_exp_f32_e32 v198, v198\n"
"v_mfma_f32_16x16x32_bf16 v[104:107], a[208:211], v[144:147], v[250:253]\n"
"ds_read_b64_tr_b16 a[200:201], v229 offset:8192\n"
"v_mfma_f32_16x16x32_bf16 v[104:107], a[212:215], v[148:151], v[104:107]\n"
"v_exp_f32_e32 v199, v199\n"
"v_mfma_f32_16x16x32_bf16 v[108:111], a[212:215], v[156:159], v[250:253]\n"
"buffer_store_dwordx4 a[12:15], v255, s[68:71], s94 offen offset:0 sc1\n"
"v_mfma_f32_16x16x32_bf16 v[108:111], a[208:211], v[152:155], v[108:111]\n"
"v_exp_f32_e32 v200, v200\n"
"v_mfma_f32_16x16x32_bf16 v[124:127], a[216:219], v[152:155], v[250:253]\n"
"ds_read_b64_tr_b16 a[202:203], v229 offset:24576\n"
"v_perm_b32 v175, v199, v198, s93\n"
"v_mfma_f32_16x16x32_bf16 v[124:127], a[220:223], v[156:159], v[124:127]\n"
"v_exp_f32_e32 v201, v201\n"
"v_mfma_f32_16x16x32_bf16 v[120:123], a[220:223], v[148:151], v[250:253]\n"
"ds_read_b64_tr_b16 a[204:205], v229 offset:40960\n"
"v_perm_b32 v182, v201, v200, s93\n"
"v_mfma_f32_16x16x32_bf16 v[120:123], a[216:219], v[144:147], v[120:123]\n"
"v_exp_f32_e32 v202, v202\n"
"v_mfma_f32_16x16x32_bf16 v[116:119], a[216:219], v[136:139], v[250:253]\n"
"ds_read_b64_tr_b16 a[206:207], v229 offset:57344\n"
"v_mfma_f32_16x16x32_bf16 v[116:119], a[220:223], v[140:143], v[116:119]\n"
"v_exp_f32_e32 v203, v203\n"
"v_mfma_f32_16x16x32_bf16 v[112:115], a[220:223], v[132:135], v[250:253]\n"
"v_perm_b32 v183, v203, v202, s93\n"
"v_mfma_f32_16x16x32_bf16 v[112:115], a[216:219], v[128:131], v[112:115]\n"
"v_exp_f32_e32 v204, v204\n"
"v_smfmac_f32_16x16x64_bf16 a[124:127], a[92:95], v[152:159], v244\n"
"s_waitcnt lgkmcnt(8)\n"
"v_mfma_f32_16x16x32_bf16 v[192:195], a[224:227], a[128:131], v[208:211]\n"
"ds_read_b128 a[240:243], v224 offset:24576\n"
"v_mov_b32_e32 v248, 2139193217\n"
"v_mfma_f32_16x16x32_bf16 v[192:195], a[228:231], a[132:135], v[192:195]\n"
"v_exp_f32_e32 v205, v205\n"
"v_mfma_f32_16x16x32_bf16 v[192:195], a[232:235], a[136:139], v[192:195]\n"
"ds_read_b128 a[244:247], v225 offset:24576\n"
"v_perm_b32 v190, v205, v204, s93\n"
"v_mfma_f32_16x16x32_bf16 v[192:195], a[236:239], a[140:143], v[192:195]\n"
"s_add_u32 m0, s46, 0\n"
"v_exp_f32_e32 v206, v206\n"
"v_mfma_f32_16x16x32_bf16 v[196:199], a[236:239], a[156:159], v[212:215]\n"
"s_add_u32 s44, s44, 16384\n"
"ds_read_b128 a[248:251], v226 offset:24576\n"
"v_accvgpr_write_b32 a4, v248\n"
"v_mfma_f32_16x16x32_bf16 v[196:199], a[232:235], a[152:155], v[196:199]\n"
"v_exp_f32_e32 v207, v207\n"
"v_mfma_f32_16x16x32_bf16 v[196:199], a[228:231], a[148:151], v[196:199]\n"
"buffer_load_dwordx4 v236, s[36:39], s44 offen offset:0 lds\n"
"v_mfma_f32_16x16x32_bf16 v[196:199], a[224:227], a[144:147], v[196:199]\n"
"v_exp_f32_e32 v192, v192\n"
"v_mfma_f32_16x16x32_bf16 v[200:203], a[224:227], a[160:163], v[216:219]\n"
"ds_read_b128 a[252:255], v227 offset:24576\n"
"v_accvgpr_write_b32 a5, v248\n"
"v_mfma_f32_16x16x32_bf16 v[200:203], a[228:231], a[164:167], v[200:203]\n"
"v_exp_f32_e32 v193, v193\n"
"v_mfma_f32_16x16x32_bf16 v[200:203], a[232:235], a[168:171], v[200:203]\n"
"ds_read_b64_tr_b16 a[208:209], v230 offset:8192\n"
"v_perm_b32 v191, v207, v206, s93\n"
"v_mfma_f32_16x16x32_bf16 v[200:203], a[236:239], a[172:175], v[200:203]\n"
"v_exp_f32_e32 v194, v194\n"
"v_mfma_f32_16x16x32_bf16 v[204:207], a[236:239], a[188:191], v[220:223]\n"
"ds_read_b64_tr_b16 a[210:211], v230 offset:24576\n"
"v_accvgpr_write_b32 a6, v248\n"
"v_mfma_f32_16x16x32_bf16 v[204:207], a[232:235], a[184:187], v[204:207]\n"
"v_exp_f32_e32 v195, v195\n"
"v_mfma_f32_16x16x32_bf16 v[204:207], a[228:231], a[180:183], v[204:207]\n"
"buffer_load_dwordx4 v237, s[36:39], s44 offen offset:1024 lds\n"
"v_mfma_f32_16x16x32_bf16 v[204:207], a[224:227], a[176:179], v[204:207]\n"
"s_waitcnt lgkmcnt(6)\n"
"v_exp_f32_e32 v196, v196\n"
"v_mfma_f32_16x16x32_bf16 v[0:3], a[192:195], v[160:163], v[0:3]\n"
"ds_read_b64_tr_b16 a[212:213], v230 offset:40960\n"
"v_accvgpr_write_b32 a7, v248\n"
"v_mfma_f32_16x16x32_bf16 v[0:3], a[196:199], v[164:167], v[0:3]\n"
"v_exp_f32_e32 v197, v197\n"
"v_mfma_f32_16x16x32_bf16 v[4:7], a[196:199], v[172:175], v[4:7]\n"
"ds_read_b64_tr_b16 a[214:215], v230 offset:57344\n"
"v_perm_b32 v128, v193, v192, s93\n"
"v_lshlrev_b32_e32 v248, 1, v255\n"
"v_mfma_f32_16x16x32_bf16 v[4:7], a[192:195], v[168:171], v[4:7]\n"
"v_exp_f32_e32 v198, v198\n"
"v_mfma_f32_16x16x32_bf16 v[8:11], a[192:195], v[176:179], v[8:11]\n"
"ds_read_b64_tr_b16 a[216:217], v231 offset:8192\n"
"v_and_b32_e32 v247, 0x3e0, v255\n"
"v_mfma_f32_16x16x32_bf16 v[8:11], a[196:199], v[180:183], v[8:11]\n"
"v_exp_f32_e32 v199, v199\n"
"v_mfma_f32_16x16x32_bf16 v[12:15], a[196:199], v[188:191], v[12:15]\n"
"buffer_load_dwordx4 v238, s[36:39], s44 offen offset:2048 lds\n"
"v_mfma_f32_16x16x32_bf16 v[12:15], a[192:195], v[184:187], v[12:15]\n"
"v_exp_f32_e32 v200, v200\n"
"v_mfma_f32_16x16x32_bf16 v[28:31], a[200:203], v[184:187], v[28:31]\n"
"ds_read_b64_tr_b16 a[218:219], v231 offset:24576\n"
"v_perm_b32 v129, v195, v194, s93\n"
"v_lshlrev_b32_e32 v247, 20, v247\n"
"v_lshl_or_b32 v247, v255, 6, v247\n"
"v_mfma_f32_16x16x32_bf16 v[28:31], a[204:207], v[188:191], v[28:31]\n"
"v_exp_f32_e32 v201, v201\n"
"v_mfma_f32_16x16x32_bf16 v[24:27], a[204:207], v[180:183], v[24:27]\n"
"ds_read_b64_tr_b16 a[220:221], v231 offset:40960\n"
"v_perm_b32 v136, v197, v196, s93\n"
"v_mfma_f32_16x16x32_bf16 v[24:27], a[200:203], v[176:179], v[24:27]\n"
"v_exp_f32_e32 v202, v202\n"
"v_mfma_f32_16x16x32_bf16 v[20:23], a[200:203], v[168:171], v[20:23]\n"
"ds_read_b64_tr_b16 a[222:223], v231 offset:57344\n"
"v_perm_b32 v137, v199, v198, s93\n"
"v_mfma_f32_16x16x32_bf16 v[20:23], a[204:207], v[172:175], v[20:23]\n"
"v_exp_f32_e32 v203, v203\n"
"v_mfma_f32_16x16x32_bf16 v[16:19], a[204:207], v[164:167], v[16:19]\n"
"v_perm_b32 v144, v201, v200, s93\n"
"v_perm_b32 v145, v203, v202, s93\n"
"v_mfma_f32_16x16x32_bf16 v[16:19], a[200:203], v[160:163], v[16:19]\n"
"v_exp_f32_e32 v204, v204\n"
"v_smfmac_f32_16x16x64_bf16 a[112:115], a[92:95], v[160:167], v244\n"
"s_waitcnt lgkmcnt(8)\n"
"v_mfma_f32_16x16x32_bf16 v[192:195], a[240:243], a[128:131], v[208:211]\n"
"ds_read_b128 a[224:227], v224 offset:40960\n"
"v_mfma_f32_16x16x32_bf16 v[192:195], a[244:247], a[132:135], v[192:195]\n"
"v_exp_f32_e32 v205, v205\n"
"v_mfma_f32_16x16x32_bf16 v[192:195], a[248:251], a[136:139], v[192:195]\n"
"ds_read_b128 a[228:231], v225 offset:40960\n"
"v_perm_b32 v152, v205, v204, s93\n"
"v_mfma_f32_16x16x32_bf16 v[192:195], a[252:255], a[140:143], v[192:195]\n"
"v_exp_f32_e32 v206, v206\n"
"v_mfma_f32_16x16x32_bf16 v[196:199], a[252:255], a[156:159], v[212:215]\n"
"ds_read_b128 a[232:235], v226 offset:40960\n"
"v_mfma_f32_16x16x32_bf16 v[196:199], a[248:251], a[152:155], v[196:199]\n"
"v_exp_f32_e32 v207, v207\n"
"v_mfma_f32_16x16x32_bf16 v[196:199], a[244:247], a[148:151], v[196:199]\n"
"buffer_store_dwordx4 a[16:19], v255, s[68:71], s94 offen offset:1024 sc1\n"
"v_mfma_f32_16x16x32_bf16 v[196:199], a[240:243], a[144:147], v[196:199]\n"
"v_exp_f32_e32 v192, v192\n"
"v_mfma_f32_16x16x32_bf16 v[200:203], a[240:243], a[160:163], v[216:219]\n"
"ds_read_b128 a[236:239], v227 offset:40960\n"
"v_perm_b32 v153, v207, v206, s93\n"
"v_mfma_f32_16x16x32_bf16 v[200:203], a[244:247], a[164:167], v[200:203]\n"
"v_exp_f32_e32 v193, v193\n"
"v_mfma_f32_16x16x32_bf16 v[200:203], a[248:251], a[168:171], v[200:203]\n"
"ds_read_b64_tr_b16 a[192:193], v232 offset:8192\n"
"v_perm_b32 v130, v193, v192, s93\n"
"v_mfma_f32_16x16x32_bf16 v[200:203], a[252:255], a[172:175], v[200:203]\n"
"v_exp_f32_e32 v194, v194\n"
"v_mfma_f32_16x16x32_bf16 v[204:207], a[252:255], a[188:191], v[220:223]\n"
"ds_read_b64_tr_b16 a[194:195], v232 offset:24576\n"
"v_mfma_f32_16x16x32_bf16 v[204:207], a[248:251], a[184:187], v[204:207]\n"
"v_exp_f32_e32 v195, v195\n"
"v_mfma_f32_16x16x32_bf16 v[204:207], a[244:247], a[180:183], v[204:207]\n"
"buffer_load_dwordx4 v239, s[36:39], s44 offen offset:3072 lds\n"
"v_mfma_f32_16x16x32_bf16 v[204:207], a[240:243], a[176:179], v[204:207]\n"
"s_waitcnt lgkmcnt(6)\n"
"v_exp_f32_e32 v196, v196\n"
"v_mfma_f32_16x16x32_bf16 v[32:35], a[208:211], v[160:163], v[32:35]\n"
"ds_read_b64_tr_b16 a[196:197], v232 offset:40960\n"
"v_perm_b32 v131, v195, v194, s93\n"
"v_mfma_f32_16x16x32_bf16 v[32:35], a[212:215], v[164:167], v[32:35]\n"
"v_exp_f32_e32 v197, v197\n"
"v_mfma_f32_16x16x32_bf16 v[36:39], a[212:215], v[172:175], v[36:39]\n"
"ds_read_b64_tr_b16 a[198:199], v232 offset:57344\n"
"v_perm_b32 v138, v197, v196, s93\n"
"v_mfma_f32_16x16x32_bf16 v[36:39], a[208:211], v[168:171], v[36:39]\n"
"s_add_u32 m0, s46, 65536\n"
"v_exp_f32_e32 v198, v198\n"
"v_mfma_f32_16x16x32_bf16 v[40:43], a[208:211], v[176:179], v[40:43]\n"
"s_add_u32 s45, s45, 16384\n"
"ds_read_b64_tr_b16 a[200:201], v233 offset:8192\n"
"v_mfma_f32_16x16x32_bf16 v[40:43], a[212:215], v[180:183], v[40:43]\n"
"v_exp_f32_e32 v199, v199\n"
"v_mfma_f32_16x16x32_bf16 v[44:47], a[212:215], v[188:191], v[44:47]\n"
"buffer_load_dwordx4 v240, s[40:43], s45 offen offset:0 lds\n"
"v_mfma_f32_16x16x32_bf16 v[44:47], a[208:211], v[184:187], v[44:47]\n"
"v_exp_f32_e32 v200, v200\n"
"v_mfma_f32_16x16x32_bf16 v[60:63], a[216:219], v[184:187], v[60:63]\n"
"ds_read_b64_tr_b16 a[202:203], v233 offset:24576\n"
"v_perm_b32 v139, v199, v198, s93\n"
"v_mfma_f32_16x16x32_bf16 v[60:63], a[220:223], v[188:191], v[60:63]\n"
"v_exp_f32_e32 v201, v201\n"
"v_mfma_f32_16x16x32_bf16 v[56:59], a[220:223], v[180:183], v[56:59]\n"
"ds_read_b64_tr_b16 a[204:205], v233 offset:40960\n"
"v_perm_b32 v146, v201, v200, s93\n"
"v_mfma_f32_16x16x32_bf16 v[56:59], a[216:219], v[176:179], v[56:59]\n"
"v_exp_f32_e32 v202, v202\n"
"v_mfma_f32_16x16x32_bf16 v[52:55], a[216:219], v[168:171], v[52:55]\n"
"ds_read_b64_tr_b16 a[206:207], v233 offset:57344\n"
"v_mfma_f32_16x16x32_bf16 v[52:55], a[220:223], v[172:175], v[52:55]\n"
"v_exp_f32_e32 v203, v203\n"
"v_mfma_f32_16x16x32_bf16 v[48:51], a[220:223], v[164:167], v[48:51]\n"
"buffer_load_dwordx4 v241, s[40:43], s45 offen offset:1024 lds\n"
"v_mfma_f32_16x16x32_bf16 v[48:51], a[216:219], v[160:163], v[48:51]\n"
"v_exp_f32_e32 v204, v204\n"
"v_smfmac_f32_16x16x64_bf16 a[116:119], a[92:95], v[168:175], v244\n"
"s_waitcnt lgkmcnt(8)\n"
"v_perm_b32 v147, v203, v202, s93\n"
"v_mfma_f32_16x16x32_bf16 v[192:195], a[224:227], a[128:131], v[208:211]\n"
"ds_read_b128 a[240:243], v224 offset:57344\n"
"v_mfma_f32_16x16x32_bf16 v[192:195], a[228:231], a[132:135], v[192:195]\n"
"v_exp_f32_e32 v205, v205\n"
"v_mfma_f32_16x16x32_bf16 v[192:195], a[232:235], a[136:139], v[192:195]\n"
"ds_read_b128 a[244:247], v225 offset:57344\n"
"v_perm_b32 v154, v205, v204, s93\n"
"v_mfma_f32_16x16x32_bf16 v[192:195], a[236:239], a[140:143], v[192:195]\n"
"v_exp_f32_e32 v206, v206\n"
"v_mfma_f32_16x16x32_bf16 v[196:199], a[236:239], a[156:159], v[212:215]\n"
"ds_read_b128 a[248:251], v226 offset:57344\n"
"v_mfma_f32_16x16x32_bf16 v[196:199], a[232:235], a[152:155], v[196:199]\n"
"v_exp_f32_e32 v207, v207\n"
"v_mfma_f32_16x16x32_bf16 v[196:199], a[228:231], a[148:151], v[196:199]\n"
"buffer_load_dwordx4 v240, s[40:43], s45 offen offset:2048 lds\n"
"v_mfma_f32_16x16x32_bf16 v[196:199], a[224:227], a[144:147], v[196:199]\n"
"v_exp_f32_e32 v192, v192\n"
"v_mfma_f32_16x16x32_bf16 v[200:203], a[224:227], a[160:163], v[216:219]\n"
"ds_read_b128 a[252:255], v227 offset:57344\n"
"v_perm_b32 v155, v207, v206, s93\n"
"v_mfma_f32_16x16x32_bf16 v[200:203], a[228:231], a[164:167], v[200:203]\n"
"v_exp_f32_e32 v193, v193\n"
"v_mfma_f32_16x16x32_bf16 v[200:203], a[232:235], a[168:171], v[200:203]\n"
"ds_read_b64_tr_b16 a[208:209], v234 offset:8192\n"
"v_perm_b32 v132, v193, v192, s93\n"
"v_mfma_f32_16x16x32_bf16 v[200:203], a[236:239], a[172:175], v[200:203]\n"
"v_exp_f32_e32 v194, v194\n"
"v_mfma_f32_16x16x32_bf16 v[204:207], a[236:239], a[188:191], v[220:223]\n"
"ds_read_b64_tr_b16 a[210:211], v234 offset:24576\n"
"v_mfma_f32_16x16x32_bf16 v[204:207], a[232:235], a[184:187], v[204:207]\n"
"v_exp_f32_e32 v195, v195\n"
"v_mfma_f32_16x16x32_bf16 v[204:207], a[228:231], a[180:183], v[204:207]\n"
"buffer_load_dwordx4 v241, s[40:43], s45 offen offset:3072 lds\n"
"v_mfma_f32_16x16x32_bf16 v[204:207], a[224:227], a[176:179], v[204:207]\n"
"s_waitcnt lgkmcnt(6)\n"
"v_exp_f32_e32 v196, v196\n"
"v_mfma_f32_16x16x32_bf16 v[64:67], a[192:195], v[160:163], v[64:67]\n"
"ds_read_b64_tr_b16 a[212:213], v234 offset:40960\n"
"v_perm_b32 v133, v195, v194, s93\n"
"v_mfma_f32_16x16x32_bf16 v[64:67], a[196:199], v[164:167], v[64:67]\n"
"v_exp_f32_e32 v197, v197\n"
"v_mfma_f32_16x16x32_bf16 v[68:71], a[196:199], v[172:175], v[68:71]\n"
"ds_read_b64_tr_b16 a[214:215], v234 offset:57344\n"
"v_perm_b32 v140, v197, v196, s93\n"
"v_mfma_f32_16x16x32_bf16 v[68:71], a[192:195], v[168:171], v[68:71]\n"
"s_add_u32 m0, s46, 4096\n"
"v_exp_f32_e32 v198, v198\n"
"v_mfma_f32_16x16x32_bf16 v[72:75], a[192:195], v[176:179], v[72:75]\n"
"s_add_u32 s44, s44, 16384\n"
"ds_read_b64_tr_b16 a[216:217], v235 offset:8192\n"
"v_mfma_f32_16x16x32_bf16 v[72:75], a[196:199], v[180:183], v[72:75]\n"
"v_exp_f32_e32 v199, v199\n"
"v_mfma_f32_16x16x32_bf16 v[76:79], a[196:199], v[188:191], v[76:79]\n"
"buffer_load_dwordx4 v236, s[36:39], s44 offen offset:0 lds\n"
"v_mfma_f32_16x16x32_bf16 v[76:79], a[192:195], v[184:187], v[76:79]\n"
"v_exp_f32_e32 v200, v200\n"
"v_mfma_f32_16x16x32_bf16 v[92:95], a[200:203], v[184:187], v[92:95]\n"
"ds_read_b64_tr_b16 a[218:219], v235 offset:24576\n"
"v_perm_b32 v141, v199, v198, s93\n"
"v_mfma_f32_16x16x32_bf16 v[92:95], a[204:207], v[188:191], v[92:95]\n"
"v_exp_f32_e32 v201, v201\n"
"v_mfma_f32_16x16x32_bf16 v[88:91], a[204:207], v[180:183], v[88:91]\n"
"ds_read_b64_tr_b16 a[220:221], v235 offset:40960\n"
"v_perm_b32 v148, v201, v200, s93\n"
"v_mfma_f32_16x16x32_bf16 v[88:91], a[200:203], v[176:179], v[88:91]\n"
"v_exp_f32_e32 v202, v202\n"
"v_mfma_f32_16x16x32_bf16 v[84:87], a[200:203], v[168:171], v[84:87]\n"
"ds_read_b64_tr_b16 a[222:223], v235 offset:57344\n"
"v_mfma_f32_16x16x32_bf16 v[84:87], a[204:207], v[172:175], v[84:87]\n"
"v_exp_f32_e32 v203, v203\n"
"v_mfma_f32_16x16x32_bf16 v[80:83], a[204:207], v[164:167], v[80:83]\n"
"v_perm_b32 v149, v203, v202, s93\n"
"v_mfma_f32_16x16x32_bf16 v[80:83], a[200:203], v[160:163], v[80:83]\n"
"v_exp_f32_e32 v204, v204\n"
"v_smfmac_f32_16x16x64_bf16 a[120:123], a[92:95], v[176:183], v244\n"
"s_waitcnt lgkmcnt(8)\n"
"v_mfma_f32_16x16x32_bf16 v[192:195], a[240:243], a[128:131], v[208:211]\n"
"ds_read_b128 a[224:227], v224 offset:12288\n"
"v_mfma_f32_16x16x32_bf16 v[192:195], a[244:247], a[132:135], v[192:195]\n"
"v_exp_f32_e32 v205, v205\n"
"v_mfma_f32_16x16x32_bf16 v[192:195], a[248:251], a[136:139], v[192:195]\n"
"ds_read_b128 a[228:231], v225 offset:12288\n"
"v_perm_b32 v156, v205, v204, s93\n"
"v_mfma_f32_16x16x32_bf16 v[192:195], a[252:255], a[140:143], v[192:195]\n"
"s_add_u32 s94, s90, 18432\n"
"v_exp_f32_e32 v206, v206\n"
"v_mfma_f32_16x16x32_bf16 v[196:199], a[252:255], a[156:159], v[212:215]\n"
"ds_read_b128 a[232:235], v226 offset:12288\n"
"v_mfma_f32_16x16x32_bf16 v[196:199], a[248:251], a[152:155], v[196:199]\n"
"v_exp_f32_e32 v207, v207\n"
"v_mfma_f32_16x16x32_bf16 v[196:199], a[244:247], a[148:151], v[196:199]\n"
"buffer_store_dwordx4 a[20:23], v255, s[68:71], s94 offen offset:0 sc1\n"
"v_mfma_f32_16x16x32_bf16 v[196:199], a[240:243], a[144:147], v[196:199]\n"
"v_exp_f32_e32 v192, v192\n"
"v_mfma_f32_16x16x32_bf16 v[200:203], a[240:243], a[160:163], v[216:219]\n"
"ds_read_b128 a[236:239], v227 offset:12288\n"
"v_perm_b32 v157, v207, v206, s93\n"
"v_mfma_f32_16x16x32_bf16 v[200:203], a[244:247], a[164:167], v[200:203]\n"
"v_exp_f32_e32 v193, v193\n"
"v_mfma_f32_16x16x32_bf16 v[200:203], a[248:251], a[168:171], v[200:203]\n"
"ds_read_b64_tr_b16 a[192:193], v228 offset:12288\n"
"v_perm_b32 v134, v193, v192, s93\n"
"v_mfma_f32_16x16x32_bf16 v[200:203], a[252:255], a[172:175], v[200:203]\n"
"v_exp_f32_e32 v194, v194\n"
"v_mfma_f32_16x16x32_bf16 v[204:207], a[252:255], a[188:191], v[220:223]\n"
"ds_read_b64_tr_b16 a[194:195], v228 offset:28672\n"
"v_mfma_f32_16x16x32_bf16 v[204:207], a[248:251], a[184:187], v[204:207]\n"
"v_exp_f32_e32 v195, v195\n"
"v_mfma_f32_16x16x32_bf16 v[204:207], a[244:247], a[180:183], v[204:207]\n"
"buffer_load_dwordx4 v237, s[36:39], s44 offen offset:1024 lds\n"
"v_mfma_f32_16x16x32_bf16 v[204:207], a[240:243], a[176:179], v[204:207]\n"
"s_waitcnt lgkmcnt(6)\n"
"v_exp_f32_e32 v196, v196\n"
"v_mfma_f32_16x16x32_bf16 v[96:99], a[208:211], v[160:163], v[96:99]\n"
"ds_read_b64_tr_b16 a[196:197], v228 offset:45056\n"
"v_perm_b32 v135, v195, v194, s93\n"
"v_mfma_f32_16x16x32_bf16 v[96:99], a[212:215], v[164:167], v[96:99]\n"
"v_exp_f32_e32 v197, v197\n"
"v_mfma_f32_16x16x32_bf16 v[100:103], a[212:215], v[172:175], v[100:103]\n"
"ds_read_b64_tr_b16 a[198:199], v228 offset:61440\n"
"v_perm_b32 v142, v197, v196, s93\n"
"v_mfma_f32_16x16x32_bf16 v[100:103], a[208:211], v[168:171], v[100:103]\n"
"v_exp_f32_e32 v198, v198\n"
"v_mfma_f32_16x16x32_bf16 v[104:107], a[208:211], v[176:179], v[104:107]\n"
"ds_read_b64_tr_b16 a[200:201], v229 offset:12288\n"
"v_mfma_f32_16x16x32_bf16 v[104:107], a[212:215], v[180:183], v[104:107]\n"
"v_exp_f32_e32 v199, v199\n"
"v_mfma_f32_16x16x32_bf16 v[108:111], a[212:215], v[188:191], v[108:111]\n"
"buffer_load_dwordx4 v238, s[36:39], s44 offen offset:2048 lds\n"
"v_mfma_f32_16x16x32_bf16 v[108:111], a[208:211], v[184:187], v[108:111]\n"
"v_exp_f32_e32 v200, v200\n"
"v_mfma_f32_16x16x32_bf16 v[124:127], a[216:219], v[184:187], v[124:127]\n"
"ds_read_b64_tr_b16 a[202:203], v229 offset:28672\n"
"v_perm_b32 v143, v199, v198, s93\n"
"v_mfma_f32_16x16x32_bf16 v[124:127], a[220:223], v[188:191], v[124:127]\n"
"v_exp_f32_e32 v201, v201\n"
"v_mfma_f32_16x16x32_bf16 v[120:123], a[220:223], v[180:183], v[120:123]\n"
"ds_read_b64_tr_b16 a[204:205], v229 offset:45056\n"
"v_perm_b32 v150, v201, v200, s93\n"
"v_mfma_f32_16x16x32_bf16 v[120:123], a[216:219], v[176:179], v[120:123]\n"
"v_exp_f32_e32 v202, v202\n"
"v_mfma_f32_16x16x32_bf16 v[116:119], a[216:219], v[168:171], v[116:119]\n"
"ds_read_b64_tr_b16 a[206:207], v229 offset:61440\n"
"v_mfma_f32_16x16x32_bf16 v[116:119], a[220:223], v[172:175], v[116:119]\n"
"v_exp_f32_e32 v203, v203\n"
"v_mfma_f32_16x16x32_bf16 v[112:115], a[220:223], v[164:167], v[112:115]\n"
"buffer_load_dwordx4 v239, s[36:39], s44 offen offset:3072 lds\n"
"v_mfma_f32_16x16x32_bf16 v[112:115], a[216:219], v[160:163], v[112:115]\n"
"v_exp_f32_e32 v204, v204\n"
"v_smfmac_f32_16x16x64_bf16 a[124:127], a[92:95], v[184:191], v244\n"
"s_waitcnt lgkmcnt(8)\n"
"v_perm_b32 v151, v203, v202, s93\n"
"v_mfma_f32_16x16x32_bf16 v[192:195], a[224:227], a[128:131], v[208:211]\n"
"ds_read_b128 a[240:243], v224 offset:28672\n"
"v_mfma_f32_16x16x32_bf16 v[192:195], a[228:231], a[132:135], v[192:195]\n"
"v_exp_f32_e32 v205, v205\n"
"v_mfma_f32_16x16x32_bf16 v[192:195], a[232:235], a[136:139], v[192:195]\n"
"ds_read_b128 a[244:247], v225 offset:28672\n"
"v_perm_b32 v158, v205, v204, s93\n"
"v_mfma_f32_16x16x32_bf16 v[192:195], a[236:239], a[140:143], v[192:195]\n"
"s_add_u32 m0, s46, 69632\n"
"v_exp_f32_e32 v206, v206\n"
"v_mfma_f32_16x16x32_bf16 v[196:199], a[236:239], a[156:159], v[212:215]\n"
"s_add_u32 s45, s45, 16384\n"
"ds_read_b128 a[248:251], v226 offset:28672\n"
"v_mfma_f32_16x16x32_bf16 v[196:199], a[232:235], a[152:155], v[196:199]\n"
"v_exp_f32_e32 v207, v207\n"
"v_mfma_f32_16x16x32_bf16 v[196:199], a[228:231], a[148:151], v[196:199]\n"
"buffer_load_dwordx4 v240, s[40:43], s45 offen offset:0 lds\n"
"v_mfma_f32_16x16x32_bf16 v[196:199], a[224:227], a[144:147], v[196:199]\n"
"v_exp_f32_e32 v192, v192\n"
"v_mfma_f32_16x16x32_bf16 v[200:203], a[224:227], a[160:163], v[216:219]\n"
"ds_read_b128 a[252:255], v227 offset:28672\n"
"v_perm_b32 v159, v207, v206, s93\n"
"v_mfma_f32_16x16x32_bf16 v[200:203], a[228:231], a[164:167], v[200:203]\n"
"v_exp_f32_e32 v193, v193\n"
"v_mfma_f32_16x16x32_bf16 v[200:203], a[232:235], a[168:171], v[200:203]\n"
"ds_read_b64_tr_b16 a[208:209], v230 offset:12288\n"
"v_perm_b32 v160, v193, v192, s93\n"
"v_mfma_f32_16x16x32_bf16 v[200:203], a[236:239], a[172:175], v[200:203]\n"
"v_exp_f32_e32 v194, v194\n"
"v_mfma_f32_16x16x32_bf16 v[204:207], a[236:239], a[188:191], v[220:223]\n"
"ds_read_b64_tr_b16 a[210:211], v230 offset:28672\n"
"v_mfma_f32_16x16x32_bf16 v[204:207], a[232:235], a[184:187], v[204:207]\n"
"v_exp_f32_e32 v195, v195\n"
"v_mfma_f32_16x16x32_bf16 v[204:207], a[228:231], a[180:183], v[204:207]\n"
"buffer_load_dwordx4 v241, s[40:43], s45 offen offset:1024 lds\n"
"v_mfma_f32_16x16x32_bf16 v[204:207], a[224:227], a[176:179], v[204:207]\n"
"s_waitcnt lgkmcnt(6)\n"
"v_exp_f32_e32 v196, v196\n"
"v_mfma_f32_16x16x32_bf16 v[0:3], a[192:195], v[128:131], v[0:3]\n"
"ds_read_b64_tr_b16 a[212:213], v230 offset:45056\n"
"v_perm_b32 v161, v195, v194, s93\n"
"v_mfma_f32_16x16x32_bf16 v[0:3], a[196:199], v[132:135], v[0:3]\n"
"v_exp_f32_e32 v197, v197\n"
"v_mfma_f32_16x16x32_bf16 v[4:7], a[196:199], v[140:143], v[4:7]\n"
"ds_read_b64_tr_b16 a[214:215], v230 offset:61440\n"
"v_perm_b32 v168, v197, v196, s93\n"
"v_mfma_f32_16x16x32_bf16 v[4:7], a[192:195], v[136:139], v[4:7]\n"
"v_exp_f32_e32 v198, v198\n"
"v_mfma_f32_16x16x32_bf16 v[8:11], a[192:195], v[144:147], v[8:11]\n"
"ds_read_b64_tr_b16 a[216:217], v231 offset:12288\n"
"v_mfma_f32_16x16x32_bf16 v[8:11], a[196:199], v[148:151], v[8:11]\n"
"v_exp_f32_e32 v199, v199\n"
"v_mfma_f32_16x16x32_bf16 v[12:15], a[196:199], v[156:159], v[12:15]\n"
"buffer_load_dwordx4 v240, s[40:43], s45 offen offset:2048 lds\n"
"v_mfma_f32_16x16x32_bf16 v[12:15], a[192:195], v[152:155], v[12:15]\n"
"v_exp_f32_e32 v200, v200\n"
"v_mfma_f32_16x16x32_bf16 v[28:31], a[200:203], v[152:155], v[28:31]\n"
"ds_read_b64_tr_b16 a[218:219], v231 offset:28672\n"
"v_perm_b32 v169, v199, v198, s93\n"
"v_mfma_f32_16x16x32_bf16 v[28:31], a[204:207], v[156:159], v[28:31]\n"
"v_exp_f32_e32 v201, v201\n"
"v_mfma_f32_16x16x32_bf16 v[24:27], a[204:207], v[148:151], v[24:27]\n"
"ds_read_b64_tr_b16 a[220:221], v231 offset:45056\n"
"v_perm_b32 v176, v201, v200, s93\n"
"v_mfma_f32_16x16x32_bf16 v[24:27], a[200:203], v[144:147], v[24:27]\n"
"v_exp_f32_e32 v202, v202\n"
"v_mfma_f32_16x16x32_bf16 v[20:23], a[200:203], v[136:139], v[20:23]\n"
"ds_read_b64_tr_b16 a[222:223], v231 offset:61440\n"
"v_mfma_f32_16x16x32_bf16 v[20:23], a[204:207], v[140:143], v[20:23]\n"
"v_exp_f32_e32 v203, v203\n"
"v_mfma_f32_16x16x32_bf16 v[16:19], a[204:207], v[132:135], v[16:19]\n"
"buffer_load_dwordx4 v241, s[40:43], s45 offen offset:3072 lds\n"
"v_mfma_f32_16x16x32_bf16 v[16:19], a[200:203], v[128:131], v[16:19]\n"
"v_exp_f32_e32 v204, v204\n"
"v_smfmac_f32_16x16x64_bf16 a[112:115], a[92:95], v[128:135], v244\n"
"s_waitcnt lgkmcnt(8)\n"
"v_perm_b32 v177, v203, v202, s93\n"
"v_mfma_f32_16x16x32_bf16 v[192:195], a[240:243], a[128:131], v[208:211]\n"
"ds_read_b128 a[224:227], v224 offset:45056\n"
"v_mfma_f32_16x16x32_bf16 v[192:195], a[244:247], a[132:135], v[192:195]\n"
"v_exp_f32_e32 v205, v205\n"
"v_mfma_f32_16x16x32_bf16 v[192:195], a[248:251], a[136:139], v[192:195]\n"
"ds_read_b128 a[228:231], v225 offset:45056\n"
"v_perm_b32 v184, v205, v204, s93\n"
"v_mfma_f32_16x16x32_bf16 v[192:195], a[252:255], a[140:143], v[192:195]\n"
"v_exp_f32_e32 v206, v206\n"
"v_mfma_f32_16x16x32_bf16 v[196:199], a[252:255], a[156:159], v[212:215]\n"
"ds_read_b128 a[232:235], v226 offset:45056\n"
"v_mfma_f32_16x16x32_bf16 v[196:199], a[248:251], a[152:155], v[196:199]\n"
"v_exp_f32_e32 v207, v207\n"
"v_mfma_f32_16x16x32_bf16 v[196:199], a[244:247], a[148:151], v[196:199]\n"
"buffer_store_dwordx4 a[24:27], v255, s[68:71], s94 offen offset:1024 sc1\n"
"v_mfma_f32_16x16x32_bf16 v[196:199], a[240:243], a[144:147], v[196:199]\n"
"v_exp_f32_e32 v192, v192\n"
"v_mfma_f32_16x16x32_bf16 v[200:203], a[240:243], a[160:163], v[216:219]\n"
"ds_read_b128 a[236:239], v227 offset:45056\n"
"v_perm_b32 v185, v207, v206, s93\n"
"v_mfma_f32_16x16x32_bf16 v[200:203], a[244:247], a[164:167], v[200:203]\n"
"v_exp_f32_e32 v193, v193\n"
"v_mfma_f32_16x16x32_bf16 v[200:203], a[248:251], a[168:171], v[200:203]\n"
"ds_read_b64_tr_b16 a[192:193], v232 offset:12288\n"
"v_perm_b32 v162, v193, v192, s93\n"
"v_mfma_f32_16x16x32_bf16 v[200:203], a[252:255], a[172:175], v[200:203]\n"
"v_exp_f32_e32 v194, v194\n"
"v_mfma_f32_16x16x32_bf16 v[204:207], a[252:255], a[188:191], v[220:223]\n"
"ds_read_b64_tr_b16 a[194:195], v232 offset:28672\n"
"v_mfma_f32_16x16x32_bf16 v[204:207], a[248:251], a[184:187], v[204:207]\n"
"v_exp_f32_e32 v195, v195\n"
"v_mfma_f32_16x16x32_bf16 v[204:207], a[244:247], a[180:183], v[204:207]\n"
"v_perm_b32 v163, v195, v194, s93\n"
"v_mfma_f32_16x16x32_bf16 v[204:207], a[240:243], a[176:179], v[204:207]\n"
"s_waitcnt lgkmcnt(6)\n"
"v_exp_f32_e32 v196, v196\n"
"v_mfma_f32_16x16x32_bf16 v[32:35], a[208:211], v[128:131], v[32:35]\n"
"ds_read_b64_tr_b16 a[196:197], v232 offset:45056\n"
"v_mfma_f32_16x16x32_bf16 v[32:35], a[212:215], v[132:135], v[32:35]\n"
"v_exp_f32_e32 v197, v197\n"
"v_mfma_f32_16x16x32_bf16 v[36:39], a[212:215], v[140:143], v[36:39]\n"
"ds_read_b64_tr_b16 a[198:199], v232 offset:61440\n"
"v_perm_b32 v170, v197, v196, s93\n"
"v_mfma_f32_16x16x32_bf16 v[36:39], a[208:211], v[136:139], v[36:39]\n"
"v_exp_f32_e32 v198, v198\n"
"v_mfma_f32_16x16x32_bf16 v[40:43], a[208:211], v[144:147], v[40:43]\n"
"ds_read_b64_tr_b16 a[200:201], v233 offset:12288\n"
"v_mfma_f32_16x16x32_bf16 v[40:43], a[212:215], v[148:151], v[40:43]\n"
"v_exp_f32_e32 v199, v199\n"
"v_mfma_f32_16x16x32_bf16 v[44:47], a[212:215], v[156:159], v[44:47]\n"
"v_perm_b32 v171, v199, v198, s93\n"
"v_mfma_f32_16x16x32_bf16 v[44:47], a[208:211], v[152:155], v[44:47]\n"
"v_exp_f32_e32 v200, v200\n"
"v_mfma_f32_16x16x32_bf16 v[60:63], a[216:219], v[152:155], v[60:63]\n"
"ds_read_b64_tr_b16 a[202:203], v233 offset:28672\n"
"v_mfma_f32_16x16x32_bf16 v[60:63], a[220:223], v[156:159], v[60:63]\n"
"v_exp_f32_e32 v201, v201\n"
"v_mfma_f32_16x16x32_bf16 v[56:59], a[220:223], v[148:151], v[56:59]\n"
"ds_read_b64_tr_b16 a[204:205], v233 offset:45056\n"
"v_perm_b32 v178, v201, v200, s93\n"
"v_mfma_f32_16x16x32_bf16 v[56:59], a[216:219], v[144:147], v[56:59]\n"
"v_exp_f32_e32 v202, v202\n"
"v_mfma_f32_16x16x32_bf16 v[52:55], a[216:219], v[136:139], v[52:55]\n"
"ds_read_b64_tr_b16 a[206:207], v233 offset:61440\n"
"v_mfma_f32_16x16x32_bf16 v[52:55], a[220:223], v[140:143], v[52:55]\n"
"v_exp_f32_e32 v203, v203\n"
"v_mfma_f32_16x16x32_bf16 v[48:51], a[220:223], v[132:135], v[48:51]\n"
"v_perm_b32 v179, v203, v202, s93\n"
"v_mfma_f32_16x16x32_bf16 v[48:51], a[216:219], v[128:131], v[48:51]\n"
"v_exp_f32_e32 v204, v204\n"
"v_smfmac_f32_16x16x64_bf16 a[116:119], a[92:95], v[136:143], v244\n"
"s_waitcnt lgkmcnt(8)\n"
"v_mfma_f32_16x16x32_bf16 v[192:195], a[224:227], a[128:131], v[208:211]\n"
"ds_read_b128 a[240:243], v224 offset:61440\n"
"v_mfma_f32_16x16x32_bf16 v[192:195], a[228:231], a[132:135], v[192:195]\n"
"v_exp_f32_e32 v205, v205\n"
"v_mfma_f32_16x16x32_bf16 v[192:195], a[232:235], a[136:139], v[192:195]\n"
"ds_read_b128 a[244:247], v225 offset:61440\n"
"v_perm_b32 v186, v205, v204, s93\n"
"v_mfma_f32_16x16x32_bf16 v[192:195], a[236:239], a[140:143], v[192:195]\n"
"v_exp_f32_e32 v206, v206\n"
"v_mfma_f32_16x16x32_bf16 v[196:199], a[236:239], a[156:159], v[212:215]\n"
"ds_read_b128 a[248:251], v226 offset:61440\n"
"v_mfma_f32_16x16x32_bf16 v[196:199], a[232:235], a[152:155], v[196:199]\n"
"v_exp_f32_e32 v207, v207\n"
"v_mfma_f32_16x16x32_bf16 v[196:199], a[228:231], a[148:151], v[196:199]\n"
"v_perm_b32 v187, v207, v206, s93\n"
"v_mfma_f32_16x16x32_bf16 v[196:199], a[224:227], a[144:147], v[196:199]\n"
"v_exp_f32_e32 v192, v192\n"
"v_mfma_f32_16x16x32_bf16 v[200:203], a[224:227], a[160:163], v[216:219]\n"
"ds_read_b128 a[252:255], v227 offset:61440\n"
"v_mfma_f32_16x16x32_bf16 v[200:203], a[228:231], a[164:167], v[200:203]\n"
"v_exp_f32_e32 v193, v193\n"
"v_mfma_f32_16x16x32_bf16 v[200:203], a[232:235], a[168:171], v[200:203]\n"
"ds_read_b64_tr_b16 a[208:209], v234 offset:12288\n"
"v_perm_b32 v164, v193, v192, s93\n"
"v_mfma_f32_16x16x32_bf16 v[200:203], a[236:239], a[172:175], v[200:203]\n"
"v_exp_f32_e32 v194, v194\n"
"v_mfma_f32_16x16x32_bf16 v[204:207], a[236:239], a[188:191], v[220:223]\n"
"ds_read_b64_tr_b16 a[210:211], v234 offset:28672\n"
"v_mfma_f32_16x16x32_bf16 v[204:207], a[232:235], a[184:187], v[204:207]\n"
"v_exp_f32_e32 v195, v195\n"
"v_mfma_f32_16x16x32_bf16 v[204:207], a[228:231], a[180:183], v[204:207]\n"
"v_perm_b32 v165, v195, v194, s93\n"
"v_mfma_f32_16x16x32_bf16 v[204:207], a[224:227], a[176:179], v[204:207]\n"
"s_waitcnt lgkmcnt(6)\n"
"v_exp_f32_e32 v196, v196\n"
"v_mfma_f32_16x16x32_bf16 v[64:67], a[192:195], v[128:131], v[64:67]\n"
"ds_read_b64_tr_b16 a[212:213], v234 offset:45056\n"
"v_mfma_f32_16x16x32_bf16 v[64:67], a[196:199], v[132:135], v[64:67]\n"
"v_exp_f32_e32 v197, v197\n"
"v_mfma_f32_16x16x32_bf16 v[68:71], a[196:199], v[140:143], v[68:71]\n"
"ds_read_b64_tr_b16 a[214:215], v234 offset:61440\n"
"v_perm_b32 v172, v197, v196, s93\n"
"v_mfma_f32_16x16x32_bf16 v[68:71], a[192:195], v[136:139], v[68:71]\n"
"v_exp_f32_e32 v198, v198\n"
"v_mfma_f32_16x16x32_bf16 v[72:75], a[192:195], v[144:147], v[72:75]\n"
"ds_read_b64_tr_b16 a[216:217], v235 offset:12288\n"
"v_mfma_f32_16x16x32_bf16 v[72:75], a[196:199], v[148:151], v[72:75]\n"
"v_exp_f32_e32 v199, v199\n"
"v_mfma_f32_16x16x32_bf16 v[76:79], a[196:199], v[156:159], v[76:79]\n"
"v_perm_b32 v173, v199, v198, s93\n"
"v_mfma_f32_16x16x32_bf16 v[76:79], a[192:195], v[152:155], v[76:79]\n"
"v_exp_f32_e32 v200, v200\n"
"v_mfma_f32_16x16x32_bf16 v[92:95], a[200:203], v[152:155], v[92:95]\n"
"ds_read_b64_tr_b16 a[218:219], v235 offset:28672\n"
"v_mfma_f32_16x16x32_bf16 v[92:95], a[204:207], v[156:159], v[92:95]\n"
"v_exp_f32_e32 v201, v201\n"
"v_mfma_f32_16x16x32_bf16 v[88:91], a[204:207], v[148:151], v[88:91]\n"
"ds_read_b64_tr_b16 a[220:221], v235 offset:45056\n"
"v_perm_b32 v180, v201, v200, s93\n"
"v_mfma_f32_16x16x32_bf16 v[88:91], a[200:203], v[144:147], v[88:91]\n"
"v_exp_f32_e32 v202, v202\n"
"v_mfma_f32_16x16x32_bf16 v[84:87], a[200:203], v[136:139], v[84:87]\n"
"ds_read_b64_tr_b16 a[222:223], v235 offset:61440\n"
"v_mfma_f32_16x16x32_bf16 v[84:87], a[204:207], v[140:143], v[84:87]\n"
"v_exp_f32_e32 v203, v203\n"
"v_mfma_f32_16x16x32_bf16 v[80:83], a[204:207], v[132:135], v[80:83]\n"
"v_perm_b32 v181, v203, v202, s93\n"
"v_mfma_f32_16x16x32_bf16 v[80:83], a[200:203], v[128:131], v[80:83]\n"
"s_waitcnt vmcnt(1) lgkmcnt(0)\n"
"v_exp_f32_e32 v204, v204\n"
"v_smfmac_f32_16x16x64_bf16 a[120:123], a[92:95], v[144:151], v244\n"
"s_waitcnt lgkmcnt(8)\n"
"v_mfma_f32_16x16x32_bf16 v[192:195], a[240:243], a[128:131], v[208:211]\n"
"s_barrier\n"
"ds_read_b128 a[224:227], v224 offset:0\n"
"v_mfma_f32_16x16x32_bf16 v[192:195], a[244:247], a[132:135], v[192:195]\n"
"v_exp_f32_e32 v205, v205\n"
"v_mfma_f32_16x16x32_bf16 v[192:195], a[248:251], a[136:139], v[192:195]\n"
".LPG8_%=:\n"
"s_cbranch_execz .LPGR_%=\n"
"ds_read_b128 a[228:231], v225 offset:0\n"
"v_add_u32_e32 v243, v242, v243\n"
"v_mfma_f32_16x16x32_bf16 v[192:195], a[252:255], a[140:143], v[192:195]\n"
"v_exp_f32_e32 v206, v206\n"
"v_mfma_f32_16x16x32_bf16 v[196:199], a[252:255], a[156:159], v[212:215]\n"
"ds_read_b128 a[232:235], v226 offset:0\n"
"v_perm_b32 v188, v205, v204, s93\n"
"v_mfma_f32_16x16x32_bf16 v[196:199], a[248:251], a[152:155], v[196:199]\n"
"v_exp_f32_e32 v207, v207\n"
"v_mfma_f32_16x16x32_bf16 v[196:199], a[244:247], a[148:151], v[196:199]\n"
"buffer_store_dword v243, v247, s[24:27], s31 offen sc1\n"
"v_mfma_f32_16x16x32_bf16 v[196:199], a[240:243], a[144:147], v[196:199]\n"
"v_exp_f32_e32 v192, v192\n"
"v_mfma_f32_16x16x32_bf16 v[200:203], a[240:243], a[160:163], v[216:219]\n"
"ds_read_b128 a[236:239], v227 offset:0\n"
"v_perm_b32 v189, v207, v206, s93\n"
"v_mfma_f32_16x16x32_bf16 v[200:203], a[244:247], a[164:167], v[200:203]\n"
"v_exp_f32_e32 v193, v193\n"
"v_mfma_f32_16x16x32_bf16 v[200:203], a[248:251], a[168:171], v[200:203]\n"
"ds_read_b64_tr_b16 a[192:193], v228 offset:0\n"
"v_readlane_b32 s32, v243, 0\n"
"v_mfma_f32_16x16x32_bf16 v[200:203], a[252:255], a[172:175], v[200:203]\n"
"s_add_u32 s94, s90, 36864\n"
"v_exp_f32_e32 v194, v194\n"
"v_mfma_f32_16x16x32_bf16 v[204:207], a[252:255], a[188:191], v[220:223]\n"
"ds_read_b64_tr_b16 a[194:195], v228 offset:16384\n"
"v_perm_b32 v166, v193, v192, s93\n"
"v_mfma_f32_16x16x32_bf16 v[204:207], a[248:251], a[184:187], v[204:207]\n"
"v_exp_f32_e32 v195, v195\n"
"v_mfma_f32_16x16x32_bf16 v[204:207], a[244:247], a[180:183], v[204:207]\n"
"buffer_store_dwordx4 a[28:31], v255, s[68:71], s94 offen offset:0 sc1\n"
"v_mfma_f32_16x16x32_bf16 v[204:207], a[240:243], a[176:179], v[204:207]\n"
"s_waitcnt lgkmcnt(6)\n"
"v_exp_f32_e32 v196, v196\n"
"v_mfma_f32_16x16x32_bf16 v[96:99], a[208:211], v[128:131], v[96:99]\n"
"ds_read_b64_tr_b16 a[196:197], v228 offset:32768\n"
"v_readlane_b32 s33, v243, 1\n"
"v_mfma_f32_16x16x32_bf16 v[96:99], a[212:215], v[132:135], v[96:99]\n"
"v_exp_f32_e32 v197, v197\n"
"v_mfma_f32_16x16x32_bf16 v[100:103], a[212:215], v[140:143], v[100:103]\n"
"ds_read_b64_tr_b16 a[198:199], v228 offset:49152\n"
"v_perm_b32 v167, v195, v194, s93\n"
"v_mfma_f32_16x16x32_bf16 v[100:103], a[208:211], v[136:139], v[100:103]\n"
"s_add_u32 s76, s76, 65536\n"
"v_exp_f32_e32 v198, v198\n"
"v_mfma_f32_16x16x32_bf16 v[104:107], a[208:211], v[144:147], v[104:107]\n"
"ds_read_b64_tr_b16 a[200:201], v229 offset:0\n"
"v_perm_b32 v174, v197, v196, s93\n"
"v_mfma_f32_16x16x32_bf16 v[104:107], a[212:215], v[148:151], v[104:107]\n"
"v_exp_f32_e32 v199, v199\n"
"v_mfma_f32_16x16x32_bf16 v[108:111], a[212:215], v[156:159], v[108:111]\n"
"buffer_load_dword a9, v248, s[72:75], s76 offen\n"
"v_mfma_f32_16x16x32_bf16 v[108:111], a[208:211], v[152:155], v[108:111]\n"
"v_exp_f32_e32 v200, v200\n"
"v_mfma_f32_16x16x32_bf16 v[124:127], a[216:219], v[152:155], v[124:127]\n"
"ds_read_b64_tr_b16 a[202:203], v229 offset:16384\n"
"v_perm_b32 v175, v199, v198, s93\n"
"v_mfma_f32_16x16x32_bf16 v[124:127], a[220:223], v[156:159], v[124:127]\n"
"v_exp_f32_e32 v201, v201\n"
"v_mfma_f32_16x16x32_bf16 v[120:123], a[220:223], v[148:151], v[120:123]\n"
"ds_read_b64_tr_b16 a[204:205], v229 offset:32768\n"
"v_perm_b32 v182, v201, v200, s93\n"
"v_mfma_f32_16x16x32_bf16 v[120:123], a[216:219], v[144:147], v[120:123]\n"
"v_exp_f32_e32 v202, v202\n"
"v_mfma_f32_16x16x32_bf16 v[116:119], a[216:219], v[136:139], v[116:119]\n"
"ds_read_b64_tr_b16 a[206:207], v229 offset:49152\n"
"v_mfma_f32_16x16x32_bf16 v[116:119], a[220:223], v[140:143], v[116:119]\n"
"v_exp_f32_e32 v203, v203\n"
"v_mfma_f32_16x16x32_bf16 v[112:115], a[220:223], v[132:135], v[112:115]\n"
"v_perm_b32 v183, v203, v202, s93\n"
"v_mfma_f32_16x16x32_bf16 v[112:115], a[216:219], v[128:131], v[112:115]\n"
"v_exp_f32_e32 v204, v204\n"
"v_smfmac_f32_16x16x64_bf16 a[124:127], a[92:95], v[152:159], v244\n"
"s_waitcnt lgkmcnt(8)\n"
"v_mfma_f32_16x16x32_bf16 v[192:195], a[224:227], a[128:131], v[208:211]\n"
"ds_read_b128 a[240:243], v224 offset:16384\n"
"v_mfma_f32_16x16x32_bf16 v[192:195], a[228:231], a[132:135], v[192:195]\n"
"v_exp_f32_e32 v205, v205\n"
"v_mfma_f32_16x16x32_bf16 v[192:195], a[232:235], a[136:139], v[192:195]\n"
"ds_read_b128 a[244:247], v225 offset:16384\n"
"v_perm_b32 v190, v205, v204, s93\n"
"v_mfma_f32_16x16x32_bf16 v[192:195], a[236:239], a[140:143], v[192:195]\n"
"v_exp_f32_e32 v206, v206\n"
"v_mfma_f32_16x16x32_bf16 v[196:199], a[236:239], a[156:159], v[212:215]\n"
"ds_read_b128 a[248:251], v226 offset:16384\n"
"v_mfma_f32_16x16x32_bf16 v[196:199], a[232:235], a[152:155], v[196:199]\n"
"v_exp_f32_e32 v207, v207\n"
"v_mfma_f32_16x16x32_bf16 v[196:199], a[228:231], a[148:151], v[196:199]\n"
"buffer_store_dwordx4 a[32:35], v255, s[68:71], s94 offen offset:1024 sc1\n"
"v_mfma_f32_16x16x32_bf16 v[196:199], a[224:227], a[144:147], v[196:199]\n"
"v_exp_f32_e32 v192, v192\n"
"v_mfma_f32_16x16x32_bf16 v[200:203], a[224:227], a[160:163], v[216:219]\n"
"ds_read_b128 a[252:255], v227 offset:16384\n"
"v_perm_b32 v191, v207, v206, s93\n"
"v_mfma_f32_16x16x32_bf16 v[200:203], a[228:231], a[164:167], v[200:203]\n"
"v_exp_f32_e32 v193, v193\n"
"v_mfma_f32_16x16x32_bf16 v[200:203], a[232:235], a[168:171], v[200:203]\n"
"ds_read_b64_tr_b16 a[208:209], v230 offset:0\n"
"v_perm_b32 v128, v193, v192, s93\n"
"v_mfma_f32_16x16x32_bf16 v[200:203], a[236:239], a[172:175], v[200:203]\n"
"s_add_u32 m0, s46, 8192\n"
"v_exp_f32_e32 v194, v194\n"
"v_mfma_f32_16x16x32_bf16 v[204:207], a[236:239], a[188:191], v[220:223]\n"
"s_add_u32 s44, s44, 16384\n"
"ds_read_b64_tr_b16 a[210:211], v230 offset:16384\n"
"v_mfma_f32_16x16x32_bf16 v[204:207], a[232:235], a[184:187], v[204:207]\n"
"v_exp_f32_e32 v195, v195\n"
"v_mfma_f32_16x16x32_bf16 v[204:207], a[228:231], a[180:183], v[204:207]\n"
"buffer_load_dwordx4 v236, s[36:39], s44 offen offset:0 lds\n"
"v_mfma_f32_16x16x32_bf16 v[204:207], a[224:227], a[176:179], v[204:207]\n"
"s_waitcnt lgkmcnt(6)\n"
"v_exp_f32_e32 v196, v196\n"
"v_mfma_f32_16x16x32_bf16 v[0:3], a[192:195], v[160:163], v[0:3]\n"
"ds_read_b64_tr_b16 a[212:213], v230 offset:32768\n"
"v_perm_b32 v129, v195, v194, s93\n"
"v_mfma_f32_16x16x32_bf16 v[0:3], a[196:199], v[164:167], v[0:3]\n"
"v_exp_f32_e32 v197, v197\n"
"v_mfma_f32_16x16x32_bf16 v[4:7], a[196:199], v[172:175], v[4:7]\n"
"ds_read_b64_tr_b16 a[214:215], v230 offset:49152\n"
"v_perm_b32 v136, v197, v196, s93\n"
"v_mfma_f32_16x16x32_bf16 v[4:7], a[192:195], v[168:171], v[4:7]\n"
"v_exp_f32_e32 v198, v198\n"
"v_mfma_f32_16x16x32_bf16 v[8:11], a[192:195], v[176:179], v[8:11]\n"
"ds_read_b64_tr_b16 a[216:217], v231 offset:0\n"
"v_mfma_f32_16x16x32_bf16 v[8:11], a[196:199], v[180:183], v[8:11]\n"
"v_exp_f32_e32 v199, v199\n"
"v_mfma_f32_16x16x32_bf16 v[12:15], a[196:199], v[188:191], v[12:15]\n"
"buffer_load_dwordx4 v237, s[36:39], s44 offen offset:1024 lds\n"
"v_mfma_f32_16x16x32_bf16 v[12:15], a[192:195], v[184:187], v[12:15]\n"
"v_exp_f32_e32 v200, v200\n"
"v_mfma_f32_16x16x32_bf16 v[28:31], a[200:203], v[184:187], v[28:31]\n"
"ds_read_b64_tr_b16 a[218:219], v231 offset:16384\n"
"v_perm_b32 v137, v199, v198, s93\n"
"v_mfma_f32_16x16x32_bf16 v[28:31], a[204:207], v[188:191], v[28:31]\n"
"v_exp_f32_e32 v201, v201\n"
"v_mfma_f32_16x16x32_bf16 v[24:27], a[204:207], v[180:183], v[24:27]\n"
"ds_read_b64_tr_b16 a[220:221], v231 offset:32768\n"
"v_perm_b32 v144, v201, v200, s93\n"
"v_mfma_f32_16x16x32_bf16 v[24:27], a[200:203], v[176:179], v[24:27]\n"
"v_exp_f32_e32 v202, v202\n"
"v_mfma_f32_16x16x32_bf16 v[20:23], a[200:203], v[168:171], v[20:23]\n"
"ds_read_b64_tr_b16 a[222:223], v231 offset:49152\n"
"v_mfma_f32_16x16x32_bf16 v[20:23], a[204:207], v[172:175], v[20:23]\n"
"v_exp_f32_e32 v203, v203\n"
"v_mfma_f32_16x16x32_bf16 v[16:19], a[204:207], v[164:167], v[16:19]\n"
"buffer_load_dwordx4 v238, s[36:39], s44 offen offset:2048 lds\n"
"v_mfma_f32_16x16x32_bf16 v[16:19], a[200:203], v[160:163], v[16:19]\n"
"v_exp_f32_e32 v204, v204\n"
"v_smfmac_f32_16x16x64_bf16 a[112:115], a[92:95], v[160:167], v244\n"
"s_waitcnt lgkmcnt(8)\n"
"v_perm_b32 v145, v203, v202, s93\n"
"v_mfma_f32_16x16x32_bf16 v[192:195], a[240:243], a[128:131], v[208:211]\n"
"ds_read_b128 a[224:227], v224 offset:32768\n"
"v_mfma_f32_16x16x32_bf16 v[192:195], a[244:247], a[132:135], v[192:195]\n"
"v_exp_f32_e32 v205, v205\n"
"v_mfma_f32_16x16x32_bf16 v[192:195], a[248:251], a[136:139], v[192:195]\n"
"ds_read_b128 a[228:231], v225 offset:32768\n"
"v_perm_b32 v152, v205, v204, s93\n"
"v_mfma_f32_16x16x32_bf16 v[192:195], a[252:255], a[140:143], v[192:195]\n"
"v_exp_f32_e32 v206, v206\n"
"v_mfma_f32_16x16x32_bf16 v[196:199], a[252:255], a[156:159], v[212:215]\n"
"ds_read_b128 a[232:235], v226 offset:32768\n"
"v_mfma_f32_16x16x32_bf16 v[196:199], a[248:251], a[152:155], v[196:199]\n"
"v_exp_f32_e32 v207, v207\n"
"v_mfma_f32_16x16x32_bf16 v[196:199], a[244:247], a[148:151], v[196:199]\n"
"buffer_load_dwordx4 v239, s[36:39], s44 offen offset:3072 lds\n"
"v_mfma_f32_16x16x32_bf16 v[196:199], a[240:243], a[144:147], v[196:199]\n"
"v_exp_f32_e32 v192, v192\n"
"v_mfma_f32_16x16x32_bf16 v[200:203], a[240:243], a[160:163], v[216:219]\n"
"ds_read_b128 a[236:239], v227 offset:32768\n"
"v_perm_b32 v153, v207, v206, s93\n"
"v_mfma_f32_16x16x32_bf16 v[200:203], a[244:247], a[164:167], v[200:203]\n"
"v_exp_f32_e32 v193, v193\n"
"v_mfma_f32_16x16x32_bf16 v[200:203], a[248:251], a[168:171], v[200:203]\n"
"ds_read_b64_tr_b16 a[192:193], v232 offset:0\n"
"v_perm_b32 v130, v193, v192, s93\n"
"v_mfma_f32_16x16x32_bf16 v[200:203], a[252:255], a[172:175], v[200:203]\n"
"s_add_u32 m0, s46, 73728\n"
"v_exp_f32_e32 v194, v194\n"
"v_mfma_f32_16x16x32_bf16 v[204:207], a[252:255], a[188:191], v[220:223]\n"
"s_add_u32 s45, s45, 16384\n"
"ds_read_b64_tr_b16 a[194:195], v232 offset:16384\n"
"v_mfma_f32_16x16x32_bf16 v[204:207], a[248:251], a[184:187], v[204:207]\n"
"v_exp_f32_e32 v195, v195\n"
"v_mfma_f32_16x16x32_bf16 v[204:207], a[244:247], a[180:183], v[204:207]\n"
"buffer_load_dwordx4 v240, s[40:43], s45 offen offset:0 lds\n"
"v_mfma_f32_16x16x32_bf16 v[204:207], a[240:243], a[176:179], v[204:207]\n"
"s_waitcnt lgkmcnt(6)\n"
"v_exp_f32_e32 v196, v196\n"
"v_mfma_f32_16x16x32_bf16 v[32:35], a[208:211], v[160:163], v[32:35]\n"
"ds_read_b64_tr_b16 a[196:197], v232 offset:32768\n"
"v_perm_b32 v131, v195, v194, s93\n"
"v_mfma_f32_16x16x32_bf16 v[32:35], a[212:215], v[164:167], v[32:35]\n"
"v_exp_f32_e32 v197, v197\n"
"v_mfma_f32_16x16x32_bf16 v[36:39], a[212:215], v[172:175], v[36:39]\n"
"ds_read_b64_tr_b16 a[198:199], v232 offset:49152\n"
"v_perm_b32 v138, v197, v196, s93\n"
"v_mfma_f32_16x16x32_bf16 v[36:39], a[208:211], v[168:171], v[36:39]\n"
"v_exp_f32_e32 v198, v198\n"
"v_mfma_f32_16x16x32_bf16 v[40:43], a[208:211], v[176:179], v[40:43]\n"
"ds_read_b64_tr_b16 a[200:201], v233 offset:0\n"
"v_mfma_f32_16x16x32_bf16 v[40:43], a[212:215], v[180:183], v[40:43]\n"
"v_exp_f32_e32 v199, v199\n"
"v_mfma_f32_16x16x32_bf16 v[44:47], a[212:215], v[188:191], v[44:47]\n"
"buffer_load_dwordx4 v241, s[40:43], s45 offen offset:1024 lds\n"
"v_mfma_f32_16x16x32_bf16 v[44:47], a[208:211], v[184:187], v[44:47]\n"
"v_exp_f32_e32 v200, v200\n"
"v_mfma_f32_16x16x32_bf16 v[60:63], a[216:219], v[184:187], v[60:63]\n"
"ds_read_b64_tr_b16 a[202:203], v233 offset:16384\n"
"v_perm_b32 v139, v199, v198, s93\n"
"v_mfma_f32_16x16x32_bf16 v[60:63], a[220:223], v[188:191], v[60:63]\n"
"v_exp_f32_e32 v201, v201\n"
"v_mfma_f32_16x16x32_bf16 v[56:59], a[220:223], v[180:183], v[56:59]\n"
"ds_read_b64_tr_b16 a[204:205], v233 offset:32768\n"
"v_perm_b32 v146, v201, v200, s93\n"
"v_mfma_f32_16x16x32_bf16 v[56:59], a[216:219], v[176:179], v[56:59]\n"
"v_exp_f32_e32 v202, v202\n"
"v_mfma_f32_16x16x32_bf16 v[52:55], a[216:219], v[168:171], v[52:55]\n"
"ds_read_b64_tr_b16 a[206:207], v233 offset:49152\n"
"v_mfma_f32_16x16x32_bf16 v[52:55], a[220:223], v[172:175], v[52:55]\n"
"v_exp_f32_e32 v203, v203\n"
"v_mfma_f32_16x16x32_bf16 v[48:51], a[220:223], v[164:167], v[48:51]\n"
"v_perm_b32 v147, v203, v202, s93\n"
"v_mfma_f32_16x16x32_bf16 v[48:51], a[216:219], v[160:163], v[48:51]\n"
"v_exp_f32_e32 v204, v204\n"
"v_smfmac_f32_16x16x64_bf16 a[116:119], a[92:95], v[168:175], v244\n"
"s_waitcnt lgkmcnt(8)\n"
"v_mfma_f32_16x16x32_bf16 v[192:195], a[224:227], a[128:131], v[208:211]\n"
"ds_read_b128 a[240:243], v224 offset:49152\n"
"v_mfma_f32_16x16x32_bf16 v[192:195], a[228:231], a[132:135], v[192:195]\n"
"v_exp_f32_e32 v205, v205\n"
"v_mfma_f32_16x16x32_bf16 v[192:195], a[232:235], a[136:139], v[192:195]\n"
"ds_read_b128 a[244:247], v225 offset:49152\n"
"v_perm_b32 v154, v205, v204, s93\n"
"v_mfma_f32_16x16x32_bf16 v[192:195], a[236:239], a[140:143], v[192:195]\n"
"s_add_u32 s94, s90, 55296\n"
"v_exp_f32_e32 v206, v206\n"
"v_mfma_f32_16x16x32_bf16 v[196:199], a[236:239], a[156:159], v[212:215]\n"
"ds_read_b128 a[248:251], v226 offset:49152\n"
"v_mfma_f32_16x16x32_bf16 v[196:199], a[232:235], a[152:155], v[196:199]\n"
"v_exp_f32_e32 v207, v207\n"
"v_mfma_f32_16x16x32_bf16 v[196:199], a[228:231], a[148:151], v[196:199]\n"
"buffer_store_dwordx4 a[36:39], v255, s[68:71], s94 offen offset:0 sc1\n"
"v_mfma_f32_16x16x32_bf16 v[196:199], a[224:227], a[144:147], v[196:199]\n"
"v_exp_f32_e32 v192, v192\n"
"v_mfma_f32_16x16x32_bf16 v[200:203], a[224:227], a[160:163], v[216:219]\n"
"ds_read_b128 a[252:255], v227 offset:49152\n"
"v_perm_b32 v155, v207, v206, s93\n"
"v_mfma_f32_16x16x32_bf16 v[200:203], a[228:231], a[164:167], v[200:203]\n"
"v_exp_f32_e32 v193, v193\n"
"v_mfma_f32_16x16x32_bf16 v[200:203], a[232:235], a[168:171], v[200:203]\n"
"ds_read_b64_tr_b16 a[208:209], v234 offset:0\n"
"v_perm_b32 v132, v193, v192, s93\n"
"v_mfma_f32_16x16x32_bf16 v[200:203], a[236:239], a[172:175], v[200:203]\n"
"v_exp_f32_e32 v194, v194\n"
"v_mfma_f32_16x16x32_bf16 v[204:207], a[236:239], a[188:191], v[220:223]\n"
"ds_read_b64_tr_b16 a[210:211], v234 offset:16384\n"
"v_mfma_f32_16x16x32_bf16 v[204:207], a[232:235], a[184:187], v[204:207]\n"
"v_exp_f32_e32 v195, v195\n"
"v_mfma_f32_16x16x32_bf16 v[204:207], a[228:231], a[180:183], v[204:207]\n"
"buffer_load_dwordx4 v240, s[40:43], s45 offen offset:2048 lds\n"
"v_mfma_f32_16x16x32_bf16 v[204:207], a[224:227], a[176:179], v[204:207]\n"
"s_waitcnt lgkmcnt(6)\n"
"v_exp_f32_e32 v196, v196\n"
"v_mfma_f32_16x16x32_bf16 v[64:67], a[192:195], v[160:163], v[64:67]\n"
"ds_read_b64_tr_b16 a[212:213], v234 offset:32768\n"
"v_perm_b32 v133, v195, v194, s93\n"
"v_mfma_f32_16x16x32_bf16 v[64:67], a[196:199], v[164:167], v[64:67]\n"
"v_exp_f32_e32 v197, v197\n"
"v_mfma_f32_16x16x32_bf16 v[68:71], a[196:199], v[172:175], v[68:71]\n"
"ds_read_b64_tr_b16 a[214:215], v234 offset:49152\n"
"v_perm_b32 v140, v197, v196, s93\n"
"v_mfma_f32_16x16x32_bf16 v[68:71], a[192:195], v[168:171], v[68:71]\n"
"v_exp_f32_e32 v198, v198\n"
"v_mfma_f32_16x16x32_bf16 v[72:75], a[192:195], v[176:179], v[72:75]\n"
"ds_read_b64_tr_b16 a[216:217], v235 offset:0\n"
"v_mfma_f32_16x16x32_bf16 v[72:75], a[196:199], v[180:183], v[72:75]\n"
"v_exp_f32_e32 v199, v199\n"
"v_mfma_f32_16x16x32_bf16 v[76:79], a[196:199], v[188:191], v[76:79]\n"
"buffer_load_dwordx4 v241, s[40:43], s45 offen offset:3072 lds\n"
"v_mfma_f32_16x16x32_bf16 v[76:79], a[192:195], v[184:187], v[76:79]\n"
"v_exp_f32_e32 v200, v200\n"
"v_mfma_f32_16x16x32_bf16 v[92:95], a[200:203], v[184:187], v[92:95]\n"
"ds_read_b64_tr_b16 a[218:219], v235 offset:16384\n"
"v_perm_b32 v141, v199, v198, s93\n"
"v_mfma_f32_16x16x32_bf16 v[92:95], a[204:207], v[188:191], v[92:95]\n"
"v_exp_f32_e32 v201, v201\n"
"v_mfma_f32_16x16x32_bf16 v[88:91], a[204:207], v[180:183], v[88:91]\n"
"ds_read_b64_tr_b16 a[220:221], v235 offset:32768\n"
"v_perm_b32 v148, v201, v200, s93\n"
"v_mfma_f32_16x16x32_bf16 v[88:91], a[200:203], v[176:179], v[88:91]\n"
"s_add_u32 m0, s46, 12288\n"
"v_exp_f32_e32 v202, v202\n"
"v_mfma_f32_16x16x32_bf16 v[84:87], a[200:203], v[168:171], v[84:87]\n"
"s_add_u32 s44, s44, 16384\n"
"ds_read_b64_tr_b16 a[222:223], v235 offset:49152\n"
"v_mfma_f32_16x16x32_bf16 v[84:87], a[204:207], v[172:175], v[84:87]\n"
"v_exp_f32_e32 v203, v203\n"
"v_mfma_f32_16x16x32_bf16 v[80:83], a[204:207], v[164:167], v[80:83]\n"
"buffer_load_dwordx4 v236, s[36:39], s44 offen offset:0 lds\n"
"v_mfma_f32_16x16x32_bf16 v[80:83], a[200:203], v[160:163], v[80:83]\n"
"v_exp_f32_e32 v204, v204\n"
"v_smfmac_f32_16x16x64_bf16 a[120:123], a[92:95], v[176:183], v244\n"
"s_waitcnt lgkmcnt(8)\n"
"v_perm_b32 v149, v203, v202, s93\n"
"v_mfma_f32_16x16x32_bf16 v[192:195], a[240:243], a[128:131], v[208:211]\n"
"ds_read_b128 a[224:227], v224 offset:4096\n"
"v_mfma_f32_16x16x32_bf16 v[192:195], a[244:247], a[132:135], v[192:195]\n"
"v_exp_f32_e32 v205, v205\n"
"v_mfma_f32_16x16x32_bf16 v[192:195], a[248:251], a[136:139], v[192:195]\n"
"ds_read_b128 a[228:231], v225 offset:4096\n"
"v_perm_b32 v156, v205, v204, s93\n"
"v_mfma_f32_16x16x32_bf16 v[192:195], a[252:255], a[140:143], v[192:195]\n"
"v_exp_f32_e32 v206, v206\n"
"v_mfma_f32_16x16x32_bf16 v[196:199], a[252:255], a[156:159], v[212:215]\n"
"ds_read_b128 a[232:235], v226 offset:4096\n"
"v_mfma_f32_16x16x32_bf16 v[196:199], a[248:251], a[152:155], v[196:199]\n"
"v_exp_f32_e32 v207, v207\n"
"v_mfma_f32_16x16x32_bf16 v[196:199], a[244:247], a[148:151], v[196:199]\n"
"buffer_load_dwordx4 v237, s[36:39], s44 offen offset:1024 lds\n"
"v_mfma_f32_16x16x32_bf16 v[196:199], a[240:243], a[144:147], v[196:199]\n"
"v_exp_f32_e32 v192, v192\n"
"v_mfma_f32_16x16x32_bf16 v[200:203], a[240:243], a[160:163], v[216:219]\n"
"ds_read_b128 a[236:239], v227 offset:4096\n"
"v_perm_b32 v157, v207, v206, s93\n"
"v_mfma_f32_16x16x32_bf16 v[200:203], a[244:247], a[164:167], v[200:203]\n"
"v_exp_f32_e32 v193, v193\n"
"v_mfma_f32_16x16x32_bf16 v[200:203], a[248:251], a[168:171], v[200:203]\n"
"ds_read_b64_tr_b16 a[192:193], v228 offset:4096\n"
"v_perm_b32 v134, v193, v192, s93\n"
"v_mfma_f32_16x16x32_bf16 v[200:203], a[252:255], a[172:175], v[200:203]\n"
"v_exp_f32_e32 v194, v194\n"
"v_mfma_f32_16x16x32_bf16 v[204:207], a[252:255], a[188:191], v[220:223]\n"
"ds_read_b64_tr_b16 a[194:195], v228 offset:20480\n"
"v_mfma_f32_16x16x32_bf16 v[204:207], a[248:251], a[184:187], v[204:207]\n"
"v_exp_f32_e32 v195, v195\n"
"v_mfma_f32_16x16x32_bf16 v[204:207], a[244:247], a[180:183], v[204:207]\n"
"buffer_load_dwordx4 v238, s[36:39], s44 offen offset:2048 lds\n"
"v_mfma_f32_16x16x32_bf16 v[204:207], a[240:243], a[176:179], v[204:207]\n"
"s_waitcnt lgkmcnt(6)\n"
"v_exp_f32_e32 v196, v196\n"
"v_mfma_f32_16x16x32_bf16 v[96:99], a[208:211], v[160:163], v[96:99]\n"
"ds_read_b64_tr_b16 a[196:197], v228 offset:36864\n"
"v_perm_b32 v135, v195, v194, s93\n"
"v_mfma_f32_16x16x32_bf16 v[96:99], a[212:215], v[164:167], v[96:99]\n"
"v_exp_f32_e32 v197, v197\n"
"v_mfma_f32_16x16x32_bf16 v[100:103], a[212:215], v[172:175], v[100:103]\n"
"ds_read_b64_tr_b16 a[198:199], v228 offset:53248\n"
"v_perm_b32 v142, v197, v196, s93\n"
"v_mfma_f32_16x16x32_bf16 v[100:103], a[208:211], v[168:171], v[100:103]\n"
"v_exp_f32_e32 v198, v198\n"
"v_mfma_f32_16x16x32_bf16 v[104:107], a[208:211], v[176:179], v[104:107]\n"
"ds_read_b64_tr_b16 a[200:201], v229 offset:4096\n"
"v_mfma_f32_16x16x32_bf16 v[104:107], a[212:215], v[180:183], v[104:107]\n"
"v_exp_f32_e32 v199, v199\n"
"v_mfma_f32_16x16x32_bf16 v[108:111], a[212:215], v[188:191], v[108:111]\n"
"buffer_load_dwordx4 v239, s[36:39], s44 offen offset:3072 lds\n"
"v_mfma_f32_16x16x32_bf16 v[108:111], a[208:211], v[184:187], v[108:111]\n"
"v_exp_f32_e32 v200, v200\n"
"v_mfma_f32_16x16x32_bf16 v[124:127], a[216:219], v[184:187], v[124:127]\n"
"ds_read_b64_tr_b16 a[202:203], v229 offset:20480\n"
"v_perm_b32 v143, v199, v198, s93\n"
"v_mfma_f32_16x16x32_bf16 v[124:127], a[220:223], v[188:191], v[124:127]\n"
"v_exp_f32_e32 v201, v201\n"
"v_mfma_f32_16x16x32_bf16 v[120:123], a[220:223], v[180:183], v[120:123]\n"
"ds_read_b64_tr_b16 a[204:205], v229 offset:36864\n"
"v_perm_b32 v150, v201, v200, s93\n"
"v_mfma_f32_16x16x32_bf16 v[120:123], a[216:219], v[176:179], v[120:123]\n"
"v_exp_f32_e32 v202, v202\n"
"v_mfma_f32_16x16x32_bf16 v[116:119], a[216:219], v[168:171], v[116:119]\n"
"ds_read_b64_tr_b16 a[206:207], v229 offset:53248\n"
"v_mfma_f32_16x16x32_bf16 v[116:119], a[220:223], v[172:175], v[116:119]\n"
"v_exp_f32_e32 v203, v203\n"
"v_mfma_f32_16x16x32_bf16 v[112:115], a[220:223], v[164:167], v[112:115]\n"
"v_perm_b32 v151, v203, v202, s93\n"
"v_mfma_f32_16x16x32_bf16 v[112:115], a[216:219], v[160:163], v[112:115]\n"
"v_exp_f32_e32 v204, v204\n"
"v_smfmac_f32_16x16x64_bf16 a[124:127], a[92:95], v[184:191], v244\n"
"s_waitcnt lgkmcnt(8)\n"
"v_mfma_f32_16x16x32_bf16 v[192:195], a[224:227], a[128:131], v[208:211]\n"
"ds_read_b128 a[240:243], v224 offset:20480\n"
"v_mfma_f32_16x16x32_bf16 v[192:195], a[228:231], a[132:135], v[192:195]\n"
"v_exp_f32_e32 v205, v205\n"
"v_mfma_f32_16x16x32_bf16 v[192:195], a[232:235], a[136:139], v[192:195]\n"
"ds_read_b128 a[244:247], v225 offset:20480\n"
"v_perm_b32 v158, v205, v204, s93\n"
"v_mfma_f32_16x16x32_bf16 v[192:195], a[236:239], a[140:143], v[192:195]\n"
"s_add_u32 m0, s46, 77824\n"
"v_exp_f32_e32 v206, v206\n"
"v_mfma_f32_16x16x32_bf16 v[196:199], a[236:239], a[156:159], v[212:215]\n"
"s_add_u32 s45, s45, 16384\n"
"ds_read_b128 a[248:251], v226 offset:20480\n"
"v_mfma_f32_16x16x32_bf16 v[196:199], a[232:235], a[152:155], v[196:199]\n"
"v_exp_f32_e32 v207, v207\n"
"v_mfma_f32_16x16x32_bf16 v[196:199], a[228:231], a[148:151], v[196:199]\n"
"buffer_load_dwordx4 v240, s[40:43], s45 offen offset:0 lds\n"
"v_mfma_f32_16x16x32_bf16 v[196:199], a[224:227], a[144:147], v[196:199]\n"
"v_exp_f32_e32 v192, v192\n"
"v_mfma_f32_16x16x32_bf16 v[200:203], a[224:227], a[160:163], v[216:219]\n"
"ds_read_b128 a[252:255], v227 offset:20480\n"
"v_perm_b32 v159, v207, v206, s93\n"
"v_mfma_f32_16x16x32_bf16 v[200:203], a[228:231], a[164:167], v[200:203]\n"
"v_exp_f32_e32 v193, v193\n"
"v_mfma_f32_16x16x32_bf16 v[200:203], a[232:235], a[168:171], v[200:203]\n"
"ds_read_b64_tr_b16 a[208:209], v230 offset:4096\n"
"v_perm_b32 v160, v193, v192, s93\n"
"v_mfma_f32_16x16x32_bf16 v[200:203], a[236:239], a[172:175], v[200:203]\n"
"v_exp_f32_e32 v194, v194\n"
"v_mfma_f32_16x16x32_bf16 v[204:207], a[236:239], a[188:191], v[220:223]\n"
"ds_read_b64_tr_b16 a[210:211], v230 offset:20480\n"
"v_mfma_f32_16x16x32_bf16 v[204:207], a[232:235], a[184:187], v[204:207]\n"
"v_exp_f32_e32 v195, v195\n"
"v_mfma_f32_16x16x32_bf16 v[204:207], a[228:231], a[180:183], v[204:207]\n"
"buffer_load_dwordx4 v241, s[40:43], s45 offen offset:1024 lds\n"
"v_mfma_f32_16x16x32_bf16 v[204:207], a[224:227], a[176:179], v[204:207]\n"
"s_waitcnt lgkmcnt(6)\n"
"v_exp_f32_e32 v196, v196\n"
"v_mfma_f32_16x16x32_bf16 v[0:3], a[192:195], v[128:131], v[0:3]\n"
"ds_read_b64_tr_b16 a[212:213], v230 offset:36864\n"
"v_perm_b32 v161, v195, v194, s93\n"
"v_mfma_f32_16x16x32_bf16 v[0:3], a[196:199], v[132:135], v[0:3]\n"
"v_exp_f32_e32 v197, v197\n"
"v_mfma_f32_16x16x32_bf16 v[4:7], a[196:199], v[140:143], v[4:7]\n"
"ds_read_b64_tr_b16 a[214:215], v230 offset:53248\n"
"v_perm_b32 v168, v197, v196, s93\n"
"v_mfma_f32_16x16x32_bf16 v[4:7], a[192:195], v[136:139], v[4:7]\n"
"v_exp_f32_e32 v198, v198\n"
"v_mfma_f32_16x16x32_bf16 v[8:11], a[192:195], v[144:147], v[8:11]\n"
"ds_read_b64_tr_b16 a[216:217], v231 offset:4096\n"
"v_mfma_f32_16x16x32_bf16 v[8:11], a[196:199], v[148:151], v[8:11]\n"
"v_exp_f32_e32 v199, v199\n"
"v_mfma_f32_16x16x32_bf16 v[12:15], a[196:199], v[156:159], v[12:15]\n"
"buffer_load_dwordx4 v240, s[40:43], s45 offen offset:2048 lds\n"
"v_mfma_f32_16x16x32_bf16 v[12:15], a[192:195], v[152:155], v[12:15]\n"
"v_exp_f32_e32 v200, v200\n"
"v_mfma_f32_16x16x32_bf16 v[28:31], a[200:203], v[152:155], v[28:31]\n"
"ds_read_b64_tr_b16 a[218:219], v231 offset:20480\n"
"v_perm_b32 v169, v199, v198, s93\n"
"v_mfma_f32_16x16x32_bf16 v[28:31], a[204:207], v[156:159], v[28:31]\n"
"v_exp_f32_e32 v201, v201\n"
"v_mfma_f32_16x16x32_bf16 v[24:27], a[204:207], v[148:151], v[24:27]\n"
"ds_read_b64_tr_b16 a[220:221], v231 offset:36864\n"
"v_perm_b32 v176, v201, v200, s93\n"
"v_mfma_f32_16x16x32_bf16 v[24:27], a[200:203], v[144:147], v[24:27]\n"
"v_exp_f32_e32 v202, v202\n"
"v_mfma_f32_16x16x32_bf16 v[20:23], a[200:203], v[136:139], v[20:23]\n"
"ds_read_b64_tr_b16 a[222:223], v231 offset:53248\n"
"v_mfma_f32_16x16x32_bf16 v[20:23], a[204:207], v[140:143], v[20:23]\n"
"v_exp_f32_e32 v203, v203\n"
"v_mfma_f32_16x16x32_bf16 v[16:19], a[204:207], v[132:135], v[16:19]\n"
"buffer_load_dwordx4 v241, s[40:43], s45 offen offset:3072 lds\n"
"v_mfma_f32_16x16x32_bf16 v[16:19], a[200:203], v[128:131], v[16:19]\n"
"v_exp_f32_e32 v204, v204\n"
"v_smfmac_f32_16x16x64_bf16 a[112:115], a[92:95], v[128:135], v244\n"
"s_waitcnt lgkmcnt(8)\n"
"v_perm_b32 v177, v203, v202, s93\n"
"v_mfma_f32_16x16x32_bf16 v[192:195], a[240:243], a[128:131], v[208:211]\n"
"ds_read_b128 a[224:227], v224 offset:36864\n"
"v_mfma_f32_16x16x32_bf16 v[192:195], a[244:247], a[132:135], v[192:195]\n"
"v_exp_f32_e32 v205, v205\n"
"v_mfma_f32_16x16x32_bf16 v[192:195], a[248:251], a[136:139], v[192:195]\n"
"ds_read_b128 a[228:231], v225 offset:36864\n"
"v_perm_b32 v184, v205, v204, s93\n"
"v_mfma_f32_16x16x32_bf16 v[192:195], a[252:255], a[140:143], v[192:195]\n"
"v_exp_f32_e32 v206, v206\n"
"v_mfma_f32_16x16x32_bf16 v[196:199], a[252:255], a[156:159], v[212:215]\n"
"ds_read_b128 a[232:235], v226 offset:36864\n"
"v_mfma_f32_16x16x32_bf16 v[196:199], a[248:251], a[152:155], v[196:199]\n"
"v_exp_f32_e32 v207, v207\n"
"v_mfma_f32_16x16x32_bf16 v[196:199], a[244:247], a[148:151], v[196:199]\n"
"buffer_store_dwordx4 a[40:43], v255, s[68:71], s94 offen offset:1024 sc1\n"
"v_mfma_f32_16x16x32_bf16 v[196:199], a[240:243], a[144:147], v[196:199]\n"
"v_exp_f32_e32 v192, v192\n"
"v_mfma_f32_16x16x32_bf16 v[200:203], a[240:243], a[160:163], v[216:219]\n"
"ds_read_b128 a[236:239], v227 offset:36864\n"
"v_perm_b32 v185, v207, v206, s93\n"
"v_mfma_f32_16x16x32_bf16 v[200:203], a[244:247], a[164:167], v[200:203]\n"
"v_exp_f32_e32 v193, v193\n"
"v_mfma_f32_16x16x32_bf16 v[200:203], a[248:251], a[168:171], v[200:203]\n"
"ds_read_b64_tr_b16 a[192:193], v232 offset:4096\n"
"v_perm_b32 v162, v193, v192, s93\n"
"v_mfma_f32_16x16x32_bf16 v[200:203], a[252:255], a[172:175], v[200:203]\n"
"v_exp_f32_e32 v194, v194\n"
"v_mfma_f32_16x16x32_bf16 v[204:207], a[252:255], a[188:191], v[220:223]\n"
"ds_read_b64_tr_b16 a[194:195], v232 offset:20480\n"
"v_mfma_f32_16x16x32_bf16 v[204:207], a[248:251], a[184:187], v[204:207]\n"
"v_exp_f32_e32 v195, v195\n"
"v_mfma_f32_16x16x32_bf16 v[204:207], a[244:247], a[180:183], v[204:207]\n"
"v_perm_b32 v163, v195, v194, s93\n"
"v_mfma_f32_16x16x32_bf16 v[204:207], a[240:243], a[176:179], v[204:207]\n"
"s_waitcnt lgkmcnt(6)\n"
"v_exp_f32_e32 v196, v196\n"
"v_mfma_f32_16x16x32_bf16 v[32:35], a[208:211], v[128:131], v[32:35]\n"
"ds_read_b64_tr_b16 a[196:197], v232 offset:36864\n"
"v_mfma_f32_16x16x32_bf16 v[32:35], a[212:215], v[132:135], v[32:35]\n"
"v_exp_f32_e32 v197, v197\n"
"v_mfma_f32_16x16x32_bf16 v[36:39], a[212:215], v[140:143], v[36:39]\n"
"ds_read_b64_tr_b16 a[198:199], v232 offset:53248\n"
"v_perm_b32 v170, v197, v196, s93\n"
"v_mfma_f32_16x16x32_bf16 v[36:39], a[208:211], v[136:139], v[36:39]\n"
"v_exp_f32_e32 v198, v198\n"
"v_mfma_f32_16x16x32_bf16 v[40:43], a[208:211], v[144:147], v[40:43]\n"
"ds_read_b64_tr_b16 a[200:201], v233 offset:4096\n"
"v_mfma_f32_16x16x32_bf16 v[40:43], a[212:215], v[148:151], v[40:43]\n"
"v_exp_f32_e32 v199, v199\n"
"v_mfma_f32_16x16x32_bf16 v[44:47], a[212:215], v[156:159], v[44:47]\n"
"v_perm_b32 v171, v199, v198, s93\n"
"v_mfma_f32_16x16x32_bf16 v[44:47], a[208:211], v[152:155], v[44:47]\n"
"v_exp_f32_e32 v200, v200\n"
"v_mfma_f32_16x16x32_bf16 v[60:63], a[216:219], v[152:155], v[60:63]\n"
"ds_read_b64_tr_b16 a[202:203], v233 offset:20480\n"
"v_mfma_f32_16x16x32_bf16 v[60:63], a[220:223], v[156:159], v[60:63]\n"
"v_exp_f32_e32 v201, v201\n"
"v_mfma_f32_16x16x32_bf16 v[56:59], a[220:223], v[148:151], v[56:59]\n"
"ds_read_b64_tr_b16 a[204:205], v233 offset:36864\n"
"v_perm_b32 v178, v201, v200, s93\n"
"v_mfma_f32_16x16x32_bf16 v[56:59], a[216:219], v[144:147], v[56:59]\n"
"v_exp_f32_e32 v202, v202\n"
"v_mfma_f32_16x16x32_bf16 v[52:55], a[216:219], v[136:139], v[52:55]\n"
"ds_read_b64_tr_b16 a[206:207], v233 offset:53248\n"
"v_mfma_f32_16x16x32_bf16 v[52:55], a[220:223], v[140:143], v[52:55]\n"
"v_exp_f32_e32 v203, v203\n"
"v_mfma_f32_16x16x32_bf16 v[48:51], a[220:223], v[132:135], v[48:51]\n"
"v_perm_b32 v179, v203, v202, s93\n"
"v_mfma_f32_16x16x32_bf16 v[48:51], a[216:219], v[128:131], v[48:51]\n"
"v_exp_f32_e32 v204, v204\n"
"v_smfmac_f32_16x16x64_bf16 a[116:119], a[92:95], v[136:143], v244\n"
"s_waitcnt lgkmcnt(8)\n"
"v_mfma_f32_16x16x32_bf16 v[192:195], a[224:227], a[128:131], v[208:211]\n"
"ds_read_b128 a[240:243], v224 offset:53248\n"
"v_mfma_f32_16x16x32_bf16 v[192:195], a[228:231], a[132:135], v[192:195]\n"
"v_exp_f32_e32 v205, v205\n"
"v_mfma_f32_16x16x32_bf16 v[192:195], a[232:235], a[136:139], v[192:195]\n"
"ds_read_b128 a[244:247], v225 offset:53248\n"
"v_perm_b32 v186, v205, v204, s93\n"
"v_mfma_f32_16x16x32_bf16 v[192:195], a[236:239], a[140:143], v[192:195]\n"
"s_add_u32 s34, s34, 1024\n"
"v_exp_f32_e32 v206, v206\n"
"v_mfma_f32_16x16x32_bf16 v[196:199], a[236:239], a[156:159], v[212:215]\n"
"ds_read_b128 a[248:251], v226 offset:53248\n"
"v_mfma_f32_16x16x32_bf16 v[196:199], a[232:235], a[152:155], v[196:199]\n"
"v_exp_f32_e32 v207, v207\n"
"v_mfma_f32_16x16x32_bf16 v[196:199], a[228:231], a[148:151], v[196:199]\n"
"buffer_store_dwordx4 a[4:7], v255, s[52:55], s34 offen sc1\n"
"v_mfma_f32_16x16x32_bf16 v[196:199], a[224:227], a[144:147], v[196:199]\n"
"v_exp_f32_e32 v192, v192\n"
"v_mfma_f32_16x16x32_bf16 v[200:203], a[224:227], a[160:163], v[216:219]\n"
"ds_read_b128 a[252:255], v227 offset:53248\n"
"v_perm_b32 v187, v207, v206, s93\n"
"v_mfma_f32_16x16x32_bf16 v[200:203], a[228:231], a[164:167], v[200:203]\n"
"v_exp_f32_e32 v193, v193\n"
"v_mfma_f32_16x16x32_bf16 v[200:203], a[232:235], a[168:171], v[200:203]\n"
"ds_read_b64_tr_b16 a[208:209], v234 offset:4096\n"
"v_perm_b32 v164, v193, v192, s93\n"
"v_mfma_f32_16x16x32_bf16 v[200:203], a[236:239], a[172:175], v[200:203]\n"
"v_exp_f32_e32 v194, v194\n"
"v_mfma_f32_16x16x32_bf16 v[204:207], a[236:239], a[188:191], v[220:223]\n"
"ds_read_b64_tr_b16 a[210:211], v234 offset:20480\n"
"v_mfma_f32_16x16x32_bf16 v[204:207], a[232:235], a[184:187], v[204:207]\n"
"v_exp_f32_e32 v195, v195\n"
"v_mfma_f32_16x16x32_bf16 v[204:207], a[228:231], a[180:183], v[204:207]\n"
"v_perm_b32 v165, v195, v194, s93\n"
"v_mfma_f32_16x16x32_bf16 v[204:207], a[224:227], a[176:179], v[204:207]\n"
"s_waitcnt lgkmcnt(6)\n"
"v_exp_f32_e32 v196, v196\n"
"v_mfma_f32_16x16x32_bf16 v[64:67], a[192:195], v[128:131], v[64:67]\n"
"ds_read_b64_tr_b16 a[212:213], v234 offset:36864\n"
"v_mfma_f32_16x16x32_bf16 v[64:67], a[196:199], v[132:135], v[64:67]\n"
"v_exp_f32_e32 v197, v197\n"
"v_mfma_f32_16x16x32_bf16 v[68:71], a[196:199], v[140:143], v[68:71]\n"
"ds_read_b64_tr_b16 a[214:215], v234 offset:53248\n"
"v_perm_b32 v172, v197, v196, s93\n"
"v_mfma_f32_16x16x32_bf16 v[68:71], a[192:195], v[136:139], v[68:71]\n"
"v_exp_f32_e32 v198, v198\n"
"v_mfma_f32_16x16x32_bf16 v[72:75], a[192:195], v[144:147], v[72:75]\n"
"ds_read_b64_tr_b16 a[216:217], v235 offset:4096\n"
"v_mfma_f32_16x16x32_bf16 v[72:75], a[196:199], v[148:151], v[72:75]\n"
"v_exp_f32_e32 v199, v199\n"
"v_mfma_f32_16x16x32_bf16 v[76:79], a[196:199], v[156:159], v[76:79]\n"
"v_perm_b32 v173, v199, v198, s93\n"
"v_mfma_f32_16x16x32_bf16 v[76:79], a[192:195], v[152:155], v[76:79]\n"
".LPG9_%=:\n"
"s_cbranch_execz .LPGR_%=\n"
"v_exp_f32_e32 v200, v200\n"
"v_mfma_f32_16x16x32_bf16 v[92:95], a[200:203], v[152:155], v[92:95]\n"
"ds_read_b64_tr_b16 a[218:219], v235 offset:20480\n"
"v_mfma_f32_16x16x32_bf16 v[92:95], a[204:207], v[156:159], v[92:95]\n"
"v_exp_f32_e32 v201, v201\n"
"v_mfma_f32_16x16x32_bf16 v[88:91], a[204:207], v[148:151], v[88:91]\n"
"ds_read_b64_tr_b16 a[220:221], v235 offset:36864\n"
"v_perm_b32 v180, v201, v200, s93\n"
"v_mfma_f32_16x16x32_bf16 v[88:91], a[200:203], v[144:147], v[88:91]\n"
"v_exp_f32_e32 v202, v202\n"
"v_mfma_f32_16x16x32_bf16 v[84:87], a[200:203], v[136:139], v[84:87]\n"
"ds_read_b64_tr_b16 a[222:223], v235 offset:53248\n"
"v_mfma_f32_16x16x32_bf16 v[84:87], a[204:207], v[140:143], v[84:87]\n"
"v_exp_f32_e32 v203, v203\n"
"v_mfma_f32_16x16x32_bf16 v[80:83], a[204:207], v[132:135], v[80:83]\n"
"v_perm_b32 v181, v203, v202, s93\n"
"v_mfma_f32_16x16x32_bf16 v[80:83], a[200:203], v[128:131], v[80:83]\n"
"s_waitcnt vmcnt(2) lgkmcnt(0)\n"
"v_exp_f32_e32 v204, v204\n"
"v_smfmac_f32_16x16x64_bf16 a[120:123], a[92:95], v[144:151], v244\n"
"s_waitcnt lgkmcnt(8)\n"
"v_mfma_f32_16x16x32_bf16 v[192:195], a[240:243], a[128:131], v[208:211]\n"
"s_barrier\n"
"ds_read_b128 a[224:227], v224 offset:8192\n"
"v_mfma_f32_16x16x32_bf16 v[192:195], a[244:247], a[132:135], v[192:195]\n"
"v_exp_f32_e32 v205, v205\n"
"v_mfma_f32_16x16x32_bf16 v[192:195], a[248:251], a[136:139], v[192:195]\n"
"ds_read_b128 a[228:231], v225 offset:8192\n"
"v_perm_b32 v188, v205, v204, s93\n"
"v_mfma_f32_16x16x32_bf16 v[192:195], a[252:255], a[140:143], v[192:195]\n"
"s_add_u32 s34, s34, 1024\n"
"v_exp_f32_e32 v206, v206\n"
"v_mfma_f32_16x16x32_bf16 v[196:199], a[252:255], a[156:159], v[212:215]\n"
"ds_read_b128 a[232:235], v226 offset:8192\n"
"v_mfma_f32_16x16x32_bf16 v[196:199], a[248:251], a[152:155], v[196:199]\n"
"v_exp_f32_e32 v207, v207\n"
"v_mfma_f32_16x16x32_bf16 v[196:199], a[244:247], a[148:151], v[196:199]\n"
"buffer_store_dwordx4 a[4:7], v255, s[52:55], s34 offen sc1\n"
"v_mfma_f32_16x16x32_bf16 v[196:199], a[240:243], a[144:147], v[196:199]\n"
"v_exp_f32_e32 v192, v192\n"
"v_mfma_f32_16x16x32_bf16 v[200:203], a[240:243], a[160:163], v[216:219]\n"
"ds_read_b128 a[236:239], v227 offset:8192\n"
"v_perm_b32 v189, v207, v206, s93\n"
"v_mfma_f32_16x16x32_bf16 v[200:203], a[244:247], a[164:167], v[200:203]\n"
"v_exp_f32_e32 v193, v193\n"
"v_mfma_f32_16x16x32_bf16 v[200:203], a[248:251], a[168:171], v[200:203]\n"
"ds_read_b64_tr_b16 a[192:193], v228 offset:8192\n"
"v_perm_b32 v166, v193, v192, s93\n"
"v_mfma_f32_16x16x32_bf16 v[200:203], a[252:255], a[172:175], v[200:203]\n"
"v_exp_f32_e32 v194, v194\n"
"v_mfma_f32_16x16x32_bf16 v[204:207], a[252:255], a[188:191], v[220:223]\n"
"ds_read_b64_tr_b16 a[194:195], v228 offset:24576\n"
"v_mfma_f32_16x16x32_bf16 v[204:207], a[248:251], a[184:187], v[204:207]\n"
"v_exp_f32_e32 v195, v195\n"
"v_mfma_f32_16x16x32_bf16 v[204:207], a[244:247], a[180:183], v[204:207]\n"
"v_perm_b32 v167, v195, v194, s93\n"
"v_mfma_f32_16x16x32_bf16 v[204:207], a[240:243], a[176:179], v[204:207]\n"
"s_waitcnt lgkmcnt(6)\n"
"v_exp_f32_e32 v196, v196\n"
"v_mfma_f32_16x16x32_bf16 v[96:99], a[208:211], v[128:131], v[96:99]\n"
"ds_read_b64_tr_b16 a[196:197], v228 offset:40960\n"
"v_mfma_f32_16x16x32_bf16 v[96:99], a[212:215], v[132:135], v[96:99]\n"
"v_exp_f32_e32 v197, v197\n"
"v_mfma_f32_16x16x32_bf16 v[100:103], a[212:215], v[140:143], v[100:103]\n"
"ds_read_b64_tr_b16 a[198:199], v228 offset:57344\n"
"v_perm_b32 v174, v197, v196, s93\n"
"v_mfma_f32_16x16x32_bf16 v[100:103], a[208:211], v[136:139], v[100:103]\n"
"v_exp_f32_e32 v198, v198\n"
"v_mfma_f32_16x16x32_bf16 v[104:107], a[208:211], v[144:147], v[104:107]\n"
"ds_read_b64_tr_b16 a[200:201], v229 offset:8192\n"
"v_mfma_f32_16x16x32_bf16 v[104:107], a[212:215], v[148:151], v[104:107]\n"
"v_exp_f32_e32 v199, v199\n"
"v_mfma_f32_16x16x32_bf16 v[108:111], a[212:215], v[156:159], v[108:111]\n"
"v_perm_b32 v175, v199, v198, s93\n"
"v_mfma_f32_16x16x32_bf16 v[108:111], a[208:211], v[152:155], v[108:111]\n"
"v_exp_f32_e32 v200, v200\n"
"v_mfma_f32_16x16x32_bf16 v[124:127], a[216:219], v[152:155], v[124:127]\n"
"ds_read_b64_tr_b16 a[202:203], v229 offset:24576\n"
"v_mfma_f32_16x16x32_bf16 v[124:127], a[220:223], v[156:159], v[124:127]\n"
"v_exp_f32_e32 v201, v201\n"
"v_mfma_f32_16x16x32_bf16 v[120:123], a[220:223], v[148:151], v[120:123]\n"
"ds_read_b64_tr_b16 a[204:205], v229 offset:40960\n"
"v_perm_b32 v182, v201, v200, s93\n"
"v_mfma_f32_16x16x32_bf16 v[120:123], a[216:219], v[144:147], v[120:123]\n"
"v_exp_f32_e32 v202, v202\n"
"v_mfma_f32_16x16x32_bf16 v[116:119], a[216:219], v[136:139], v[116:119]\n"
"ds_read_b64_tr_b16 a[206:207], v229 offset:57344\n"
"v_mfma_f32_16x16x32_bf16 v[116:119], a[220:223], v[140:143], v[116:119]\n"
"v_exp_f32_e32 v203, v203\n"
"v_mfma_f32_16x16x32_bf16 v[112:115], a[220:223], v[132:135], v[112:115]\n"
"v_perm_b32 v183, v203, v202, s93\n"
"v_mfma_f32_16x16x32_bf16 v[112:115], a[216:219], v[128:131], v[112:115]\n"
"v_exp_f32_e32 v204, v204\n"
"v_smfmac_f32_16x16x64_bf16 a[124:127], a[92:95], v[152:159], v244\n"
"s_waitcnt lgkmcnt(8)\n"
".p2align 6\n"
".LLOOP_%=:\n"
"v_mfma_f32_16x16x32_bf16 v[192:195], a[224:227], a[128:131], v[208:211]\n"
"ds_read_b128 a[240:243], v224 offset:24576\n"
"v_mfma_f32_16x16x32_bf16 v[192:195], a[228:231], a[132:135], v[192:195]\n"
"v_exp_f32_e32 v205, v205\n"
"v_mfma_f32_16x16x32_bf16 v[192:195], a[232:235], a[136:139], v[192:195]\n"
"ds_read_b128 a[244:247], v225 offset:24576\n"
"v_perm_b32 v190, v205, v204, s93\n"
"v_mfma_f32_16x16x32_bf16 v[192:195], a[236:239], a[140:143], v[192:195]\n"
"v_exp_f32_e32 v206, v206\n"
"v_mfma_f32_16x16x32_bf16 v[196:199], a[236:239], a[156:159], v[212:215]\n"
"ds_read_b128 a[248:251], v226 offset:24576\n"
"v_mfma_f32_16x16x32_bf16 v[196:199], a[232:235], a[152:155], v[196:199]\n"
"v_exp_f32_e32 v207, v207\n"
"v_mfma_f32_16x16x32_bf16 v[196:199], a[228:231], a[148:151], v[196:199]\n"
"buffer_load_dword a0, v246, s[24:27], s30 offen sc1\n"
"v_mfma_f32_16x16x32_bf16 v[196:199], a[224:227], a[144:147], v[196:199]\n"
"v_exp_f32_e32 v192, v192\n"
"v_mfma_f32_16x16x32_bf16 v[200:203], a[224:227], a[160:163], v[216:219]\n"
"ds_read_b128 a[252:255], v227 offset:24576\n"
"v_perm_b32 v191, v207, v206, s93\n"
"v_mfma_f32_16x16x32_bf16 v[200:203], a[228:231], a[164:167], v[200:203]\n"
"v_exp_f32_e32 v193, v193\n"
"v_mfma_f32_16x16x32_bf16 v[200:203], a[232:235], a[168:171], v[200:203]\n"
"ds_read_b64_tr_b16 a[208:209], v230 offset:8192\n"
"v_perm_b32 v128, v193, v192, s93\n"
"v_mfma_f32_16x16x32_bf16 v[200:203], a[236:239], a[172:175], v[200:203]\n"
"s_add_u32 m0, s46, 0\n"
"v_exp_f32_e32 v194, v194\n"
"v_mfma_f32_16x16x32_bf16 v[204:207], a[236:239], a[188:191], v[220:223]\n"
"s_add_u32 s44, s44, 16384\n"
"ds_read_b64_tr_b16 a[210:211], v230 offset:24576\n"
"v_mfma_f32_16x16x32_bf16 v[204:207], a[232:235], a[184:187], v[204:207]\n"
"v_exp_f32_e32 v195, v195\n"
"v_mfma_f32_16x16x32_bf16 v[204:207], a[228:231], a[180:183], v[204:207]\n"
"buffer_load_dwordx4 v236, s[36:39], s44 offen offset:0 lds\n"
"v_mfma_f32_16x16x32_bf16 v[204:207], a[224:227], a[176:179], v[204:207]\n"
"s_waitcnt lgkmcnt(6)\n"
"v_exp_f32_e32 v196, v196\n"
"v_mfma_f32_16x16x32_bf16 v[0:3], a[192:195], v[160:163], v[0:3]\n"
"ds_read_b64_tr_b16 a[212:213], v230 offset:40960\n"
"v_perm_b32 v129, v195, v194, s93\n"
"v_mfma_f32_16x16x32_bf16 v[0:3], a[196:199], v[164:167], v[0:3]\n"
"v_exp_f32_e32 v197, v197\n"
"v_mfma_f32_16x16x32_bf16 v[4:7], a[196:199], v[172:175], v[4:7]\n"
"ds_read_b64_tr_b16 a[214:215], v230 offset:57344\n"
"v_perm_b32 v136, v197, v196, s93\n"
"v_mfma_f32_16x16x32_bf16 v[4:7], a[192:195], v[168:171], v[4:7]\n"
"v_exp_f32_e32 v198, v198\n"
"v_mfma_f32_16x16x32_bf16 v[8:11], a[192:195], v[176:179], v[8:11]\n"
"ds_read_b64_tr_b16 a[216:217], v231 offset:8192\n"
"v_mfma_f32_16x16x32_bf16 v[8:11], a[196:199], v[180:183], v[8:11]\n"
"v_exp_f32_e32 v199, v199\n"
"v_mfma_f32_16x16x32_bf16 v[12:15], a[196:199], v[188:191], v[12:15]\n"
"buffer_load_dwordx4 v237, s[36:39], s44 offen offset:1024 lds\n"
"v_mfma_f32_16x16x32_bf16 v[12:15], a[192:195], v[184:187], v[12:15]\n"
"v_exp_f32_e32 v200, v200\n"
"v_mfma_f32_16x16x32_bf16 v[28:31], a[200:203], v[184:187], v[28:31]\n"
"ds_read_b64_tr_b16 a[218:219], v231 offset:24576\n"
"v_perm_b32 v137, v199, v198, s93\n"
"v_mfma_f32_16x16x32_bf16 v[28:31], a[204:207], v[188:191], v[28:31]\n"
"v_exp_f32_e32 v201, v201\n"
"v_mfma_f32_16x16x32_bf16 v[24:27], a[204:207], v[180:183], v[24:27]\n"
"ds_read_b64_tr_b16 a[220:221], v231 offset:40960\n"
"v_perm_b32 v144, v201, v200, s93\n"
"v_mfma_f32_16x16x32_bf16 v[24:27], a[200:203], v[176:179], v[24:27]\n"
"v_exp_f32_e32 v202, v202\n"
"v_mfma_f32_16x16x32_bf16 v[20:23], a[200:203], v[168:171], v[20:23]\n"
"ds_read_b64_tr_b16 a[222:223], v231 offset:57344\n"
"v_mfma_f32_16x16x32_bf16 v[20:23], a[204:207], v[172:175], v[20:23]\n"
"v_exp_f32_e32 v203, v203\n"
"v_mfma_f32_16x16x32_bf16 v[16:19], a[204:207], v[164:167], v[16:19]\n"
"buffer_load_dwordx4 v238, s[36:39], s44 offen offset:2048 lds\n"
"v_mfma_f32_16x16x32_bf16 v[16:19], a[200:203], v[160:163], v[16:19]\n"
"v_exp_f32_e32 v204, v204\n"
"v_smfmac_f32_16x16x64_bf16 a[112:115], a[92:95], v[160:167], v244\n"
"s_waitcnt lgkmcnt(8)\n"
"v_perm_b32 v145, v203, v202, s93\n"
"v_mfma_f32_16x16x32_bf16 v[192:195], a[240:243], a[128:131], v[208:211]\n"
"ds_read_b128 a[224:227], v224 offset:40960\n"
"v_mfma_f32_16x16x32_bf16 v[192:195], a[244:247], a[132:135], v[192:195]\n"
"v_exp_f32_e32 v205, v205\n"
"v_mfma_f32_16x16x32_bf16 v[192:195], a[248:251], a[136:139], v[192:195]\n"
"ds_read_b128 a[228:231], v225 offset:40960\n"
"v_perm_b32 v152, v205, v204, s93\n"
"v_mfma_f32_16x16x32_bf16 v[192:195], a[252:255], a[140:143], v[192:195]\n"
"v_exp_f32_e32 v206, v206\n"
"v_mfma_f32_16x16x32_bf16 v[196:199], a[252:255], a[156:159], v[212:215]\n"
"ds_read_b128 a[232:235], v226 offset:40960\n"
"v_mfma_f32_16x16x32_bf16 v[196:199], a[248:251], a[152:155], v[196:199]\n"
"v_exp_f32_e32 v207, v207\n"
"v_mfma_f32_16x16x32_bf16 v[196:199], a[244:247], a[148:151], v[196:199]\n"
"buffer_load_dwordx4 v239, s[36:39], s44 offen offset:3072 lds\n"
"v_mfma_f32_16x16x32_bf16 v[196:199], a[240:243], a[144:147], v[196:199]\n"
"v_exp_f32_e32 v192, v192\n"
"v_mfma_f32_16x16x32_bf16 v[200:203], a[240:243], a[160:163], v[216:219]\n"
"ds_read_b128 a[236:239], v227 offset:40960\n"
"v_perm_b32 v153, v207, v206, s93\n"
"v_mfma_f32_16x16x32_bf16 v[200:203], a[244:247], a[164:167], v[200:203]\n"
"v_exp_f32_e32 v193, v193\n"
"v_mfma_f32_16x16x32_bf16 v[200:203], a[248:251], a[168:171], v[200:203]\n"
"ds_read_b64_tr_b16 a[192:193], v232 offset:8192\n"
"v_perm_b32 v130, v193, v192, s93\n"
"v_mfma_f32_16x16x32_bf16 v[200:203], a[252:255], a[172:175], v[200:203]\n"
"s_add_u32 m0, s46, 65536\n"
"v_exp_f32_e32 v194, v194\n"
"v_mfma_f32_16x16x32_bf16 v[204:207], a[252:255], a[188:191], v[220:223]\n"
"s_add_u32 s45, s45, 16384\n"
"ds_read_b64_tr_b16 a[194:195], v232 offset:24576\n"
"v_mfma_f32_16x16x32_bf16 v[204:207], a[248:251], a[184:187], v[204:207]\n"
"v_exp_f32_e32 v195, v195\n"
"v_mfma_f32_16x16x32_bf16 v[204:207], a[244:247], a[180:183], v[204:207]\n"
"buffer_load_dwordx4 v240, s[40:43], s45 offen offset:0 lds\n"
"v_mfma_f32_16x16x32_bf16 v[204:207], a[240:243], a[176:179], v[204:207]\n"
"s_waitcnt lgkmcnt(6)\n"
"v_exp_f32_e32 v196, v196\n"
"v_mfma_f32_16x16x32_bf16 v[32:35], a[208:211], v[160:163], v[32:35]\n"
"ds_read_b64_tr_b16 a[196:197], v232 offset:40960\n"
"v_perm_b32 v131, v195, v194, s93\n"
"v_mfma_f32_16x16x32_bf16 v[32:35], a[212:215], v[164:167], v[32:35]\n"
"v_exp_f32_e32 v197, v197\n"
"v_mfma_f32_16x16x32_bf16 v[36:39], a[212:215], v[172:175], v[36:39]\n"
"ds_read_b64_tr_b16 a[198:199], v232 offset:57344\n"
"v_perm_b32 v138, v197, v196, s93\n"
"v_mfma_f32_16x16x32_bf16 v[36:39], a[208:211], v[168:171], v[36:39]\n"
"v_exp_f32_e32 v198, v198\n"
"v_mfma_f32_16x16x32_bf16 v[40:43], a[208:211], v[176:179], v[40:43]\n"
"ds_read_b64_tr_b16 a[200:201], v233 offset:8192\n"
"v_mfma_f32_16x16x32_bf16 v[40:43], a[212:215], v[180:183], v[40:43]\n"
"v_exp_f32_e32 v199, v199\n"
"v_mfma_f32_16x16x32_bf16 v[44:47], a[212:215], v[188:191], v[44:47]\n"
"buffer_load_dwordx4 v241, s[40:43], s45 offen offset:1024 lds\n"
"v_mfma_f32_16x16x32_bf16 v[44:47], a[208:211], v[184:187], v[44:47]\n"
"v_exp_f32_e32 v200, v200\n"
"v_mfma_f32_16x16x32_bf16 v[60:63], a[216:219], v[184:187], v[60:63]\n"
"ds_read_b64_tr_b16 a[202:203], v233 offset:24576\n"
"v_perm_b32 v139, v199, v198, s93\n"
"v_mfma_f32_16x16x32_bf16 v[60:63], a[220:223], v[188:191], v[60:63]\n"
"v_exp_f32_e32 v201, v201\n"
"v_mfma_f32_16x16x32_bf16 v[56:59], a[220:223], v[180:183], v[56:59]\n"
"ds_read_b64_tr_b16 a[204:205], v233 offset:40960\n"
"v_perm_b32 v146, v201, v200, s93\n"
"v_mfma_f32_16x16x32_bf16 v[56:59], a[216:219], v[176:179], v[56:59]\n"
"v_exp_f32_e32 v202, v202\n"
"v_mfma_f32_16x16x32_bf16 v[52:55], a[216:219], v[168:171], v[52:55]\n"
"ds_read_b64_tr_b16 a[206:207], v233 offset:57344\n"
"v_mfma_f32_16x16x32_bf16 v[52:55], a[220:223], v[172:175], v[52:55]\n"
"v_exp_f32_e32 v203, v203\n"
"v_mfma_f32_16x16x32_bf16 v[48:51], a[220:223], v[164:167], v[48:51]\n"
"buffer_load_dwordx4 v240, s[40:43], s45 offen offset:2048 lds\n"
"v_mfma_f32_16x16x32_bf16 v[48:51], a[216:219], v[160:163], v[48:51]\n"
"v_exp_f32_e32 v204, v204\n"
"v_smfmac_f32_16x16x64_bf16 a[116:119], a[92:95], v[168:175], v244\n"
"s_waitcnt lgkmcnt(8)\n"
"v_perm_b32 v147, v203, v202, s93\n"
"v_mfma_f32_16x16x32_bf16 v[192:195], a[224:227], a[128:131], v[208:211]\n"
"ds_read_b128 a[240:243], v224 offset:57344\n"
"v_mfma_f32_16x16x32_bf16 v[192:195], a[228:231], a[132:135], v[192:195]\n"
"v_exp_f32_e32 v205, v205\n"
"v_mfma_f32_16x16x32_bf16 v[192:195], a[232:235], a[136:139], v[192:195]\n"
"ds_read_b128 a[244:247], v225 offset:57344\n"
"v_perm_b32 v154, v205, v204, s93\n"
"v_mfma_f32_16x16x32_bf16 v[192:195], a[236:239], a[140:143], v[192:195]\n"
"v_exp_f32_e32 v206, v206\n"
"v_mfma_f32_16x16x32_bf16 v[196:199], a[236:239], a[156:159], v[212:215]\n"
"ds_read_b128 a[248:251], v226 offset:57344\n"
"v_mfma_f32_16x16x32_bf16 v[196:199], a[232:235], a[152:155], v[196:199]\n"
"v_exp_f32_e32 v207, v207\n"
"v_mfma_f32_16x16x32_bf16 v[196:199], a[228:231], a[148:151], v[196:199]\n"
"buffer_load_dwordx4 v241, s[40:43], s45 offen offset:3072 lds\n"
"v_mfma_f32_16x16x32_bf16 v[196:199], a[224:227], a[144:147], v[196:199]\n"
"v_exp_f32_e32 v192, v192\n"
"v_mfma_f32_16x16x32_bf16 v[200:203], a[224:227], a[160:163], v[216:219]\n"
"ds_read_b128 a[252:255], v227 offset:57344\n"
"v_perm_b32 v155, v207, v206, s93\n"
"v_mfma_f32_16x16x32_bf16 v[200:203], a[228:231], a[164:167], v[200:203]\n"
"v_exp_f32_e32 v193, v193\n"
"v_mfma_f32_16x16x32_bf16 v[200:203], a[232:235], a[168:171], v[200:203]\n"
"ds_read_b64_tr_b16 a[208:209], v234 offset:8192\n"
"v_perm_b32 v132, v193, v192, s93\n"
"v_mfma_f32_16x16x32_bf16 v[200:203], a[236:239], a[172:175], v[200:203]\n"
"s_add_u32 m0, s46, 4096\n"
"v_exp_f32_e32 v194, v194\n"
"v_mfma_f32_16x16x32_bf16 v[204:207], a[236:239], a[188:191], v[220:223]\n"
"s_add_u32 s44, s44, 16384\n"
"ds_read_b64_tr_b16 a[210:211], v234 offset:24576\n"
"v_mfma_f32_16x16x32_bf16 v[204:207], a[232:235], a[184:187], v[204:207]\n"
"v_exp_f32_e32 v195, v195\n"
"v_mfma_f32_16x16x32_bf16 v[204:207], a[228:231], a[180:183], v[204:207]\n"
"buffer_load_dwordx4 v236, s[36:39], s44 offen offset:0 lds\n"
"v_mfma_f32_16x16x32_bf16 v[204:207], a[224:227], a[176:179], v[204:207]\n"
"s_waitcnt lgkmcnt(6)\n"
"v_exp_f32_e32 v196, v196\n"
"v_mfma_f32_16x16x32_bf16 v[64:67], a[192:195], v[160:163], v[64:67]\n"
"ds_read_b64_tr_b16 a[212:213], v234 offset:40960\n"
"v_perm_b32 v133, v195, v194, s93\n"
"v_mfma_f32_16x16x32_bf16 v[64:67], a[196:199], v[164:167], v[64:67]\n"
"v_exp_f32_e32 v197, v197\n"
"v_mfma_f32_16x16x32_bf16 v[68:71], a[196:199], v[172:175], v[68:71]\n"
"ds_read_b64_tr_b16 a[214:215], v234 offset:57344\n"
"v_perm_b32 v140, v197, v196, s93\n"
"v_mfma_f32_16x16x32_bf16 v[68:71], a[192:195], v[168:171], v[68:71]\n"
"v_exp_f32_e32 v198, v198\n"
"v_mfma_f32_16x16x32_bf16 v[72:75], a[192:195], v[176:179], v[72:75]\n"
"ds_read_b64_tr_b16 a[216:217], v235 offset:8192\n"
"v_mfma_f32_16x16x32_bf16 v[72:75], a[196:199], v[180:183], v[72:75]\n"
"v_exp_f32_e32 v199, v199\n"
"v_mfma_f32_16x16x32_bf16 v[76:79], a[196:199], v[188:191], v[76:79]\n"
"buffer_load_dwordx4 v237, s[36:39], s44 offen offset:1024 lds\n"
"v_mfma_f32_16x16x32_bf16 v[76:79], a[192:195], v[184:187], v[76:79]\n"
"v_exp_f32_e32 v200, v200\n"
"v_mfma_f32_16x16x32_bf16 v[92:95], a[200:203], v[184:187], v[92:95]\n"
"ds_read_b64_tr_b16 a[218:219], v235 offset:24576\n"
"v_perm_b32 v141, v199, v198, s93\n"
"v_mfma_f32_16x16x32_bf16 v[92:95], a[204:207], v[188:191], v[92:95]\n"
"v_exp_f32_e32 v201, v201\n"
"v_mfma_f32_16x16x32_bf16 v[88:91], a[204:207], v[180:183], v[88:91]\n"
"ds_read_b64_tr_b16 a[220:221], v235 offset:40960\n"
"v_perm_b32 v148, v201, v200, s93\n"
"v_mfma_f32_16x16x32_bf16 v[88:91], a[200:203], v[176:179], v[88:91]\n"
"v_exp_f32_e32 v202, v202\n"
"v_mfma_f32_16x16x32_bf16 v[84:87], a[200:203], v[168:171], v[84:87]\n"
"ds_read_b64_tr_b16 a[222:223], v235 offset:57344\n"
"v_mfma_f32_16x16x32_bf16 v[84:87], a[204:207], v[172:175], v[84:87]\n"
"v_exp_f32_e32 v203, v203\n"
"v_mfma_f32_16x16x32_bf16 v[80:83], a[204:207], v[164:167], v[80:83]\n"
"v_perm_b32 v149, v203, v202, s93\n"
"v_mfma_f32_16x16x32_bf16 v[80:83], a[200:203], v[160:163], v[80:83]\n"
"v_exp_f32_e32 v204, v204\n"
"v_smfmac_f32_16x16x64_bf16 a[120:123], a[92:95], v[176:183], v244\n"
"s_waitcnt lgkmcnt(8)\n"
"v_mfma_f32_16x16x32_bf16 v[192:195], a[240:243], a[128:131], v[208:211]\n"
"ds_read_b128 a[224:227], v224 offset:12288\n"
"v_mfma_f32_16x16x32_bf16 v[192:195], a[244:247], a[132:135], v[192:195]\n"
"v_exp_f32_e32 v205, v205\n"
"v_mfma_f32_16x16x32_bf16 v[192:195], a[248:251], a[136:139], v[192:195]\n"
"ds_read_b128 a[228:231], v225 offset:12288\n"
"v_perm_b32 v156, v205, v204, s93\n"
"v_mfma_f32_16x16x32_bf16 v[192:195], a[252:255], a[140:143], v[192:195]\n"
"v_exp_f32_e32 v206, v206\n"
"v_mfma_f32_16x16x32_bf16 v[196:199], a[252:255], a[156:159], v[212:215]\n"
"ds_read_b128 a[232:235], v226 offset:12288\n"
"v_mfma_f32_16x16x32_bf16 v[196:199], a[248:251], a[152:155], v[196:199]\n"
"v_exp_f32_e32 v207, v207\n"
"v_mfma_f32_16x16x32_bf16 v[196:199], a[244:247], a[148:151], v[196:199]\n"
"buffer_load_dwordx4 v238, s[36:39], s44 offen offset:2048 lds\n"
"v_mfma_f32_16x16x32_bf16 v[196:199], a[240:243], a[144:147], v[196:199]\n"
"v_exp_f32_e32 v192, v192\n"
"v_mfma_f32_16x16x32_bf16 v[200:203], a[240:243], a[160:163], v[216:219]\n"
"ds_read_b128 a[236:239], v227 offset:12288\n"
"v_perm_b32 v157, v207, v206, s93\n"
"v_mfma_f32_16x16x32_bf16 v[200:203], a[244:247], a[164:167], v[200:203]\n"
"v_exp_f32_e32 v193, v193\n"
"v_mfma_f32_16x16x32_bf16 v[200:203], a[248:251], a[168:171], v[200:203]\n"
"ds_read_b64_tr_b16 a[192:193], v228 offset:12288\n"
"v_perm_b32 v134, v193, v192, s93\n"
"v_mfma_f32_16x16x32_bf16 v[200:203], a[252:255], a[172:175], v[200:203]\n"
"v_exp_f32_e32 v194, v194\n"
"v_mfma_f32_16x16x32_bf16 v[204:207], a[252:255], a[188:191], v[220:223]\n"
"ds_read_b64_tr_b16 a[194:195], v228 offset:28672\n"
"v_mfma_f32_16x16x32_bf16 v[204:207], a[248:251], a[184:187], v[204:207]\n"
"v_exp_f32_e32 v195, v195\n"
"v_mfma_f32_16x16x32_bf16 v[204:207], a[244:247], a[180:183], v[204:207]\n"
"buffer_load_dwordx4 v239, s[36:39], s44 offen offset:3072 lds\n"
"v_mfma_f32_16x16x32_bf16 v[204:207], a[240:243], a[176:179], v[204:207]\n"
"s_waitcnt lgkmcnt(6)\n"
"v_exp_f32_e32 v196, v196\n"
"v_mfma_f32_16x16x32_bf16 v[96:99], a[208:211], v[160:163], v[96:99]\n"
"ds_read_b64_tr_b16 a[196:197], v228 offset:45056\n"
"v_perm_b32 v135, v195, v194, s93\n"
"v_mfma_f32_16x16x32_bf16 v[96:99], a[212:215], v[164:167], v[96:99]\n"
"v_exp_f32_e32 v197, v197\n"
"v_mfma_f32_16x16x32_bf16 v[100:103], a[212:215], v[172:175], v[100:103]\n"
"ds_read_b64_tr_b16 a[198:199], v228 offset:61440\n"
"v_perm_b32 v142, v197, v196, s93\n"
"v_mfma_f32_16x16x32_bf16 v[100:103], a[208:211], v[168:171], v[100:103]\n"
"s_add_u32 m0, s46, 69632\n"
"v_exp_f32_e32 v198, v198\n"
"v_mfma_f32_16x16x32_bf16 v[104:107], a[208:211], v[176:179], v[104:107]\n"
"s_add_u32 s45, s45, 16384\n"
"ds_read_b64_tr_b16 a[200:201], v229 offset:12288\n"
"v_mfma_f32_16x16x32_bf16 v[104:107], a[212:215], v[180:183], v[104:107]\n"
"v_exp_f32_e32 v199, v199\n"
"v_mfma_f32_16x16x32_bf16 v[108:111], a[212:215], v[188:191], v[108:111]\n"
"buffer_load_dwordx4 v240, s[40:43], s45 offen offset:0 lds\n"
"v_mfma_f32_16x16x32_bf16 v[108:111], a[208:211], v[184:187], v[108:111]\n"
"v_exp_f32_e32 v200, v200\n"
"v_mfma_f32_16x16x32_bf16 v[124:127], a[216:219], v[184:187], v[124:127]\n"
"ds_read_b64_tr_b16 a[202:203], v229 offset:28672\n"
"v_perm_b32 v143, v199, v198, s93\n"
"v_mfma_f32_16x16x32_bf16 v[124:127], a[220:223], v[188:191], v[124:127]\n"
"v_exp_f32_e32 v201, v201\n"
"v_mfma_f32_16x16x32_bf16 v[120:123], a[220:223], v[180:183], v[120:123]\n"
"ds_read_b64_tr_b16 a[204:205], v229 offset:45056\n"
"v_perm_b32 v150, v201, v200, s93\n"
"v_mfma_f32_16x16x32_bf16 v[120:123], a[216:219], v[176:179], v[120:123]\n"
"v_exp_f32_e32 v202, v202\n"
"v_mfma_f32_16x16x32_bf16 v[116:119], a[216:219], v[168:171], v[116:119]\n"
"ds_read_b64_tr_b16 a[206:207], v229 offset:61440\n"
"v_mfma_f32_16x16x32_bf16 v[116:119], a[220:223], v[172:175], v[116:119]\n"
"v_exp_f32_e32 v203, v203\n"
"v_mfma_f32_16x16x32_bf16 v[112:115], a[220:223], v[164:167], v[112:115]\n"
"buffer_load_dwordx4 v241, s[40:43], s45 offen offset:1024 lds\n"
"v_mfma_f32_16x16x32_bf16 v[112:115], a[216:219], v[160:163], v[112:115]\n"
"v_exp_f32_e32 v204, v204\n"
"v_smfmac_f32_16x16x64_bf16 a[124:127], a[92:95], v[184:191], v244\n"
"s_waitcnt lgkmcnt(8)\n"
"v_perm_b32 v151, v203, v202, s93\n"
"v_mfma_f32_16x16x32_bf16 v[192:195], a[224:227], a[128:131], v[208:211]\n"
"ds_read_b128 a[240:243], v224 offset:28672\n"
"v_mfma_f32_16x16x32_bf16 v[192:195], a[228:231], a[132:135], v[192:195]\n"
"v_exp_f32_e32 v205, v205\n"
"v_mfma_f32_16x16x32_bf16 v[192:195], a[232:235], a[136:139], v[192:195]\n"
"ds_read_b128 a[244:247], v225 offset:28672\n"
"v_perm_b32 v158, v205, v204, s93\n"
"v_mfma_f32_16x16x32_bf16 v[192:195], a[236:239], a[140:143], v[192:195]\n"
"v_exp_f32_e32 v206, v206\n"
"v_mfma_f32_16x16x32_bf16 v[196:199], a[236:239], a[156:159], v[212:215]\n"
"ds_read_b128 a[248:251], v226 offset:28672\n"
"v_mfma_f32_16x16x32_bf16 v[196:199], a[232:235], a[152:155], v[196:199]\n"
"v_exp_f32_e32 v207, v207\n"
"v_mfma_f32_16x16x32_bf16 v[196:199], a[228:231], a[148:151], v[196:199]\n"
"buffer_load_dwordx4 v240, s[40:43], s45 offen offset:2048 lds\n"
"v_mfma_f32_16x16x32_bf16 v[196:199], a[224:227], a[144:147], v[196:199]\n"
"v_exp_f32_e32 v192, v192\n"
"v_mfma_f32_16x16x32_bf16 v[200:203], a[224:227], a[160:163], v[216:219]\n"
"ds_read_b128 a[252:255], v227 offset:28672\n"
"v_perm_b32 v159, v207, v206, s93\n"
"v_mfma_f32_16x16x32_bf16 v[200:203], a[228:231], a[164:167], v[200:203]\n"
"v_exp_f32_e32 v193, v193\n"
"v_mfma_f32_16x16x32_bf16 v[200:203], a[232:235], a[168:171], v[200:203]\n"
"ds_read_b64_tr_b16 a[208:209], v230 offset:12288\n"
"v_perm_b32 v160, v193, v192, s93\n"
"v_mfma_f32_16x16x32_bf16 v[200:203], a[236:239], a[172:175], v[200:203]\n"
"v_exp_f32_e32 v194, v194\n"
"v_mfma_f32_16x16x32_bf16 v[204:207], a[236:239], a[188:191], v[220:223]\n"
"ds_read_b64_tr_b16 a[210:211], v230 offset:28672\n"
"v_mfma_f32_16x16x32_bf16 v[204:207], a[232:235], a[184:187], v[204:207]\n"
"v_exp_f32_e32 v195, v195\n"
"v_mfma_f32_16x16x32_bf16 v[204:207], a[228:231], a[180:183], v[204:207]\n"
"v_perm_b32 v161, v195, v194, s93\n"
"v_mfma_f32_16x16x32_bf16 v[204:207], a[224:227], a[176:179], v[204:207]\n"
"s_waitcnt lgkmcnt(6)\n"
"v_exp_f32_e32 v196, v196\n"
"v_mfma_f32_16x16x32_bf16 v[0:3], a[192:195], v[128:131], v[0:3]\n"
"ds_read_b64_tr_b16 a[212:213], v230 offset:45056\n"
"v_mfma_f32_16x16x32_bf16 v[0:3], a[196:199], v[132:135], v[0:3]\n"
"v_exp_f32_e32 v197, v197\n"
"v_mfma_f32_16x16x32_bf16 v[4:7], a[196:199], v[140:143], v[4:7]\n"
"ds_read_b64_tr_b16 a[214:215], v230 offset:61440\n"
"v_perm_b32 v168, v197, v196, s93\n"
"v_mfma_f32_16x16x32_bf16 v[4:7], a[192:195], v[136:139], v[4:7]\n"
"v_exp_f32_e32 v198, v198\n"
"v_mfma_f32_16x16x32_bf16 v[8:11], a[192:195], v[144:147], v[8:11]\n"
"ds_read_b64_tr_b16 a[216:217], v231 offset:12288\n"
"v_mfma_f32_16x16x32_bf16 v[8:11], a[196:199], v[148:151], v[8:11]\n"
"v_exp_f32_e32 v199, v199\n"
"v_mfma_f32_16x16x32_bf16 v[12:15], a[196:199], v[156:159], v[12:15]\n"
"buffer_load_dwordx4 v241, s[40:43], s45 offen offset:3072 lds\n"
"v_mfma_f32_16x16x32_bf16 v[12:15], a[192:195], v[152:155], v[12:15]\n"
"v_exp_f32_e32 v200, v200\n"
"v_mfma_f32_16x16x32_bf16 v[28:31], a[200:203], v[152:155], v[28:31]\n"
"ds_read_b64_tr_b16 a[218:219], v231 offset:28672\n"
"v_perm_b32 v169, v199, v198, s93\n"
"v_mfma_f32_16x16x32_bf16 v[28:31], a[204:207], v[156:159], v[28:31]\n"
"v_exp_f32_e32 v201, v201\n"
"v_mfma_f32_16x16x32_bf16 v[24:27], a[204:207], v[148:151], v[24:27]\n"
"ds_read_b64_tr_b16 a[220:221], v231 offset:45056\n"
"v_perm_b32 v176, v201, v200, s93\n"
"v_mfma_f32_16x16x32_bf16 v[24:27], a[200:203], v[144:147], v[24:27]\n"
"v_exp_f32_e32 v202, v202\n"
"v_mfma_f32_16x16x32_bf16 v[20:23], a[200:203], v[136:139], v[20:23]\n"
"ds_read_b64_tr_b16 a[222:223], v231 offset:61440\n"
"v_mfma_f32_16x16x32_bf16 v[20:23], a[204:207], v[140:143], v[20:23]\n"
"v_exp_f32_e32 v203, v203\n"
"v_mfma_f32_16x16x32_bf16 v[16:19], a[204:207], v[132:135], v[16:19]\n"
"v_perm_b32 v177, v203, v202, s93\n"
"v_mfma_f32_16x16x32_bf16 v[16:19], a[200:203], v[128:131], v[16:19]\n"
"v_exp_f32_e32 v204, v204\n"
"v_smfmac_f32_16x16x64_bf16 a[112:115], a[92:95], v[128:135], v244\n"
"s_waitcnt lgkmcnt(8)\n"
"v_mfma_f32_16x16x32_bf16 v[192:195], a[240:243], a[128:131], v[208:211]\n"
"ds_read_b128 a[224:227], v224 offset:45056\n"
"v_mfma_f32_16x16x32_bf16 v[192:195], a[244:247], a[132:135], v[192:195]\n"
"v_exp_f32_e32 v205, v205\n"
"v_mfma_f32_16x16x32_bf16 v[192:195], a[248:251], a[136:139], v[192:195]\n"
"ds_read_b128 a[228:231], v225 offset:45056\n"
"v_perm_b32 v184, v205, v204, s93\n"
"v_mfma_f32_16x16x32_bf16 v[192:195], a[252:255], a[140:143], v[192:195]\n"
"v_exp_f32_e32 v206, v206\n"
"v_mfma_f32_16x16x32_bf16 v[196:199], a[252:255], a[156:159], v[212:215]\n"
"ds_read_b128 a[232:235], v226 offset:45056\n"
"v_mfma_f32_16x16x32_bf16 v[196:199], a[248:251], a[152:155], v[196:199]\n"
"v_exp_f32_e32 v207, v207\n"
"v_mfma_f32_16x16x32_bf16 v[196:199], a[244:247], a[148:151], v[196:199]\n"
"v_perm_b32 v185, v207, v206, s93\n"
"v_mfma_f32_16x16x32_bf16 v[196:199], a[240:243], a[144:147], v[196:199]\n"
"v_exp_f32_e32 v192, v192\n"
"v_mfma_f32_16x16x32_bf16 v[200:203], a[240:243], a[160:163], v[216:219]\n"
"ds_read_b128 a[236:239], v227 offset:45056\n"
"v_mfma_f32_16x16x32_bf16 v[200:203], a[244:247], a[164:167], v[200:203]\n"
"v_exp_f32_e32 v193, v193\n"
"v_mfma_f32_16x16x32_bf16 v[200:203], a[248:251], a[168:171], v[200:203]\n"
"ds_read_b64_tr_b16 a[192:193], v232 offset:12288\n"
"v_perm_b32 v162, v193, v192, s93\n"
"v_mfma_f32_16x16x32_bf16 v[200:203], a[252:255], a[172:175], v[200:203]\n"
"v_exp_f32_e32 v194, v194\n"
"v_mfma_f32_16x16x32_bf16 v[204:207], a[252:255], a[188:191], v[220:223]\n"
"ds_read_b64_tr_b16 a[194:195], v232 offset:28672\n"
"v_mfma_f32_16x16x32_bf16 v[204:207], a[248:251], a[184:187], v[204:207]\n"
"v_exp_f32_e32 v195, v195\n"
"v_mfma_f32_16x16x32_bf16 v[204:207], a[244:247], a[180:183], v[204:207]\n"
"v_perm_b32 v163, v195, v194, s93\n"
"v_mfma_f32_16x16x32_bf16 v[204:207], a[240:243], a[176:179], v[204:207]\n"
"s_waitcnt lgkmcnt(6)\n"
"v_exp_f32_e32 v196, v196\n"
"v_mfma_f32_16x16x32_bf16 v[32:35], a[208:211], v[128:131], v[32:35]\n"
"ds_read_b64_tr_b16 a[196:197], v232 offset:45056\n"
"v_mfma_f32_16x16x32_bf16 v[32:35], a[212:215], v[132:135], v[32:35]\n"
"v_exp_f32_e32 v197, v197\n"
"v_mfma_f32_16x16x32_bf16 v[36:39], a[212:215], v[140:143], v[36:39]\n"
"ds_read_b64_tr_b16 a[198:199], v232 offset:61440\n"
"v_perm_b32 v170, v197, v196, s93\n"
"v_mfma_f32_16x16x32_bf16 v[36:39], a[208:211], v[136:139], v[36:39]\n"
"v_exp_f32_e32 v198, v198\n"
"v_mfma_f32_16x16x32_bf16 v[40:43], a[208:211], v[144:147], v[40:43]\n"
"ds_read_b64_tr_b16 a[200:201], v233 offset:12288\n"
"v_mfma_f32_16x16x32_bf16 v[40:43], a[212:215], v[148:151], v[40:43]\n"
"v_exp_f32_e32 v199, v199\n"
"v_mfma_f32_16x16x32_bf16 v[44:47], a[212:215], v[156:159], v[44:47]\n"
"v_perm_b32 v171, v199, v198, s93\n"
"v_mfma_f32_16x16x32_bf16 v[44:47], a[208:211], v[152:155], v[44:47]\n"
"v_exp_f32_e32 v200, v200\n"
"v_mfma_f32_16x16x32_bf16 v[60:63], a[216:219], v[152:155], v[60:63]\n"
"ds_read_b64_tr_b16 a[202:203], v233 offset:28672\n"
"v_mfma_f32_16x16x32_bf16 v[60:63], a[220:223], v[156:159], v[60:63]\n"
"v_exp_f32_e32 v201, v201\n"
"v_mfma_f32_16x16x32_bf16 v[56:59], a[220:223], v[148:151], v[56:59]\n"
"ds_read_b64_tr_b16 a[204:205], v233 offset:45056\n"
"v_perm_b32 v178, v201, v200, s93\n"
"v_mfma_f32_16x16x32_bf16 v[56:59], a[216:219], v[144:147], v[56:59]\n"
"v_exp_f32_e32 v202, v202\n"
"v_mfma_f32_16x16x32_bf16 v[52:55], a[216:219], v[136:139], v[52:55]\n"
"ds_read_b64_tr_b16 a[206:207], v233 offset:61440\n"
"v_mfma_f32_16x16x32_bf16 v[52:55], a[220:223], v[140:143], v[52:55]\n"
"v_exp_f32_e32 v203, v203\n"
"v_mfma_f32_16x16x32_bf16 v[48:51], a[220:223], v[132:135], v[48:51]\n"
"v_perm_b32 v179, v203, v202, s93\n"
"v_mfma_f32_16x16x32_bf16 v[48:51], a[216:219], v[128:131], v[48:51]\n"
"v_exp_f32_e32 v204, v204\n"
"v_smfmac_f32_16x16x64_bf16 a[116:119], a[92:95], v[136:143], v244\n"
"s_waitcnt lgkmcnt(8)\n"
"v_mfma_f32_16x16x32_bf16 v[192:195], a[224:227], a[128:131], v[208:211]\n"
"ds_read_b128 a[240:243], v224 offset:61440\n"
"v_mfma_f32_16x16x32_bf16 v[192:195], a[228:231], a[132:135], v[192:195]\n"
"v_exp_f32_e32 v205, v205\n"
"v_mfma_f32_16x16x32_bf16 v[192:195], a[232:235], a[136:139], v[192:195]\n"
"ds_read_b128 a[244:247], v225 offset:61440\n"
"v_perm_b32 v186, v205, v204, s93\n"
"v_mfma_f32_16x16x32_bf16 v[192:195], a[236:239], a[140:143], v[192:195]\n"
"v_exp_f32_e32 v206, v206\n"
"v_mfma_f32_16x16x32_bf16 v[196:199], a[236:239], a[156:159], v[212:215]\n"
"ds_read_b128 a[248:251], v226 offset:61440\n"
"v_mfma_f32_16x16x32_bf16 v[196:199], a[232:235], a[152:155], v[196:199]\n"
"v_exp_f32_e32 v207, v207\n"
"v_mfma_f32_16x16x32_bf16 v[196:199], a[228:231], a[148:151], v[196:199]\n"
"v_perm_b32 v187, v207, v206, s93\n"
"v_mfma_f32_16x16x32_bf16 v[196:199], a[224:227], a[144:147], v[196:199]\n"
"v_exp_f32_e32 v192, v192\n"
"v_mfma_f32_16x16x32_bf16 v[200:203], a[224:227], a[160:163], v[216:219]\n"
"ds_read_b128 a[252:255], v227 offset:61440\n"
"v_mfma_f32_16x16x32_bf16 v[200:203], a[228:231], a[164:167], v[200:203]\n"
"v_exp_f32_e32 v193, v193\n"
"v_mfma_f32_16x16x32_bf16 v[200:203], a[232:235], a[168:171], v[200:203]\n"
"ds_read_b64_tr_b16 a[208:209], v234 offset:12288\n"
"v_perm_b32 v164, v193, v192, s93\n"
"v_mfma_f32_16x16x32_bf16 v[200:203], a[236:239], a[172:175], v[200:203]\n"
"v_exp_f32_e32 v194, v194\n"
"v_mfma_f32_16x16x32_bf16 v[204:207], a[236:239], a[188:191], v[220:223]\n"
"ds_read_b64_tr_b16 a[210:211], v234 offset:28672\n"
"v_mfma_f32_16x16x32_bf16 v[204:207], a[232:235], a[184:187], v[204:207]\n"
"v_exp_f32_e32 v195, v195\n"
"v_mfma_f32_16x16x32_bf16 v[204:207], a[228:231], a[180:183], v[204:207]\n"
".LPG10_%=:\n"
"s_cbranch_execz .LPGR_%=\n"
"v_perm_b32 v165, v195, v194, s93\n"
"v_mfma_f32_16x16x32_bf16 v[204:207], a[224:227], a[176:179], v[204:207]\n"
"s_waitcnt lgkmcnt(6)\n"
"v_exp_f32_e32 v196, v196\n"
"v_mfma_f32_16x16x32_bf16 v[64:67], a[192:195], v[128:131], v[64:67]\n"
"ds_read_b64_tr_b16 a[212:213], v234 offset:45056\n"
"v_mfma_f32_16x16x32_bf16 v[64:67], a[196:199], v[132:135], v[64:67]\n"
"v_exp_f32_e32 v197, v197\n"
"v_mfma_f32_16x16x32_bf16 v[68:71], a[196:199], v[140:143], v[68:71]\n"
"ds_read_b64_tr_b16 a[214:215], v234 offset:61440\n"
"v_perm_b32 v172, v197, v196, s93\n"
"v_mfma_f32_16x16x32_bf16 v[68:71], a[192:195], v[136:139], v[68:71]\n"
"v_exp_f32_e32 v198, v198\n"
"v_mfma_f32_16x16x32_bf16 v[72:75], a[192:195], v[144:147], v[72:75]\n"
"ds_read_b64_tr_b16 a[216:217], v235 offset:12288\n"
"v_mfma_f32_16x16x32_bf16 v[72:75], a[196:199], v[148:151], v[72:75]\n"
"v_exp_f32_e32 v199, v199\n"
"v_mfma_f32_16x16x32_bf16 v[76:79], a[196:199], v[156:159], v[76:79]\n"
"v_perm_b32 v173, v199, v198, s93\n"
"v_mfma_f32_16x16x32_bf16 v[76:79], a[192:195], v[152:155], v[76:79]\n"
"v_exp_f32_e32 v200, v200\n"
"v_mfma_f32_16x16x32_bf16 v[92:95], a[200:203], v[152:155], v[92:95]\n"
"ds_read_b64_tr_b16 a[218:219], v235 offset:28672\n"
"v_mfma_f32_16x16x32_bf16 v[92:95], a[204:207], v[156:159], v[92:95]\n"
"v_exp_f32_e32 v201, v201\n"
"v_mfma_f32_16x16x32_bf16 v[88:91], a[204:207], v[148:151], v[88:91]\n"
"ds_read_b64_tr_b16 a[220:221], v235 offset:45056\n"
"v_perm_b32 v180, v201, v200, s93\n"
"v_mfma_f32_16x16x32_bf16 v[88:91], a[200:203], v[144:147], v[88:91]\n"
"v_exp_f32_e32 v202, v202\n"
"v_mfma_f32_16x16x32_bf16 v[84:87], a[200:203], v[136:139], v[84:87]\n"
"ds_read_b64_tr_b16 a[222:223], v235 offset:61440\n"
"v_mfma_f32_16x16x32_bf16 v[84:87], a[204:207], v[140:143], v[84:87]\n"
"v_exp_f32_e32 v203, v203\n"
"v_mfma_f32_16x16x32_bf16 v[80:83], a[204:207], v[132:135], v[80:83]\n"
"v_perm_b32 v181, v203, v202, s93\n"
"v_mfma_f32_16x16x32_bf16 v[80:83], a[200:203], v[128:131], v[80:83]\n"
"s_waitcnt vmcnt(0) lgkmcnt(0)\n"
"v_exp_f32_e32 v204, v204\n"
"v_smfmac_f32_16x16x64_bf16 a[120:123], a[92:95], v[144:151], v244\n"
"v_mfma_f32_16x16x32_bf16 v[192:195], a[240:243], a[128:131], v[208:211]\n"
"s_barrier\n"
"ds_read_b128 a[224:227], v224 offset:0\n"
"v_mfma_f32_16x16x32_bf16 v[192:195], a[244:247], a[132:135], v[192:195]\n"
"v_exp_f32_e32 v205, v205\n"
"v_mfma_f32_16x16x32_bf16 v[192:195], a[248:251], a[136:139], v[192:195]\n"
"ds_read_b128 a[228:231], v225 offset:0\n"
"v_perm_b32 v188, v205, v204, s93\n"
"v_mfma_f32_16x16x32_bf16 v[192:195], a[252:255], a[140:143], v[192:195]\n"
"s_add_u32 s76, s76, 65536\n"
"v_exp_f32_e32 v206, v206\n"
"v_mfma_f32_16x16x32_bf16 v[196:199], a[252:255], a[156:159], v[212:215]\n"
"ds_read_b128 a[232:235], v226 offset:0\n"
"v_mfma_f32_16x16x32_bf16 v[196:199], a[248:251], a[152:155], v[196:199]\n"
"v_exp_f32_e32 v207, v207\n"
"v_mfma_f32_16x16x32_bf16 v[196:199], a[244:247], a[148:151], v[196:199]\n"
"buffer_load_dword a8, v248, s[72:75], s76 offen\n"
"v_mfma_f32_16x16x32_bf16 v[196:199], a[240:243], a[144:147], v[196:199]\n"
"v_exp_f32_e32 v192, v192\n"
"v_mfma_f32_16x16x32_bf16 v[200:203], a[240:243], a[160:163], v[216:219]\n"
"ds_read_b128 a[236:239], v227 offset:0\n"
"v_perm_b32 v189, v207, v206, s93\n"
"v_mfma_f32_16x16x32_bf16 v[200:203], a[244:247], a[164:167], v[200:203]\n"
"v_exp_f32_e32 v193, v193\n"
"v_mfma_f32_16x16x32_bf16 v[200:203], a[248:251], a[168:171], v[200:203]\n"
"ds_read_b64_tr_b16 a[192:193], v228 offset:0\n"
"v_perm_b32 v166, v193, v192, s93\n"
"v_mfma_f32_16x16x32_bf16 v[200:203], a[252:255], a[172:175], v[200:203]\n"
"s_sub_u32 s58, s47, 2\n"
"v_exp_f32_e32 v194, v194\n"
"v_mfma_f32_16x16x32_bf16 v[204:207], a[252:255], a[188:191], v[220:223]\n"
"s_and_b32 s58, s58, 0x4000000\n"
"ds_read_b64_tr_b16 a[194:195], v228 offset:16384\n"
"v_mfma_f32_16x16x32_bf16 v[204:207], a[248:251], a[184:187], v[204:207]\n"
"v_exp_f32_e32 v195, v195\n"
"v_mfma_f32_16x16x32_bf16 v[204:207], a[244:247], a[180:183], v[204:207]\n"
"buffer_store_dword a0, v247, s[56:59], s29 offen sc1\n"
"v_mfma_f32_16x16x32_bf16 v[204:207], a[240:243], a[176:179], v[204:207]\n"
"v_exp_f32_e32 v196, v196\n"
"v_mfma_f32_16x16x32_bf16 v[96:99], a[208:211], v[128:131], v[96:99]\n"
"ds_read_b64_tr_b16 a[196:197], v228 offset:32768\n"
"v_perm_b32 v167, v195, v194, s93\n"
"v_mfma_f32_16x16x32_bf16 v[96:99], a[212:215], v[132:135], v[96:99]\n"
"v_exp_f32_e32 v197, v197\n"
"v_mfma_f32_16x16x32_bf16 v[100:103], a[212:215], v[140:143], v[100:103]\n"
"ds_read_b64_tr_b16 a[198:199], v228 offset:49152\n"
"v_perm_b32 v174, v197, v196, s93\n"
"v_mfma_f32_16x16x32_bf16 v[100:103], a[208:211], v[136:139], v[100:103]\n"
"s_add_u32 s34, s34, 1024\n"
"v_exp_f32_e32 v198, v198\n"
"v_mfma_f32_16x16x32_bf16 v[104:107], a[208:211], v[144:147], v[104:107]\n"
"ds_read_b64_tr_b16 a[200:201], v229 offset:0\n"
"v_mfma_f32_16x16x32_bf16 v[104:107], a[212:215], v[148:151], v[104:107]\n"
"v_exp_f32_e32 v199, v199\n"
"v_mfma_f32_16x16x32_bf16 v[108:111], a[212:215], v[156:159], v[108:111]\n"
"buffer_store_dwordx4 a[4:7], v255, s[52:55], s34 offen sc1\n"
"v_mfma_f32_16x16x32_bf16 v[108:111], a[208:211], v[152:155], v[108:111]\n"
"v_exp_f32_e32 v200, v200\n"
"v_mfma_f32_16x16x32_bf16 v[124:127], a[216:219], v[152:155], v[124:127]\n"
"ds_read_b64_tr_b16 a[202:203], v229 offset:16384\n"
"v_perm_b32 v175, v199, v198, s93\n"
"v_mfma_f32_16x16x32_bf16 v[124:127], a[220:223], v[156:159], v[124:127]\n"
"v_exp_f32_e32 v201, v201\n"
"v_mfma_f32_16x16x32_bf16 v[120:123], a[220:223], v[148:151], v[120:123]\n"
"ds_read_b64_tr_b16 a[204:205], v229 offset:32768\n"
"v_perm_b32 v182, v201, v200, s93\n"
"v_mfma_f32_16x16x32_bf16 v[120:123], a[216:219], v[144:147], v[120:123]\n"
"v_exp_f32_e32 v202, v202\n"
"v_mfma_f32_16x16x32_bf16 v[116:119], a[216:219], v[136:139], v[116:119]\n"
"ds_read_b64_tr_b16 a[206:207], v229 offset:49152\n"
"v_mfma_f32_16x16x32_bf16 v[116:119], a[220:223], v[140:143], v[116:119]\n"
"v_exp_f32_e32 v203, v203\n"
"v_mfma_f32_16x16x32_bf16 v[112:115], a[220:223], v[132:135], v[112:115]\n"
"v_perm_b32 v183, v203, v202, s93\n"
"v_mfma_f32_16x16x32_bf16 v[112:115], a[216:219], v[128:131], v[112:115]\n"
"v_exp_f32_e32 v204, v204\n"
"v_smfmac_f32_16x16x64_bf16 a[124:127], a[92:95], v[152:159], v244\n"
"s_waitcnt lgkmcnt(8)\n"
"v_mfma_f32_16x16x32_bf16 v[192:195], a[224:227], a[128:131], v[208:211]\n"
"ds_read_b128 a[240:243], v224 offset:16384\n"
"v_mfma_f32_16x16x32_bf16 v[192:195], a[228:231], a[132:135], v[192:195]\n"
"v_exp_f32_e32 v205, v205\n"
"v_mfma_f32_16x16x32_bf16 v[192:195], a[232:235], a[136:139], v[192:195]\n"
"ds_read_b128 a[244:247], v225 offset:16384\n"
"v_perm_b32 v190, v205, v204, s93\n"
"v_mfma_f32_16x16x32_bf16 v[192:195], a[236:239], a[140:143], v[192:195]\n"
"s_add_u32 m0, s46, 8192\n"
"v_exp_f32_e32 v206, v206\n"
"v_mfma_f32_16x16x32_bf16 v[196:199], a[236:239], a[156:159], v[212:215]\n"
"s_add_u32 s44, s44, 16384\n"
"ds_read_b128 a[248:251], v226 offset:16384\n"
"v_mfma_f32_16x16x32_bf16 v[196:199], a[232:235], a[152:155], v[196:199]\n"
"v_exp_f32_e32 v207, v207\n"
"v_mfma_f32_16x16x32_bf16 v[196:199], a[228:231], a[148:151], v[196:199]\n"
"buffer_load_dwordx4 v236, s[36:39], s44 offen offset:0 lds\n"
"v_mfma_f32_16x16x32_bf16 v[196:199], a[224:227], a[144:147], v[196:199]\n"
"v_exp_f32_e32 v192, v192\n"
"v_mfma_f32_16x16x32_bf16 v[200:203], a[224:227], a[160:163], v[216:219]\n"
"ds_read_b128 a[252:255], v227 offset:16384\n"
"v_perm_b32 v191, v207, v206, s93\n"
"v_mfma_f32_16x16x32_bf16 v[200:203], a[228:231], a[164:167], v[200:203]\n"
"v_exp_f32_e32 v193, v193\n"
"v_mfma_f32_16x16x32_bf16 v[200:203], a[232:235], a[168:171], v[200:203]\n"
"ds_read_b64_tr_b16 a[208:209], v230 offset:0\n"
"v_perm_b32 v128, v193, v192, s93\n"
"v_mfma_f32_16x16x32_bf16 v[200:203], a[236:239], a[172:175], v[200:203]\n"
"v_exp_f32_e32 v194, v194\n"
"v_mfma_f32_16x16x32_bf16 v[204:207], a[236:239], a[188:191], v[220:223]\n"
"ds_read_b64_tr_b16 a[210:211], v230 offset:16384\n"
"v_mfma_f32_16x16x32_bf16 v[204:207], a[232:235], a[184:187], v[204:207]\n"
"v_exp_f32_e32 v195, v195\n"
"v_mfma_f32_16x16x32_bf16 v[204:207], a[228:231], a[180:183], v[204:207]\n"
"buffer_load_dwordx4 v237, s[36:39], s44 offen offset:1024 lds\n"
"v_mfma_f32_16x16x32_bf16 v[204:207], a[224:227], a[176:179], v[204:207]\n"
"s_waitcnt lgkmcnt(6)\n"
"v_exp_f32_e32 v196, v196\n"
"v_mfma_f32_16x16x32_bf16 v[0:3], a[192:195], v[160:163], v[0:3]\n"
"ds_read_b64_tr_b16 a[212:213], v230 offset:32768\n"
"v_perm_b32 v129, v195, v194, s93\n"
"v_mfma_f32_16x16x32_bf16 v[0:3], a[196:199], v[164:167], v[0:3]\n"
"v_exp_f32_e32 v197, v197\n"
"v_mfma_f32_16x16x32_bf16 v[4:7], a[196:199], v[172:175], v[4:7]\n"
"ds_read_b64_tr_b16 a[214:215], v230 offset:49152\n"
"v_perm_b32 v136, v197, v196, s93\n"
"v_mfma_f32_16x16x32_bf16 v[4:7], a[192:195], v[168:171], v[4:7]\n"
"v_exp_f32_e32 v198, v198\n"
"v_mfma_f32_16x16x32_bf16 v[8:11], a[192:195], v[176:179], v[8:11]\n"
"ds_read_b64_tr_b16 a[216:217], v231 offset:0\n"
"v_mfma_f32_16x16x32_bf16 v[8:11], a[196:199], v[180:183], v[8:11]\n"
"v_exp_f32_e32 v199, v199\n"
"v_mfma_f32_16x16x32_bf16 v[12:15], a[196:199], v[188:191], v[12:15]\n"
"buffer_load_dwordx4 v238, s[36:39], s44 offen offset:2048 lds\n"
"v_mfma_f32_16x16x32_bf16 v[12:15], a[192:195], v[184:187], v[12:15]\n"
"v_exp_f32_e32 v200, v200\n"
"v_mfma_f32_16x16x32_bf16 v[28:31], a[200:203], v[184:187], v[28:31]\n"
"ds_read_b64_tr_b16 a[218:219], v231 offset:16384\n"
"v_perm_b32 v137, v199, v198, s93\n"
"v_mfma_f32_16x16x32_bf16 v[28:31], a[204:207], v[188:191], v[28:31]\n"
"v_exp_f32_e32 v201, v201\n"
"v_mfma_f32_16x16x32_bf16 v[24:27], a[204:207], v[180:183], v[24:27]\n"
"ds_read_b64_tr_b16 a[220:221], v231 offset:32768\n"
"v_perm_b32 v144, v201, v200, s93\n"
"v_mfma_f32_16x16x32_bf16 v[24:27], a[200:203], v[176:179], v[24:27]\n"
"v_exp_f32_e32 v202, v202\n"
"v_mfma_f32_16x16x32_bf16 v[20:23], a[200:203], v[168:171], v[20:23]\n"
"ds_read_b64_tr_b16 a[222:223], v231 offset:49152\n"
"v_mfma_f32_16x16x32_bf16 v[20:23], a[204:207], v[172:175], v[20:23]\n"
"v_exp_f32_e32 v203, v203\n"
"v_mfma_f32_16x16x32_bf16 v[16:19], a[204:207], v[164:167], v[16:19]\n"
"v_perm_b32 v145, v203, v202, s93\n"
"v_mfma_f32_16x16x32_bf16 v[16:19], a[200:203], v[160:163], v[16:19]\n"
"v_exp_f32_e32 v204, v204\n"
"v_smfmac_f32_16x16x64_bf16 a[112:115], a[92:95], v[160:167], v244\n"
"s_waitcnt lgkmcnt(8)\n"
"v_mfma_f32_16x16x32_bf16 v[192:195], a[240:243], a[128:131], v[208:211]\n"
"ds_read_b128 a[224:227], v224 offset:32768\n"
"v_mfma_f32_16x16x32_bf16 v[192:195], a[244:247], a[132:135], v[192:195]\n"
"v_exp_f32_e32 v205, v205\n"
"v_mfma_f32_16x16x32_bf16 v[192:195], a[248:251], a[136:139], v[192:195]\n"
"ds_read_b128 a[228:231], v225 offset:32768\n"
"v_perm_b32 v152, v205, v204, s93\n"
"v_mfma_f32_16x16x32_bf16 v[192:195], a[252:255], a[140:143], v[192:195]\n"
"v_exp_f32_e32 v206, v206\n"
"v_mfma_f32_16x16x32_bf16 v[196:199], a[252:255], a[156:159], v[212:215]\n"
"ds_read_b128 a[232:235], v226 offset:32768\n"
"v_mfma_f32_16x16x32_bf16 v[196:199], a[248:251], a[152:155], v[196:199]\n"
"v_exp_f32_e32 v207, v207\n"
"v_mfma_f32_16x16x32_bf16 v[196:199], a[244:247], a[148:151], v[196:199]\n"
"buffer_load_dwordx4 v239, s[36:39], s44 offen offset:3072 lds\n"
"v_mfma_f32_16x16x32_bf16 v[196:199], a[240:243], a[144:147], v[196:199]\n"
"v_exp_f32_e32 v192, v192\n"
"v_mfma_f32_16x16x32_bf16 v[200:203], a[240:243], a[160:163], v[216:219]\n"
"ds_read_b128 a[236:239], v227 offset:32768\n"
"v_perm_b32 v153, v207, v206, s93\n"
"v_mfma_f32_16x16x32_bf16 v[200:203], a[244:247], a[164:167], v[200:203]\n"
"v_exp_f32_e32 v193, v193\n"
"v_mfma_f32_16x16x32_bf16 v[200:203], a[248:251], a[168:171], v[200:203]\n"
"ds_read_b64_tr_b16 a[192:193], v232 offset:0\n"
"v_perm_b32 v130, v193, v192, s93\n"
"v_mfma_f32_16x16x32_bf16 v[200:203], a[252:255], a[172:175], v[200:203]\n"
"s_add_u32 m0, s46, 73728\n"
"v_exp_f32_e32 v194, v194\n"
"v_mfma_f32_16x16x32_bf16 v[204:207], a[252:255], a[188:191], v[220:223]\n"
"s_add_u32 s45, s45, 16384\n"
"ds_read_b64_tr_b16 a[194:195], v232 offset:16384\n"
"v_mfma_f32_16x16x32_bf16 v[204:207], a[248:251], a[184:187], v[204:207]\n"
"v_exp_f32_e32 v195, v195\n"
"v_mfma_f32_16x16x32_bf16 v[204:207], a[244:247], a[180:183], v[204:207]\n"
"buffer_load_dwordx4 v240, s[40:43], s45 offen offset:0 lds\n"
"v_mfma_f32_16x16x32_bf16 v[204:207], a[240:243], a[176:179], v[204:207]\n"
"s_waitcnt lgkmcnt(6)\n"
"v_exp_f32_e32 v196, v196\n"
"v_mfma_f32_16x16x32_bf16 v[32:35], a[208:211], v[160:163], v[32:35]\n"
"ds_read_b64_tr_b16 a[196:197], v232 offset:32768\n"
"v_perm_b32 v131, v195, v194, s93\n"
"v_mfma_f32_16x16x32_bf16 v[32:35], a[212:215], v[164:167], v[32:35]\n"
"v_exp_f32_e32 v197, v197\n"
"v_mfma_f32_16x16x32_bf16 v[36:39], a[212:215], v[172:175], v[36:39]\n"
"ds_read_b64_tr_b16 a[198:199], v232 offset:49152\n"
"v_perm_b32 v138, v197, v196, s93\n"
"v_mfma_f32_16x16x32_bf16 v[36:39], a[208:211], v[168:171], v[36:39]\n"
"v_exp_f32_e32 v198, v198\n"
"v_mfma_f32_16x16x32_bf16 v[40:43], a[208:211], v[176:179], v[40:43]\n"
"ds_read_b64_tr_b16 a[200:201], v233 offset:0\n"
"v_mfma_f32_16x16x32_bf16 v[40:43], a[212:215], v[180:183], v[40:43]\n"
"v_exp_f32_e32 v199, v199\n"
"v_mfma_f32_16x16x32_bf16 v[44:47], a[212:215], v[188:191], v[44:47]\n"
"buffer_load_dwordx4 v241, s[40:43], s45 offen offset:1024 lds\n"
"v_mfma_f32_16x16x32_bf16 v[44:47], a[208:211], v[184:187], v[44:47]\n"
"v_exp_f32_e32 v200, v200\n"
"v_mfma_f32_16x16x32_bf16 v[60:63], a[216:219], v[184:187], v[60:63]\n"
"ds_read_b64_tr_b16 a[202:203], v233 offset:16384\n"
"v_perm_b32 v139, v199, v198, s93\n"
"v_mfma_f32_16x16x32_bf16 v[60:63], a[220:223], v[188:191], v[60:63]\n"
"v_exp_f32_e32 v201, v201\n"
"v_mfma_f32_16x16x32_bf16 v[56:59], a[220:223], v[180:183], v[56:59]\n"
"ds_read_b64_tr_b16 a[204:205], v233 offset:32768\n"
"v_perm_b32 v146, v201, v200, s93\n"
"v_mfma_f32_16x16x32_bf16 v[56:59], a[216:219], v[176:179], v[56:59]\n"
"v_exp_f32_e32 v202, v202\n"
"v_mfma_f32_16x16x32_bf16 v[52:55], a[216:219], v[168:171], v[52:55]\n"
"ds_read_b64_tr_b16 a[206:207], v233 offset:49152\n"
"v_mfma_f32_16x16x32_bf16 v[52:55], a[220:223], v[172:175], v[52:55]\n"
"v_exp_f32_e32 v203, v203\n"
"v_mfma_f32_16x16x32_bf16 v[48:51], a[220:223], v[164:167], v[48:51]\n"
"buffer_load_dwordx4 v240, s[40:43], s45 offen offset:2048 lds\n"
"v_mfma_f32_16x16x32_bf16 v[48:51], a[216:219], v[160:163], v[48:51]\n"
"v_exp_f32_e32 v204, v204\n"
"v_smfmac_f32_16x16x64_bf16 a[116:119], a[92:95], v[168:175], v244\n"
"s_waitcnt lgkmcnt(8)\n"
"v_perm_b32 v147, v203, v202, s93\n"
"v_mfma_f32_16x16x32_bf16 v[192:195], a[224:227], a[128:131], v[208:211]\n"
"ds_read_b128 a[240:243], v224 offset:49152\n"
"v_mfma_f32_16x16x32_bf16 v[192:195], a[228:231], a[132:135], v[192:195]\n"
"v_exp_f32_e32 v205, v205\n"
"v_mfma_f32_16x16x32_bf16 v[192:195], a[232:235], a[136:139], v[192:195]\n"
"ds_read_b128 a[244:247], v225 offset:49152\n"
"v_perm_b32 v154, v205, v204, s93\n"
"v_mfma_f32_16x16x32_bf16 v[192:195], a[236:239], a[140:143], v[192:195]\n"
"s_add_u32 s34, s34, 1024\n"
"v_exp_f32_e32 v206, v206\n"
"v_mfma_f32_16x16x32_bf16 v[196:199], a[236:239], a[156:159], v[212:215]\n"
"ds_read_b128 a[248:251], v226 offset:49152\n"
"v_mfma_f32_16x16x32_bf16 v[196:199], a[232:235], a[152:155], v[196:199]\n"
"v_exp_f32_e32 v207, v207\n"
"v_mfma_f32_16x16x32_bf16 v[196:199], a[228:231], a[148:151], v[196:199]\n"
"buffer_store_dwordx4 a[4:7], v255, s[52:55], s34 offen sc1\n"
"v_mfma_f32_16x16x32_bf16 v[196:199], a[224:227], a[144:147], v[196:199]\n"
"v_exp_f32_e32 v192, v192\n"
"v_mfma_f32_16x16x32_bf16 v[200:203], a[224:227], a[160:163], v[216:219]\n"
"ds_read_b128 a[252:255], v227 offset:49152\n"
"v_perm_b32 v155, v207, v206, s93\n"
"v_mfma_f32_16x16x32_bf16 v[200:203], a[228:231], a[164:167], v[200:203]\n"
"v_exp_f32_e32 v193, v193\n"
"v_mfma_f32_16x16x32_bf16 v[200:203], a[232:235], a[168:171], v[200:203]\n"
"ds_read_b64_tr_b16 a[208:209], v234 offset:0\n"
"v_perm_b32 v132, v193, v192, s93\n"
"v_mfma_f32_16x16x32_bf16 v[200:203], a[236:239], a[172:175], v[200:203]\n"
"v_exp_f32_e32 v194, v194\n"
"v_mfma_f32_16x16x32_bf16 v[204:207], a[236:239], a[188:191], v[220:223]\n"
"ds_read_b64_tr_b16 a[210:211], v234 offset:16384\n"
"v_mfma_f32_16x16x32_bf16 v[204:207], a[232:235], a[184:187], v[204:207]\n"
"v_exp_f32_e32 v195, v195\n"
"v_mfma_f32_16x16x32_bf16 v[204:207], a[228:231], a[180:183], v[204:207]\n"
"buffer_load_dwordx4 v241, s[40:43], s45 offen offset:3072 lds\n"
"v_mfma_f32_16x16x32_bf16 v[204:207], a[224:227], a[176:179], v[204:207]\n"
"s_waitcnt lgkmcnt(6)\n"
"v_exp_f32_e32 v196, v196\n"
"v_mfma_f32_16x16x32_bf16 v[64:67], a[192:195], v[160:163], v[64:67]\n"
"ds_read_b64_tr_b16 a[212:213], v234 offset:32768\n"
"v_perm_b32 v133, v195, v194, s93\n"
"v_mfma_f32_16x16x32_bf16 v[64:67], a[196:199], v[164:167], v[64:67]\n"
"v_exp_f32_e32 v197, v197\n"
"v_mfma_f32_16x16x32_bf16 v[68:71], a[196:199], v[172:175], v[68:71]\n"
"ds_read_b64_tr_b16 a[214:215], v234 offset:49152\n"
"v_perm_b32 v140, v197, v196, s93\n"
"v_mfma_f32_16x16x32_bf16 v[68:71], a[192:195], v[168:171], v[68:71]\n"
"s_add_u32 m0, s46, 12288\n"
"v_exp_f32_e32 v198, v198\n"
"v_mfma_f32_16x16x32_bf16 v[72:75], a[192:195], v[176:179], v[72:75]\n"
"s_add_u32 s44, s44, 16384\n"
"ds_read_b64_tr_b16 a[216:217], v235 offset:0\n"
"v_mfma_f32_16x16x32_bf16 v[72:75], a[196:199], v[180:183], v[72:75]\n"
"v_exp_f32_e32 v199, v199\n"
"v_mfma_f32_16x16x32_bf16 v[76:79], a[196:199], v[188:191], v[76:79]\n"
"buffer_load_dwordx4 v236, s[36:39], s44 offen offset:0 lds\n"
"v_mfma_f32_16x16x32_bf16 v[76:79], a[192:195], v[184:187], v[76:79]\n"
"v_exp_f32_e32 v200, v200\n"
"v_mfma_f32_16x16x32_bf16 v[92:95], a[200:203], v[184:187], v[92:95]\n"
"ds_read_b64_tr_b16 a[218:219], v235 offset:16384\n"
"v_perm_b32 v141, v199, v198, s93\n"
"v_mfma_f32_16x16x32_bf16 v[92:95], a[204:207], v[188:191], v[92:95]\n"
"v_exp_f32_e32 v201, v201\n"
"v_mfma_f32_16x16x32_bf16 v[88:91], a[204:207], v[180:183], v[88:91]\n"
"ds_read_b64_tr_b16 a[220:221], v235 offset:32768\n"
"v_perm_b32 v148, v201, v200, s93\n"
"v_mfma_f32_16x16x32_bf16 v[88:91], a[200:203], v[176:179], v[88:91]\n"
"v_exp_f32_e32 v202, v202\n"
"v_mfma_f32_16x16x32_bf16 v[84:87], a[200:203], v[168:171], v[84:87]\n"
"ds_read_b64_tr_b16 a[222:223], v235 offset:49152\n"
"v_mfma_f32_16x16x32_bf16 v[84:87], a[204:207], v[172:175], v[84:87]\n"
"v_exp_f32_e32 v203, v203\n"
"v_mfma_f32_16x16x32_bf16 v[80:83], a[204:207], v[164:167], v[80:83]\n"
"buffer_load_dwordx4 v237, s[36:39], s44 offen offset:1024 lds\n"
"v_mfma_f32_16x16x32_bf16 v[80:83], a[200:203], v[160:163], v[80:83]\n"
"v_exp_f32_e32 v204, v204\n"
"v_smfmac_f32_16x16x64_bf16 a[120:123], a[92:95], v[176:183], v244\n"
"s_waitcnt lgkmcnt(8)\n"
"v_perm_b32 v149, v203, v202, s93\n"
"v_mfma_f32_16x16x32_bf16 v[192:195], a[240:243], a[128:131], v[208:211]\n"
"ds_read_b128 a[224:227], v224 offset:4096\n"
"v_mfma_f32_16x16x32_bf16 v[192:195], a[244:247], a[132:135], v[192:195]\n"
"v_exp_f32_e32 v205, v205\n"
"v_mfma_f32_16x16x32_bf16 v[192:195], a[248:251], a[136:139], v[192:195]\n"
"ds_read_b128 a[228:231], v225 offset:4096\n"
"v_perm_b32 v156, v205, v204, s93\n"
"v_mfma_f32_16x16x32_bf16 v[192:195], a[252:255], a[140:143], v[192:195]\n"
"v_exp_f32_e32 v206, v206\n"
"v_mfma_f32_16x16x32_bf16 v[196:199], a[252:255], a[156:159], v[212:215]\n"
"ds_read_b128 a[232:235], v226 offset:4096\n"
"v_mfma_f32_16x16x32_bf16 v[196:199], a[248:251], a[152:155], v[196:199]\n"
"v_exp_f32_e32 v207, v207\n"
"v_mfma_f32_16x16x32_bf16 v[196:199], a[244:247], a[148:151], v[196:199]\n"
"buffer_load_dwordx4 v238, s[36:39], s44 offen offset:2048 lds\n"
"v_mfma_f32_16x16x32_bf16 v[196:199], a[240:243], a[144:147], v[196:199]\n"
"v_exp_f32_e32 v192, v192\n"
"v_mfma_f32_16x16x32_bf16 v[200:203], a[240:243], a[160:163], v[216:219]\n"
"ds_read_b128 a[236:239], v227 offset:4096\n"
"v_perm_b32 v157, v207, v206, s93\n"
"v_mfma_f32_16x16x32_bf16 v[200:203], a[244:247], a[164:167], v[200:203]\n"
"v_exp_f32_e32 v193, v193\n"
"v_mfma_f32_16x16x32_bf16 v[200:203], a[248:251], a[168:171], v[200:203]\n"
"ds_read_b64_tr_b16 a[192:193], v228 offset:4096\n"
"v_perm_b32 v134, v193, v192, s93\n"
"v_mfma_f32_16x16x32_bf16 v[200:203], a[252:255], a[172:175], v[200:203]\n"
"v_exp_f32_e32 v194, v194\n"
"v_mfma_f32_16x16x32_bf16 v[204:207], a[252:255], a[188:191], v[220:223]\n"
"ds_read_b64_tr_b16 a[194:195], v228 offset:20480\n"
"v_mfma_f32_16x16x32_bf16 v[204:207], a[248:251], a[184:187], v[204:207]\n"
"v_exp_f32_e32 v195, v195\n"
"v_mfma_f32_16x16x32_bf16 v[204:207], a[244:247], a[180:183], v[204:207]\n"
"buffer_load_dwordx4 v239, s[36:39], s44 offen offset:3072 lds\n"
"v_mfma_f32_16x16x32_bf16 v[204:207], a[240:243], a[176:179], v[204:207]\n"
"s_waitcnt lgkmcnt(6)\n"
"v_exp_f32_e32 v196, v196\n"
"v_mfma_f32_16x16x32_bf16 v[96:99], a[208:211], v[160:163], v[96:99]\n"
"ds_read_b64_tr_b16 a[196:197], v228 offset:36864\n"
"v_perm_b32 v135, v195, v194, s93\n"
"v_mfma_f32_16x16x32_bf16 v[96:99], a[212:215], v[164:167], v[96:99]\n"
"v_exp_f32_e32 v197, v197\n"
"v_mfma_f32_16x16x32_bf16 v[100:103], a[212:215], v[172:175], v[100:103]\n"
"ds_read_b64_tr_b16 a[198:199], v228 offset:53248\n"
"v_perm_b32 v142, v197, v196, s93\n"
"v_mfma_f32_16x16x32_bf16 v[100:103], a[208:211], v[168:171], v[100:103]\n"
"s_add_u32 m0, s46, 77824\n"
"v_exp_f32_e32 v198, v198\n"
"v_mfma_f32_16x16x32_bf16 v[104:107], a[208:211], v[176:179], v[104:107]\n"
"s_add_u32 s45, s45, 16384\n"
"ds_read_b64_tr_b16 a[200:201], v229 offset:4096\n"
"v_mfma_f32_16x16x32_bf16 v[104:107], a[212:215], v[180:183], v[104:107]\n"
"v_exp_f32_e32 v199, v199\n"
"v_mfma_f32_16x16x32_bf16 v[108:111], a[212:215], v[188:191], v[108:111]\n"
"buffer_load_dwordx4 v240, s[40:43], s45 offen offset:0 lds\n"
"v_mfma_f32_16x16x32_bf16 v[108:111], a[208:211], v[184:187], v[108:111]\n"
"v_exp_f32_e32 v200, v200\n"
"v_mfma_f32_16x16x32_bf16 v[124:127], a[216:219], v[184:187], v[124:127]\n"
"ds_read_b64_tr_b16 a[202:203], v229 offset:20480\n"
"v_perm_b32 v143, v199, v198, s93\n"
"v_mfma_f32_16x16x32_bf16 v[124:127], a[220:223], v[188:191], v[124:127]\n"
"v_exp_f32_e32 v201, v201\n"
"v_mfma_f32_16x16x32_bf16 v[120:123], a[220:223], v[180:183], v[120:123]\n"
"ds_read_b64_tr_b16 a[204:205], v229 offset:36864\n"
"v_perm_b32 v150, v201, v200, s93\n"
"v_mfma_f32_16x16x32_bf16 v[120:123], a[216:219], v[176:179], v[120:123]\n"
"v_exp_f32_e32 v202, v202\n"
"v_mfma_f32_16x16x32_bf16 v[116:119], a[216:219], v[168:171], v[116:119]\n"
"ds_read_b64_tr_b16 a[206:207], v229 offset:53248\n"
"v_mfma_f32_16x16x32_bf16 v[116:119], a[220:223], v[172:175], v[116:119]\n"
"v_exp_f32_e32 v203, v203\n"
"v_mfma_f32_16x16x32_bf16 v[112:115], a[220:223], v[164:167], v[112:115]\n"
"buffer_load_dwordx4 v241, s[40:43], s45 offen offset:1024 lds\n"
"v_mfma_f32_16x16x32_bf16 v[112:115], a[216:219], v[160:163], v[112:115]\n"
"v_exp_f32_e32 v204, v204\n"
"v_smfmac_f32_16x16x64_bf16 a[124:127], a[92:95], v[184:191], v244\n"
"s_waitcnt lgkmcnt(8)\n"
"s_sub_u32 s47, s47, 1\n"
"v_perm_b32 v151, v203, v202, s93\n"
"v_mfma_f32_16x16x32_bf16 v[192:195], a[224:227], a[128:131], v[208:211]\n"
"ds_read_b128 a[240:243], v224 offset:20480\n"
"v_mfma_f32_16x16x32_bf16 v[192:195], a[228:231], a[132:135], v[192:195]\n"
"v_exp_f32_e32 v205, v205\n"
"v_mfma_f32_16x16x32_bf16 v[192:195], a[232:235], a[136:139], v[192:195]\n"
"ds_read_b128 a[244:247], v225 offset:20480\n"
"v_perm_b32 v158, v205, v204, s93\n"
"v_mfma_f32_16x16x32_bf16 v[192:195], a[236:239], a[140:143], v[192:195]\n"
"v_exp_f32_e32 v206, v206\n"
"v_mfma_f32_16x16x32_bf16 v[196:199], a[236:239], a[156:159], v[212:215]\n"
"ds_read_b128 a[248:251], v226 offset:20480\n"
"v_mfma_f32_16x16x32_bf16 v[196:199], a[232:235], a[152:155], v[196:199]\n"
"v_exp_f32_e32 v207, v207\n"
"v_mfma_f32_16x16x32_bf16 v[196:199], a[228:231], a[148:151], v[196:199]\n"
"buffer_load_dwordx4 v240, s[40:43], s45 offen offset:2048 lds\n"
"v_mfma_f32_16x16x32_bf16 v[196:199], a[224:227], a[144:147], v[196:199]\n"
"v_exp_f32_e32 v192, v192\n"
"v_mfma_f32_16x16x32_bf16 v[200:203], a[224:227], a[160:163], v[216:219]\n"
"ds_read_b128 a[252:255], v227 offset:20480\n"
"v_perm_b32 v159, v207, v206, s93\n"
"v_mfma_f32_16x16x32_bf16 v[200:203], a[228:231], a[164:167], v[200:203]\n"
"v_exp_f32_e32 v193, v193\n"
"v_mfma_f32_16x16x32_bf16 v[200:203], a[232:235], a[168:171], v[200:203]\n"
"ds_read_b64_tr_b16 a[208:209], v230 offset:4096\n"
"v_perm_b32 v160, v193, v192, s93\n"
"v_mfma_f32_16x16x32_bf16 v[200:203], a[236:239], a[172:175], v[200:203]\n"
"v_exp_f32_e32 v194, v194\n"
"v_mfma_f32_16x16x32_bf16 v[204:207], a[236:239], a[188:191], v[220:223]\n"
"ds_read_b64_tr_b16 a[210:211], v230 offset:20480\n"
"v_mfma_f32_16x16x32_bf16 v[204:207], a[232:235], a[184:187], v[204:207]\n"
"v_exp_f32_e32 v195, v195\n"
"v_mfma_f32_16x16x32_bf16 v[204:207], a[228:231], a[180:183], v[204:207]\n"
"v_perm_b32 v161, v195, v194, s93\n"
"v_mfma_f32_16x16x32_bf16 v[204:207], a[224:227], a[176:179], v[204:207]\n"
"s_waitcnt lgkmcnt(6)\n"
"v_exp_f32_e32 v196, v196\n"
"v_mfma_f32_16x16x32_bf16 v[0:3], a[192:195], v[128:131], v[0:3]\n"
"ds_read_b64_tr_b16 a[212:213], v230 offset:36864\n"
"v_mfma_f32_16x16x32_bf16 v[0:3], a[196:199], v[132:135], v[0:3]\n"
"v_exp_f32_e32 v197, v197\n"
"v_mfma_f32_16x16x32_bf16 v[4:7], a[196:199], v[140:143], v[4:7]\n"
"ds_read_b64_tr_b16 a[214:215], v230 offset:53248\n"
"v_perm_b32 v168, v197, v196, s93\n"
"v_mfma_f32_16x16x32_bf16 v[4:7], a[192:195], v[136:139], v[4:7]\n"
"v_exp_f32_e32 v198, v198\n"
"v_mfma_f32_16x16x32_bf16 v[8:11], a[192:195], v[144:147], v[8:11]\n"
"ds_read_b64_tr_b16 a[216:217], v231 offset:4096\n"
"v_mfma_f32_16x16x32_bf16 v[8:11], a[196:199], v[148:151], v[8:11]\n"
"v_exp_f32_e32 v199, v199\n"
"v_mfma_f32_16x16x32_bf16 v[12:15], a[196:199], v[156:159], v[12:15]\n"
"buffer_load_dwordx4 v241, s[40:43], s45 offen offset:3072 lds\n"
"v_mfma_f32_16x16x32_bf16 v[12:15], a[192:195], v[152:155], v[12:15]\n"
"v_exp_f32_e32 v200, v200\n"
"v_mfma_f32_16x16x32_bf16 v[28:31], a[200:203], v[152:155], v[28:31]\n"
"ds_read_b64_tr_b16 a[218:219], v231 offset:20480\n"
"v_perm_b32 v169, v199, v198, s93\n"
"v_mfma_f32_16x16x32_bf16 v[28:31], a[204:207], v[156:159], v[28:31]\n"
"v_exp_f32_e32 v201, v201\n"
"v_mfma_f32_16x16x32_bf16 v[24:27], a[204:207], v[148:151], v[24:27]\n"
"ds_read_b64_tr_b16 a[220:221], v231 offset:36864\n"
"v_perm_b32 v176, v201, v200, s93\n"
"v_mfma_f32_16x16x32_bf16 v[24:27], a[200:203], v[144:147], v[24:27]\n"
"v_exp_f32_e32 v202, v202\n"
"v_mfma_f32_16x16x32_bf16 v[20:23], a[200:203], v[136:139], v[20:23]\n"
"ds_read_b64_tr_b16 a[222:223], v231 offset:53248\n"
"v_mfma_f32_16x16x32_bf16 v[20:23], a[204:207], v[140:143], v[20:23]\n"
"v_exp_f32_e32 v203, v203\n"
"v_mfma_f32_16x16x32_bf16 v[16:19], a[204:207], v[132:135], v[16:19]\n"
"v_perm_b32 v177, v203, v202, s93\n"
"v_mfma_f32_16x16x32_bf16 v[16:19], a[200:203], v[128:131], v[16:19]\n"
"v_exp_f32_e32 v204, v204\n"
"v_smfmac_f32_16x16x64_bf16 a[112:115], a[92:95], v[128:135], v244\n"
"s_waitcnt lgkmcnt(8)\n"
"v_mfma_f32_16x16x32_bf16 v[192:195], a[240:243], a[128:131], v[208:211]\n"
"ds_read_b128 a[224:227], v224 offset:36864\n"
"v_mfma_f32_16x16x32_bf16 v[192:195], a[244:247], a[132:135], v[192:195]\n"
"v_exp_f32_e32 v205, v205\n"
"v_mfma_f32_16x16x32_bf16 v[192:195], a[248:251], a[136:139], v[192:195]\n"
"ds_read_b128 a[228:231], v225 offset:36864\n"
"v_perm_b32 v184, v205, v204, s93\n"
"v_mfma_f32_16x16x32_bf16 v[192:195], a[252:255], a[140:143], v[192:195]\n"
"v_exp_f32_e32 v206, v206\n"
"v_mfma_f32_16x16x32_bf16 v[196:199], a[252:255], a[156:159], v[212:215]\n"
"ds_read_b128 a[232:235], v226 offset:36864\n"
"v_mfma_f32_16x16x32_bf16 v[196:199], a[248:251], a[152:155], v[196:199]\n"
"v_exp_f32_e32 v207, v207\n"
"v_mfma_f32_16x16x32_bf16 v[196:199], a[244:247], a[148:151], v[196:199]\n"
"v_perm_b32 v185, v207, v206, s93\n"
"v_mfma_f32_16x16x32_bf16 v[196:199], a[240:243], a[144:147], v[196:199]\n"
"v_exp_f32_e32 v192, v192\n"
"v_mfma_f32_16x16x32_bf16 v[200:203], a[240:243], a[160:163], v[216:219]\n"
"ds_read_b128 a[236:239], v227 offset:36864\n"
"v_mfma_f32_16x16x32_bf16 v[200:203], a[244:247], a[164:167], v[200:203]\n"
"v_exp_f32_e32 v193, v193\n"
"v_mfma_f32_16x16x32_bf16 v[200:203], a[248:251], a[168:171], v[200:203]\n"
"ds_read_b64_tr_b16 a[192:193], v232 offset:4096\n"
"v_perm_b32 v162, v193, v192, s93\n"
"v_mfma_f32_16x16x32_bf16 v[200:203], a[252:255], a[172:175], v[200:203]\n"
"v_exp_f32_e32 v194, v194\n"
"v_mfma_f32_16x16x32_bf16 v[204:207], a[252:255], a[188:191], v[220:223]\n"
"ds_read_b64_tr_b16 a[194:195], v232 offset:20480\n"
"v_mfma_f32_16x16x32_bf16 v[204:207], a[248:251], a[184:187], v[204:207]\n"
"v_exp_f32_e32 v195, v195\n"
"v_mfma_f32_16x16x32_bf16 v[204:207], a[244:247], a[180:183], v[204:207]\n"
"v_perm_b32 v163, v195, v194, s93\n"
"v_mfma_f32_16x16x32_bf16 v[204:207], a[240:243], a[176:179], v[204:207]\n"
"s_waitcnt lgkmcnt(6)\n"
"v_exp_f32_e32 v196, v196\n"
"v_mfma_f32_16x16x32_bf16 v[32:35], a[208:211], v[128:131], v[32:35]\n"
"ds_read_b64_tr_b16 a[196:197], v232 offset:36864\n"
"v_mfma_f32_16x16x32_bf16 v[32:35], a[212:215], v[132:135], v[32:35]\n"
"v_exp_f32_e32 v197, v197\n"
"v_mfma_f32_16x16x32_bf16 v[36:39], a[212:215], v[140:143], v[36:39]\n"
"ds_read_b64_tr_b16 a[198:199], v232 offset:53248\n"
"v_perm_b32 v170, v197, v196, s93\n"
"v_mfma_f32_16x16x32_bf16 v[36:39], a[208:211], v[136:139], v[36:39]\n"
"v_exp_f32_e32 v198, v198\n"
"v_mfma_f32_16x16x32_bf16 v[40:43], a[208:211], v[144:147], v[40:43]\n"
"ds_read_b64_tr_b16 a[200:201], v233 offset:4096\n"
"v_mfma_f32_16x16x32_bf16 v[40:43], a[212:215], v[148:151], v[40:43]\n"
"v_exp_f32_e32 v199, v199\n"
"v_mfma_f32_16x16x32_bf16 v[44:47], a[212:215], v[156:159], v[44:47]\n"
"v_perm_b32 v171, v199, v198, s93\n"
"v_mfma_f32_16x16x32_bf16 v[44:47], a[208:211], v[152:155], v[44:47]\n"
"v_exp_f32_e32 v200, v200\n"
"v_mfma_f32_16x16x32_bf16 v[60:63], a[216:219], v[152:155], v[60:63]\n"
"ds_read_b64_tr_b16 a[202:203], v233 offset:20480\n"
"v_mfma_f32_16x16x32_bf16 v[60:63], a[220:223], v[156:159], v[60:63]\n"
"v_exp_f32_e32 v201, v201\n"
"v_mfma_f32_16x16x32_bf16 v[56:59], a[220:223], v[148:151], v[56:59]\n"
"ds_read_b64_tr_b16 a[204:205], v233 offset:36864\n"
"v_perm_b32 v178, v201, v200, s93\n"
"v_mfma_f32_16x16x32_bf16 v[56:59], a[216:219], v[144:147], v[56:59]\n"
"v_exp_f32_e32 v202, v202\n"
"v_mfma_f32_16x16x32_bf16 v[52:55], a[216:219], v[136:139], v[52:55]\n"
"ds_read_b64_tr_b16 a[206:207], v233 offset:53248\n"
"v_mfma_f32_16x16x32_bf16 v[52:55], a[220:223], v[140:143], v[52:55]\n"
"v_exp_f32_e32 v203, v203\n"
"v_mfma_f32_16x16x32_bf16 v[48:51], a[220:223], v[132:135], v[48:51]\n"
"v_perm_b32 v179, v203, v202, s93\n"
"v_mfma_f32_16x16x32_bf16 v[48:51], a[216:219], v[128:131], v[48:51]\n"
"v_exp_f32_e32 v204, v204\n"
"v_smfmac_f32_16x16x64_bf16 a[116:119], a[92:95], v[136:143], v244\n"
"s_waitcnt lgkmcnt(8)\n"
"v_mfma_f32_16x16x32_bf16 v[192:195], a[224:227], a[128:131], v[208:211]\n"
"ds_read_b128 a[240:243], v224 offset:53248\n"
"v_mfma_f32_16x16x32_bf16 v[192:195], a[228:231], a[132:135], v[192:195]\n"
"v_exp_f32_e32 v205, v205\n"
"v_mfma_f32_16x16x32_bf16 v[192:195], a[232:235], a[136:139], v[192:195]\n"
"ds_read_b128 a[244:247], v225 offset:53248\n"
"v_perm_b32 v186, v205, v204, s93\n"
"v_mfma_f32_16x16x32_bf16 v[192:195], a[236:239], a[140:143], v[192:195]\n"
"v_exp_f32_e32 v206, v206\n"
"v_mfma_f32_16x16x32_bf16 v[196:199], a[236:239], a[156:159], v[212:215]\n"
"ds_read_b128 a[248:251], v226 offset:53248\n"
"v_mfma_f32_16x16x32_bf16 v[196:199], a[232:235], a[152:155], v[196:199]\n"
".LPG11_%=:\n"
"s_cbranch_execz .LPGR_%=\n"
"v_exp_f32_e32 v207, v207\n"
"v_mfma_f32_16x16x32_bf16 v[196:199], a[228:231], a[148:151], v[196:199]\n"
"v_perm_b32 v187, v207, v206, s93\n"
"v_mfma_f32_16x16x32_bf16 v[196:199], a[224:227], a[144:147], v[196:199]\n"
"v_exp_f32_e32 v192, v192\n"
"v_mfma_f32_16x16x32_bf16 v[200:203], a[224:227], a[160:163], v[216:219]\n"
"ds_read_b128 a[252:255], v227 offset:53248\n"
"v_mfma_f32_16x16x32_bf16 v[200:203], a[228:231], a[164:167], v[200:203]\n"
"v_exp_f32_e32 v193, v193\n"
"v_mfma_f32_16x16x32_bf16 v[200:203], a[232:235], a[168:171], v[200:203]\n"
"ds_read_b64_tr_b16 a[208:209], v234 offset:4096\n"
"v_perm_b32 v164, v193, v192, s93\n"
"v_mfma_f32_16x16x32_bf16 v[200:203], a[236:239], a[172:175], v[200:203]\n"
"v_exp_f32_e32 v194, v194\n"
"v_mfma_f32_16x16x32_bf16 v[204:207], a[236:239], a[188:191], v[220:223]\n"
"ds_read_b64_tr_b16 a[210:211], v234 offset:20480\n"
"v_mfma_f32_16x16x32_bf16 v[204:207], a[232:235], a[184:187], v[204:207]\n"
"v_exp_f32_e32 v195, v195\n"
"v_mfma_f32_16x16x32_bf16 v[204:207], a[228:231], a[180:183], v[204:207]\n"
"v_perm_b32 v165, v195, v194, s93\n"
"v_mfma_f32_16x16x32_bf16 v[204:207], a[224:227], a[176:179], v[204:207]\n"
"s_waitcnt lgkmcnt(6)\n"
"v_exp_f32_e32 v196, v196\n"
"v_mfma_f32_16x16x32_bf16 v[64:67], a[192:195], v[128:131], v[64:67]\n"
"ds_read_b64_tr_b16 a[212:213], v234 offset:36864\n"
"v_mfma_f32_16x16x32_bf16 v[64:67], a[196:199], v[132:135], v[64:67]\n"
"v_exp_f32_e32 v197, v197\n"
"v_mfma_f32_16x16x32_bf16 v[68:71], a[196:199], v[140:143], v[68:71]\n"
"ds_read_b64_tr_b16 a[214:215], v234 offset:53248\n"
"v_perm_b32 v172, v197, v196, s93\n"
"v_mfma_f32_16x16x32_bf16 v[68:71], a[192:195], v[136:139], v[68:71]\n"
"v_exp_f32_e32 v198, v198\n"
"v_mfma_f32_16x16x32_bf16 v[72:75], a[192:195], v[144:147], v[72:75]\n"
"ds_read_b64_tr_b16 a[216:217], v235 offset:4096\n"
"v_mfma_f32_16x16x32_bf16 v[72:75], a[196:199], v[148:151], v[72:75]\n"
"v_exp_f32_e32 v199, v199\n"
"v_mfma_f32_16x16x32_bf16 v[76:79], a[196:199], v[156:159], v[76:79]\n"
"v_perm_b32 v173, v199, v198, s93\n"
"v_mfma_f32_16x16x32_bf16 v[76:79], a[192:195], v[152:155], v[76:79]\n"
"v_exp_f32_e32 v200, v200\n"
"v_mfma_f32_16x16x32_bf16 v[92:95], a[200:203], v[152:155], v[92:95]\n"
"ds_read_b64_tr_b16 a[218:219], v235 offset:20480\n"
"v_mfma_f32_16x16x32_bf16 v[92:95], a[204:207], v[156:159], v[92:95]\n"
"v_exp_f32_e32 v201, v201\n"
"v_mfma_f32_16x16x32_bf16 v[88:91], a[204:207], v[148:151], v[88:91]\n"
"ds_read_b64_tr_b16 a[220:221], v235 offset:36864\n"
"v_perm_b32 v180, v201, v200, s93\n"
"v_mfma_f32_16x16x32_bf16 v[88:91], a[200:203], v[144:147], v[88:91]\n"
"v_exp_f32_e32 v202, v202\n"
"v_mfma_f32_16x16x32_bf16 v[84:87], a[200:203], v[136:139], v[84:87]\n"
"ds_read_b64_tr_b16 a[222:223], v235 offset:53248\n"
"v_mfma_f32_16x16x32_bf16 v[84:87], a[204:207], v[140:143], v[84:87]\n"
"v_exp_f32_e32 v203, v203\n"
"v_mfma_f32_16x16x32_bf16 v[80:83], a[204:207], v[132:135], v[80:83]\n"
"v_perm_b32 v181, v203, v202, s93\n"
"v_mfma_f32_16x16x32_bf16 v[80:83], a[200:203], v[128:131], v[80:83]\n"
"s_waitcnt vmcnt(0) lgkmcnt(0)\n"
"v_exp_f32_e32 v204, v204\n"
"v_smfmac_f32_16x16x64_bf16 a[120:123], a[92:95], v[144:151], v244\n"
"v_mfma_f32_16x16x32_bf16 v[192:195], a[240:243], a[128:131], v[208:211]\n"
"s_barrier\n"
"ds_read_b128 a[224:227], v224 offset:8192\n"
"v_mfma_f32_16x16x32_bf16 v[192:195], a[244:247], a[132:135], v[192:195]\n"
"v_exp_f32_e32 v205, v205\n"
"v_mfma_f32_16x16x32_bf16 v[192:195], a[248:251], a[136:139], v[192:195]\n"
"ds_read_b128 a[228:231], v225 offset:8192\n"
"v_perm_b32 v188, v205, v204, s93\n"
"v_mfma_f32_16x16x32_bf16 v[192:195], a[252:255], a[140:143], v[192:195]\n"
"s_add_u32 s85, s85, 2048\n"
"v_exp_f32_e32 v206, v206\n"
"v_mfma_f32_16x16x32_bf16 v[196:199], a[252:255], a[156:159], v[212:215]\n"
"ds_read_b128 a[232:235], v226 offset:8192\n"
"v_mfma_f32_16x16x32_bf16 v[196:199], a[248:251], a[152:155], v[196:199]\n"
"v_exp_f32_e32 v207, v207\n"
"v_mfma_f32_16x16x32_bf16 v[196:199], a[244:247], a[148:151], v[196:199]\n"
"buffer_load_dword a9, v248, s[64:67], s85 offen\n"
"v_mfma_f32_16x16x32_bf16 v[196:199], a[240:243], a[144:147], v[196:199]\n"
"v_exp_f32_e32 v192, v192\n"
"v_mfma_f32_16x16x32_bf16 v[200:203], a[240:243], a[160:163], v[216:219]\n"
"ds_read_b128 a[236:239], v227 offset:8192\n"
"v_perm_b32 v189, v207, v206, s93\n"
"v_mfma_f32_16x16x32_bf16 v[200:203], a[244:247], a[164:167], v[200:203]\n"
"v_exp_f32_e32 v193, v193\n"
"v_mfma_f32_16x16x32_bf16 v[200:203], a[248:251], a[168:171], v[200:203]\n"
"ds_read_b64_tr_b16 a[192:193], v228 offset:8192\n"
"v_perm_b32 v166, v193, v192, s93\n"
"v_mfma_f32_16x16x32_bf16 v[200:203], a[252:255], a[172:175], v[200:203]\n"
"v_exp_f32_e32 v194, v194\n"
"v_mfma_f32_16x16x32_bf16 v[204:207], a[252:255], a[188:191], v[220:223]\n"
"ds_read_b64_tr_b16 a[194:195], v228 offset:24576\n"
"v_mfma_f32_16x16x32_bf16 v[204:207], a[248:251], a[184:187], v[204:207]\n"
"v_exp_f32_e32 v195, v195\n"
"v_mfma_f32_16x16x32_bf16 v[204:207], a[244:247], a[180:183], v[204:207]\n"
"v_perm_b32 v167, v195, v194, s93\n"
"v_mfma_f32_16x16x32_bf16 v[204:207], a[240:243], a[176:179], v[204:207]\n"
"v_exp_f32_e32 v196, v196\n"
"v_mfma_f32_16x16x32_bf16 v[96:99], a[208:211], v[128:131], v[96:99]\n"
"ds_read_b64_tr_b16 a[196:197], v228 offset:40960\n"
"v_mfma_f32_16x16x32_bf16 v[96:99], a[212:215], v[132:135], v[96:99]\n"
"v_exp_f32_e32 v197, v197\n"
"v_mfma_f32_16x16x32_bf16 v[100:103], a[212:215], v[140:143], v[100:103]\n"
"ds_read_b64_tr_b16 a[198:199], v228 offset:57344\n"
"v_perm_b32 v174, v197, v196, s93\n"
"v_mfma_f32_16x16x32_bf16 v[100:103], a[208:211], v[136:139], v[100:103]\n"
"s_add_u32 s34, s34, 1024\n"
"v_exp_f32_e32 v198, v198\n"
"v_mfma_f32_16x16x32_bf16 v[104:107], a[208:211], v[144:147], v[104:107]\n"
"ds_read_b64_tr_b16 a[200:201], v229 offset:8192\n"
"v_mfma_f32_16x16x32_bf16 v[104:107], a[212:215], v[148:151], v[104:107]\n"
"v_exp_f32_e32 v199, v199\n"
"v_mfma_f32_16x16x32_bf16 v[108:111], a[212:215], v[156:159], v[108:111]\n"
"buffer_store_dwordx4 a[4:7], v255, s[52:55], s34 offen sc1\n"
"v_mfma_f32_16x16x32_bf16 v[108:111], a[208:211], v[152:155], v[108:111]\n"
"v_exp_f32_e32 v200, v200\n"
"v_mfma_f32_16x16x32_bf16 v[124:127], a[216:219], v[152:155], v[124:127]\n"
"ds_read_b64_tr_b16 a[202:203], v229 offset:24576\n"
"v_perm_b32 v175, v199, v198, s93\n"
"v_mfma_f32_16x16x32_bf16 v[124:127], a[220:223], v[156:159], v[124:127]\n"
"v_exp_f32_e32 v201, v201\n"
"v_mfma_f32_16x16x32_bf16 v[120:123], a[220:223], v[148:151], v[120:123]\n"
"ds_read_b64_tr_b16 a[204:205], v229 offset:40960\n"
"v_perm_b32 v182, v201, v200, s93\n"
"v_mfma_f32_16x16x32_bf16 v[120:123], a[216:219], v[144:147], v[120:123]\n"
"v_exp_f32_e32 v202, v202\n"
"v_mfma_f32_16x16x32_bf16 v[116:119], a[216:219], v[136:139], v[116:119]\n"
"ds_read_b64_tr_b16 a[206:207], v229 offset:57344\n"
"v_mfma_f32_16x16x32_bf16 v[116:119], a[220:223], v[140:143], v[116:119]\n"
"v_exp_f32_e32 v203, v203\n"
"v_mfma_f32_16x16x32_bf16 v[112:115], a[220:223], v[132:135], v[112:115]\n"
"v_perm_b32 v183, v203, v202, s93\n"
"v_mfma_f32_16x16x32_bf16 v[112:115], a[216:219], v[128:131], v[112:115]\n"
"v_exp_f32_e32 v204, v204\n"
"v_smfmac_f32_16x16x64_bf16 a[124:127], a[92:95], v[152:159], v244\n"
"s_waitcnt lgkmcnt(8)\n"
"s_cmp_lg_u32 s47, 0\n"
"s_cbranch_scc1 .LLOOP_%=\n"
".LLATE_%=:\n"
"v_mfma_f32_16x16x32_bf16 v[192:195], a[224:227], a[128:131], v[208:211]\n"
"s_mov_b32 s54, 41943040\n"
"ds_read_b128 a[240:243], v224 offset:24576\n"
"v_mfma_f32_16x16x32_bf16 v[192:195], a[228:231], a[132:135], v[192:195]\n"
"v_exp_f32_e32 v205, v205\n"
"v_mfma_f32_16x16x32_bf16 v[192:195], a[232:235], a[136:139], v[192:195]\n"
"ds_read_b128 a[244:247], v225 offset:24576\n"
"v_perm_b32 v190, v205, v204, s93\n"
"v_mfma_f32_16x16x32_bf16 v[192:195], a[236:239], a[140:143], v[192:195]\n"
"v_exp_f32_e32 v206, v206\n"
"v_mfma_f32_16x16x32_bf16 v[196:199], a[236:239], a[156:159], v[212:215]\n"
"ds_read_b128 a[248:251], v226 offset:24576\n"
"v_mfma_f32_16x16x32_bf16 v[196:199], a[232:235], a[152:155], v[196:199]\n"
"v_exp_f32_e32 v207, v207\n"
"v_mfma_f32_16x16x32_bf16 v[196:199], a[228:231], a[148:151], v[196:199]\n"
"buffer_load_dword v249, v246, s[24:27], s28 offen sc1\n"
"v_mfma_f32_16x16x32_bf16 v[196:199], a[224:227], a[144:147], v[196:199]\n"
"v_exp_f32_e32 v192, v192\n"
"v_mfma_f32_16x16x32_bf16 v[200:203], a[224:227], a[160:163], v[216:219]\n"
"ds_read_b128 a[252:255], v227 offset:24576\n"
"v_perm_b32 v191, v207, v206, s93\n"
"v_mfma_f32_16x16x32_bf16 v[200:203], a[228:231], a[164:167], v[200:203]\n"
"v_exp_f32_e32 v193, v193\n"
"v_mfma_f32_16x16x32_bf16 v[200:203], a[232:235], a[168:171], v[200:203]\n"
"ds_read_b64_tr_b16 a[208:209], v230 offset:8192\n"
"v_perm_b32 v128, v193, v192, s93\n"
"v_mfma_f32_16x16x32_bf16 v[200:203], a[236:239], a[172:175], v[200:203]\n"
"s_add_u32 m0, s46, 0\n"
"v_exp_f32_e32 v194, v194\n"
"v_mfma_f32_16x16x32_bf16 v[204:207], a[236:239], a[188:191], v[220:223]\n"
"s_add_u32 s44, s44, 16384\n"
"ds_read_b64_tr_b16 a[210:211], v230 offset:24576\n"
"v_mfma_f32_16x16x32_bf16 v[204:207], a[232:235], a[184:187], v[204:207]\n"
"v_exp_f32_e32 v195, v195\n"
"v_mfma_f32_16x16x32_bf16 v[204:207], a[228:231], a[180:183], v[204:207]\n"
"buffer_load_dwordx4 v236, s[36:39], s44 offen offset:0 lds\n"
"v_mfma_f32_16x16x32_bf16 v[204:207], a[224:227], a[176:179], v[204:207]\n"
"s_waitcnt lgkmcnt(6)\n"
"v_exp_f32_e32 v196, v196\n"
"v_mfma_f32_16x16x32_bf16 v[0:3], a[192:195], v[160:163], v[0:3]\n"
"ds_read_b64_tr_b16 a[212:213], v230 offset:40960\n"
"v_perm_b32 v129, v195, v194, s93\n"
"v_mfma_f32_16x16x32_bf16 v[0:3], a[196:199], v[164:167], v[0:3]\n"
"v_exp_f32_e32 v197, v197\n"
"v_mfma_f32_16x16x32_bf16 v[4:7], a[196:199], v[172:175], v[4:7]\n"
"ds_read_b64_tr_b16 a[214:215], v230 offset:57344\n"
"v_perm_b32 v136, v197, v196, s93\n"
"v_mfma_f32_16x16x32_bf16 v[4:7], a[192:195], v[168:171], v[4:7]\n"
"v_exp_f32_e32 v198, v198\n"
"v_mfma_f32_16x16x32_bf16 v[8:11], a[192:195], v[176:179], v[8:11]\n"
"ds_read_b64_tr_b16 a[216:217], v231 offset:8192\n"
"v_mfma_f32_16x16x32_bf16 v[8:11], a[196:199], v[180:183], v[8:11]\n"
"v_exp_f32_e32 v199, v199\n"
"v_mfma_f32_16x16x32_bf16 v[12:15], a[196:199], v[188:191], v[12:15]\n"
"buffer_load_dwordx4 v237, s[36:39], s44 offen offset:1024 lds\n"
"v_mfma_f32_16x16x32_bf16 v[12:15], a[192:195], v[184:187], v[12:15]\n"
"v_exp_f32_e32 v200, v200\n"
"v_mfma_f32_16x16x32_bf16 v[28:31], a[200:203], v[184:187], v[28:31]\n"
"ds_read_b64_tr_b16 a[218:219], v231 offset:24576\n"
"v_perm_b32 v137, v199, v198, s93\n"
"v_mfma_f32_16x16x32_bf16 v[28:31], a[204:207], v[188:191], v[28:31]\n"
"v_exp_f32_e32 v201, v201\n"
"v_mfma_f32_16x16x32_bf16 v[24:27], a[204:207], v[180:183], v[24:27]\n"
"ds_read_b64_tr_b16 a[220:221], v231 offset:40960\n"
"v_perm_b32 v144, v201, v200, s93\n"
"v_mfma_f32_16x16x32_bf16 v[24:27], a[200:203], v[176:179], v[24:27]\n"
"v_exp_f32_e32 v202, v202\n"
"v_mfma_f32_16x16x32_bf16 v[20:23], a[200:203], v[168:171], v[20:23]\n"
"ds_read_b64_tr_b16 a[222:223], v231 offset:57344\n"
"v_mfma_f32_16x16x32_bf16 v[20:23], a[204:207], v[172:175], v[20:23]\n"
"v_exp_f32_e32 v203, v203\n"
"v_mfma_f32_16x16x32_bf16 v[16:19], a[204:207], v[164:167], v[16:19]\n"
"buffer_load_dwordx4 v238, s[36:39], s44 offen offset:2048 lds\n"
"v_mfma_f32_16x16x32_bf16 v[16:19], a[200:203], v[160:163], v[16:19]\n"
"v_exp_f32_e32 v204, v204\n"
"v_smfmac_f32_16x16x64_bf16 a[112:115], a[92:95], v[160:167], v244\n"
"s_waitcnt lgkmcnt(8)\n"
"v_perm_b32 v145, v203, v202, s93\n"
"v_mfma_f32_16x16x32_bf16 v[192:195], a[240:243], a[128:131], v[208:211]\n"
"ds_read_b128 a[224:227], v224 offset:40960\n"
"v_mfma_f32_16x16x32_bf16 v[192:195], a[244:247], a[132:135], v[192:195]\n"
"v_exp_f32_e32 v205, v205\n"
"v_mfma_f32_16x16x32_bf16 v[192:195], a[248:251], a[136:139], v[192:195]\n"
"ds_read_b128 a[228:231], v225 offset:40960\n"
"v_perm_b32 v152, v205, v204, s93\n"
"v_mfma_f32_16x16x32_bf16 v[192:195], a[252:255], a[140:143], v[192:195]\n"
"v_exp_f32_e32 v206, v206\n"
"v_mfma_f32_16x16x32_bf16 v[196:199], a[252:255], a[156:159], v[212:215]\n"
"ds_read_b128 a[232:235], v226 offset:40960\n"
"v_mfma_f32_16x16x32_bf16 v[196:199], a[248:251], a[152:155], v[196:199]\n"
"v_exp_f32_e32 v207, v207\n"
"v_mfma_f32_16x16x32_bf16 v[196:199], a[244:247], a[148:151], v[196:199]\n"
"buffer_load_dwordx4 v239, s[36:39], s44 offen offset:3072 lds\n"
"v_mfma_f32_16x16x32_bf16 v[196:199], a[240:243], a[144:147], v[196:199]\n"
"v_exp_f32_e32 v192, v192\n"
"v_mfma_f32_16x16x32_bf16 v[200:203], a[240:243], a[160:163], v[216:219]\n"
"ds_read_b128 a[236:239], v227 offset:40960\n"
"v_perm_b32 v153, v207, v206, s93\n"
"v_mfma_f32_16x16x32_bf16 v[200:203], a[244:247], a[164:167], v[200:203]\n"
"v_exp_f32_e32 v193, v193\n"
"v_mfma_f32_16x16x32_bf16 v[200:203], a[248:251], a[168:171], v[200:203]\n"
"ds_read_b64_tr_b16 a[192:193], v232 offset:8192\n"
"v_perm_b32 v130, v193, v192, s93\n"
"v_mfma_f32_16x16x32_bf16 v[200:203], a[252:255], a[172:175], v[200:203]\n"
"s_add_u32 m0, s46, 65536\n"
"v_exp_f32_e32 v194, v194\n"
"v_mfma_f32_16x16x32_bf16 v[204:207], a[252:255], a[188:191], v[220:223]\n"
"s_add_u32 s45, s45, 16384\n"
"ds_read_b64_tr_b16 a[194:195], v232 offset:24576\n"
"v_mfma_f32_16x16x32_bf16 v[204:207], a[248:251], a[184:187], v[204:207]\n"
"v_exp_f32_e32 v195, v195\n"
"v_mfma_f32_16x16x32_bf16 v[204:207], a[244:247], a[180:183], v[204:207]\n"
"buffer_load_dwordx4 v240, s[40:43], s45 offen offset:0 lds\n"
"v_mfma_f32_16x16x32_bf16 v[204:207], a[240:243], a[176:179], v[204:207]\n"
"s_waitcnt lgkmcnt(6)\n"
"v_exp_f32_e32 v196, v196\n"
"v_mfma_f32_16x16x32_bf16 v[32:35], a[208:211], v[160:163], v[32:35]\n"
"ds_read_b64_tr_b16 a[196:197], v232 offset:40960\n"
"v_perm_b32 v131, v195, v194, s93\n"
"v_mfma_f32_16x16x32_bf16 v[32:35], a[212:215], v[164:167], v[32:35]\n"
"v_exp_f32_e32 v197, v197\n"
"v_mfma_f32_16x16x32_bf16 v[36:39], a[212:215], v[172:175], v[36:39]\n"
"ds_read_b64_tr_b16 a[198:199], v232 offset:57344\n"
"v_perm_b32 v138, v197, v196, s93\n"
"v_mfma_f32_16x16x32_bf16 v[36:39], a[208:211], v[168:171], v[36:39]\n"
"v_exp_f32_e32 v198, v198\n"
"v_mfma_f32_16x16x32_bf16 v[40:43], a[208:211], v[176:179], v[40:43]\n"
"ds_read_b64_tr_b16 a[200:201], v233 offset:8192\n"
"v_mfma_f32_16x16x32_bf16 v[40:43], a[212:215], v[180:183], v[40:43]\n"
"v_exp_f32_e32 v199, v199\n"
"v_mfma_f32_16x16x32_bf16 v[44:47], a[212:215], v[188:191], v[44:47]\n"
"buffer_load_dwordx4 v241, s[40:43], s45 offen offset:1024 lds\n"
"v_mfma_f32_16x16x32_bf16 v[44:47], a[208:211], v[184:187], v[44:47]\n"
"v_exp_f32_e32 v200, v200\n"
"v_mfma_f32_16x16x32_bf16 v[60:63], a[216:219], v[184:187], v[60:63]\n"
"ds_read_b64_tr_b16 a[202:203], v233 offset:24576\n"
"v_perm_b32 v139, v199, v198, s93\n"
"v_mfma_f32_16x16x32_bf16 v[60:63], a[220:223], v[188:191], v[60:63]\n"
"v_exp_f32_e32 v201, v201\n"
"v_mfma_f32_16x16x32_bf16 v[56:59], a[220:223], v[180:183], v[56:59]\n"
"ds_read_b64_tr_b16 a[204:205], v233 offset:40960\n"
"v_perm_b32 v146, v201, v200, s93\n"
"v_mfma_f32_16x16x32_bf16 v[56:59], a[216:219], v[176:179], v[56:59]\n"
"v_exp_f32_e32 v202, v202\n"
"v_mfma_f32_16x16x32_bf16 v[52:55], a[216:219], v[168:171], v[52:55]\n"
"ds_read_b64_tr_b16 a[206:207], v233 offset:57344\n"
"v_mfma_f32_16x16x32_bf16 v[52:55], a[220:223], v[172:175], v[52:55]\n"
"v_exp_f32_e32 v203, v203\n"
"v_mfma_f32_16x16x32_bf16 v[48:51], a[220:223], v[164:167], v[48:51]\n"
"v_perm_b32 v147, v203, v202, s93\n"
"v_mfma_f32_16x16x32_bf16 v[48:51], a[216:219], v[160:163], v[48:51]\n"
"v_exp_f32_e32 v204, v204\n"
"v_smfmac_f32_16x16x64_bf16 a[116:119], a[92:95], v[168:175], v244\n"
"s_waitcnt lgkmcnt(8)\n"
"v_mfma_f32_16x16x32_bf16 v[192:195], a[224:227], a[128:131], v[208:211]\n"
"ds_read_b128 a[240:243], v224 offset:57344\n"
"v_mfma_f32_16x16x32_bf16 v[192:195], a[228:231], a[132:135], v[192:195]\n"
"v_exp_f32_e32 v205, v205\n"
"v_mfma_f32_16x16x32_bf16 v[192:195], a[232:235], a[136:139], v[192:195]\n"
"ds_read_b128 a[244:247], v225 offset:57344\n"
"v_perm_b32 v154, v205, v204, s93\n"
"v_mfma_f32_16x16x32_bf16 v[192:195], a[236:239], a[140:143], v[192:195]\n"
"v_exp_f32_e32 v206, v206\n"
"v_mfma_f32_16x16x32_bf16 v[196:199], a[236:239], a[156:159], v[212:215]\n"
"ds_read_b128 a[248:251], v226 offset:57344\n"
"v_mfma_f32_16x16x32_bf16 v[196:199], a[232:235], a[152:155], v[196:199]\n"
"v_exp_f32_e32 v207, v207\n"
"v_mfma_f32_16x16x32_bf16 v[196:199], a[228:231], a[148:151], v[196:199]\n"
"buffer_load_dwordx4 v240, s[40:43], s45 offen offset:2048 lds\n"
"v_mfma_f32_16x16x32_bf16 v[196:199], a[224:227], a[144:147], v[196:199]\n"
"v_exp_f32_e32 v192, v192\n"
"v_mfma_f32_16x16x32_bf16 v[200:203], a[224:227], a[160:163], v[216:219]\n"
"ds_read_b128 a[252:255], v227 offset:57344\n"
"v_perm_b32 v155, v207, v206, s93\n"
"v_mfma_f32_16x16x32_bf16 v[200:203], a[228:231], a[164:167], v[200:203]\n"
"v_exp_f32_e32 v193, v193\n"
"v_mfma_f32_16x16x32_bf16 v[200:203], a[232:235], a[168:171], v[200:203]\n"
"ds_read_b64_tr_b16 a[208:209], v234 offset:8192\n"
"v_perm_b32 v132, v193, v192, s93\n"
"v_mfma_f32_16x16x32_bf16 v[200:203], a[236:239], a[172:175], v[200:203]\n"
"v_exp_f32_e32 v194, v194\n"
"v_mfma_f32_16x16x32_bf16 v[204:207], a[236:239], a[188:191], v[220:223]\n"
"ds_read_b64_tr_b16 a[210:211], v234 offset:24576\n"
"v_mfma_f32_16x16x32_bf16 v[204:207], a[232:235], a[184:187], v[204:207]\n"
"v_exp_f32_e32 v195, v195\n"
"v_mfma_f32_16x16x32_bf16 v[204:207], a[228:231], a[180:183], v[204:207]\n"
"buffer_load_dwordx4 v241, s[40:43], s45 offen offset:3072 lds\n"
"v_mfma_f32_16x16x32_bf16 v[204:207], a[224:227], a[176:179], v[204:207]\n"
"s_waitcnt lgkmcnt(6)\n"
"v_exp_f32_e32 v196, v196\n"
"v_mfma_f32_16x16x32_bf16 v[64:67], a[192:195], v[160:163], v[64:67]\n"
"ds_read_b64_tr_b16 a[212:213], v234 offset:40960\n"
"v_perm_b32 v133, v195, v194, s93\n"
"v_mfma_f32_16x16x32_bf16 v[64:67], a[196:199], v[164:167], v[64:67]\n"
"v_exp_f32_e32 v197, v197\n"
"v_mfma_f32_16x16x32_bf16 v[68:71], a[196:199], v[172:175], v[68:71]\n"
"ds_read_b64_tr_b16 a[214:215], v234 offset:57344\n"
"v_perm_b32 v140, v197, v196, s93\n"
"v_mfma_f32_16x16x32_bf16 v[68:71], a[192:195], v[168:171], v[68:71]\n"
"s_add_u32 m0, s46, 4096\n"
"v_exp_f32_e32 v198, v198\n"
"v_mfma_f32_16x16x32_bf16 v[72:75], a[192:195], v[176:179], v[72:75]\n"
"s_add_u32 s44, s44, 16384\n"
"ds_read_b64_tr_b16 a[216:217], v235 offset:8192\n"
"v_mfma_f32_16x16x32_bf16 v[72:75], a[196:199], v[180:183], v[72:75]\n"
"v_exp_f32_e32 v199, v199\n"
"v_mfma_f32_16x16x32_bf16 v[76:79], a[196:199], v[188:191], v[76:79]\n"
"buffer_load_dwordx4 v236, s[36:39], s44 offen offset:0 lds\n"
"v_mfma_f32_16x16x32_bf16 v[76:79], a[192:195], v[184:187], v[76:79]\n"
"v_exp_f32_e32 v200, v200\n"
"v_mfma_f32_16x16x32_bf16 v[92:95], a[200:203], v[184:187], v[92:95]\n"
"ds_read_b64_tr_b16 a[218:219], v235 offset:24576\n"
"v_perm_b32 v141, v199, v198, s93\n"
"v_mfma_f32_16x16x32_bf16 v[92:95], a[204:207], v[188:191], v[92:95]\n"
"v_exp_f32_e32 v201, v201\n"
"v_mfma_f32_16x16x32_bf16 v[88:91], a[204:207], v[180:183], v[88:91]\n"
"ds_read_b64_tr_b16 a[220:221], v235 offset:40960\n"
"v_perm_b32 v148, v201, v200, s93\n"
"v_mfma_f32_16x16x32_bf16 v[88:91], a[200:203], v[176:179], v[88:91]\n"
"v_exp_f32_e32 v202, v202\n"
"v_mfma_f32_16x16x32_bf16 v[84:87], a[200:203], v[168:171], v[84:87]\n"
"ds_read_b64_tr_b16 a[222:223], v235 offset:57344\n"
"v_mfma_f32_16x16x32_bf16 v[84:87], a[204:207], v[172:175], v[84:87]\n"
"v_exp_f32_e32 v203, v203\n"
"v_mfma_f32_16x16x32_bf16 v[80:83], a[204:207], v[164:167], v[80:83]\n"
"v_perm_b32 v149, v203, v202, s93\n"
"v_mfma_f32_16x16x32_bf16 v[80:83], a[200:203], v[160:163], v[80:83]\n"
"v_exp_f32_e32 v204, v204\n"
"v_smfmac_f32_16x16x64_bf16 a[120:123], a[92:95], v[176:183], v244\n"
"s_waitcnt lgkmcnt(8)\n"
"v_mfma_f32_16x16x32_bf16 v[192:195], a[240:243], a[128:131], v[208:211]\n"
"ds_read_b128 a[224:227], v224 offset:12288\n"
"v_mfma_f32_16x16x32_bf16 v[192:195], a[244:247], a[132:135], v[192:195]\n"
"v_exp_f32_e32 v205, v205\n"
"v_mfma_f32_16x16x32_bf16 v[192:195], a[248:251], a[136:139], v[192:195]\n"
"ds_read_b128 a[228:231], v225 offset:12288\n"
"v_perm_b32 v156, v205, v204, s93\n"
"v_mfma_f32_16x16x32_bf16 v[192:195], a[252:255], a[140:143], v[192:195]\n"
"v_exp_f32_e32 v206, v206\n"
"v_mfma_f32_16x16x32_bf16 v[196:199], a[252:255], a[156:159], v[212:215]\n"
"ds_read_b128 a[232:235], v226 offset:12288\n"
"v_mfma_f32_16x16x32_bf16 v[196:199], a[248:251], a[152:155], v[196:199]\n"
"v_exp_f32_e32 v207, v207\n"
"v_mfma_f32_16x16x32_bf16 v[196:199], a[244:247], a[148:151], v[196:199]\n"
"buffer_load_dwordx4 v237, s[36:39], s44 offen offset:1024 lds\n"
"v_mfma_f32_16x16x32_bf16 v[196:199], a[240:243], a[144:147], v[196:199]\n"
"v_exp_f32_e32 v192, v192\n"
"v_mfma_f32_16x16x32_bf16 v[200:203], a[240:243], a[160:163], v[216:219]\n"
"ds_read_b128 a[236:239], v227 offset:12288\n"
"v_perm_b32 v157, v207, v206, s93\n"
"v_mfma_f32_16x16x32_bf16 v[200:203], a[244:247], a[164:167], v[200:203]\n"
"v_exp_f32_e32 v193, v193\n"
"v_mfma_f32_16x16x32_bf16 v[200:203], a[248:251], a[168:171], v[200:203]\n"
"ds_read_b64_tr_b16 a[192:193], v228 offset:12288\n"
"v_perm_b32 v134, v193, v192, s93\n"
"v_mfma_f32_16x16x32_bf16 v[200:203], a[252:255], a[172:175], v[200:203]\n"
"v_exp_f32_e32 v194, v194\n"
"v_mfma_f32_16x16x32_bf16 v[204:207], a[252:255], a[188:191], v[220:223]\n"
"ds_read_b64_tr_b16 a[194:195], v228 offset:28672\n"
"v_mfma_f32_16x16x32_bf16 v[204:207], a[248:251], a[184:187], v[204:207]\n"
"v_exp_f32_e32 v195, v195\n"
"v_mfma_f32_16x16x32_bf16 v[204:207], a[244:247], a[180:183], v[204:207]\n"
"buffer_load_dwordx4 v238, s[36:39], s44 offen offset:2048 lds\n"
"v_mfma_f32_16x16x32_bf16 v[204:207], a[240:243], a[176:179], v[204:207]\n"
"s_waitcnt lgkmcnt(6)\n"
"v_exp_f32_e32 v196, v196\n"
"v_mfma_f32_16x16x32_bf16 v[96:99], a[208:211], v[160:163], v[96:99]\n"
"ds_read_b64_tr_b16 a[196:197], v228 offset:45056\n"
"v_perm_b32 v135, v195, v194, s93\n"
"v_mfma_f32_16x16x32_bf16 v[96:99], a[212:215], v[164:167], v[96:99]\n"
"v_exp_f32_e32 v197, v197\n"
"v_mfma_f32_16x16x32_bf16 v[100:103], a[212:215], v[172:175], v[100:103]\n"
"ds_read_b64_tr_b16 a[198:199], v228 offset:61440\n"
"v_perm_b32 v142, v197, v196, s93\n"
"v_mfma_f32_16x16x32_bf16 v[100:103], a[208:211], v[168:171], v[100:103]\n"
"v_exp_f32_e32 v198, v198\n"
"v_mfma_f32_16x16x32_bf16 v[104:107], a[208:211], v[176:179], v[104:107]\n"
"ds_read_b64_tr_b16 a[200:201], v229 offset:12288\n"
"v_mfma_f32_16x16x32_bf16 v[104:107], a[212:215], v[180:183], v[104:107]\n"
"v_exp_f32_e32 v199, v199\n"
"v_mfma_f32_16x16x32_bf16 v[108:111], a[212:215], v[188:191], v[108:111]\n"
"buffer_load_dwordx4 v239, s[36:39], s44 offen offset:3072 lds\n"
"v_mfma_f32_16x16x32_bf16 v[108:111], a[208:211], v[184:187], v[108:111]\n"
"v_exp_f32_e32 v200, v200\n"
"v_mfma_f32_16x16x32_bf16 v[124:127], a[216:219], v[184:187], v[124:127]\n"
"ds_read_b64_tr_b16 a[202:203], v229 offset:28672\n"
"v_perm_b32 v143, v199, v198, s93\n"
"v_mfma_f32_16x16x32_bf16 v[124:127], a[220:223], v[188:191], v[124:127]\n"
"v_exp_f32_e32 v201, v201\n"
"v_mfma_f32_16x16x32_bf16 v[120:123], a[220:223], v[180:183], v[120:123]\n"
"ds_read_b64_tr_b16 a[204:205], v229 offset:45056\n"
"v_perm_b32 v150, v201, v200, s93\n"
"v_mfma_f32_16x16x32_bf16 v[120:123], a[216:219], v[176:179], v[120:123]\n"
"v_exp_f32_e32 v202, v202\n"
"v_mfma_f32_16x16x32_bf16 v[116:119], a[216:219], v[168:171], v[116:119]\n"
"ds_read_b64_tr_b16 a[206:207], v229 offset:61440\n"
"v_mfma_f32_16x16x32_bf16 v[116:119], a[220:223], v[172:175], v[116:119]\n"
"v_exp_f32_e32 v203, v203\n"
"v_mfma_f32_16x16x32_bf16 v[112:115], a[220:223], v[164:167], v[112:115]\n"
"v_perm_b32 v151, v203, v202, s93\n"
"v_mfma_f32_16x16x32_bf16 v[112:115], a[216:219], v[160:163], v[112:115]\n"
"v_exp_f32_e32 v204, v204\n"
"v_smfmac_f32_16x16x64_bf16 a[124:127], a[92:95], v[184:191], v244\n"
"s_waitcnt lgkmcnt(8)\n"
"v_mfma_f32_16x16x32_bf16 v[192:195], a[224:227], a[128:131], v[208:211]\n"
"ds_read_b128 a[240:243], v224 offset:28672\n"
"v_mfma_f32_16x16x32_bf16 v[192:195], a[228:231], a[132:135], v[192:195]\n"
"v_exp_f32_e32 v205, v205\n"
"v_mfma_f32_16x16x32_bf16 v[192:195], a[232:235], a[136:139], v[192:195]\n"
"ds_read_b128 a[244:247], v225 offset:28672\n"
"v_perm_b32 v158, v205, v204, s93\n"
"v_mfma_f32_16x16x32_bf16 v[192:195], a[236:239], a[140:143], v[192:195]\n"
"s_add_u32 m0, s46, 69632\n"
"v_exp_f32_e32 v206, v206\n"
"v_mfma_f32_16x16x32_bf16 v[196:199], a[236:239], a[156:159], v[212:215]\n"
"s_add_u32 s45, s45, 16384\n"
"ds_read_b128 a[248:251], v226 offset:28672\n"
"v_mfma_f32_16x16x32_bf16 v[196:199], a[232:235], a[152:155], v[196:199]\n"
"v_exp_f32_e32 v207, v207\n"
"v_mfma_f32_16x16x32_bf16 v[196:199], a[228:231], a[148:151], v[196:199]\n"
"buffer_load_dwordx4 v240, s[40:43], s45 offen offset:0 lds\n"
"v_mfma_f32_16x16x32_bf16 v[196:199], a[224:227], a[144:147], v[196:199]\n"
"v_exp_f32_e32 v192, v192\n"
"v_mfma_f32_16x16x32_bf16 v[200:203], a[224:227], a[160:163], v[216:219]\n"
"ds_read_b128 a[252:255], v227 offset:28672\n"
"v_perm_b32 v159, v207, v206, s93\n"
"v_mfma_f32_16x16x32_bf16 v[200:203], a[228:231], a[164:167], v[200:203]\n"
"v_exp_f32_e32 v193, v193\n"
"v_mfma_f32_16x16x32_bf16 v[200:203], a[232:235], a[168:171], v[200:203]\n"
"ds_read_b64_tr_b16 a[208:209], v230 offset:12288\n"
"v_perm_b32 v160, v193, v192, s93\n"
"v_mfma_f32_16x16x32_bf16 v[200:203], a[236:239], a[172:175], v[200:203]\n"
"v_exp_f32_e32 v194, v194\n"
"v_mfma_f32_16x16x32_bf16 v[204:207], a[236:239], a[188:191], v[220:223]\n"
"ds_read_b64_tr_b16 a[210:211], v230 offset:28672\n"
"v_mfma_f32_16x16x32_bf16 v[204:207], a[232:235], a[184:187], v[204:207]\n"
"v_exp_f32_e32 v195, v195\n"
"v_mfma_f32_16x16x32_bf16 v[204:207], a[228:231], a[180:183], v[204:207]\n"
"v_perm_b32 v161, v195, v194, s93\n"
"v_mfma_f32_16x16x32_bf16 v[204:207], a[224:227], a[176:179], v[204:207]\n"
"s_waitcnt lgkmcnt(6)\n"
"v_exp_f32_e32 v196, v196\n"
"v_mfma_f32_16x16x32_bf16 v[0:3], a[192:195], v[128:131], v[0:3]\n"
"ds_read_b64_tr_b16 a[212:213], v230 offset:45056\n"
"v_mfma_f32_16x16x32_bf16 v[0:3], a[196:199], v[132:135], v[0:3]\n"
"v_exp_f32_e32 v197, v197\n"
"v_mfma_f32_16x16x32_bf16 v[4:7], a[196:199], v[140:143], v[4:7]\n"
"ds_read_b64_tr_b16 a[214:215], v230 offset:61440\n"
"v_perm_b32 v168, v197, v196, s93\n"
"v_mfma_f32_16x16x32_bf16 v[4:7], a[192:195], v[136:139], v[4:7]\n"
"v_exp_f32_e32 v198, v198\n"
"v_mfma_f32_16x16x32_bf16 v[8:11], a[192:195], v[144:147], v[8:11]\n"
"ds_read_b64_tr_b16 a[216:217], v231 offset:12288\n"
"v_mfma_f32_16x16x32_bf16 v[8:11], a[196:199], v[148:151], v[8:11]\n"
"v_exp_f32_e32 v199, v199\n"
"v_mfma_f32_16x16x32_bf16 v[12:15], a[196:199], v[156:159], v[12:15]\n"
"buffer_load_dwordx4 v241, s[40:43], s45 offen offset:1024 lds\n"
"v_mfma_f32_16x16x32_bf16 v[12:15], a[192:195], v[152:155], v[12:15]\n"
"v_exp_f32_e32 v200, v200\n"
"v_mfma_f32_16x16x32_bf16 v[28:31], a[200:203], v[152:155], v[28:31]\n"
"ds_read_b64_tr_b16 a[218:219], v231 offset:28672\n"
"v_perm_b32 v169, v199, v198, s93\n"
"v_mfma_f32_16x16x32_bf16 v[28:31], a[204:207], v[156:159], v[28:31]\n"
"v_exp_f32_e32 v201, v201\n"
"v_mfma_f32_16x16x32_bf16 v[24:27], a[204:207], v[148:151], v[24:27]\n"
"ds_read_b64_tr_b16 a[220:221], v231 offset:45056\n"
"v_perm_b32 v176, v201, v200, s93\n"
"v_mfma_f32_16x16x32_bf16 v[24:27], a[200:203], v[144:147], v[24:27]\n"
"v_exp_f32_e32 v202, v202\n"
"v_mfma_f32_16x16x32_bf16 v[20:23], a[200:203], v[136:139], v[20:23]\n"
"ds_read_b64_tr_b16 a[222:223], v231 offset:61440\n"
"v_mfma_f32_16x16x32_bf16 v[20:23], a[204:207], v[140:143], v[20:23]\n"
"v_exp_f32_e32 v203, v203\n"
"v_mfma_f32_16x16x32_bf16 v[16:19], a[204:207], v[132:135], v[16:19]\n"
"v_perm_b32 v177, v203, v202, s93\n"
"v_mfma_f32_16x16x32_bf16 v[16:19], a[200:203], v[128:131], v[16:19]\n"
"v_exp_f32_e32 v204, v204\n"
"v_smfmac_f32_16x16x64_bf16 a[112:115], a[92:95], v[128:135], v244\n"
"s_waitcnt lgkmcnt(8)\n"
"v_mfma_f32_16x16x32_bf16 v[192:195], a[240:243], a[128:131], v[208:211]\n"
"ds_read_b128 a[224:227], v224 offset:45056\n"
"v_mfma_f32_16x16x32_bf16 v[192:195], a[244:247], a[132:135], v[192:195]\n"
"v_exp_f32_e32 v205, v205\n"
"v_mfma_f32_16x16x32_bf16 v[192:195], a[248:251], a[136:139], v[192:195]\n"
"ds_read_b128 a[228:231], v225 offset:45056\n"
"v_perm_b32 v184, v205, v204, s93\n"
"v_mfma_f32_16x16x32_bf16 v[192:195], a[252:255], a[140:143], v[192:195]\n"
"v_exp_f32_e32 v206, v206\n"
"v_mfma_f32_16x16x32_bf16 v[196:199], a[252:255], a[156:159], v[212:215]\n"
"ds_read_b128 a[232:235], v226 offset:45056\n"
"v_mfma_f32_16x16x32_bf16 v[196:199], a[248:251], a[152:155], v[196:199]\n"
"v_exp_f32_e32 v207, v207\n"
"v_mfma_f32_16x16x32_bf16 v[196:199], a[244:247], a[148:151], v[196:199]\n"
"buffer_load_dwordx4 v240, s[40:43], s45 offen offset:2048 lds\n"
"v_mfma_f32_16x16x32_bf16 v[196:199], a[240:243], a[144:147], v[196:199]\n"
"v_exp_f32_e32 v192, v192\n"
"v_mfma_f32_16x16x32_bf16 v[200:203], a[240:243], a[160:163], v[216:219]\n"
"ds_read_b128 a[236:239], v227 offset:45056\n"
"v_perm_b32 v185, v207, v206, s93\n"
"v_mfma_f32_16x16x32_bf16 v[200:203], a[244:247], a[164:167], v[200:203]\n"
"v_exp_f32_e32 v193, v193\n"
"v_mfma_f32_16x16x32_bf16 v[200:203], a[248:251], a[168:171], v[200:203]\n"
"ds_read_b64_tr_b16 a[192:193], v232 offset:12288\n"
"v_perm_b32 v162, v193, v192, s93\n"
"v_mfma_f32_16x16x32_bf16 v[200:203], a[252:255], a[172:175], v[200:203]\n"
"v_exp_f32_e32 v194, v194\n"
"v_mfma_f32_16x16x32_bf16 v[204:207], a[252:255], a[188:191], v[220:223]\n"
"ds_read_b64_tr_b16 a[194:195], v232 offset:28672\n"
"v_mfma_f32_16x16x32_bf16 v[204:207], a[248:251], a[184:187], v[204:207]\n"
"v_exp_f32_e32 v195, v195\n"
"v_mfma_f32_16x16x32_bf16 v[204:207], a[244:247], a[180:183], v[204:207]\n"
"v_perm_b32 v163, v195, v194, s93\n"
"v_mfma_f32_16x16x32_bf16 v[204:207], a[240:243], a[176:179], v[204:207]\n"
"s_waitcnt lgkmcnt(6)\n"
"v_exp_f32_e32 v196, v196\n"
"v_mfma_f32_16x16x32_bf16 v[32:35], a[208:211], v[128:131], v[32:35]\n"
"ds_read_b64_tr_b16 a[196:197], v232 offset:45056\n"
"v_mfma_f32_16x16x32_bf16 v[32:35], a[212:215], v[132:135], v[32:35]\n"
"v_exp_f32_e32 v197, v197\n"
"v_mfma_f32_16x16x32_bf16 v[36:39], a[212:215], v[140:143], v[36:39]\n"
"ds_read_b64_tr_b16 a[198:199], v232 offset:61440\n"
"v_perm_b32 v170, v197, v196, s93\n"
"v_mfma_f32_16x16x32_bf16 v[36:39], a[208:211], v[136:139], v[36:39]\n"
"v_exp_f32_e32 v198, v198\n"
"v_mfma_f32_16x16x32_bf16 v[40:43], a[208:211], v[144:147], v[40:43]\n"
"ds_read_b64_tr_b16 a[200:201], v233 offset:12288\n"
"v_mfma_f32_16x16x32_bf16 v[40:43], a[212:215], v[148:151], v[40:43]\n"
"v_exp_f32_e32 v199, v199\n"
"v_mfma_f32_16x16x32_bf16 v[44:47], a[212:215], v[156:159], v[44:47]\n"
"buffer_load_dwordx4 v241, s[40:43], s45 offen offset:3072 lds\n"
"v_mfma_f32_16x16x32_bf16 v[44:47], a[208:211], v[152:155], v[44:47]\n"
"v_exp_f32_e32 v200, v200\n"
"v_mfma_f32_16x16x32_bf16 v[60:63], a[216:219], v[152:155], v[60:63]\n"
"ds_read_b64_tr_b16 a[202:203], v233 offset:28672\n"
"v_perm_b32 v171, v199, v198, s93\n"
"v_mfma_f32_16x16x32_bf16 v[60:63], a[220:223], v[156:159], v[60:63]\n"
"v_exp_f32_e32 v201, v201\n"
"v_mfma_f32_16x16x32_bf16 v[56:59], a[220:223], v[148:151], v[56:59]\n"
"ds_read_b64_tr_b16 a[204:205], v233 offset:45056\n"
"v_perm_b32 v178, v201, v200, s93\n"
"v_mfma_f32_16x16x32_bf16 v[56:59], a[216:219], v[144:147], v[56:59]\n"
"v_exp_f32_e32 v202, v202\n"
"v_mfma_f32_16x16x32_bf16 v[52:55], a[216:219], v[136:139], v[52:55]\n"
"ds_read_b64_tr_b16 a[206:207], v233 offset:61440\n"
"v_mfma_f32_16x16x32_bf16 v[52:55], a[220:223], v[140:143], v[52:55]\n"
"v_exp_f32_e32 v203, v203\n"
"v_mfma_f32_16x16x32_bf16 v[48:51], a[220:223], v[132:135], v[48:51]\n"
".LPG12_%=:\n"
"s_cbranch_execz .LPGR_%=\n"
"v_perm_b32 v179, v203, v202, s93\n"
"v_mfma_f32_16x16x32_bf16 v[48:51], a[216:219], v[128:131], v[48:51]\n"
"v_exp_f32_e32 v204, v204\n"
"v_smfmac_f32_16x16x64_bf16 a[116:119], a[92:95], v[136:143], v244\n"
"s_waitcnt lgkmcnt(8)\n"
"s_waitcnt vmcnt(16)\n"
"v_readlane_b32 s82, v249, 0\n"
"v_readlane_b32 s83, v249, 1\n"
"s_nop 0\n"
"s_cmp_eq_u64 s[82:83], s[32:33]\n"
"s_cbranch_scc0 .LHS_%=\n"
".LHSOK_%=:\n"
"v_mfma_f32_16x16x32_bf16 v[192:195], a[224:227], a[128:131], v[208:211]\n"
"ds_read_b128 a[240:243], v224 offset:61440\n"
"v_mfma_f32_16x16x32_bf16 v[192:195], a[228:231], a[132:135], v[192:195]\n"
"v_exp_f32_e32 v205, v205\n"
"v_mfma_f32_16x16x32_bf16 v[192:195], a[232:235], a[136:139], v[192:195]\n"
"ds_read_b128 a[244:247], v225 offset:61440\n"
"v_perm_b32 v186, v205, v204, s93\n"
"v_mfma_f32_16x16x32_bf16 v[192:195], a[236:239], a[140:143], v[192:195]\n"
"v_exp_f32_e32 v206, v206\n"
"v_mfma_f32_16x16x32_bf16 v[196:199], a[236:239], a[156:159], v[212:215]\n"
"ds_read_b128 a[248:251], v226 offset:61440\n"
"v_mfma_f32_16x16x32_bf16 v[196:199], a[232:235], a[152:155], v[196:199]\n"
"v_exp_f32_e32 v207, v207\n"
"v_mfma_f32_16x16x32_bf16 v[196:199], a[228:231], a[148:151], v[196:199]\n"
"v_perm_b32 v187, v207, v206, s93\n"
"v_mfma_f32_16x16x32_bf16 v[196:199], a[224:227], a[144:147], v[196:199]\n"
"v_exp_f32_e32 v192, v192\n"
"v_mfma_f32_16x16x32_bf16 v[200:203], a[224:227], a[160:163], v[216:219]\n"
"ds_read_b128 a[252:255], v227 offset:61440\n"
"v_mfma_f32_16x16x32_bf16 v[200:203], a[228:231], a[164:167], v[200:203]\n"
"v_exp_f32_e32 v193, v193\n"
"v_mfma_f32_16x16x32_bf16 v[200:203], a[232:235], a[168:171], v[200:203]\n"
"ds_read_b64_tr_b16 a[208:209], v234 offset:12288\n"
"v_perm_b32 v164, v193, v192, s93\n"
"v_mfma_f32_16x16x32_bf16 v[200:203], a[236:239], a[172:175], v[200:203]\n"
"v_exp_f32_e32 v194, v194\n"
"v_mfma_f32_16x16x32_bf16 v[204:207], a[236:239], a[188:191], v[220:223]\n"
"ds_read_b64_tr_b16 a[210:211], v234 offset:28672\n"
"v_mfma_f32_16x16x32_bf16 v[204:207], a[232:235], a[184:187], v[204:207]\n"
"v_exp_f32_e32 v195, v195\n"
"v_mfma_f32_16x16x32_bf16 v[204:207], a[228:231], a[180:183], v[204:207]\n"
"v_perm_b32 v165, v195, v194, s93\n"
"v_mfma_f32_16x16x32_bf16 v[204:207], a[224:227], a[176:179], v[204:207]\n"
"s_waitcnt lgkmcnt(6)\n"
"v_exp_f32_e32 v196, v196\n"
"v_mfma_f32_16x16x32_bf16 v[64:67], a[192:195], v[128:131], v[64:67]\n"
"ds_read_b64_tr_b16 a[212:213], v234 offset:45056\n"
"v_mfma_f32_16x16x32_bf16 v[64:67], a[196:199], v[132:135], v[64:67]\n"
"v_exp_f32_e32 v197, v197\n"
"v_mfma_f32_16x16x32_bf16 v[68:71], a[196:199], v[140:143], v[68:71]\n"
"ds_read_b64_tr_b16 a[214:215], v234 offset:61440\n"
"v_perm_b32 v172, v197, v196, s93\n"
"v_mfma_f32_16x16x32_bf16 v[68:71], a[192:195], v[136:139], v[68:71]\n"
"v_exp_f32_e32 v198, v198\n"
"v_mfma_f32_16x16x32_bf16 v[72:75], a[192:195], v[144:147], v[72:75]\n"
"ds_read_b64_tr_b16 a[216:217], v235 offset:12288\n"
"v_mfma_f32_16x16x32_bf16 v[72:75], a[196:199], v[148:151], v[72:75]\n"
"v_exp_f32_e32 v199, v199\n"
"v_mfma_f32_16x16x32_bf16 v[76:79], a[196:199], v[156:159], v[76:79]\n"
"v_perm_b32 v173, v199, v198, s93\n"
"v_mfma_f32_16x16x32_bf16 v[76:79], a[192:195], v[152:155], v[76:79]\n"
"v_exp_f32_e32 v200, v200\n"
"v_mfma_f32_16x16x32_bf16 v[92:95], a[200:203], v[152:155], v[92:95]\n"
"ds_read_b64_tr_b16 a[218:219], v235 offset:28672\n"
"v_mfma_f32_16x16x32_bf16 v[92:95], a[204:207], v[156:159], v[92:95]\n"
"v_exp_f32_e32 v201, v201\n"
"v_mfma_f32_16x16x32_bf16 v[88:91], a[204:207], v[148:151], v[88:91]\n"
"ds_read_b64_tr_b16 a[220:221], v235 offset:45056\n"
"v_perm_b32 v180, v201, v200, s93\n"
"v_mfma_f32_16x16x32_bf16 v[88:91], a[200:203], v[144:147], v[88:91]\n"
"v_exp_f32_e32 v202, v202\n"
"v_mfma_f32_16x16x32_bf16 v[84:87], a[200:203], v[136:139], v[84:87]\n"
"ds_read_b64_tr_b16 a[222:223], v235 offset:61440\n"
"v_mfma_f32_16x16x32_bf16 v[84:87], a[204:207], v[140:143], v[84:87]\n"
"v_exp_f32_e32 v203, v203\n"
"v_mfma_f32_16x16x32_bf16 v[80:83], a[204:207], v[132:135], v[80:83]\n"
"v_perm_b32 v181, v203, v202, s93\n"
"v_mfma_f32_16x16x32_bf16 v[80:83], a[200:203], v[128:131], v[80:83]\n"
"s_waitcnt vmcnt(0) lgkmcnt(0)\n"
"v_exp_f32_e32 v204, v204\n"
"v_smfmac_f32_16x16x64_bf16 a[120:123], a[92:95], v[144:151], v244\n"
"v_mfma_f32_16x16x32_bf16 v[192:195], a[240:243], a[128:131], v[208:211]\n"
"s_barrier\n"
"ds_read_b128 a[224:227], v224 offset:0\n"
"v_mfma_f32_16x16x32_bf16 v[192:195], a[244:247], a[132:135], v[192:195]\n"
"v_exp_f32_e32 v205, v205\n"
"v_mfma_f32_16x16x32_bf16 v[192:195], a[248:251], a[136:139], v[192:195]\n"
"ds_read_b128 a[228:231], v225 offset:0\n"
"v_perm_b32 v188, v205, v204, s93\n"
"v_mfma_f32_16x16x32_bf16 v[192:195], a[252:255], a[140:143], v[192:195]\n"
"v_exp_f32_e32 v206, v206\n"
"v_mfma_f32_16x16x32_bf16 v[196:199], a[252:255], a[156:159], v[212:215]\n"
"ds_read_b128 a[232:235], v226 offset:0\n"
"v_mfma_f32_16x16x32_bf16 v[196:199], a[248:251], a[152:155], v[196:199]\n"
"v_exp_f32_e32 v207, v207\n"
"v_mfma_f32_16x16x32_bf16 v[196:199], a[244:247], a[148:151], v[196:199]\n"
"buffer_load_dword a0, v246, s[24:27], s30 offen sc1\n"
"v_mfma_f32_16x16x32_bf16 v[196:199], a[240:243], a[144:147], v[196:199]\n"
"v_exp_f32_e32 v192, v192\n"
"v_mfma_f32_16x16x32_bf16 v[200:203], a[240:243], a[160:163], v[216:219]\n"
"ds_read_b128 a[236:239], v227 offset:0\n"
"v_perm_b32 v189, v207, v206, s93\n"
"v_mfma_f32_16x16x32_bf16 v[200:203], a[244:247], a[164:167], v[200:203]\n"
"v_exp_f32_e32 v193, v193\n"
"v_mfma_f32_16x16x32_bf16 v[200:203], a[248:251], a[168:171], v[200:203]\n"
"ds_read_b64_tr_b16 a[192:193], v228 offset:0\n"
"v_perm_b32 v166, v193, v192, s93\n"
"v_mfma_f32_16x16x32_bf16 v[200:203], a[252:255], a[172:175], v[200:203]\n"
"s_add_u32 s94, s22, 20971520\n"
"v_exp_f32_e32 v194, v194\n"
"v_mfma_f32_16x16x32_bf16 v[204:207], a[252:255], a[188:191], v[220:223]\n"
"ds_read_b64_tr_b16 a[194:195], v228 offset:16384\n"
"v_mfma_f32_16x16x32_bf16 v[204:207], a[248:251], a[184:187], v[204:207]\n"
"v_exp_f32_e32 v195, v195\n"
"v_mfma_f32_16x16x32_bf16 v[204:207], a[244:247], a[180:183], v[204:207]\n"
"buffer_load_dwordx4 a[12:15], v255, s[52:55], s94 offen offset:0 sc1\n"
"v_mfma_f32_16x16x32_bf16 v[204:207], a[240:243], a[176:179], v[204:207]\n"
"v_exp_f32_e32 v196, v196\n"
"v_mfma_f32_16x16x32_bf16 v[96:99], a[208:211], v[128:131], v[96:99]\n"
"ds_read_b64_tr_b16 a[196:197], v228 offset:32768\n"
"v_perm_b32 v167, v195, v194, s93\n"
"v_mfma_f32_16x16x32_bf16 v[96:99], a[212:215], v[132:135], v[96:99]\n"
"v_exp_f32_e32 v197, v197\n"
"v_mfma_f32_16x16x32_bf16 v[100:103], a[212:215], v[140:143], v[100:103]\n"
"ds_read_b64_tr_b16 a[198:199], v228 offset:49152\n"
"v_perm_b32 v174, v197, v196, s93\n"
"v_mfma_f32_16x16x32_bf16 v[100:103], a[208:211], v[136:139], v[100:103]\n"
"v_exp_f32_e32 v198, v198\n"
"v_mfma_f32_16x16x32_bf16 v[104:107], a[208:211], v[144:147], v[104:107]\n"
"ds_read_b64_tr_b16 a[200:201], v229 offset:0\n"
"v_mfma_f32_16x16x32_bf16 v[104:107], a[212:215], v[148:151], v[104:107]\n"
"v_exp_f32_e32 v199, v199\n"
"v_mfma_f32_16x16x32_bf16 v[108:111], a[212:215], v[156:159], v[108:111]\n"
"buffer_load_dword a10, v255, s[60:63], s86 offen\n"
"v_mfma_f32_16x16x32_bf16 v[108:111], a[208:211], v[152:155], v[108:111]\n"
"v_exp_f32_e32 v200, v200\n"
"v_mfma_f32_16x16x32_bf16 v[124:127], a[216:219], v[152:155], v[124:127]\n"
"ds_read_b64_tr_b16 a[202:203], v229 offset:16384\n"
"v_perm_b32 v175, v199, v198, s93\n"
"v_mfma_f32_16x16x32_bf16 v[124:127], a[220:223], v[156:159], v[124:127]\n"
"v_exp_f32_e32 v201, v201\n"
"v_mfma_f32_16x16x32_bf16 v[120:123], a[220:223], v[148:151], v[120:123]\n"
"ds_read_b64_tr_b16 a[204:205], v229 offset:32768\n"
"v_perm_b32 v182, v201, v200, s93\n"
"v_mfma_f32_16x16x32_bf16 v[120:123], a[216:219], v[144:147], v[120:123]\n"
"v_exp_f32_e32 v202, v202\n"
"v_mfma_f32_16x16x32_bf16 v[116:119], a[216:219], v[136:139], v[116:119]\n"
"ds_read_b64_tr_b16 a[206:207], v229 offset:49152\n"
"v_mfma_f32_16x16x32_bf16 v[116:119], a[220:223], v[140:143], v[116:119]\n"
"v_exp_f32_e32 v203, v203\n"
"v_mfma_f32_16x16x32_bf16 v[112:115], a[220:223], v[132:135], v[112:115]\n"
"v_perm_b32 v183, v203, v202, s93\n"
"v_mfma_f32_16x16x32_bf16 v[112:115], a[216:219], v[128:131], v[112:115]\n"
"v_exp_f32_e32 v204, v204\n"
"v_smfmac_f32_16x16x64_bf16 a[124:127], a[92:95], v[152:159], v244\n"
"s_waitcnt lgkmcnt(8)\n"
"v_mfma_f32_16x16x32_bf16 v[192:195], a[224:227], a[128:131], v[208:211]\n"
"ds_read_b128 a[240:243], v224 offset:16384\n"
"v_accvgpr_write_b32 a96, 0\n"
"v_mfma_f32_16x16x32_bf16 v[192:195], a[228:231], a[132:135], v[192:195]\n"
"v_exp_f32_e32 v205, v205\n"
"v_mfma_f32_16x16x32_bf16 v[192:195], a[232:235], a[136:139], v[192:195]\n"
"ds_read_b128 a[244:247], v225 offset:16384\n"
"v_perm_b32 v190, v205, v204, s93\n"
"v_mfma_f32_16x16x32_bf16 v[192:195], a[236:239], a[140:143], v[192:195]\n"
"v_exp_f32_e32 v206, v206\n"
"v_mfma_f32_16x16x32_bf16 v[196:199], a[236:239], a[156:159], v[212:215]\n"
"ds_read_b128 a[248:251], v226 offset:16384\n"
"v_mfma_f32_16x16x32_bf16 v[196:199], a[232:235], a[152:155], v[196:199]\n"
"v_exp_f32_e32 v207, v207\n"
"v_mfma_f32_16x16x32_bf16 v[196:199], a[228:231], a[148:151], v[196:199]\n"
"buffer_load_dwordx4 a[44:47], v255, s[52:55], s94 offen offset:1024 sc1\n"
"v_mfma_f32_16x16x32_bf16 v[196:199], a[224:227], a[144:147], v[196:199]\n"
"v_exp_f32_e32 v192, v192\n"
"v_mfma_f32_16x16x32_bf16 v[200:203], a[224:227], a[160:163], v[216:219]\n"
"ds_read_b128 a[252:255], v227 offset:16384\n"
"v_perm_b32 v191, v207, v206, s93\n"
"v_mfma_f32_16x16x32_bf16 v[200:203], a[228:231], a[164:167], v[200:203]\n"
"v_exp_f32_e32 v193, v193\n"
"v_mfma_f32_16x16x32_bf16 v[200:203], a[232:235], a[168:171], v[200:203]\n"
"ds_read_b64_tr_b16 a[208:209], v230 offset:0\n"
"v_accvgpr_write_b32 a97, 0\n"
"v_mfma_f32_16x16x32_bf16 v[200:203], a[236:239], a[172:175], v[200:203]\n"
"s_add_u32 m0, s46, 8192\n"
"v_exp_f32_e32 v194, v194\n"
"v_mfma_f32_16x16x32_bf16 v[204:207], a[236:239], a[188:191], v[220:223]\n"
"s_add_u32 s44, s44, 16384\n"
"ds_read_b64_tr_b16 a[210:211], v230 offset:16384\n"
"v_perm_b32 v128, v193, v192, s93\n"
"v_mfma_f32_16x16x32_bf16 v[204:207], a[232:235], a[184:187], v[204:207]\n"
"v_exp_f32_e32 v195, v195\n"
"v_mfma_f32_16x16x32_bf16 v[204:207], a[228:231], a[180:183], v[204:207]\n"
"buffer_load_dwordx4 v236, s[36:39], s44 offen offset:0 lds\n"
"v_mfma_f32_16x16x32_bf16 v[204:207], a[224:227], a[176:179], v[204:207]\n"
"s_waitcnt lgkmcnt(6)\n"
"v_exp_f32_e32 v196, v196\n"
"v_mfma_f32_16x16x32_bf16 v[0:3], a[192:195], v[160:163], v[0:3]\n"
"ds_read_b64_tr_b16 a[212:213], v230 offset:32768\n"
"v_perm_b32 v129, v195, v194, s93\n"
"v_mfma_f32_16x16x32_bf16 v[0:3], a[196:199], v[164:167], v[0:3]\n"
"v_exp_f32_e32 v197, v197\n"
"v_mfma_f32_16x16x32_bf16 v[4:7], a[196:199], v[172:175], v[4:7]\n"
"ds_read_b64_tr_b16 a[214:215], v230 offset:49152\n"
"v_accvgpr_write_b32 a98, 0\n"
"v_mfma_f32_16x16x32_bf16 v[4:7], a[192:195], v[168:171], v[4:7]\n"
"v_exp_f32_e32 v198, v198\n"
"v_mfma_f32_16x16x32_bf16 v[8:11], a[192:195], v[176:179], v[8:11]\n"
"ds_read_b64_tr_b16 a[216:217], v231 offset:0\n"
"v_perm_b32 v136, v197, v196, s93\n"
"v_mfma_f32_16x16x32_bf16 v[8:11], a[196:199], v[180:183], v[8:11]\n"
"v_exp_f32_e32 v199, v199\n"
"v_mfma_f32_16x16x32_bf16 v[12:15], a[196:199], v[188:191], v[12:15]\n"
"buffer_load_dwordx4 v237, s[36:39], s44 offen offset:1024 lds\n"
"v_mfma_f32_16x16x32_bf16 v[12:15], a[192:195], v[184:187], v[12:15]\n"
"v_exp_f32_e32 v200, v200\n"
"v_mfma_f32_16x16x32_bf16 v[28:31], a[200:203], v[184:187], v[28:31]\n"
"ds_read_b64_tr_b16 a[218:219], v231 offset:16384\n"
"v_perm_b32 v137, v199, v198, s93\n"
"v_mfma_f32_16x16x32_bf16 v[28:31], a[204:207], v[188:191], v[28:31]\n"
"v_exp_f32_e32 v201, v201\n"
"v_mfma_f32_16x16x32_bf16 v[24:27], a[204:207], v[180:183], v[24:27]\n"
"ds_read_b64_tr_b16 a[220:221], v231 offset:32768\n"
"v_accvgpr_write_b32 a99, 0\n"
"v_mfma_f32_16x16x32_bf16 v[24:27], a[200:203], v[176:179], v[24:27]\n"
"v_exp_f32_e32 v202, v202\n"
"v_mfma_f32_16x16x32_bf16 v[20:23], a[200:203], v[168:171], v[20:23]\n"
"ds_read_b64_tr_b16 a[222:223], v231 offset:49152\n"
"v_perm_b32 v144, v201, v200, s93\n"
"v_mfma_f32_16x16x32_bf16 v[20:23], a[204:207], v[172:175], v[20:23]\n"
"v_exp_f32_e32 v203, v203\n"
"v_mfma_f32_16x16x32_bf16 v[16:19], a[204:207], v[164:167], v[16:19]\n"
"buffer_load_dwordx4 v238, s[36:39], s44 offen offset:2048 lds\n"
"v_mfma_f32_16x16x32_bf16 v[16:19], a[200:203], v[160:163], v[16:19]\n"
"v_exp_f32_e32 v204, v204\n"
"v_smfmac_f32_16x16x64_bf16 a[112:115], a[92:95], v[160:167], v244\n"
"s_waitcnt lgkmcnt(8)\n"
"v_perm_b32 v145, v203, v202, s93\n"
"v_mfma_f32_16x16x32_bf16 v[192:195], a[240:243], a[128:131], v[208:211]\n"
"ds_read_b128 a[224:227], v224 offset:32768\n"
"v_accvgpr_write_b32 a100, 0\n"
"v_mfma_f32_16x16x32_bf16 v[192:195], a[244:247], a[132:135], v[192:195]\n"
"v_exp_f32_e32 v205, v205\n"
"v_mfma_f32_16x16x32_bf16 v[192:195], a[248:251], a[136:139], v[192:195]\n"
"ds_read_b128 a[228:231], v225 offset:32768\n"
"v_perm_b32 v152, v205, v204, s93\n"
"v_mfma_f32_16x16x32_bf16 v[192:195], a[252:255], a[140:143], v[192:195]\n"
"v_exp_f32_e32 v206, v206\n"
"v_mfma_f32_16x16x32_bf16 v[196:199], a[252:255], a[156:159], v[212:215]\n"
"ds_read_b128 a[232:235], v226 offset:32768\n"
"v_mfma_f32_16x16x32_bf16 v[196:199], a[248:251], a[152:155], v[196:199]\n"
"v_exp_f32_e32 v207, v207\n"
"v_mfma_f32_16x16x32_bf16 v[196:199], a[244:247], a[148:151], v[196:199]\n"
"buffer_load_dwordx4 a[16:19], v255, s[52:55], s94 offen offset:2048 sc1\n"
"v_mfma_f32_16x16x32_bf16 v[196:199], a[240:243], a[144:147], v[196:199]\n"
"v_exp_f32_e32 v192, v192\n"
"v_mfma_f32_16x16x32_bf16 v[200:203], a[240:243], a[160:163], v[216:219]\n"
"ds_read_b128 a[236:239], v227 offset:32768\n"
"v_perm_b32 v153, v207, v206, s93\n"
"v_mfma_f32_16x16x32_bf16 v[200:203], a[244:247], a[164:167], v[200:203]\n"
"v_exp_f32_e32 v193, v193\n"
"v_mfma_f32_16x16x32_bf16 v[200:203], a[248:251], a[168:171], v[200:203]\n"
"ds_read_b64_tr_b16 a[192:193], v232 offset:0\n"
"v_accvgpr_write_b32 a101, 0\n"
"v_mfma_f32_16x16x32_bf16 v[200:203], a[252:255], a[172:175], v[200:203]\n"
"v_exp_f32_e32 v194, v194\n"
"v_mfma_f32_16x16x32_bf16 v[204:207], a[252:255], a[188:191], v[220:223]\n"
"ds_read_b64_tr_b16 a[194:195], v232 offset:16384\n"
"v_perm_b32 v130, v193, v192, s93\n"
"v_mfma_f32_16x16x32_bf16 v[204:207], a[248:251], a[184:187], v[204:207]\n"
"v_exp_f32_e32 v195, v195\n"
"v_mfma_f32_16x16x32_bf16 v[204:207], a[244:247], a[180:183], v[204:207]\n"
"buffer_load_dwordx4 v239, s[36:39], s44 offen offset:3072 lds\n"
"v_mfma_f32_16x16x32_bf16 v[204:207], a[240:243], a[176:179], v[204:207]\n"
"s_waitcnt lgkmcnt(6)\n"
"v_exp_f32_e32 v196, v196\n"
"v_mfma_f32_16x16x32_bf16 v[32:35], a[208:211], v[160:163], v[32:35]\n"
"ds_read_b64_tr_b16 a[196:197], v232 offset:32768\n"
"v_perm_b32 v131, v195, v194, s93\n"
"v_mfma_f32_16x16x32_bf16 v[32:35], a[212:215], v[164:167], v[32:35]\n"
"v_exp_f32_e32 v197, v197\n"
"v_mfma_f32_16x16x32_bf16 v[36:39], a[212:215], v[172:175], v[36:39]\n"
"ds_read_b64_tr_b16 a[198:199], v232 offset:49152\n"
"v_accvgpr_write_b32 a102, 0\n"
"v_mfma_f32_16x16x32_bf16 v[36:39], a[208:211], v[168:171], v[36:39]\n"
"s_add_u32 m0, s46, 73728\n"
"v_exp_f32_e32 v198, v198\n"
"v_mfma_f32_16x16x32_bf16 v[40:43], a[208:211], v[176:179], v[40:43]\n"
"s_add_u32 s45, s45, 16384\n"
"ds_read_b64_tr_b16 a[200:201], v233 offset:0\n"
"v_perm_b32 v138, v197, v196, s93\n"
"v_mfma_f32_16x16x32_bf16 v[40:43], a[212:215], v[180:183], v[40:43]\n"
"v_exp_f32_e32 v199, v199\n"
"v_mfma_f32_16x16x32_bf16 v[44:47], a[212:215], v[188:191], v[44:47]\n"
"buffer_load_dwordx4 v240, s[40:43], s45 offen offset:0 lds\n"
"v_mfma_f32_16x16x32_bf16 v[44:47], a[208:211], v[184:187], v[44:47]\n"
"v_exp_f32_e32 v200, v200\n"
"v_mfma_f32_16x16x32_bf16 v[60:63], a[216:219], v[184:187], v[60:63]\n"
"ds_read_b64_tr_b16 a[202:203], v233 offset:16384\n"
"v_perm_b32 v139, v199, v198, s93\n"
"v_mfma_f32_16x16x32_bf16 v[60:63], a[220:223], v[188:191], v[60:63]\n"
"v_exp_f32_e32 v201, v201\n"
"v_mfma_f32_16x16x32_bf16 v[56:59], a[220:223], v[180:183], v[56:59]\n"
"ds_read_b64_tr_b16 a[204:205], v233 offset:32768\n"
"v_accvgpr_write_b32 a103, 0\n"
"v_mfma_f32_16x16x32_bf16 v[56:59], a[216:219], v[176:179], v[56:59]\n"
"v_exp_f32_e32 v202, v202\n"
"v_mfma_f32_16x16x32_bf16 v[52:55], a[216:219], v[168:171], v[52:55]\n"
"ds_read_b64_tr_b16 a[206:207], v233 offset:49152\n"
"v_perm_b32 v146, v201, v200, s93\n"
"v_mfma_f32_16x16x32_bf16 v[52:55], a[220:223], v[172:175], v[52:55]\n"
"v_exp_f32_e32 v203, v203\n"
"v_mfma_f32_16x16x32_bf16 v[48:51], a[220:223], v[164:167], v[48:51]\n"
"buffer_load_dwordx4 v241, s[40:43], s45 offen offset:1024 lds\n"
"v_mfma_f32_16x16x32_bf16 v[48:51], a[216:219], v[160:163], v[48:51]\n"
"v_exp_f32_e32 v204, v204\n"
"v_smfmac_f32_16x16x64_bf16 a[116:119], a[92:95], v[168:175], v244\n"
"s_waitcnt lgkmcnt(8)\n"
"v_perm_b32 v147, v203, v202, s93\n"
"v_mfma_f32_16x16x32_bf16 v[192:195], a[224:227], a[128:131], v[208:211]\n"
"ds_read_b128 a[240:243], v224 offset:49152\n"
"v_accvgpr_write_b32 a104, 0\n"
"v_mfma_f32_16x16x32_bf16 v[192:195], a[228:231], a[132:135], v[192:195]\n"
"v_exp_f32_e32 v205, v205\n"
"v_mfma_f32_16x16x32_bf16 v[192:195], a[232:235], a[136:139], v[192:195]\n"
"ds_read_b128 a[244:247], v225 offset:49152\n"
"v_perm_b32 v154, v205, v204, s93\n"
"v_mfma_f32_16x16x32_bf16 v[192:195], a[236:239], a[140:143], v[192:195]\n"
"v_exp_f32_e32 v206, v206\n"
"v_mfma_f32_16x16x32_bf16 v[196:199], a[236:239], a[156:159], v[212:215]\n"
"ds_read_b128 a[248:251], v226 offset:49152\n"
"v_mfma_f32_16x16x32_bf16 v[196:199], a[232:235], a[152:155], v[196:199]\n"
"v_exp_f32_e32 v207, v207\n"
"v_mfma_f32_16x16x32_bf16 v[196:199], a[228:231], a[148:151], v[196:199]\n"
"buffer_load_dwordx4 a[48:51], v255, s[52:55], s94 offen offset:3072 sc1\n"
"v_mfma_f32_16x16x32_bf16 v[196:199], a[224:227], a[144:147], v[196:199]\n"
"v_exp_f32_e32 v192, v192\n"
"v_mfma_f32_16x16x32_bf16 v[200:203], a[224:227], a[160:163], v[216:219]\n"
"ds_read_b128 a[252:255], v227 offset:49152\n"
"v_perm_b32 v155, v207, v206, s93\n"
"v_mfma_f32_16x16x32_bf16 v[200:203], a[228:231], a[164:167], v[200:203]\n"
"v_exp_f32_e32 v193, v193\n"
"v_mfma_f32_16x16x32_bf16 v[200:203], a[232:235], a[168:171], v[200:203]\n"
"ds_read_b64_tr_b16 a[208:209], v234 offset:0\n"
"v_accvgpr_write_b32 a105, 0\n"
"v_mfma_f32_16x16x32_bf16 v[200:203], a[236:239], a[172:175], v[200:203]\n"
"v_exp_f32_e32 v194, v194\n"
"v_mfma_f32_16x16x32_bf16 v[204:207], a[236:239], a[188:191], v[220:223]\n"
"ds_read_b64_tr_b16 a[210:211], v234 offset:16384\n"
"v_perm_b32 v132, v193, v192, s93\n"
"v_mfma_f32_16x16x32_bf16 v[204:207], a[232:235], a[184:187], v[204:207]\n"
"v_exp_f32_e32 v195, v195\n"
"v_mfma_f32_16x16x32_bf16 v[204:207], a[228:231], a[180:183], v[204:207]\n"
"buffer_load_dwordx4 v240, s[40:43], s45 offen offset:2048 lds\n"
"v_mfma_f32_16x16x32_bf16 v[204:207], a[224:227], a[176:179], v[204:207]\n"
"s_waitcnt lgkmcnt(6)\n"
"v_exp_f32_e32 v196, v196\n"
"v_mfma_f32_16x16x32_bf16 v[64:67], a[192:195], v[160:163], v[64:67]\n"
"ds_read_b64_tr_b16 a[212:213], v234 offset:32768\n"
"v_perm_b32 v133, v195, v194, s93\n"
"v_mfma_f32_16x16x32_bf16 v[64:67], a[196:199], v[164:167], v[64:67]\n"
"v_exp_f32_e32 v197, v197\n"
"v_mfma_f32_16x16x32_bf16 v[68:71], a[196:199], v[172:175], v[68:71]\n"
"ds_read_b64_tr_b16 a[214:215], v234 offset:49152\n"
"v_accvgpr_write_b32 a106, 0\n"
"v_mfma_f32_16x16x32_bf16 v[68:71], a[192:195], v[168:171], v[68:71]\n"
"v_exp_f32_e32 v198, v198\n"
"v_mfma_f32_16x16x32_bf16 v[72:75], a[192:195], v[176:179], v[72:75]\n"
"ds_read_b64_tr_b16 a[216:217], v235 offset:0\n"
"v_perm_b32 v140, v197, v196, s93\n"
"v_mfma_f32_16x16x32_bf16 v[72:75], a[196:199], v[180:183], v[72:75]\n"
"v_exp_f32_e32 v199, v199\n"
"v_mfma_f32_16x16x32_bf16 v[76:79], a[196:199], v[188:191], v[76:79]\n"
"buffer_load_dwordx4 v241, s[40:43], s45 offen offset:3072 lds\n"
"v_mfma_f32_16x16x32_bf16 v[76:79], a[192:195], v[184:187], v[76:79]\n"
"v_exp_f32_e32 v200, v200\n"
"v_mfma_f32_16x16x32_bf16 v[92:95], a[200:203], v[184:187], v[92:95]\n"
"ds_read_b64_tr_b16 a[218:219], v235 offset:16384\n"
"v_perm_b32 v141, v199, v198, s93\n"
"v_mfma_f32_16x16x32_bf16 v[92:95], a[204:207], v[188:191], v[92:95]\n"
"v_exp_f32_e32 v201, v201\n"
"v_mfma_f32_16x16x32_bf16 v[88:91], a[204:207], v[180:183], v[88:91]\n"
"ds_read_b64_tr_b16 a[220:221], v235 offset:32768\n"
"v_accvgpr_write_b32 a107, 0\n"
"v_mfma_f32_16x16x32_bf16 v[88:91], a[200:203], v[176:179], v[88:91]\n"
"s_add_u32 m0, s46, 12288\n"
"v_exp_f32_e32 v202, v202\n"
"v_mfma_f32_16x16x32_bf16 v[84:87], a[200:203], v[168:171], v[84:87]\n"
"s_add_u32 s44, s44, 16384\n"
"ds_read_b64_tr_b16 a[222:223], v235 offset:49152\n"
"v_perm_b32 v148, v201, v200, s93\n"
"v_mfma_f32_16x16x32_bf16 v[84:87], a[204:207], v[172:175], v[84:87]\n"
"v_exp_f32_e32 v203, v203\n"
"v_mfma_f32_16x16x32_bf16 v[80:83], a[204:207], v[164:167], v[80:83]\n"
"buffer_load_dwordx4 v236, s[36:39], s44 offen offset:0 lds\n"
"v_mfma_f32_16x16x32_bf16 v[80:83], a[200:203], v[160:163], v[80:83]\n"
"v_exp_f32_e32 v204, v204\n"
"v_smfmac_f32_16x16x64_bf16 a[120:123], a[92:95], v[176:183], v244\n"
"s_waitcnt lgkmcnt(8)\n"
"v_perm_b32 v149, v203, v202, s93\n"
"v_mfma_f32_16x16x32_bf16 v[192:195], a[240:243], a[128:131], v[208:211]\n"
"ds_read_b128 a[224:227], v224 offset:4096\n"
"v_accvgpr_write_b32 a108, 0\n"
"v_mfma_f32_16x16x32_bf16 v[192:195], a[244:247], a[132:135], v[192:195]\n"
"v_exp_f32_e32 v205, v205\n"
"v_mfma_f32_16x16x32_bf16 v[192:195], a[248:251], a[136:139], v[192:195]\n"
"ds_read_b128 a[228:231], v225 offset:4096\n"
"v_perm_b32 v156, v205, v204, s93\n"
"v_mfma_f32_16x16x32_bf16 v[192:195], a[252:255], a[140:143], v[192:195]\n"
"s_add_u32 s94, s22, 20975616\n"
"v_exp_f32_e32 v206, v206\n"
"v_mfma_f32_16x16x32_bf16 v[196:199], a[252:255], a[156:159], v[212:215]\n"
"ds_read_b128 a[232:235], v226 offset:4096\n"
"v_mfma_f32_16x16x32_bf16 v[196:199], a[248:251], a[152:155], v[196:199]\n"
"v_exp_f32_e32 v207, v207\n"
"v_mfma_f32_16x16x32_bf16 v[196:199], a[244:247], a[148:151], v[196:199]\n"
"buffer_load_dwordx4 a[20:23], v255, s[52:55], s94 offen offset:0 sc1\n"
"v_mfma_f32_16x16x32_bf16 v[196:199], a[240:243], a[144:147], v[196:199]\n"
"v_exp_f32_e32 v192, v192\n"
"v_mfma_f32_16x16x32_bf16 v[200:203], a[240:243], a[160:163], v[216:219]\n"
"ds_read_b128 a[236:239], v227 offset:4096\n"
"v_accvgpr_write_b32 a109, 0\n"
"v_mfma_f32_16x16x32_bf16 v[200:203], a[244:247], a[164:167], v[200:203]\n"
"v_exp_f32_e32 v193, v193\n"
"v_mfma_f32_16x16x32_bf16 v[200:203], a[248:251], a[168:171], v[200:203]\n"
"ds_read_b64_tr_b16 a[192:193], v228 offset:4096\n"
"v_perm_b32 v157, v207, v206, s93\n"
"v_mfma_f32_16x16x32_bf16 v[200:203], a[252:255], a[172:175], v[200:203]\n"
"v_exp_f32_e32 v194, v194\n"
"v_mfma_f32_16x16x32_bf16 v[204:207], a[252:255], a[188:191], v[220:223]\n"
"ds_read_b64_tr_b16 a[194:195], v228 offset:20480\n"
"v_perm_b32 v134, v193, v192, s93\n"
"v_mfma_f32_16x16x32_bf16 v[204:207], a[248:251], a[184:187], v[204:207]\n"
"v_exp_f32_e32 v195, v195\n"
"v_mfma_f32_16x16x32_bf16 v[204:207], a[244:247], a[180:183], v[204:207]\n"
"buffer_load_dwordx4 v237, s[36:39], s44 offen offset:1024 lds\n"
"v_mfma_f32_16x16x32_bf16 v[204:207], a[240:243], a[176:179], v[204:207]\n"
"s_waitcnt lgkmcnt(6)\n"
"v_exp_f32_e32 v196, v196\n"
"v_mfma_f32_16x16x32_bf16 v[96:99], a[208:211], v[160:163], v[96:99]\n"
"ds_read_b64_tr_b16 a[196:197], v228 offset:36864\n"
"v_accvgpr_write_b32 a110, 0\n"
"v_mfma_f32_16x16x32_bf16 v[96:99], a[212:215], v[164:167], v[96:99]\n"
"v_exp_f32_e32 v197, v197\n"
"v_mfma_f32_16x16x32_bf16 v[100:103], a[212:215], v[172:175], v[100:103]\n"
"ds_read_b64_tr_b16 a[198:199], v228 offset:53248\n"
"v_perm_b32 v135, v195, v194, s93\n"
"v_mfma_f32_16x16x32_bf16 v[100:103], a[208:211], v[168:171], v[100:103]\n"
"v_exp_f32_e32 v198, v198\n"
"v_mfma_f32_16x16x32_bf16 v[104:107], a[208:211], v[176:179], v[104:107]\n"
"ds_read_b64_tr_b16 a[200:201], v229 offset:4096\n"
"v_perm_b32 v142, v197, v196, s93\n"
"v_mfma_f32_16x16x32_bf16 v[104:107], a[212:215], v[180:183], v[104:107]\n"
"v_exp_f32_e32 v199, v199\n"
"v_mfma_f32_16x16x32_bf16 v[108:111], a[212:215], v[188:191], v[108:111]\n"
"buffer_load_dwordx4 v238, s[36:39], s44 offen offset:2048 lds\n"
"v_mfma_f32_16x16x32_bf16 v[108:111], a[208:211], v[184:187], v[108:111]\n"
"v_exp_f32_e32 v200, v200\n"
"v_mfma_f32_16x16x32_bf16 v[124:127], a[216:219], v[184:187], v[124:127]\n"
"ds_read_b64_tr_b16 a[202:203], v229 offset:20480\n"
"v_accvgpr_write_b32 a111, 0\n"
"v_mfma_f32_16x16x32_bf16 v[124:127], a[220:223], v[188:191], v[124:127]\n"
"v_exp_f32_e32 v201, v201\n"
"v_mfma_f32_16x16x32_bf16 v[120:123], a[220:223], v[180:183], v[120:123]\n"
"ds_read_b64_tr_b16 a[204:205], v229 offset:36864\n"
"v_perm_b32 v143, v199, v198, s93\n"
"v_mfma_f32_16x16x32_bf16 v[120:123], a[216:219], v[176:179], v[120:123]\n"
"v_exp_f32_e32 v202, v202\n"
"v_mfma_f32_16x16x32_bf16 v[116:119], a[216:219], v[168:171], v[116:119]\n"
"ds_read_b64_tr_b16 a[206:207], v229 offset:53248\n"
"v_perm_b32 v150, v201, v200, s93\n"
"v_mfma_f32_16x16x32_bf16 v[116:119], a[220:223], v[172:175], v[116:119]\n"
"v_exp_f32_e32 v203, v203\n"
"v_mfma_f32_16x16x32_bf16 v[112:115], a[220:223], v[164:167], v[112:115]\n"
"buffer_load_dwordx4 v239, s[36:39], s44 offen offset:3072 lds\n"
"v_mfma_f32_16x16x32_bf16 v[112:115], a[216:219], v[160:163], v[112:115]\n"
"v_exp_f32_e32 v204, v204\n"
"v_smfmac_f32_16x16x64_bf16 a[124:127], a[92:95], v[184:191], v244\n"
"s_waitcnt lgkmcnt(8)\n"
"v_perm_b32 v151, v203, v202, s93\n"
"v_mfma_f32_16x16x32_bf16 v[192:195], a[224:227], a[128:131], v[208:211]\n"
"s_mul_i32 s34, s79, 65536\n"
"ds_read_b128 a[240:243], v224 offset:20480\n"
"v_lshrrev_b32_e32 v250, 1, v255\n"
"v_mfma_f32_16x16x32_bf16 v[192:195], a[228:231], a[132:135], v[192:195]\n"
"s_mul_i32 s83, s84, 128\n"
"v_exp_f32_e32 v205, v205\n"
"v_mfma_f32_16x16x32_bf16 v[192:195], a[232:235], a[136:139], v[192:195]\n"
"s_add_u32 s34, s34, s83\n"
"ds_read_b128 a[244:247], v225 offset:20480\n"
"v_perm_b32 v158, v205, v204, s93\n"
"v_mfma_f32_16x16x32_bf16 v[192:195], a[236:239], a[140:143], v[192:195]\n"
"s_add_u32 m0, s46, 77824\n"
"v_exp_f32_e32 v206, v206\n"
"v_mfma_f32_16x16x32_bf16 v[196:199], a[236:239], a[156:159], v[212:215]\n"
"s_add_u32 s45, s45, 16384\n"
"ds_read_b128 a[248:251], v226 offset:20480\n"
"v_bfe_u32 v242, v255, 4, 5\n"
"v_mfma_f32_16x16x32_bf16 v[196:199], a[232:235], a[152:155], v[196:199]\n"
"s_mul_i32 s83, s77, 32\n"
"v_exp_f32_e32 v207, v207\n"
"v_mfma_f32_16x16x32_bf16 v[196:199], a[228:231], a[148:151], v[196:199]\n"
"s_add_u32 s34, s34, s83\n"
"buffer_load_dwordx4 v240, s[40:43], s45 offen offset:0 lds\n"
"v_mfma_f32_16x16x32_bf16 v[196:199], a[224:227], a[144:147], v[196:199]\n"
"s_mul_i32 s82, s78, 524288\n"
"v_exp_f32_e32 v192, v192\n"
"v_mfma_f32_16x16x32_bf16 v[200:203], a[224:227], a[160:163], v[216:219]\n"
"s_mul_i32 s83, s77, 131072\n"
"ds_read_b128 a[252:255], v227 offset:20480\n"
"v_perm_b32 v159, v207, v206, s93\n"
"v_mfma_f32_16x16x32_bf16 v[200:203], a[228:231], a[164:167], v[200:203]\n"
"s_add_u32 s82, s82, s83\n"
"v_exp_f32_e32 v193, v193\n"
"v_mfma_f32_16x16x32_bf16 v[200:203], a[232:235], a[168:171], v[200:203]\n"
"s_xor_b32 s68, s78, 1\n"
"ds_read_b64_tr_b16 a[208:209], v230 offset:4096\n"
"v_xor_b32_e32 v250, v250, v255\n"
"v_mfma_f32_16x16x32_bf16 v[200:203], a[236:239], a[172:175], v[200:203]\n"
"s_add_u32 s68, s68, s79\n"
"v_exp_f32_e32 v194, v194\n"
"v_mfma_f32_16x16x32_bf16 v[204:207], a[236:239], a[188:191], v[220:223]\n"
"s_and_b32 s68, s68, 7\n"
"ds_read_b64_tr_b16 a[210:211], v230 offset:20480\n"
"v_lshrrev_b32_e32 v243, 9, v255\n"
"v_mfma_f32_16x16x32_bf16 v[204:207], a[232:235], a[184:187], v[204:207]\n"
"s_lshl_b32 s68, s68, 7\n"
"v_exp_f32_e32 v195, v195\n"
"v_mfma_f32_16x16x32_bf16 v[204:207], a[228:231], a[180:183], v[204:207]\n"
"s_or_b32 s68, s68, s82\n"
"buffer_load_dwordx4 v241, s[40:43], s45 offen offset:1024 lds\n"
"v_mfma_f32_16x16x32_bf16 v[204:207], a[224:227], a[176:179], v[204:207]\n"
"s_waitcnt lgkmcnt(6)\n"
"v_exp_f32_e32 v196, v196\n"
"v_mfma_f32_16x16x32_bf16 v[0:3], a[192:195], v[128:131], v[0:3]\n"
"s_xor_b32 s69, s78, 2\n"
"ds_read_b64_tr_b16 a[212:213], v230 offset:36864\n"
"v_perm_b32 v160, v193, v192, s93\n"
"v_mfma_f32_16x16x32_bf16 v[0:3], a[196:199], v[132:135], v[0:3]\n"
"s_add_u32 s69, s69, s79\n"
"v_exp_f32_e32 v197, v197\n"
"v_mfma_f32_16x16x32_bf16 v[4:7], a[196:199], v[140:143], v[4:7]\n"
"s_and_b32 s69, s69, 7\n"
"ds_read_b64_tr_b16 a[214:215], v230 offset:53248\n"
"v_and_b32_e32 v250, 0x80, v250\n"
"v_mfma_f32_16x16x32_bf16 v[4:7], a[192:195], v[136:139], v[4:7]\n"
"s_lshl_b32 s69, s69, 7\n"
"v_exp_f32_e32 v198, v198\n"
"v_mfma_f32_16x16x32_bf16 v[8:11], a[192:195], v[144:147], v[8:11]\n"
"s_or_b32 s69, s69, s82\n"
"ds_read_b64_tr_b16 a[216:217], v231 offset:4096\n"
"v_perm_b32 v161, v195, v194, s93\n"
"v_mfma_f32_16x16x32_bf16 v[8:11], a[196:199], v[148:151], v[8:11]\n"
"s_xor_b32 s70, s78, 3\n"
"v_exp_f32_e32 v199, v199\n"
"v_mfma_f32_16x16x32_bf16 v[12:15], a[196:199], v[156:159], v[12:15]\n"
"s_add_u32 s70, s70, s79\n"
"buffer_load_dwordx4 v240, s[40:43], s45 offen offset:2048 lds\n"
"v_mfma_f32_16x16x32_bf16 v[12:15], a[192:195], v[152:155], v[12:15]\n"
"s_and_b32 s70, s70, 7\n"
"v_exp_f32_e32 v200, v200\n"
"v_mfma_f32_16x16x32_bf16 v[28:31], a[200:203], v[152:155], v[28:31]\n"
"s_lshl_b32 s70, s70, 7\n"
"ds_read_b64_tr_b16 a[218:219], v231 offset:20480\n"
"v_lshlrev_b32_e32 v243, 4, v243\n"
"v_mfma_f32_16x16x32_bf16 v[28:31], a[204:207], v[156:159], v[28:31]\n"
"s_or_b32 s70, s70, s82\n"
"v_exp_f32_e32 v201, v201\n"
"v_mfma_f32_16x16x32_bf16 v[24:27], a[204:207], v[148:151], v[24:27]\n"
"s_xor_b32 s71, s78, 4\n"
"ds_read_b64_tr_b16 a[220:221], v231 offset:36864\n"
"v_perm_b32 v168, v197, v196, s93\n"
"v_cmp_eq_u32_e32 vcc, 0, v250\n"
"v_mfma_f32_16x16x32_bf16 v[24:27], a[200:203], v[144:147], v[24:27]\n"
"s_add_u32 s71, s71, s79\n"
"v_exp_f32_e32 v202, v202\n"
"v_mfma_f32_16x16x32_bf16 v[20:23], a[200:203], v[136:139], v[20:23]\n"
"s_and_b32 s71, s71, 7\n"
"ds_read_b64_tr_b16 a[222:223], v231 offset:53248\n"
"v_lshl_add_u32 v242, v242, 10, v243\n"
"v_mfma_f32_16x16x32_bf16 v[20:23], a[204:207], v[140:143], v[20:23]\n"
"s_lshl_b32 s71, s71, 7\n"
"v_exp_f32_e32 v203, v203\n"
"v_mfma_f32_16x16x32_bf16 v[16:19], a[204:207], v[132:135], v[16:19]\n"
"s_or_b32 s71, s71, s82\n"
"buffer_load_dwordx4 v241, s[40:43], s45 offen offset:3072 lds\n"
"v_mfma_f32_16x16x32_bf16 v[16:19], a[200:203], v[128:131], v[16:19]\n"
"s_xor_b32 s87, s78, 5\n"
"v_exp_f32_e32 v204, v204\n"
"v_smfmac_f32_16x16x64_bf16 a[112:115], a[92:95], v[128:135], v244\n"
"s_waitcnt lgkmcnt(8)\n"
"v_perm_b32 v169, v199, v198, s93\n"
".LPG13_%=:\n"
"s_cbranch_execz .LPGR_%=\n"
"v_and_b32_e32 v250, 16, v255\n"
"v_mfma_f32_16x16x32_bf16 v[192:195], a[240:243], a[128:131], v[208:211]\n"
"s_add_u32 s87, s87, s79\n"
"ds_read_b128 a[224:227], v224 offset:36864\n"
"v_perm_b32 v176, v201, v200, s93\n"
"v_mfma_f32_16x16x32_bf16 v[192:195], a[244:247], a[132:135], v[192:195]\n"
"s_and_b32 s87, s87, 7\n"
"v_exp_f32_e32 v205, v205\n"
"v_mfma_f32_16x16x32_bf16 v[192:195], a[248:251], a[136:139], v[192:195]\n"
"s_lshl_b32 s87, s87, 7\n"
"ds_read_b128 a[228:231], v225 offset:36864\n"
"v_perm_b32 v177, v203, v202, s93\n"
"v_mfma_f32_16x16x32_bf16 v[192:195], a[252:255], a[140:143], v[192:195]\n"
"s_or_b32 s87, s87, s82\n"
"v_exp_f32_e32 v206, v206\n"
"v_mfma_f32_16x16x32_bf16 v[196:199], a[252:255], a[156:159], v[212:215]\n"
"s_xor_b32 s88, s78, 6\n"
"ds_read_b128 a[232:235], v226 offset:36864\n"
"v_mov_b32_e32 v251, 0x3f80\n"
"v_mfma_f32_16x16x32_bf16 v[196:199], a[248:251], a[152:155], v[196:199]\n"
"s_add_u32 s88, s88, s79\n"
"v_exp_f32_e32 v207, v207\n"
"v_mfma_f32_16x16x32_bf16 v[196:199], a[244:247], a[148:151], v[196:199]\n"
"s_and_b32 s88, s88, 7\n"
"buffer_load_dwordx4 a[52:55], v255, s[52:55], s94 offen offset:1024 sc1\n"
"v_mfma_f32_16x16x32_bf16 v[196:199], a[240:243], a[144:147], v[196:199]\n"
"s_lshl_b32 s88, s88, 7\n"
"v_exp_f32_e32 v192, v192\n"
"v_mfma_f32_16x16x32_bf16 v[200:203], a[240:243], a[160:163], v[216:219]\n"
"s_or_b32 s88, s88, s82\n"
"ds_read_b128 a[236:239], v227 offset:36864\n"
"v_perm_b32 v184, v205, v204, s93\n"
"v_mfma_f32_16x16x32_bf16 v[200:203], a[244:247], a[164:167], v[200:203]\n"
"s_xor_b32 s90, s78, 7\n"
"v_exp_f32_e32 v193, v193\n"
"v_mfma_f32_16x16x32_bf16 v[200:203], a[248:251], a[168:171], v[200:203]\n"
"s_add_u32 s90, s90, s79\n"
"ds_read_b64_tr_b16 a[192:193], v232 offset:4096\n"
"v_lshlrev_b32_e32 v250, v250, v251\n"
"v_perm_b32 v185, v207, v206, s93\n"
"v_mfma_f32_16x16x32_bf16 v[200:203], a[252:255], a[172:175], v[200:203]\n"
"s_and_b32 s90, s90, 7\n"
"v_exp_f32_e32 v194, v194\n"
"v_mfma_f32_16x16x32_bf16 v[204:207], a[252:255], a[188:191], v[220:223]\n"
"s_lshl_b32 s90, s90, 7\n"
"ds_read_b64_tr_b16 a[194:195], v232 offset:20480\n"
"v_cndmask_b32_e32 v250, 0, v250, vcc\n"
"v_mfma_f32_16x16x32_bf16 v[204:207], a[248:251], a[184:187], v[204:207]\n"
"s_or_b32 s90, s90, s82\n"
"v_exp_f32_e32 v195, v195\n"
"v_mfma_f32_16x16x32_bf16 v[204:207], a[244:247], a[180:183], v[204:207]\n"
"s_xor_b32 s91, s78, 0\n"
"buffer_load_dwordx4 a[24:27], v255, s[52:55], s94 offen offset:2048 sc1\n"
"v_mfma_f32_16x16x32_bf16 v[204:207], a[240:243], a[176:179], v[204:207]\n"
"s_waitcnt lgkmcnt(6)\n"
"v_exp_f32_e32 v196, v196\n"
"v_mfma_f32_16x16x32_bf16 v[32:35], a[208:211], v[128:131], v[32:35]\n"
"s_add_u32 s91, s91, s79\n"
"ds_read_b64_tr_b16 a[196:197], v232 offset:36864\n"
"v_perm_b32 v162, v193, v192, s93\n"
"v_mfma_f32_16x16x32_bf16 v[32:35], a[212:215], v[132:135], v[32:35]\n"
"s_and_b32 s91, s91, 7\n"
"v_exp_f32_e32 v197, v197\n"
"v_mfma_f32_16x16x32_bf16 v[36:39], a[212:215], v[140:143], v[36:39]\n"
"s_lshl_b32 s91, s91, 7\n"
"ds_read_b64_tr_b16 a[198:199], v232 offset:53248\n"
"v_perm_b32 v163, v195, v194, s93\n"
"v_mfma_f32_16x16x32_bf16 v[36:39], a[208:211], v[136:139], v[36:39]\n"
"s_or_b32 s91, s91, s82\n"
"v_exp_f32_e32 v198, v198\n"
"v_mfma_f32_16x16x32_bf16 v[40:43], a[208:211], v[144:147], v[40:43]\n"
"ds_read_b64_tr_b16 a[200:201], v233 offset:4096\n"
"v_and_b32_e32 v251, 64, v255\n"
"v_mfma_f32_16x16x32_bf16 v[40:43], a[212:215], v[148:151], v[40:43]\n"
"v_exp_f32_e32 v199, v199\n"
"v_mfma_f32_16x16x32_bf16 v[44:47], a[212:215], v[156:159], v[44:47]\n"
"buffer_load_dwordx4 a[56:59], v255, s[52:55], s94 offen offset:3072 sc1\n"
"v_mfma_f32_16x16x32_bf16 v[44:47], a[208:211], v[152:155], v[44:47]\n"
"v_exp_f32_e32 v200, v200\n"
"v_mfma_f32_16x16x32_bf16 v[60:63], a[216:219], v[152:155], v[60:63]\n"
"ds_read_b64_tr_b16 a[202:203], v233 offset:20480\n"
"v_perm_b32 v170, v197, v196, s93\n"
"v_mfma_f32_16x16x32_bf16 v[60:63], a[220:223], v[156:159], v[60:63]\n"
"v_exp_f32_e32 v201, v201\n"
"v_mfma_f32_16x16x32_bf16 v[56:59], a[220:223], v[148:151], v[56:59]\n"
"ds_read_b64_tr_b16 a[204:205], v233 offset:36864\n"
"v_cmp_eq_u32_e32 vcc, 0, v251\n"
"v_mfma_f32_16x16x32_bf16 v[56:59], a[216:219], v[144:147], v[56:59]\n"
"s_add_u32 s94, s22, 20979712\n"
"v_exp_f32_e32 v202, v202\n"
"v_mfma_f32_16x16x32_bf16 v[52:55], a[216:219], v[136:139], v[52:55]\n"
"ds_read_b64_tr_b16 a[206:207], v233 offset:53248\n"
"v_perm_b32 v171, v199, v198, s93\n"
"v_mfma_f32_16x16x32_bf16 v[52:55], a[220:223], v[140:143], v[52:55]\n"
"v_exp_f32_e32 v203, v203\n"
"v_mfma_f32_16x16x32_bf16 v[48:51], a[220:223], v[132:135], v[48:51]\n"
"buffer_load_dwordx4 a[28:31], v255, s[52:55], s94 offen offset:0 sc1\n"
"v_mfma_f32_16x16x32_bf16 v[48:51], a[216:219], v[128:131], v[48:51]\n"
"v_exp_f32_e32 v204, v204\n"
"v_smfmac_f32_16x16x64_bf16 a[116:119], a[92:95], v[136:143], v244\n"
"s_waitcnt lgkmcnt(8)\n"
"v_perm_b32 v178, v201, v200, s93\n"
"v_mfma_f32_16x16x32_bf16 v[192:195], a[224:227], a[128:131], v[208:211]\n"
"ds_read_b128 a[240:243], v224 offset:53248\n"
"v_accvgpr_write_b32 a77, 0\n"
"v_mfma_f32_16x16x32_bf16 v[192:195], a[228:231], a[132:135], v[192:195]\n"
"v_exp_f32_e32 v205, v205\n"
"v_mfma_f32_16x16x32_bf16 v[192:195], a[232:235], a[136:139], v[192:195]\n"
"ds_read_b128 a[244:247], v225 offset:53248\n"
"v_perm_b32 v179, v203, v202, s93\n"
"v_mfma_f32_16x16x32_bf16 v[192:195], a[236:239], a[140:143], v[192:195]\n"
"v_exp_f32_e32 v206, v206\n"
"v_mfma_f32_16x16x32_bf16 v[196:199], a[236:239], a[156:159], v[212:215]\n"
"ds_read_b128 a[248:251], v226 offset:53248\n"
"v_accvgpr_write_b32 a79, 0\n"
"v_mfma_f32_16x16x32_bf16 v[196:199], a[232:235], a[152:155], v[196:199]\n"
"v_exp_f32_e32 v207, v207\n"
"v_mfma_f32_16x16x32_bf16 v[196:199], a[228:231], a[148:151], v[196:199]\n"
"buffer_load_dwordx4 a[60:63], v255, s[52:55], s94 offen offset:1024 sc1\n"
"v_mfma_f32_16x16x32_bf16 v[196:199], a[224:227], a[144:147], v[196:199]\n"
"v_exp_f32_e32 v192, v192\n"
"v_mfma_f32_16x16x32_bf16 v[200:203], a[224:227], a[160:163], v[216:219]\n"
"ds_read_b128 a[252:255], v227 offset:53248\n"
"v_perm_b32 v186, v205, v204, s93\n"
"v_mfma_f32_16x16x32_bf16 v[200:203], a[228:231], a[164:167], v[200:203]\n"
"v_exp_f32_e32 v193, v193\n"
"v_mfma_f32_16x16x32_bf16 v[200:203], a[232:235], a[168:171], v[200:203]\n"
"ds_read_b64_tr_b16 a[208:209], v234 offset:4096\n"
"v_perm_b32 v187, v207, v206, s93\n"
"v_mfma_f32_16x16x32_bf16 v[200:203], a[236:239], a[172:175], v[200:203]\n"
"v_exp_f32_e32 v194, v194\n"
"v_mfma_f32_16x16x32_bf16 v[204:207], a[236:239], a[188:191], v[220:223]\n"
"ds_read_b64_tr_b16 a[210:211], v234 offset:20480\n"
"v_accvgpr_write_b32 a80, 0\n"
"v_mfma_f32_16x16x32_bf16 v[204:207], a[232:235], a[184:187], v[204:207]\n"
"v_exp_f32_e32 v195, v195\n"
"v_mfma_f32_16x16x32_bf16 v[204:207], a[228:231], a[180:183], v[204:207]\n"
"buffer_load_dwordx4 a[32:35], v255, s[52:55], s94 offen offset:2048 sc1\n"
"v_mfma_f32_16x16x32_bf16 v[204:207], a[224:227], a[176:179], v[204:207]\n"
"s_waitcnt lgkmcnt(6)\n"
"v_exp_f32_e32 v196, v196\n"
"v_mfma_f32_16x16x32_bf16 v[64:67], a[192:195], v[128:131], v[64:67]\n"
"ds_read_b64_tr_b16 a[212:213], v234 offset:36864\n"
"v_perm_b32 v164, v193, v192, s93\n"
"v_mfma_f32_16x16x32_bf16 v[64:67], a[196:199], v[132:135], v[64:67]\n"
"v_exp_f32_e32 v197, v197\n"
"v_mfma_f32_16x16x32_bf16 v[68:71], a[196:199], v[140:143], v[68:71]\n"
"ds_read_b64_tr_b16 a[214:215], v234 offset:53248\n"
"v_accvgpr_write_b32 a82, 0\n"
"v_mfma_f32_16x16x32_bf16 v[68:71], a[192:195], v[136:139], v[68:71]\n"
"v_exp_f32_e32 v198, v198\n"
"v_mfma_f32_16x16x32_bf16 v[72:75], a[192:195], v[144:147], v[72:75]\n"
"ds_read_b64_tr_b16 a[216:217], v235 offset:4096\n"
"v_perm_b32 v165, v195, v194, s93\n"
"v_mfma_f32_16x16x32_bf16 v[72:75], a[196:199], v[148:151], v[72:75]\n"
"v_exp_f32_e32 v199, v199\n"
"v_mfma_f32_16x16x32_bf16 v[76:79], a[196:199], v[156:159], v[76:79]\n"
"buffer_load_dwordx4 a[64:67], v255, s[52:55], s94 offen offset:3072 sc1\n"
"v_mfma_f32_16x16x32_bf16 v[76:79], a[192:195], v[152:155], v[76:79]\n"
"v_exp_f32_e32 v200, v200\n"
"v_mfma_f32_16x16x32_bf16 v[92:95], a[200:203], v[152:155], v[92:95]\n"
"ds_read_b64_tr_b16 a[218:219], v235 offset:20480\n"
"v_perm_b32 v172, v197, v196, s93\n"
"v_mfma_f32_16x16x32_bf16 v[92:95], a[204:207], v[156:159], v[92:95]\n"
"v_exp_f32_e32 v201, v201\n"
"v_mfma_f32_16x16x32_bf16 v[88:91], a[204:207], v[148:151], v[88:91]\n"
"ds_read_b64_tr_b16 a[220:221], v235 offset:36864\n"
"v_cndmask_b32_e32 v251, 0, v250, vcc\n"
"v_mfma_f32_16x16x32_bf16 v[88:91], a[200:203], v[144:147], v[88:91]\n"
"s_add_u32 s94, s22, 20983808\n"
"v_exp_f32_e32 v202, v202\n"
"v_mfma_f32_16x16x32_bf16 v[84:87], a[200:203], v[136:139], v[84:87]\n"
"ds_read_b64_tr_b16 a[222:223], v235 offset:53248\n"
"v_perm_b32 v173, v199, v198, s93\n"
"v_mfma_f32_16x16x32_bf16 v[84:87], a[204:207], v[140:143], v[84:87]\n"
"v_exp_f32_e32 v203, v203\n"
"v_mfma_f32_16x16x32_bf16 v[80:83], a[204:207], v[132:135], v[80:83]\n"
"buffer_load_dwordx4 a[36:39], v255, s[52:55], s94 offen offset:0 sc1\n"
"v_mfma_f32_16x16x32_bf16 v[80:83], a[200:203], v[128:131], v[80:83]\n"
"s_waitcnt vmcnt(8) lgkmcnt(0)\n"
"v_exp_f32_e32 v204, v204\n"
"v_smfmac_f32_16x16x64_bf16 a[120:123], a[92:95], v[144:151], v244\n"
"s_waitcnt lgkmcnt(8)\n"
"v_xor_b32_e32 v250, v250, v251\n"
"v_mfma_f32_16x16x32_bf16 v[192:195], a[240:243], a[128:131], v[208:211]\n"
"s_barrier\n"
"ds_read_b128 a[224:227], v224 offset:8192\n"
"v_perm_b32 v180, v201, v200, s93\n"
"v_mfma_f32_16x16x32_bf16 v[192:195], a[244:247], a[132:135], v[192:195]\n"
"v_exp_f32_e32 v205, v205\n"
"v_mfma_f32_16x16x32_bf16 v[192:195], a[248:251], a[136:139], v[192:195]\n"
"ds_read_b128 a[228:231], v225 offset:8192\n"
"v_perm_b32 v181, v203, v202, s93\n"
"v_mfma_f32_16x16x32_bf16 v[192:195], a[252:255], a[140:143], v[192:195]\n"
"v_exp_f32_e32 v206, v206\n"
"v_mfma_f32_16x16x32_bf16 v[196:199], a[252:255], a[156:159], v[212:215]\n"
"ds_read_b128 a[232:235], v226 offset:8192\n"
"v_accvgpr_write_b32 a76, v251\n"
"v_mfma_f32_16x16x32_bf16 v[196:199], a[248:251], a[152:155], v[196:199]\n"
"v_exp_f32_e32 v207, v207\n"
"v_mfma_f32_16x16x32_bf16 v[196:199], a[244:247], a[148:151], v[196:199]\n"
"buffer_store_dword a0, v247, s[24:27], s29 offen sc1\n"
"v_mfma_f32_16x16x32_bf16 v[196:199], a[240:243], a[144:147], v[196:199]\n"
"v_exp_f32_e32 v192, v192\n"
"v_mfma_f32_16x16x32_bf16 v[200:203], a[240:243], a[160:163], v[216:219]\n"
"ds_read_b128 a[236:239], v227 offset:8192\n"
"v_perm_b32 v188, v205, v204, s93\n"
"v_mfma_f32_16x16x32_bf16 v[200:203], a[244:247], a[164:167], v[200:203]\n"
"v_exp_f32_e32 v193, v193\n"
"v_mfma_f32_16x16x32_bf16 v[200:203], a[248:251], a[168:171], v[200:203]\n"
"ds_read_b64_tr_b16 a[192:193], v228 offset:8192\n"
"v_accvgpr_write_b32 a81, v251\n"
"v_perm_b32 v189, v207, v206, s93\n"
"v_mfma_f32_16x16x32_bf16 v[200:203], a[252:255], a[172:175], v[200:203]\n"
"v_exp_f32_e32 v194, v194\n"
"v_mfma_f32_16x16x32_bf16 v[204:207], a[252:255], a[188:191], v[220:223]\n"
"ds_read_b64_tr_b16 a[194:195], v228 offset:24576\n"
"v_accvgpr_write_b32 a78, v250\n"
"v_mfma_f32_16x16x32_bf16 v[204:207], a[248:251], a[184:187], v[204:207]\n"
"v_exp_f32_e32 v195, v195\n"
"v_mfma_f32_16x16x32_bf16 v[204:207], a[244:247], a[180:183], v[204:207]\n"
"v_perm_b32 v166, v193, v192, s93\n"
"v_perm_b32 v167, v195, v194, s93\n"
"v_mfma_f32_16x16x32_bf16 v[204:207], a[240:243], a[176:179], v[204:207]\n"
"s_waitcnt lgkmcnt(6)\n"
"v_exp_f32_e32 v196, v196\n"
"v_mfma_f32_16x16x32_bf16 v[96:99], a[208:211], v[128:131], v[96:99]\n"
"ds_read_b64_tr_b16 a[196:197], v228 offset:40960\n"
"v_accvgpr_write_b32 a83, v250\n"
"v_mfma_f32_16x16x32_bf16 v[96:99], a[212:215], v[132:135], v[96:99]\n"
"v_exp_f32_e32 v197, v197\n"
"v_mfma_f32_16x16x32_bf16 v[100:103], a[212:215], v[140:143], v[100:103]\n"
"ds_read_b64_tr_b16 a[198:199], v228 offset:57344\n"
"v_perm_b32 v174, v197, v196, s93\n"
"v_mfma_f32_16x16x32_bf16 v[100:103], a[208:211], v[136:139], v[100:103]\n"
"v_exp_f32_e32 v198, v198\n"
"v_mfma_f32_16x16x32_bf16 v[104:107], a[208:211], v[144:147], v[104:107]\n"
"ds_read_b64_tr_b16 a[200:201], v229 offset:8192\n"
"v_mfma_f32_16x16x32_bf16 v[104:107], a[212:215], v[148:151], v[104:107]\n"
"v_exp_f32_e32 v199, v199\n"
"v_mfma_f32_16x16x32_bf16 v[108:111], a[212:215], v[156:159], v[108:111]\n"
"buffer_load_dwordx4 a[68:71], v255, s[52:55], s94 offen offset:1024 sc1\n"
"v_mfma_f32_16x16x32_bf16 v[108:111], a[208:211], v[152:155], v[108:111]\n"
"v_exp_f32_e32 v200, v200\n"
"v_mfma_f32_16x16x32_bf16 v[124:127], a[216:219], v[152:155], v[124:127]\n"
"ds_read_b64_tr_b16 a[202:203], v229 offset:24576\n"
"v_perm_b32 v175, v199, v198, s93\n"
"v_mfma_f32_16x16x32_bf16 v[124:127], a[220:223], v[156:159], v[124:127]\n"
"v_exp_f32_e32 v201, v201\n"
"v_mfma_f32_16x16x32_bf16 v[120:123], a[220:223], v[148:151], v[120:123]\n"
"ds_read_b64_tr_b16 a[204:205], v229 offset:40960\n"
"v_perm_b32 v182, v201, v200, s93\n"
"v_mfma_f32_16x16x32_bf16 v[120:123], a[216:219], v[144:147], v[120:123]\n"
"v_exp_f32_e32 v202, v202\n"
"v_mfma_f32_16x16x32_bf16 v[116:119], a[216:219], v[136:139], v[116:119]\n"
"ds_read_b64_tr_b16 a[206:207], v229 offset:57344\n"
"v_mfma_f32_16x16x32_bf16 v[116:119], a[220:223], v[140:143], v[116:119]\n"
"v_exp_f32_e32 v203, v203\n"
"v_mfma_f32_16x16x32_bf16 v[112:115], a[220:223], v[132:135], v[112:115]\n"
"v_perm_b32 v183, v203, v202, s93\n"
"v_mfma_f32_16x16x32_bf16 v[112:115], a[216:219], v[128:131], v[112:115]\n"
"v_exp_f32_e32 v204, v204\n"
"v_smfmac_f32_16x16x64_bf16 a[124:127], a[92:95], v[152:159], v244\n"
"s_waitcnt lgkmcnt(8)\n"
"v_mfma_f32_16x16x32_bf16 v[192:195], a[224:227], a[128:131], v[208:211]\n"
"ds_read_b128 a[240:243], v224 offset:24576\n"
"v_mfma_f32_16x16x32_bf16 v[192:195], a[228:231], a[132:135], v[192:195]\n"
"v_exp_f32_e32 v205, v205\n"
"v_mfma_f32_16x16x32_bf16 v[192:195], a[232:235], a[136:139], v[192:195]\n"
"ds_read_b128 a[244:247], v225 offset:24576\n"
"v_perm_b32 v190, v205, v204, s93\n"
"v_mfma_f32_16x16x32_bf16 v[192:195], a[236:239], a[140:143], v[192:195]\n"
"v_exp_f32_e32 v206, v206\n"
"v_mfma_f32_16x16x32_bf16 v[196:199], a[236:239], a[156:159], v[212:215]\n"
"ds_read_b128 a[248:251], v226 offset:24576\n"
"v_mfma_f32_16x16x32_bf16 v[196:199], a[232:235], a[152:155], v[196:199]\n"
"v_exp_f32_e32 v207, v207\n"
"v_mfma_f32_16x16x32_bf16 v[196:199], a[228:231], a[148:151], v[196:199]\n"
"buffer_load_dwordx4 a[40:43], v255, s[52:55], s94 offen offset:2048 sc1\n"
"v_mfma_f32_16x16x32_bf16 v[196:199], a[224:227], a[144:147], v[196:199]\n"
"v_exp_f32_e32 v192, v192\n"
"v_mfma_f32_16x16x32_bf16 v[200:203], a[224:227], a[160:163], v[216:219]\n"
"ds_read_b128 a[252:255], v227 offset:24576\n"
"v_perm_b32 v191, v207, v206, s93\n"
"v_mfma_f32_16x16x32_bf16 v[200:203], a[228:231], a[164:167], v[200:203]\n"
"v_exp_f32_e32 v193, v193\n"
"v_mfma_f32_16x16x32_bf16 v[200:203], a[232:235], a[168:171], v[200:203]\n"
"ds_read_b64_tr_b16 a[208:209], v230 offset:8192\n"
"v_perm_b32 v128, v193, v192, s93\n"
"v_mfma_f32_16x16x32_bf16 v[200:203], a[236:239], a[172:175], v[200:203]\n"
"v_exp_f32_e32 v194, v194\n"
"v_mfma_f32_16x16x32_bf16 v[204:207], a[236:239], a[188:191], v[220:223]\n"
"ds_read_b64_tr_b16 a[210:211], v230 offset:24576\n"
"v_mfma_f32_16x16x32_bf16 v[204:207], a[232:235], a[184:187], v[204:207]\n"
"v_exp_f32_e32 v195, v195\n"
"v_mfma_f32_16x16x32_bf16 v[204:207], a[228:231], a[180:183], v[204:207]\n"
"buffer_load_dwordx4 a[72:75], v255, s[52:55], s94 offen offset:3072 sc1\n"
"v_mfma_f32_16x16x32_bf16 v[204:207], a[224:227], a[176:179], v[204:207]\n"
"s_waitcnt lgkmcnt(6)\n"
"v_exp_f32_e32 v196, v196\n"
"v_mfma_f32_16x16x32_bf16 v[0:3], a[192:195], v[160:163], v[0:3]\n"
"ds_read_b64_tr_b16 a[212:213], v230 offset:40960\n"
"v_perm_b32 v129, v195, v194, s93\n"
"v_mfma_f32_16x16x32_bf16 v[0:3], a[196:199], v[164:167], v[0:3]\n"
"v_exp_f32_e32 v197, v197\n"
"v_mfma_f32_16x16x32_bf16 v[4:7], a[196:199], v[172:175], v[4:7]\n"
"ds_read_b64_tr_b16 a[214:215], v230 offset:57344\n"
"v_perm_b32 v136, v197, v196, s93\n"
"v_mfma_f32_16x16x32_bf16 v[4:7], a[192:195], v[168:171], v[4:7]\n"
"s_add_u32 s94, s34, 0\n"
"v_exp_f32_e32 v198, v198\n"
"v_mfma_f32_16x16x32_bf16 v[8:11], a[192:195], v[176:179], v[8:11]\n"
"ds_read_b64_tr_b16 a[216:217], v231 offset:8192\n"
"v_mfma_f32_16x16x32_bf16 v[8:11], a[196:199], v[180:183], v[8:11]\n"
"v_exp_f32_e32 v199, v199\n"
"v_mfma_f32_16x16x32_bf16 v[12:15], a[196:199], v[188:191], v[12:15]\n"
"buffer_load_dwordx4 a[84:87], v242, s[48:51], s94 offen\n"
"v_mfma_f32_16x16x32_bf16 v[12:15], a[192:195], v[184:187], v[12:15]\n"
"v_exp_f32_e32 v200, v200\n"
"v_mfma_f32_16x16x32_bf16 v[28:31], a[200:203], v[184:187], v[28:31]\n"
"ds_read_b64_tr_b16 a[218:219], v231 offset:24576\n"
"v_perm_b32 v137, v199, v198, s93\n"
"v_mfma_f32_16x16x32_bf16 v[28:31], a[204:207], v[188:191], v[28:31]\n"
"v_exp_f32_e32 v201, v201\n"
"v_mfma_f32_16x16x32_bf16 v[24:27], a[204:207], v[180:183], v[24:27]\n"
"ds_read_b64_tr_b16 a[220:221], v231 offset:40960\n"
"v_perm_b32 v144, v201, v200, s93\n"
"v_mfma_f32_16x16x32_bf16 v[24:27], a[200:203], v[176:179], v[24:27]\n"
"s_add_u32 s94, s34, 32768\n"
"v_exp_f32_e32 v202, v202\n"
"v_mfma_f32_16x16x32_bf16 v[20:23], a[200:203], v[168:171], v[20:23]\n"
"ds_read_b64_tr_b16 a[222:223], v231 offset:57344\n"
"v_mfma_f32_16x16x32_bf16 v[20:23], a[204:207], v[172:175], v[20:23]\n"
"v_exp_f32_e32 v203, v203\n"
"v_mfma_f32_16x16x32_bf16 v[16:19], a[204:207], v[164:167], v[16:19]\n"
"buffer_load_dwordx4 a[88:91], v242, s[48:51], s94 offen\n"
"v_mfma_f32_16x16x32_bf16 v[16:19], a[200:203], v[160:163], v[16:19]\n"
"v_exp_f32_e32 v204, v204\n"
"v_smfmac_f32_16x16x64_bf16 a[112:115], a[92:95], v[160:167], v244\n"
"s_waitcnt lgkmcnt(8)\n"
"v_perm_b32 v145, v203, v202, s93\n"
"v_mfma_f32_16x16x32_bf16 v[192:195], a[240:243], a[128:131], v[208:211]\n"
"ds_read_b128 a[224:227], v224 offset:40960\n"
"v_mfma_f32_16x16x32_bf16 v[192:195], a[244:247], a[132:135], v[192:195]\n"
"v_exp_f32_e32 v205, v205\n"
"v_mfma_f32_16x16x32_bf16 v[192:195], a[248:251], a[136:139], v[192:195]\n"
"ds_read_b128 a[228:231], v225 offset:40960\n"
"v_perm_b32 v152, v205, v204, s93\n"
"v_mfma_f32_16x16x32_bf16 v[192:195], a[252:255], a[140:143], v[192:195]\n"
"s_add_u32 m0, s46, 65536\n"
"v_exp_f32_e32 v206, v206\n"
"v_mfma_f32_16x16x32_bf16 v[196:199], a[252:255], a[156:159], v[212:215]\n"
"s_add_u32 s45, s45, 16384\n"
"ds_read_b128 a[232:235], v226 offset:40960\n"
"v_mfma_f32_16x16x32_bf16 v[196:199], a[248:251], a[152:155], v[196:199]\n"
"v_exp_f32_e32 v207, v207\n"
"v_mfma_f32_16x16x32_bf16 v[196:199], a[244:247], a[148:151], v[196:199]\n"
"buffer_load_dwordx4 v240, s[40:43], s45 offen offset:0 lds\n"
"v_mfma_f32_16x16x32_bf16 v[196:199], a[240:243], a[144:147], v[196:199]\n"
"v_exp_f32_e32 v192, v192\n"
"v_mfma_f32_16x16x32_bf16 v[200:203], a[240:243], a[160:163], v[216:219]\n"
"ds_read_b128 a[236:239], v227 offset:40960\n"
"v_perm_b32 v153, v207, v206, s93\n"
"v_mfma_f32_16x16x32_bf16 v[200:203], a[244:247], a[164:167], v[200:203]\n"
"v_exp_f32_e32 v193, v193\n"
"v_mfma_f32_16x16x32_bf16 v[200:203], a[248:251], a[168:171], v[200:203]\n"
"ds_read_b64_tr_b16 a[192:193], v232 offset:8192\n"
"v_perm_b32 v130, v193, v192, s93\n"
"v_mfma_f32_16x16x32_bf16 v[200:203], a[252:255], a[172:175], v[200:203]\n"
"v_exp_f32_e32 v194, v194\n"
"v_mfma_f32_16x16x32_bf16 v[204:207], a[252:255], a[188:191], v[220:223]\n"
"ds_read_b64_tr_b16 a[194:195], v232 offset:24576\n"
"v_mfma_f32_16x16x32_bf16 v[204:207], a[248:251], a[184:187], v[204:207]\n"
"v_exp_f32_e32 v195, v195\n"
"v_mfma_f32_16x16x32_bf16 v[204:207], a[244:247], a[180:183], v[204:207]\n"
"buffer_load_dwordx4 v241, s[40:43], s45 offen offset:1024 lds\n"
"v_mfma_f32_16x16x32_bf16 v[204:207], a[240:243], a[176:179], v[204:207]\n"
"s_waitcnt lgkmcnt(6)\n"
"v_exp_f32_e32 v196, v196\n"
"v_mfma_f32_16x16x32_bf16 v[32:35], a[208:211], v[160:163], v[32:35]\n"
"ds_read_b64_tr_b16 a[196:197], v232 offset:40960\n"
"v_perm_b32 v131, v195, v194, s93\n"
"v_mfma_f32_16x16x32_bf16 v[32:35], a[212:215], v[164:167], v[32:35]\n"
"v_exp_f32_e32 v197, v197\n"
"v_mfma_f32_16x16x32_bf16 v[36:39], a[212:215], v[172:175], v[36:39]\n"
"ds_read_b64_tr_b16 a[198:199], v232 offset:57344\n"
"v_perm_b32 v138, v197, v196, s93\n"
"v_mfma_f32_16x16x32_bf16 v[36:39], a[208:211], v[168:171], v[36:39]\n"
"v_exp_f32_e32 v198, v198\n"
"v_mfma_f32_16x16x32_bf16 v[40:43], a[208:211], v[176:179], v[40:43]\n"
"ds_read_b64_tr_b16 a[200:201], v233 offset:8192\n"
"v_mfma_f32_16x16x32_bf16 v[40:43], a[212:215], v[180:183], v[40:43]\n"
"v_exp_f32_e32 v199, v199\n"
"v_mfma_f32_16x16x32_bf16 v[44:47], a[212:215], v[188:191], v[44:47]\n"
"buffer_load_dwordx4 v240, s[40:43], s45 offen offset:2048 lds\n"
"v_mfma_f32_16x16x32_bf16 v[44:47], a[208:211], v[184:187], v[44:47]\n"
"v_exp_f32_e32 v200, v200\n"
"v_mfma_f32_16x16x32_bf16 v[60:63], a[216:219], v[184:187], v[60:63]\n"
"ds_read_b64_tr_b16 a[202:203], v233 offset:24576\n"
"v_perm_b32 v139, v199, v198, s93\n"
"v_mfma_f32_16x16x32_bf16 v[60:63], a[220:223], v[188:191], v[60:63]\n"
"v_exp_f32_e32 v201, v201\n"
"v_mfma_f32_16x16x32_bf16 v[56:59], a[220:223], v[180:183], v[56:59]\n"
"ds_read_b64_tr_b16 a[204:205], v233 offset:40960\n"
"v_perm_b32 v146, v201, v200, s93\n"
"v_mfma_f32_16x16x32_bf16 v[56:59], a[216:219], v[176:179], v[56:59]\n"
"v_exp_f32_e32 v202, v202\n"
"v_mfma_f32_16x16x32_bf16 v[52:55], a[216:219], v[168:171], v[52:55]\n"
"ds_read_b64_tr_b16 a[206:207], v233 offset:57344\n"
"v_mfma_f32_16x16x32_bf16 v[52:55], a[220:223], v[172:175], v[52:55]\n"
"v_exp_f32_e32 v203, v203\n"
"v_mfma_f32_16x16x32_bf16 v[48:51], a[220:223], v[164:167], v[48:51]\n"
"buffer_load_dwordx4 v241, s[40:43], s45 offen offset:3072 lds\n"
"v_mfma_f32_16x16x32_bf16 v[48:51], a[216:219], v[160:163], v[48:51]\n"
"v_exp_f32_e32 v204, v204\n"
"v_smfmac_f32_16x16x64_bf16 a[116:119], a[92:95], v[168:175], v244\n"
"s_waitcnt lgkmcnt(8)\n"
"v_perm_b32 v147, v203, v202, s93\n"
"v_mfma_f32_16x16x32_bf16 v[192:195], a[224:227], a[128:131], v[208:211]\n"
"ds_read_b128 a[240:243], v224 offset:57344\n"
"v_mfma_f32_16x16x32_bf16 v[192:195], a[228:231], a[132:135], v[192:195]\n"
"v_exp_f32_e32 v205, v205\n"
"v_mfma_f32_16x16x32_bf16 v[192:195], a[232:235], a[136:139], v[192:195]\n"
"ds_read_b128 a[244:247], v225 offset:57344\n"
"v_perm_b32 v154, v205, v204, s93\n"
"v_mfma_f32_16x16x32_bf16 v[192:195], a[236:239], a[140:143], v[192:195]\n"
"s_add_u32 m0, s46, 0\n"
"v_exp_f32_e32 v206, v206\n"
"v_mfma_f32_16x16x32_bf16 v[196:199], a[236:239], a[156:159], v[212:215]\n"
"s_add_u32 s80, s68, 0\n"
"ds_read_b128 a[248:251], v226 offset:57344\n"
"v_mfma_f32_16x16x32_bf16 v[196:199], a[232:235], a[152:155], v[196:199]\n"
"v_exp_f32_e32 v207, v207\n"
"v_mfma_f32_16x16x32_bf16 v[196:199], a[228:231], a[148:151], v[196:199]\n"
"buffer_load_dwordx4 v254, s[64:67], s80 offen lds\n"
"v_mfma_f32_16x16x32_bf16 v[196:199], a[224:227], a[144:147], v[196:199]\n"
"v_exp_f32_e32 v192, v192\n"
"v_mfma_f32_16x16x32_bf16 v[200:203], a[224:227], a[160:163], v[216:219]\n"
"ds_read_b128 a[252:255], v227 offset:57344\n"
"v_perm_b32 v155, v207, v206, s93\n"
"v_mfma_f32_16x16x32_bf16 v[200:203], a[228:231], a[164:167], v[200:203]\n"
"v_exp_f32_e32 v193, v193\n"
"v_mfma_f32_16x16x32_bf16 v[200:203], a[232:235], a[168:171], v[200:203]\n"
"ds_read_b64_tr_b16 a[208:209], v234 offset:8192\n"
"v_perm_b32 v132, v193, v192, s93\n"
"v_mfma_f32_16x16x32_bf16 v[200:203], a[236:239], a[172:175], v[200:203]\n"
"s_addk_i32 m0, 1024\n"
"v_exp_f32_e32 v194, v194\n"
"v_mfma_f32_16x16x32_bf16 v[204:207], a[236:239], a[188:191], v[220:223]\n"
"s_addk_i32 s80, 8192\n"
"ds_read_b64_tr_b16 a[210:211], v234 offset:24576\n"
"v_mfma_f32_16x16x32_bf16 v[204:207], a[232:235], a[184:187], v[204:207]\n"
"v_exp_f32_e32 v195, v195\n"
"v_mfma_f32_16x16x32_bf16 v[204:207], a[228:231], a[180:183], v[204:207]\n"
"buffer_load_dwordx4 v254, s[64:67], s80 offen lds\n"
"v_mfma_f32_16x16x32_bf16 v[204:207], a[224:227], a[176:179], v[204:207]\n"
"s_waitcnt lgkmcnt(6)\n"
"v_exp_f32_e32 v196, v196\n"
"v_mfma_f32_16x16x32_bf16 v[64:67], a[192:195], v[160:163], v[64:67]\n"
"ds_read_b64_tr_b16 a[212:213], v234 offset:40960\n"
"v_perm_b32 v133, v195, v194, s93\n"
"v_mfma_f32_16x16x32_bf16 v[64:67], a[196:199], v[164:167], v[64:67]\n"
"v_exp_f32_e32 v197, v197\n"
"v_mfma_f32_16x16x32_bf16 v[68:71], a[196:199], v[172:175], v[68:71]\n"
"ds_read_b64_tr_b16 a[214:215], v234 offset:57344\n"
"v_perm_b32 v140, v197, v196, s93\n"
"v_mfma_f32_16x16x32_bf16 v[68:71], a[192:195], v[168:171], v[68:71]\n"
"s_addk_i32 m0, 1024\n"
"v_exp_f32_e32 v198, v198\n"
"v_mfma_f32_16x16x32_bf16 v[72:75], a[192:195], v[176:179], v[72:75]\n"
"s_addk_i32 s80, 8192\n"
"ds_read_b64_tr_b16 a[216:217], v235 offset:8192\n"
"v_mfma_f32_16x16x32_bf16 v[72:75], a[196:199], v[180:183], v[72:75]\n"
"v_exp_f32_e32 v199, v199\n"
"v_mfma_f32_16x16x32_bf16 v[76:79], a[196:199], v[188:191], v[76:79]\n"
"buffer_load_dwordx4 v254, s[64:67], s80 offen lds\n"
"v_mfma_f32_16x16x32_bf16 v[76:79], a[192:195], v[184:187], v[76:79]\n"
"v_exp_f32_e32 v200, v200\n"
"v_mfma_f32_16x16x32_bf16 v[92:95], a[200:203], v[184:187], v[92:95]\n"
"ds_read_b64_tr_b16 a[218:219], v235 offset:24576\n"
"v_perm_b32 v141, v199, v198, s93\n"
"v_mfma_f32_16x16x32_bf16 v[92:95], a[204:207], v[188:191], v[92:95]\n"
"v_exp_f32_e32 v201, v201\n"
"v_mfma_f32_16x16x32_bf16 v[88:91], a[204:207], v[180:183], v[88:91]\n"
"ds_read_b64_tr_b16 a[220:221], v235 offset:40960\n"
"v_perm_b32 v148, v201, v200, s93\n"
"v_mfma_f32_16x16x32_bf16 v[88:91], a[200:203], v[176:179], v[88:91]\n"
"s_addk_i32 m0, 1024\n"
"v_exp_f32_e32 v202, v202\n"
"v_mfma_f32_16x16x32_bf16 v[84:87], a[200:203], v[168:171], v[84:87]\n"
"s_addk_i32 s80, 8192\n"
"ds_read_b64_tr_b16 a[222:223], v235 offset:57344\n"
"v_mfma_f32_16x16x32_bf16 v[84:87], a[204:207], v[172:175], v[84:87]\n"
"v_exp_f32_e32 v203, v203\n"
"v_mfma_f32_16x16x32_bf16 v[80:83], a[204:207], v[164:167], v[80:83]\n"
"buffer_load_dwordx4 v254, s[64:67], s80 offen lds\n"
"v_mfma_f32_16x16x32_bf16 v[80:83], a[200:203], v[160:163], v[80:83]\n"
"v_exp_f32_e32 v204, v204\n"
"v_smfmac_f32_16x16x64_bf16 a[120:123], a[92:95], v[176:183], v244\n"
"s_waitcnt lgkmcnt(8)\n"
"v_perm_b32 v149, v203, v202, s93\n"
"v_mfma_f32_16x16x32_bf16 v[192:195], a[240:243], a[128:131], v[208:211]\n"
"ds_read_b128 a[224:227], v224 offset:12288\n"
"v_mfma_f32_16x16x32_bf16 v[192:195], a[244:247], a[132:135], v[192:195]\n"
"v_exp_f32_e32 v205, v205\n"
"v_mfma_f32_16x16x32_bf16 v[192:195], a[248:251], a[136:139], v[192:195]\n"
"ds_read_b128 a[228:231], v225 offset:12288\n"
"v_perm_b32 v156, v205, v204, s93\n"
"v_mfma_f32_16x16x32_bf16 v[192:195], a[252:255], a[140:143], v[192:195]\n"
"s_addk_i32 m0, 1024\n"
"v_exp_f32_e32 v206, v206\n"
"v_mfma_f32_16x16x32_bf16 v[196:199], a[252:255], a[156:159], v[212:215]\n"
"s_addk_i32 s80, 8192\n"
"ds_read_b128 a[232:235], v226 offset:12288\n"
"v_mfma_f32_16x16x32_bf16 v[196:199], a[248:251], a[152:155], v[196:199]\n"
"v_exp_f32_e32 v207, v207\n"
"v_mfma_f32_16x16x32_bf16 v[196:199], a[244:247], a[148:151], v[196:199]\n"
"buffer_load_dwordx4 v254, s[64:67], s80 offen lds\n"
"v_mfma_f32_16x16x32_bf16 v[196:199], a[240:243], a[144:147], v[196:199]\n"
"v_exp_f32_e32 v192, v192\n"
"v_mfma_f32_16x16x32_bf16 v[200:203], a[240:243], a[160:163], v[216:219]\n"
"ds_read_b128 a[236:239], v227 offset:12288\n"
"v_perm_b32 v157, v207, v206, s93\n"
"v_mfma_f32_16x16x32_bf16 v[200:203], a[244:247], a[164:167], v[200:203]\n"
"v_exp_f32_e32 v193, v193\n"
"v_mfma_f32_16x16x32_bf16 v[200:203], a[248:251], a[168:171], v[200:203]\n"
"ds_read_b64_tr_b16 a[192:193], v228 offset:12288\n"
"v_perm_b32 v134, v193, v192, s93\n"
"v_mfma_f32_16x16x32_bf16 v[200:203], a[252:255], a[172:175], v[200:203]\n"
"s_addk_i32 m0, 1024\n"
"v_exp_f32_e32 v194, v194\n"
"v_mfma_f32_16x16x32_bf16 v[204:207], a[252:255], a[188:191], v[220:223]\n"
"s_addk_i32 s80, 8192\n"
"ds_read_b64_tr_b16 a[194:195], v228 offset:28672\n"
"v_mfma_f32_16x16x32_bf16 v[204:207], a[248:251], a[184:187], v[204:207]\n"
"v_exp_f32_e32 v195, v195\n"
"v_mfma_f32_16x16x32_bf16 v[204:207], a[244:247], a[180:183], v[204:207]\n"
"buffer_load_dwordx4 v254, s[64:67], s80 offen lds\n"
"v_mfma_f32_16x16x32_bf16 v[204:207], a[240:243], a[176:179], v[204:207]\n"
"s_waitcnt lgkmcnt(6)\n"
"v_exp_f32_e32 v196, v196\n"
"v_mfma_f32_16x16x32_bf16 v[96:99], a[208:211], v[160:163], v[96:99]\n"
"ds_read_b64_tr_b16 a[196:197], v228 offset:45056\n"
"v_perm_b32 v135, v195, v194, s93\n"
"v_mfma_f32_16x16x32_bf16 v[96:99], a[212:215], v[164:167], v[96:99]\n"
"v_exp_f32_e32 v197, v197\n"
"v_mfma_f32_16x16x32_bf16 v[100:103], a[212:215], v[172:175], v[100:103]\n"
"ds_read_b64_tr_b16 a[198:199], v228 offset:61440\n"
"v_perm_b32 v142, v197, v196, s93\n"
"v_mfma_f32_16x16x32_bf16 v[100:103], a[208:211], v[168:171], v[100:103]\n"
"s_addk_i32 m0, 1024\n"
"v_exp_f32_e32 v198, v198\n"
"v_mfma_f32_16x16x32_bf16 v[104:107], a[208:211], v[176:179], v[104:107]\n"
"s_addk_i32 s80, 8192\n"
"ds_read_b64_tr_b16 a[200:201], v229 offset:12288\n"
"v_mfma_f32_16x16x32_bf16 v[104:107], a[212:215], v[180:183], v[104:107]\n"
"v_exp_f32_e32 v199, v199\n"
"v_mfma_f32_16x16x32_bf16 v[108:111], a[212:215], v[188:191], v[108:111]\n"
"buffer_load_dwordx4 v254, s[64:67], s80 offen lds\n"
"v_mfma_f32_16x16x32_bf16 v[108:111], a[208:211], v[184:187], v[108:111]\n"
"v_exp_f32_e32 v200, v200\n"
"v_mfma_f32_16x16x32_bf16 v[124:127], a[216:219], v[184:187], v[124:127]\n"
"ds_read_b64_tr_b16 a[202:203], v229 offset:28672\n"
"v_perm_b32 v143, v199, v198, s93\n"
"v_mfma_f32_16x16x32_bf16 v[124:127], a[220:223], v[188:191], v[124:127]\n"
"v_exp_f32_e32 v201, v201\n"
"v_mfma_f32_16x16x32_bf16 v[120:123], a[220:223], v[180:183], v[120:123]\n"
"ds_read_b64_tr_b16 a[204:205], v229 offset:45056\n"
"v_perm_b32 v150, v201, v200, s93\n"
"v_mfma_f32_16x16x32_bf16 v[120:123], a[216:219], v[176:179], v[120:123]\n"
"s_addk_i32 m0, 1024\n"
"v_exp_f32_e32 v202, v202\n"
"v_mfma_f32_16x16x32_bf16 v[116:119], a[216:219], v[168:171], v[116:119]\n"
"s_addk_i32 s80, 8192\n"
"ds_read_b64_tr_b16 a[206:207], v229 offset:61440\n"
"v_mfma_f32_16x16x32_bf16 v[116:119], a[220:223], v[172:175], v[116:119]\n"
"v_exp_f32_e32 v203, v203\n"
"v_mfma_f32_16x16x32_bf16 v[112:115], a[220:223], v[164:167], v[112:115]\n"
"buffer_load_dwordx4 v254, s[64:67], s80 offen lds\n"
"v_mfma_f32_16x16x32_bf16 v[112:115], a[216:219], v[160:163], v[112:115]\n"
"v_exp_f32_e32 v204, v204\n"
"v_smfmac_f32_16x16x64_bf16 a[124:127], a[92:95], v[184:191], v244\n"
"s_waitcnt lgkmcnt(8)\n"
"v_perm_b32 v151, v203, v202, s93\n"
"s_waitcnt vmcnt(15)\n"
"v_mfma_f32_16x16x32_bf16 v[192:195], a[224:227], a[128:131], v[208:211]\n"
"s_add_u32 s82, s77, 69632\n"
"ds_read_b128 a[240:243], v224 offset:28672\n"
"v_mfma_f32_16x16x32_bf16 v[192:195], a[228:231], a[132:135], v[192:195]\n"
"s_add_u32 s44, s77, 0\n"
"v_exp_f32_e32 v205, v205\n"
"v_mfma_f32_16x16x32_bf16 v[192:195], a[232:235], a[136:139], v[192:195]\n"
"s_and_b32 s44, s44, 3\n"
"ds_read_b128 a[244:247], v225 offset:28672\n"
"v_perm_b32 v158, v205, v204, s93\n"
"v_mfma_f32_16x16x32_bf16 v[192:195], a[236:239], a[140:143], v[192:195]\n"
"s_add_u32 m0, s81, 0\n"
"v_exp_f32_e32 v206, v206\n"
"v_mfma_f32_16x16x32_bf16 v[196:199], a[236:239], a[156:159], v[212:215]\n"
"s_add_u32 s80, s69, 0\n"
".LPG14_%=:\n"
"s_cbranch_execz .LPGR_%=\n"
"ds_read_b128 a[248:251], v226 offset:28672\n"
"v_mfma_f32_16x16x32_bf16 v[196:199], a[232:235], a[152:155], v[196:199]\n"
"s_lshl_b32 s44, s44, 14\n"
"v_exp_f32_e32 v207, v207\n"
"v_mfma_f32_16x16x32_bf16 v[196:199], a[228:231], a[148:151], v[196:199]\n"
"s_add_u32 s45, s77, 1\n"
"buffer_load_dwordx4 v254, s[64:67], s80 offen lds\n"
"v_mfma_f32_16x16x32_bf16 v[196:199], a[224:227], a[144:147], v[196:199]\n"
"s_and_b32 s45, s45, 3\n"
"v_exp_f32_e32 v192, v192\n"
"v_mfma_f32_16x16x32_bf16 v[200:203], a[224:227], a[160:163], v[216:219]\n"
"s_lshl_b32 s45, s45, 14\n"
"ds_read_b128 a[252:255], v227 offset:28672\n"
"v_perm_b32 v159, v207, v206, s93\n"
"v_mfma_f32_16x16x32_bf16 v[200:203], a[228:231], a[164:167], v[200:203]\n"
"s_add_u32 s76, s77, 2\n"
"v_exp_f32_e32 v193, v193\n"
"v_mfma_f32_16x16x32_bf16 v[200:203], a[232:235], a[168:171], v[200:203]\n"
"s_and_b32 s76, s76, 3\n"
"ds_read_b64_tr_b16 a[208:209], v230 offset:12288\n"
"v_perm_b32 v160, v193, v192, s93\n"
"v_mfma_f32_16x16x32_bf16 v[200:203], a[236:239], a[172:175], v[200:203]\n"
"s_addk_i32 m0, 1024\n"
"v_exp_f32_e32 v194, v194\n"
"v_mfma_f32_16x16x32_bf16 v[204:207], a[236:239], a[188:191], v[220:223]\n"
"s_addk_i32 s80, 8192\n"
"ds_read_b64_tr_b16 a[210:211], v230 offset:28672\n"
"v_bfe_u32 v242, v255, 7, 2\n"
"v_mfma_f32_16x16x32_bf16 v[204:207], a[232:235], a[184:187], v[204:207]\n"
"s_lshl_b32 s76, s76, 14\n"
"v_exp_f32_e32 v195, v195\n"
"v_mfma_f32_16x16x32_bf16 v[204:207], a[228:231], a[180:183], v[204:207]\n"
"s_add_u32 s30, s77, 3\n"
"buffer_load_dwordx4 v254, s[64:67], s80 offen lds\n"
"v_mfma_f32_16x16x32_bf16 v[204:207], a[224:227], a[176:179], v[204:207]\n"
"s_waitcnt lgkmcnt(6)\n"
"v_exp_f32_e32 v196, v196\n"
"v_mfma_f32_16x16x32_bf16 v[0:3], a[192:195], v[128:131], v[0:3]\n"
"s_and_b32 s30, s30, 3\n"
"ds_read_b64_tr_b16 a[212:213], v230 offset:45056\n"
"v_xor_b32_e32 v242, s77, v242\n"
"v_mfma_f32_16x16x32_bf16 v[0:3], a[196:199], v[132:135], v[0:3]\n"
"s_lshl_b32 s30, s30, 14\n"
"v_exp_f32_e32 v197, v197\n"
"v_mfma_f32_16x16x32_bf16 v[4:7], a[196:199], v[140:143], v[4:7]\n"
"ds_read_b64_tr_b16 a[214:215], v230 offset:61440\n"
"v_perm_b32 v161, v195, v194, s93\n"
"v_mfma_f32_16x16x32_bf16 v[4:7], a[192:195], v[136:139], v[4:7]\n"
"v_exp_f32_e32 v198, v198\n"
"v_mfma_f32_16x16x32_bf16 v[8:11], a[192:195], v[144:147], v[8:11]\n"
"ds_read_b64_tr_b16 a[216:217], v231 offset:12288\n"
"v_lshrrev_b32_e32 v243, 6, v255\n"
"v_mfma_f32_16x16x32_bf16 v[8:11], a[196:199], v[148:151], v[8:11]\n"
"v_exp_f32_e32 v199, v199\n"
"v_mfma_f32_16x16x32_bf16 v[12:15], a[196:199], v[156:159], v[12:15]\n"
"v_perm_b32 v168, v197, v196, s93\n"
"v_lshlrev_b32_e32 v243, 7, v243\n"
"v_mfma_f32_16x16x32_bf16 v[12:15], a[192:195], v[152:155], v[12:15]\n"
"v_exp_f32_e32 v200, v200\n"
"v_mfma_f32_16x16x32_bf16 v[28:31], a[200:203], v[152:155], v[28:31]\n"
"ds_read_b64_tr_b16 a[218:219], v231 offset:28672\n"
"v_perm_b32 v169, v199, v198, s93\n"
"v_mfma_f32_16x16x32_bf16 v[28:31], a[204:207], v[156:159], v[28:31]\n"
"v_exp_f32_e32 v201, v201\n"
"v_mfma_f32_16x16x32_bf16 v[24:27], a[204:207], v[148:151], v[24:27]\n"
"ds_read_b64_tr_b16 a[220:221], v231 offset:45056\n"
"v_lshl_add_u32 v242, v242, 5, v243\n"
"v_mfma_f32_16x16x32_bf16 v[24:27], a[200:203], v[144:147], v[24:27]\n"
"s_addk_i32 m0, 1024\n"
"v_exp_f32_e32 v202, v202\n"
"v_mfma_f32_16x16x32_bf16 v[20:23], a[200:203], v[136:139], v[20:23]\n"
"s_addk_i32 s80, 8192\n"
"ds_read_b64_tr_b16 a[222:223], v231 offset:61440\n"
"v_bfe_u32 v243, v255, 4, 2\n"
"v_mfma_f32_16x16x32_bf16 v[20:23], a[204:207], v[140:143], v[20:23]\n"
"v_exp_f32_e32 v203, v203\n"
"v_mfma_f32_16x16x32_bf16 v[16:19], a[204:207], v[132:135], v[16:19]\n"
"buffer_load_dwordx4 v254, s[64:67], s80 offen lds\n"
"v_mfma_f32_16x16x32_bf16 v[16:19], a[200:203], v[128:131], v[16:19]\n"
"s_addk_i32 m0, 1024\n"
"v_exp_f32_e32 v204, v204\n"
"v_smfmac_f32_16x16x64_bf16 a[112:115], a[92:95], v[128:135], v244\n"
"s_addk_i32 s80, 8192\n"
"v_perm_b32 v176, v201, v200, s93\n"
"v_smfmac_f32_16x16x64_bf16 a[96:99], a[76:79], a[12:19], v244\n"
"buffer_load_dwordx4 v254, s[64:67], s80 offen lds\n"
"v_smfmac_f32_16x16x64_bf16 a[96:99], a[76:79], a[20:27], v244\n"
"v_lshl_add_u32 v242, v243, 3, v242\n"
"v_smfmac_f32_16x16x64_bf16 a[96:99], a[76:79], a[28:35], v244\n"
"v_perm_b32 v177, v203, v202, s93\n"
"v_smfmac_f32_16x16x64_bf16 a[96:99], a[76:79], a[36:43], v244\n"
"s_waitcnt lgkmcnt(8)\n"
"v_add_u32_e32 v250, s44, v242\n"
"v_mfma_f32_16x16x32_bf16 v[192:195], a[240:243], a[128:131], v[208:211]\n"
"ds_read_b128 a[224:227], v224 offset:45056\n"
"v_add_u32_e32 v250, 65536, v250\n"
"v_mfma_f32_16x16x32_bf16 v[192:195], a[244:247], a[132:135], v[192:195]\n"
"v_exp_f32_e32 v205, v205\n"
"v_mfma_f32_16x16x32_bf16 v[192:195], a[248:251], a[136:139], v[192:195]\n"
"ds_read_b128 a[228:231], v225 offset:45056\n"
"v_add_u32_e32 v236, s44, v255\n"
"v_mfma_f32_16x16x32_bf16 v[192:195], a[252:255], a[140:143], v[192:195]\n"
"s_addk_i32 m0, 1024\n"
"v_exp_f32_e32 v206, v206\n"
"v_mfma_f32_16x16x32_bf16 v[196:199], a[252:255], a[156:159], v[212:215]\n"
"s_addk_i32 s80, 8192\n"
"ds_read_b128 a[232:235], v226 offset:45056\n"
"v_perm_b32 v184, v205, v204, s93\n"
"v_mfma_f32_16x16x32_bf16 v[196:199], a[248:251], a[152:155], v[196:199]\n"
"v_exp_f32_e32 v207, v207\n"
"v_mfma_f32_16x16x32_bf16 v[196:199], a[244:247], a[148:151], v[196:199]\n"
"buffer_load_dwordx4 v254, s[64:67], s80 offen lds\n"
"v_mfma_f32_16x16x32_bf16 v[196:199], a[240:243], a[144:147], v[196:199]\n"
"v_exp_f32_e32 v192, v192\n"
"v_mfma_f32_16x16x32_bf16 v[200:203], a[240:243], a[160:163], v[216:219]\n"
"ds_read_b128 a[236:239], v227 offset:45056\n"
"v_add_u32_e32 v251, s45, v242\n"
"v_mfma_f32_16x16x32_bf16 v[200:203], a[244:247], a[164:167], v[200:203]\n"
"v_exp_f32_e32 v193, v193\n"
"v_mfma_f32_16x16x32_bf16 v[200:203], a[248:251], a[168:171], v[200:203]\n"
"ds_read_b64_tr_b16 a[192:193], v232 offset:12288\n"
"v_perm_b32 v185, v207, v206, s93\n"
"v_mfma_f32_16x16x32_bf16 v[200:203], a[252:255], a[172:175], v[200:203]\n"
"s_addk_i32 m0, 1024\n"
"v_exp_f32_e32 v194, v194\n"
"v_mfma_f32_16x16x32_bf16 v[204:207], a[252:255], a[188:191], v[220:223]\n"
"s_addk_i32 s80, 8192\n"
"ds_read_b64_tr_b16 a[194:195], v232 offset:28672\n"
"v_add_u32_e32 v251, 65536, v251\n"
"v_mfma_f32_16x16x32_bf16 v[204:207], a[248:251], a[184:187], v[204:207]\n"
"v_exp_f32_e32 v195, v195\n"
"v_mfma_f32_16x16x32_bf16 v[204:207], a[244:247], a[180:183], v[204:207]\n"
"buffer_load_dwordx4 v254, s[64:67], s80 offen lds\n"
"v_mfma_f32_16x16x32_bf16 v[204:207], a[240:243], a[176:179], v[204:207]\n"
"s_waitcnt lgkmcnt(6)\n"
"v_exp_f32_e32 v196, v196\n"
"v_mfma_f32_16x16x32_bf16 v[32:35], a[208:211], v[128:131], v[32:35]\n"
"ds_read_b64_tr_b16 a[196:197], v232 offset:45056\n"
"v_add_u32_e32 v237, s45, v255\n"
"v_mfma_f32_16x16x32_bf16 v[32:35], a[212:215], v[132:135], v[32:35]\n"
"v_exp_f32_e32 v197, v197\n"
"v_mfma_f32_16x16x32_bf16 v[36:39], a[212:215], v[140:143], v[36:39]\n"
"ds_read_b64_tr_b16 a[198:199], v232 offset:61440\n"
"v_perm_b32 v162, v193, v192, s93\n"
"v_mfma_f32_16x16x32_bf16 v[36:39], a[208:211], v[136:139], v[36:39]\n"
"v_exp_f32_e32 v198, v198\n"
"v_mfma_f32_16x16x32_bf16 v[40:43], a[208:211], v[144:147], v[40:43]\n"
"ds_read_b64_tr_b16 a[200:201], v233 offset:12288\n"
"v_add_u32_e32 v252, s76, v242\n"
"v_mfma_f32_16x16x32_bf16 v[40:43], a[212:215], v[148:151], v[40:43]\n"
"v_exp_f32_e32 v199, v199\n"
"v_mfma_f32_16x16x32_bf16 v[44:47], a[212:215], v[156:159], v[44:47]\n"
"v_perm_b32 v163, v195, v194, s93\n"
"v_add_u32_e32 v252, 65536, v252\n"
"v_mfma_f32_16x16x32_bf16 v[44:47], a[208:211], v[152:155], v[44:47]\n"
"v_exp_f32_e32 v200, v200\n"
"v_mfma_f32_16x16x32_bf16 v[60:63], a[216:219], v[152:155], v[60:63]\n"
"ds_read_b64_tr_b16 a[202:203], v233 offset:28672\n"
"v_perm_b32 v170, v197, v196, s93\n"
"v_mfma_f32_16x16x32_bf16 v[60:63], a[220:223], v[156:159], v[60:63]\n"
"v_exp_f32_e32 v201, v201\n"
"v_mfma_f32_16x16x32_bf16 v[56:59], a[220:223], v[148:151], v[56:59]\n"
"ds_read_b64_tr_b16 a[204:205], v233 offset:45056\n"
"v_add_u32_e32 v238, s76, v255\n"
"v_mfma_f32_16x16x32_bf16 v[56:59], a[216:219], v[144:147], v[56:59]\n"
"s_addk_i32 m0, 1024\n"
"v_exp_f32_e32 v202, v202\n"
"v_mfma_f32_16x16x32_bf16 v[52:55], a[216:219], v[136:139], v[52:55]\n"
"s_addk_i32 s80, 8192\n"
"ds_read_b64_tr_b16 a[206:207], v233 offset:61440\n"
"v_add_u32_e32 v253, s30, v242\n"
"v_mfma_f32_16x16x32_bf16 v[52:55], a[220:223], v[140:143], v[52:55]\n"
"v_exp_f32_e32 v203, v203\n"
"v_mfma_f32_16x16x32_bf16 v[48:51], a[220:223], v[132:135], v[48:51]\n"
"buffer_load_dwordx4 v254, s[64:67], s80 offen lds\n"
"v_mfma_f32_16x16x32_bf16 v[48:51], a[216:219], v[128:131], v[48:51]\n"
"s_addk_i32 m0, 1024\n"
"v_exp_f32_e32 v204, v204\n"
"v_smfmac_f32_16x16x64_bf16 a[116:119], a[92:95], v[136:143], v244\n"
"s_addk_i32 s80, 8192\n"
"v_perm_b32 v171, v199, v198, s93\n"
"v_add_u32_e32 v253, 65536, v253\n"
"v_smfmac_f32_16x16x64_bf16 a[100:103], a[80:83], a[12:19], v244\n"
"buffer_load_dwordx4 v254, s[64:67], s80 offen lds\n"
"v_smfmac_f32_16x16x64_bf16 a[100:103], a[80:83], a[20:27], v244\n"
"v_perm_b32 v178, v201, v200, s93\n"
"v_smfmac_f32_16x16x64_bf16 a[100:103], a[80:83], a[28:35], v244\n"
"v_add_u32_e32 v239, s30, v255\n"
"v_smfmac_f32_16x16x64_bf16 a[100:103], a[80:83], a[36:43], v244\n"
"s_waitcnt lgkmcnt(8)\n"
"v_perm_b32 v179, v203, v202, s93\n"
"v_mfma_f32_16x16x32_bf16 v[192:195], a[224:227], a[128:131], v[208:211]\n"
"ds_read_b128 a[240:243], v224 offset:61440\n"
"v_lshrrev_b32_e64 v246, 1, s44\n"
"v_mfma_f32_16x16x32_bf16 v[192:195], a[228:231], a[132:135], v[192:195]\n"
"v_exp_f32_e32 v205, v205\n"
"v_mfma_f32_16x16x32_bf16 v[192:195], a[232:235], a[136:139], v[192:195]\n"
"ds_read_b128 a[244:247], v225 offset:61440\n"
"v_add_u32_e32 v246, v242, v246\n"
"v_mfma_f32_16x16x32_bf16 v[192:195], a[236:239], a[140:143], v[192:195]\n"
"v_exp_f32_e32 v206, v206\n"
"v_mfma_f32_16x16x32_bf16 v[196:199], a[236:239], a[156:159], v[212:215]\n"
"ds_read_b128 a[248:251], v226 offset:61440\n"
"v_add_u32_e32 v246, 131072, v246\n"
"v_mfma_f32_16x16x32_bf16 v[196:199], a[232:235], a[152:155], v[196:199]\n"
"v_exp_f32_e32 v207, v207\n"
"v_mfma_f32_16x16x32_bf16 v[196:199], a[228:231], a[148:151], v[196:199]\n"
"v_perm_b32 v186, v205, v204, s93\n"
"v_lshrrev_b32_e64 v247, 1, s45\n"
"v_mfma_f32_16x16x32_bf16 v[196:199], a[224:227], a[144:147], v[196:199]\n"
"v_exp_f32_e32 v192, v192\n"
"v_mfma_f32_16x16x32_bf16 v[200:203], a[224:227], a[160:163], v[216:219]\n"
"ds_read_b128 a[252:255], v227 offset:61440\n"
"v_add_u32_e32 v247, v242, v247\n"
"v_mfma_f32_16x16x32_bf16 v[200:203], a[228:231], a[164:167], v[200:203]\n"
"v_exp_f32_e32 v193, v193\n"
"v_mfma_f32_16x16x32_bf16 v[200:203], a[232:235], a[168:171], v[200:203]\n"
"ds_read_b64_tr_b16 a[208:209], v234 offset:12288\n"
"v_perm_b32 v187, v207, v206, s93\n"
"v_mfma_f32_16x16x32_bf16 v[200:203], a[236:239], a[172:175], v[200:203]\n"
"v_exp_f32_e32 v194, v194\n"
"v_mfma_f32_16x16x32_bf16 v[204:207], a[236:239], a[188:191], v[220:223]\n"
"ds_read_b64_tr_b16 a[210:211], v234 offset:28672\n"
"v_add_u32_e32 v247, 131072, v247\n"
"v_mfma_f32_16x16x32_bf16 v[204:207], a[232:235], a[184:187], v[204:207]\n"
"v_exp_f32_e32 v195, v195\n"
"v_mfma_f32_16x16x32_bf16 v[204:207], a[228:231], a[180:183], v[204:207]\n"
"v_perm_b32 v164, v193, v192, s93\n"
"v_lshrrev_b32_e64 v248, 1, s76\n"
"v_mfma_f32_16x16x32_bf16 v[204:207], a[224:227], a[176:179], v[204:207]\n"
"s_waitcnt lgkmcnt(6)\n"
"v_exp_f32_e32 v196, v196\n"
"v_mfma_f32_16x16x32_bf16 v[64:67], a[192:195], v[128:131], v[64:67]\n"
"ds_read_b64_tr_b16 a[212:213], v234 offset:45056\n"
"v_perm_b32 v165, v195, v194, s93\n"
"v_mfma_f32_16x16x32_bf16 v[64:67], a[196:199], v[132:135], v[64:67]\n"
"v_exp_f32_e32 v197, v197\n"
"v_mfma_f32_16x16x32_bf16 v[68:71], a[196:199], v[140:143], v[68:71]\n"
"ds_read_b64_tr_b16 a[214:215], v234 offset:61440\n"
"v_add_u32_e32 v248, v242, v248\n"
"v_mfma_f32_16x16x32_bf16 v[68:71], a[192:195], v[136:139], v[68:71]\n"
"v_exp_f32_e32 v198, v198\n"
"v_mfma_f32_16x16x32_bf16 v[72:75], a[192:195], v[144:147], v[72:75]\n"
"ds_read_b64_tr_b16 a[216:217], v235 offset:12288\n"
"v_add_u32_e32 v248, 131072, v248\n"
"v_mfma_f32_16x16x32_bf16 v[72:75], a[196:199], v[148:151], v[72:75]\n"
"v_exp_f32_e32 v199, v199\n"
"v_mfma_f32_16x16x32_bf16 v[76:79], a[196:199], v[156:159], v[76:79]\n"
"v_perm_b32 v172, v197, v196, s93\n"
"v_lshrrev_b32_e64 v249, 1, s30\n"
"v_mfma_f32_16x16x32_bf16 v[76:79], a[192:195], v[152:155], v[76:79]\n"
"v_exp_f32_e32 v200, v200\n"
"v_mfma_f32_16x16x32_bf16 v[92:95], a[200:203], v[152:155], v[92:95]\n"
"ds_read_b64_tr_b16 a[218:219], v235 offset:28672\n"
"v_perm_b32 v173, v199, v198, s93\n"
"v_mfma_f32_16x16x32_bf16 v[92:95], a[204:207], v[156:159], v[92:95]\n"
"v_exp_f32_e32 v201, v201\n"
"v_mfma_f32_16x16x32_bf16 v[88:91], a[204:207], v[148:151], v[88:91]\n"
"ds_read_b64_tr_b16 a[220:221], v235 offset:45056\n"
"v_add_u32_e32 v249, v242, v249\n"
"v_mfma_f32_16x16x32_bf16 v[88:91], a[200:203], v[144:147], v[88:91]\n"
"v_exp_f32_e32 v202, v202\n"
"v_mfma_f32_16x16x32_bf16 v[84:87], a[200:203], v[136:139], v[84:87]\n"
"ds_read_b64_tr_b16 a[222:223], v235 offset:61440\n"
"v_perm_b32 v180, v201, v200, s93\n"
"v_mfma_f32_16x16x32_bf16 v[84:87], a[204:207], v[140:143], v[84:87]\n"
"v_exp_f32_e32 v203, v203\n"
"v_mfma_f32_16x16x32_bf16 v[80:83], a[204:207], v[132:135], v[80:83]\n"
"v_add_u32_e32 v249, 131072, v249\n"
"v_mul_u32_u24_e32 v240, 16416, v243\n"
"v_mfma_f32_16x16x32_bf16 v[80:83], a[200:203], v[128:131], v[80:83]\n"
"v_exp_f32_e32 v204, v204\n"
"v_smfmac_f32_16x16x64_bf16 a[120:123], a[92:95], v[144:151], v244\n"
"v_perm_b32 v181, v203, v202, s93\n"
"v_smfmac_f32_16x16x64_bf16 a[104:107], a[76:79], a[44:51], v244\n"
"v_add_u32_e32 v240, s82, v240\n"
"v_smfmac_f32_16x16x64_bf16 a[104:107], a[76:79], a[52:59], v244\n"
"v_smfmac_f32_16x16x64_bf16 a[104:107], a[76:79], a[60:67], v244\n"
"s_waitcnt vmcnt(0) lgkmcnt(0)\n"
"v_smfmac_f32_16x16x64_bf16 a[104:107], a[76:79], a[68:75], v244\n"
"v_mfma_f32_16x16x32_bf16 v[192:195], a[240:243], a[128:131], v[208:211]\n"
"s_barrier\n"
"ds_read_b64_tr_b16 a[192:193], v228 offset:0\n"
"v_mfma_f32_16x16x32_bf16 v[192:195], a[244:247], a[132:135], v[192:195]\n"
"v_exp_f32_e32 v205, v205\n"
"v_mfma_f32_16x16x32_bf16 v[192:195], a[248:251], a[136:139], v[192:195]\n"
"ds_read_b64_tr_b16 a[194:195], v228 offset:16384\n"
"v_perm_b32 v188, v205, v204, s93\n"
"v_mfma_f32_16x16x32_bf16 v[192:195], a[252:255], a[140:143], v[192:195]\n"
"s_add_u32 m0, s46, 73728\n"
"v_exp_f32_e32 v206, v206\n"
"v_mfma_f32_16x16x32_bf16 v[196:199], a[252:255], a[156:159], v[212:215]\n"
"s_add_u32 s80, s70, 0\n"
"ds_read_b64_tr_b16 a[196:197], v228 offset:32768\n"
"v_mfma_f32_16x16x32_bf16 v[196:199], a[248:251], a[152:155], v[196:199]\n"
"v_exp_f32_e32 v207, v207\n"
"v_mfma_f32_16x16x32_bf16 v[196:199], a[244:247], a[148:151], v[196:199]\n"
"buffer_load_dwordx4 v254, s[64:67], s80 offen lds\n"
"v_mfma_f32_16x16x32_bf16 v[196:199], a[240:243], a[144:147], v[196:199]\n"
"v_exp_f32_e32 v192, v192\n"
"v_mfma_f32_16x16x32_bf16 v[200:203], a[240:243], a[160:163], v[216:219]\n"
"ds_read_b64_tr_b16 a[198:199], v228 offset:49152\n"
"v_perm_b32 v189, v207, v206, s93\n"
"v_mfma_f32_16x16x32_bf16 v[200:203], a[244:247], a[164:167], v[200:203]\n"
"v_exp_f32_e32 v193, v193\n"
"v_mfma_f32_16x16x32_bf16 v[200:203], a[248:251], a[168:171], v[200:203]\n"
"ds_read_b64_tr_b16 a[200:201], v229 offset:0\n"
"v_perm_b32 v166, v193, v192, s93\n"
"v_mfma_f32_16x16x32_bf16 v[200:203], a[252:255], a[172:175], v[200:203]\n"
"s_addk_i32 m0, 1024\n"
"v_exp_f32_e32 v194, v194\n"
"v_mfma_f32_16x16x32_bf16 v[204:207], a[252:255], a[188:191], v[220:223]\n"
"s_addk_i32 s80, 8192\n"
"ds_read_b64_tr_b16 a[202:203], v229 offset:16384\n"
"v_mfma_f32_16x16x32_bf16 v[204:207], a[248:251], a[184:187], v[204:207]\n"
"v_exp_f32_e32 v195, v195\n"
"v_mfma_f32_16x16x32_bf16 v[204:207], a[244:247], a[180:183], v[204:207]\n"
"buffer_load_dwordx4 v254, s[64:67], s80 offen lds\n"
"v_mfma_f32_16x16x32_bf16 v[204:207], a[240:243], a[176:179], v[204:207]\n"
"v_exp_f32_e32 v196, v196\n"
"v_mfma_f32_16x16x32_bf16 v[96:99], a[208:211], v[128:131], v[96:99]\n"
"ds_read_b64_tr_b16 a[204:205], v229 offset:32768\n"
"v_perm_b32 v167, v195, v194, s93\n"
"v_mfma_f32_16x16x32_bf16 v[96:99], a[212:215], v[132:135], v[96:99]\n"
"v_exp_f32_e32 v197, v197\n"
"v_mfma_f32_16x16x32_bf16 v[100:103], a[212:215], v[140:143], v[100:103]\n"
"ds_read_b64_tr_b16 a[206:207], v229 offset:49152\n"
"v_perm_b32 v174, v197, v196, s93\n"
"v_mfma_f32_16x16x32_bf16 v[100:103], a[208:211], v[136:139], v[100:103]\n"
"s_addk_i32 m0, 1024\n"
"v_exp_f32_e32 v198, v198\n"
"v_mfma_f32_16x16x32_bf16 v[104:107], a[208:211], v[144:147], v[104:107]\n"
"s_addk_i32 s80, 8192\n"
"v_mfma_f32_16x16x32_bf16 v[104:107], a[212:215], v[148:151], v[104:107]\n"
"v_exp_f32_e32 v199, v199\n"
"v_mfma_f32_16x16x32_bf16 v[108:111], a[212:215], v[156:159], v[108:111]\n"
"buffer_load_dwordx4 v254, s[64:67], s80 offen lds\n"
"v_mfma_f32_16x16x32_bf16 v[108:111], a[208:211], v[152:155], v[108:111]\n"
"v_exp_f32_e32 v200, v200\n"
"v_mfma_f32_16x16x32_bf16 v[124:127], a[216:219], v[152:155], v[124:127]\n"
"v_perm_b32 v175, v199, v198, s93\n"
"v_mfma_f32_16x16x32_bf16 v[124:127], a[220:223], v[156:159], v[124:127]\n"
"v_exp_f32_e32 v201, v201\n"
"v_mfma_f32_16x16x32_bf16 v[120:123], a[220:223], v[148:151], v[120:123]\n"
"v_subrev_u32_e32 v224, 65536, v250\n"
"v_perm_b32 v182, v201, v200, s93\n"
"v_mfma_f32_16x16x32_bf16 v[120:123], a[216:219], v[144:147], v[120:123]\n"
"s_addk_i32 m0, 1024\n"
"v_exp_f32_e32 v202, v202\n"
"v_mfma_f32_16x16x32_bf16 v[116:119], a[216:219], v[136:139], v[116:119]\n"
"s_addk_i32 s80, 8192\n"
"v_subrev_u32_e32 v225, 65536, v251\n"
"v_subrev_u32_e32 v226, 65536, v252\n"
"v_mfma_f32_16x16x32_bf16 v[116:119], a[220:223], v[140:143], v[116:119]\n"
"v_exp_f32_e32 v203, v203\n"
"v_mfma_f32_16x16x32_bf16 v[112:115], a[220:223], v[132:135], v[112:115]\n"
"buffer_load_dwordx4 v254, s[64:67], s80 offen lds\n"
"v_mfma_f32_16x16x32_bf16 v[112:115], a[216:219], v[128:131], v[112:115]\n"
"s_addk_i32 m0, 1024\n"
"v_exp_f32_e32 v204, v204\n"
"v_smfmac_f32_16x16x64_bf16 a[124:127], a[92:95], v[152:159], v244\n"
"s_addk_i32 s80, 8192\n"
"v_subrev_u32_e32 v227, 65536, v253\n"
"v_smfmac_f32_16x16x64_bf16 a[108:111], a[80:83], a[44:51], v244\n"
"buffer_load_dwordx4 v254, s[64:67], s80 offen lds\n"
"v_smfmac_f32_16x16x64_bf16 a[108:111], a[80:83], a[52:59], v244\n"
"s_addk_i32 m0, 1024\n"
"s_addk_i32 s80, 8192\n"
"buffer_load_dwordx4 v254, s[64:67], s80 offen lds\n"
"v_smfmac_f32_16x16x64_bf16 a[108:111], a[80:83], a[60:67], v244\n"
"s_addk_i32 m0, 1024\n"
"s_addk_i32 s80, 8192\n"
"buffer_load_dwordx4 v254, s[64:67], s80 offen lds\n"
"v_smfmac_f32_16x16x64_bf16 a[108:111], a[80:83], a[68:75], v244\n"
"s_addk_i32 m0, 1024\n"
"s_addk_i32 s80, 8192\n"
"buffer_load_dwordx4 v254, s[64:67], s80 offen lds\n"
"v_perm_b32 v183, v203, v202, s93\n"
"v_smfmac_f32_16x16x64_bf16 a[112:115], a[92:95], v[160:167], v244\n"
"ds_read_b64_tr_b16 a[224:225], v224 offset:0\n"
"ds_read_b64_tr_b16 a[226:227], v224 offset:2048\n"
"v_exp_f32_e32 v205, v205\n"
"v_smfmac_f32_16x16x64_bf16 a[116:119], a[92:95], v[168:175], v244\n"
"ds_read_b64_tr_b16 a[228:229], v224 offset:4096\n"
"ds_read_b64_tr_b16 a[230:231], v224 offset:6144\n"
"v_exp_f32_e32 v206, v206\n"
"v_perm_b32 v190, v205, v204, s93\n"
"v_smfmac_f32_16x16x64_bf16 a[120:123], a[92:95], v[176:183], v244\n"
"s_waitcnt lgkmcnt(6)\n"
"ds_read_b64_tr_b16 a[232:233], v225 offset:0\n"
"ds_read_b64_tr_b16 a[234:235], v225 offset:2048\n"
"v_exp_f32_e32 v207, v207\n"
"v_mfma_f32_16x16x32_bf16 v[0:3], a[192:195], v[160:163], v[0:3]\n"
"ds_read_b64_tr_b16 a[236:237], v225 offset:4096\n"
"ds_read_b64_tr_b16 a[238:239], v225 offset:6144\n"
"v_perm_b32 v191, v207, v206, s93\n"
"v_mfma_f32_16x16x32_bf16 v[0:3], a[196:199], v[164:167], v[0:3]\n"
"ds_read_b64_tr_b16 a[240:241], v226 offset:0\n"
"v_smfmac_f32_16x16x64_bf16 a[124:127], a[92:95], v[184:191], v244\n"
"ds_read_b64_tr_b16 a[242:243], v226 offset:2048\n"
"ds_read_b64_tr_b16 a[244:245], v226 offset:4096\n"
"v_mfma_f32_16x16x32_bf16 v[4:7], a[196:199], v[172:175], v[4:7]\n"
"ds_read_b64_tr_b16 a[246:247], v226 offset:6144\n"
"ds_read_b64_tr_b16 a[248:249], v227 offset:0\n"
"v_accvgpr_read_b32 v241, a112\n"
"v_mfma_f32_16x16x32_bf16 v[4:7], a[192:195], v[168:171], v[4:7]\n"
"ds_read_b64_tr_b16 a[250:251], v227 offset:2048\n"
"ds_read_b64_tr_b16 a[252:253], v227 offset:4096\n"
"v_accvgpr_read_b32 v128, a114\n"
"v_mfma_f32_16x16x32_bf16 v[8:11], a[192:195], v[176:179], v[8:11]\n"
"ds_read_b64_tr_b16 a[254:255], v227 offset:6144\n"
"v_add_f32_e32 v241, v128, v241\n"
"v_accvgpr_read_b32 v242, a116\n"
"v_mfma_f32_16x16x32_bf16 v[8:11], a[196:199], v[180:183], v[8:11]\n"
"ds_read_b64_tr_b16 a[208:209], v230 offset:0\n"
"ds_read_b64_tr_b16 a[210:211], v230 offset:16384\n"
"v_accvgpr_read_b32 v128, a118\n"
"v_mfma_f32_16x16x32_bf16 v[12:15], a[196:199], v[188:191], v[12:15]\n"
"ds_read_b64_tr_b16 a[212:213], v230 offset:32768\n"
"v_add_f32_e32 v242, v128, v242\n"
"v_accvgpr_read_b32 v243, a120\n"
"v_mfma_f32_16x16x32_bf16 v[12:15], a[192:195], v[184:187], v[12:15]\n"
"ds_read_b64_tr_b16 a[214:215], v230 offset:49152\n"
"v_accvgpr_read_b32 v128, a122\n"
"v_add_f32_e32 v243, v128, v243\n"
"v_mfma_f32_16x16x32_bf16 v[28:31], a[200:203], v[184:187], v[28:31]\n"
"s_waitcnt lgkmcnt(4)\n"
"ds_read_b64_tr_b16 a[216:217], v231 offset:0\n"
"ds_read_b64_tr_b16 a[218:219], v231 offset:16384\n"
"v_accvgpr_read_b32 v244, a124\n"
"v_mfma_f32_16x16x32_bf16 v[28:31], a[204:207], v[188:191], v[28:31]\n"
"s_barrier\n"
"ds_read_b64_tr_b16 a[220:221], v231 offset:32768\n"
"v_accvgpr_read_b32 v128, a126\n"
"v_add_f32_e32 v244, v128, v244\n"
"v_mfma_f32_16x16x32_bf16 v[24:27], a[204:207], v[180:183], v[24:27]\n"
"s_add_u32 s80, s68, 65536\n"
"ds_read_b64_tr_b16 a[222:223], v231 offset:49152\n"
"v_add_f32_e32 v128, v241, v242\n"
"v_add_f32_e32 v129, v243, v244\n"
"v_mfma_f32_16x16x32_bf16 v[24:27], a[200:203], v[176:179], v[24:27]\n"
"s_add_u32 m0, s46, 0\n"
"v_add_f32_e32 v128, v128, v129\n"
"v_rcp_f32_e32 v241, v241\n"
"v_mfma_f32_16x16x32_bf16 v[20:23], a[200:203], v[168:171], v[20:23]\n"
"buffer_load_dwordx4 v254, s[64:67], s80 offen lds\n"
"v_rcp_f32_e32 v242, v242\n"
"v_mfma_f32_16x16x32_bf16 v[20:23], a[204:207], v[172:175], v[20:23]\n"
"v_rcp_f32_e32 v243, v243\n"
"v_mfma_f32_16x16x32_bf16 v[16:19], a[204:207], v[164:167], v[16:19]\n"
"s_addk_i32 s80, 8192\n"
"v_rcp_f32_e32 v244, v244\n"
"v_cmp_nge_f32_e32 vcc, 0x5f800000, v128\n"
"v_mfma_f32_16x16x32_bf16 v[16:19], a[200:203], v[160:163], v[16:19]\n"
"s_waitcnt lgkmcnt(0)\n"
"s_addk_i32 m0, 1024\n"
"v_mul_f32_e32 v241, 0x40faf232, v241\n"
"v_mul_f32_e32 v242, 0x40faf232, v242\n"
"v_mfma_f32_16x16x32_bf16 v[32:35], a[208:211], v[160:163], v[32:35]\n"
"ds_read_b64_tr_b16 a[192:193], v232 offset:0\n"
"buffer_load_dwordx4 v254, s[64:67], s80 offen lds\n"
"v_mul_f32_e32 v243, 0x40faf232, v243\n"
"v_mfma_f32_16x16x32_bf16 v[32:35], a[212:215], v[164:167], v[32:35]\n"
"ds_read_b64_tr_b16 a[194:195], v232 offset:16384\n"
"v_mul_f32_e32 v244, 0x40faf232, v244\n"
"v_mul_f32_e32 v208, s23, v241\n"
"v_mfma_f32_16x16x32_bf16 v[36:39], a[212:215], v[172:175], v[36:39]\n"
"s_addk_i32 s80, 8192\n"
"ds_read_b64_tr_b16 a[196:197], v232 offset:32768\n"
"v_mul_f32_e32 v209, s23, v242\n"
"v_mul_f32_e32 v210, s23, v243\n"
"v_mfma_f32_16x16x32_bf16 v[36:39], a[208:211], v[168:171], v[36:39]\n"
"s_addk_i32 m0, 1024\n"
"ds_read_b64_tr_b16 a[198:199], v232 offset:49152\n"
"v_mul_f32_e32 v211, s23, v244\n"
"v_fma_f32 v0, v0, v241, -v208\n"
"v_mfma_f32_16x16x32_bf16 v[40:43], a[208:211], v[176:179], v[40:43]\n"
"ds_read_b64_tr_b16 a[200:201], v233 offset:0\n"
"buffer_load_dwordx4 v254, s[64:67], s80 offen lds\n"
"v_fma_f32 v1, v1, v241, -v208\n"
"v_mfma_f32_16x16x32_bf16 v[40:43], a[212:215], v[180:183], v[40:43]\n"
"ds_read_b64_tr_b16 a[202:203], v233 offset:16384\n"
"v_fma_f32 v2, v2, v241, -v208\n"
"v_fma_f32 v3, v3, v241, -v208\n"
"v_mfma_f32_16x16x32_bf16 v[44:47], a[212:215], v[188:191], v[44:47]\n"
"s_addk_i32 s80, 8192\n"
"ds_read_b64_tr_b16 a[204:205], v233 offset:32768\n"
"v_fma_f32 v4, v4, v242, -v209\n"
"v_fma_f32 v5, v5, v242, -v209\n"
"v_mfma_f32_16x16x32_bf16 v[44:47], a[208:211], v[184:187], v[44:47]\n"
"s_addk_i32 m0, 1024\n"
"ds_read_b64_tr_b16 a[206:207], v233 offset:49152\n"
"v_fma_f32 v6, v6, v242, -v209\n"
"v_fma_f32 v7, v7, v242, -v209\n"
"v_mfma_f32_16x16x32_bf16 v[60:63], a[216:219], v[184:187], v[60:63]\n"
"buffer_load_dwordx4 v254, s[64:67], s80 offen lds\n"
"v_fma_f32 v8, v8, v243, -v210\n"
"v_fma_f32 v9, v9, v243, -v210\n"
"v_mfma_f32_16x16x32_bf16 v[60:63], a[220:223], v[188:191], v[60:63]\n"
"v_fma_f32 v10, v10, v243, -v210\n"
"v_fma_f32 v11, v11, v243, -v210\n"
"v_fma_f32 v12, v12, v244, -v211\n"
"v_mfma_f32_16x16x32_bf16 v[56:59], a[220:223], v[180:183], v[56:59]\n"
"s_addk_i32 s80, 8192\n"
"v_fma_f32 v13, v13, v244, -v211\n"
"v_fma_f32 v14, v14, v244, -v211\n"
"v_fma_f32 v15, v15, v244, -v211\n"
"v_mfma_f32_16x16x32_bf16 v[56:59], a[216:219], v[176:179], v[56:59]\n"
"s_addk_i32 m0, 1024\n"
"s_or_b32 s83, vcc_lo, vcc_hi\n"
"s_andn2_b32 s83, s83, s92\n"
"s_min_u32 s83, s83, 1\n"
"v_mfma_f32_16x16x32_bf16 v[52:55], a[216:219], v[168:171], v[52:55]\n"
"buffer_load_dwordx4 v254, s[64:67], s80 offen lds\n"
"v_mov_b32_e32 v128, s83\n"
"v_mfma_f32_16x16x32_bf16 v[52:55], a[220:223], v[172:175], v[52:55]\n"
"ds_write_b8 v240, v128\n"
"v_fma_f32 v28, v28, v244, -v211\n"
"v_fma_f32 v29, v29, v244, -v211\n"
"v_mfma_f32_16x16x32_bf16 v[48:51], a[220:223], v[164:167], v[48:51]\n"
"v_fma_f32 v30, v30, v244, -v211\n"
"v_fma_f32 v31, v31, v244, -v211\n"
"v_cvt_pk_bf16_f32 v12, v12, v13\n"
"v_mfma_f32_16x16x32_bf16 v[48:51], a[216:219], v[160:163], v[48:51]\n"
"s_waitcnt lgkmcnt(1)\n"
"s_addk_i32 s80, 8192\n"
"v_cvt_pk_bf16_f32 v13, v14, v15\n"
"v_cvt_pk_bf16_f32 v14, v28, v29\n"
"v_mfma_f32_16x16x32_bf16 v[64:67], a[192:195], v[160:163], v[64:67]\n"
"s_addk_i32 m0, 1024\n"
"ds_read_b64_tr_b16 a[208:209], v234 offset:0\n"
"v_cvt_pk_bf16_f32 v15, v30, v31\n"
"v_mfma_f32_16x16x32_bf16 v[64:67], a[196:199], v[164:167], v[64:67]\n"
"ds_read_b64_tr_b16 a[210:211], v234 offset:16384\n"
"buffer_load_dwordx4 v254, s[64:67], s80 offen lds\n"
"ds_write_b128 v236, v[12:15] offset:11264\n"
"v_mfma_f32_16x16x32_bf16 v[68:71], a[196:199], v[172:175], v[68:71]\n"
"ds_read_b64_tr_b16 a[212:213], v234 offset:32768\n"
"v_fma_f32 v24, v24, v243, -v210\n"
"v_fma_f32 v25, v25, v243, -v210\n"
"v_mfma_f32_16x16x32_bf16 v[68:71], a[192:195], v[168:171], v[68:71]\n"
"ds_read_b64_tr_b16 a[214:215], v234 offset:49152\n"
"v_fma_f32 v26, v26, v243, -v210\n"
"v_fma_f32 v27, v27, v243, -v210\n"
"v_mfma_f32_16x16x32_bf16 v[72:75], a[192:195], v[176:179], v[72:75]\n"
"s_addk_i32 s80, 8192\n"
"ds_read_b64_tr_b16 a[216:217], v235 offset:0\n"
"v_cvt_pk_bf16_f32 v8, v8, v9\n"
"v_cvt_pk_bf16_f32 v9, v10, v11\n"
"v_mfma_f32_16x16x32_bf16 v[72:75], a[196:199], v[180:183], v[72:75]\n"
"s_addk_i32 m0, 1024\n"
"ds_read_b64_tr_b16 a[218:219], v235 offset:16384\n"
"v_cvt_pk_bf16_f32 v10, v24, v25\n"
"v_cvt_pk_bf16_f32 v11, v26, v27\n"
"v_mfma_f32_16x16x32_bf16 v[76:79], a[196:199], v[188:191], v[76:79]\n"
"ds_read_b64_tr_b16 a[220:221], v235 offset:32768\n"
"buffer_load_dwordx4 v254, s[64:67], s80 offen lds\n"
"ds_write_b128 v236, v[8:11] offset:10240\n"
"v_mfma_f32_16x16x32_bf16 v[76:79], a[192:195], v[184:187], v[76:79]\n"
"ds_read_b64_tr_b16 a[222:223], v235 offset:49152\n"
"v_fma_f32 v20, v20, v242, -v209\n"
"v_fma_f32 v21, v21, v242, -v209\n"
"v_mfma_f32_16x16x32_bf16 v[92:95], a[200:203], v[184:187], v[92:95]\n"
"v_fma_f32 v22, v22, v242, -v209\n"
"v_fma_f32 v23, v23, v242, -v209\n"
"v_cvt_pk_bf16_f32 v4, v4, v5\n"
"v_mfma_f32_16x16x32_bf16 v[92:95], a[204:207], v[188:191], v[92:95]\n"
"s_addk_i32 s80, 8192\n"
"v_cvt_pk_bf16_f32 v5, v6, v7\n"
"v_cvt_pk_bf16_f32 v6, v20, v21\n"
"v_cvt_pk_bf16_f32 v7, v22, v23\n"
"v_mfma_f32_16x16x32_bf16 v[88:91], a[204:207], v[180:183], v[88:91]\n"
"s_addk_i32 m0, 1024\n"
"ds_write_b128 v236, v[4:7] offset:9216\n"
"v_mfma_f32_16x16x32_bf16 v[88:91], a[200:203], v[176:179], v[88:91]\n"
"buffer_load_dwordx4 v254, s[64:67], s80 offen lds\n"
"v_fma_f32 v16, v16, v241, -v208\n"
"v_fma_f32 v17, v17, v241, -v208\n"
"v_mfma_f32_16x16x32_bf16 v[84:87], a[200:203], v[168:171], v[84:87]\n"
"v_fma_f32 v18, v18, v241, -v208\n"
"v_fma_f32 v19, v19, v241, -v208\n"
"v_cvt_pk_bf16_f32 v0, v0, v1\n"
"v_mfma_f32_16x16x32_bf16 v[84:87], a[204:207], v[172:175], v[84:87]\n"
"v_cvt_pk_bf16_f32 v1, v2, v3\n"
"v_cvt_pk_bf16_f32 v2, v16, v17\n"
"v_cvt_pk_bf16_f32 v3, v18, v19\n"
"v_mfma_f32_16x16x32_bf16 v[80:83], a[204:207], v[164:167], v[80:83]\n"
"ds_write_b128 v236, v[0:3] offset:8192\n"
"v_mfma_f32_16x16x32_bf16 v[80:83], a[200:203], v[160:163], v[80:83]\n"
"s_waitcnt lgkmcnt(2)\n"
"v_fma_f32 v32, v32, v241, -v208\n"
"v_fma_f32 v33, v33, v241, -v208\n"
"v_fma_f32 v34, v34, v241, -v208\n"
"v_mfma_f32_16x16x32_bf16 v[96:99], a[208:211], v[160:163], v[96:99]\n"
"v_fma_f32 v35, v35, v241, -v208\n"
"v_fma_f32 v36, v36, v242, -v209\n"
"v_fma_f32 v37, v37, v242, -v209\n"
"v_mfma_f32_16x16x32_bf16 v[96:99], a[212:215], v[164:167], v[96:99]\n"
".LPG15_%=:\n"
"s_cbranch_execz .LPGR_%=\n"
"v_fma_f32 v38, v38, v242, -v209\n"
"v_fma_f32 v39, v39, v242, -v209\n"
"v_fma_f32 v40, v40, v243, -v210\n"
"v_mfma_f32_16x16x32_bf16 v[100:103], a[212:215], v[172:175], v[100:103]\n"
"v_fma_f32 v41, v41, v243, -v210\n"
"v_fma_f32 v42, v42, v243, -v210\n"
"v_fma_f32 v43, v43, v243, -v210\n"
"v_mfma_f32_16x16x32_bf16 v[100:103], a[208:211], v[168:171], v[100:103]\n"
"v_fma_f32 v44, v44, v244, -v211\n"
"v_fma_f32 v45, v45, v244, -v211\n"
"v_fma_f32 v46, v46, v244, -v211\n"
"v_mfma_f32_16x16x32_bf16 v[104:107], a[208:211], v[176:179], v[104:107]\n"
"v_fma_f32 v47, v47, v244, -v211\n"
"v_fma_f32 v60, v60, v244, -v211\n"
"v_fma_f32 v61, v61, v244, -v211\n"
"v_mfma_f32_16x16x32_bf16 v[104:107], a[212:215], v[180:183], v[104:107]\n"
"v_fma_f32 v62, v62, v244, -v211\n"
"v_fma_f32 v63, v63, v244, -v211\n"
"v_cvt_pk_bf16_f32 v44, v44, v45\n"
"v_mfma_f32_16x16x32_bf16 v[108:111], a[212:215], v[188:191], v[108:111]\n"
"v_cvt_pk_bf16_f32 v45, v46, v47\n"
"v_cvt_pk_bf16_f32 v46, v60, v61\n"
"v_cvt_pk_bf16_f32 v47, v62, v63\n"
"v_mfma_f32_16x16x32_bf16 v[108:111], a[208:211], v[184:187], v[108:111]\n"
"ds_write_b128 v236, v[44:47] offset:15360\n"
"v_mfma_f32_16x16x32_bf16 v[124:127], a[216:219], v[184:187], v[124:127]\n"
"v_fma_f32 v56, v56, v243, -v210\n"
"v_fma_f32 v57, v57, v243, -v210\n"
"v_fma_f32 v58, v58, v243, -v210\n"
"v_mfma_f32_16x16x32_bf16 v[124:127], a[220:223], v[188:191], v[124:127]\n"
"v_fma_f32 v59, v59, v243, -v210\n"
"v_cvt_pk_bf16_f32 v40, v40, v41\n"
"v_cvt_pk_bf16_f32 v41, v42, v43\n"
"v_mfma_f32_16x16x32_bf16 v[120:123], a[220:223], v[180:183], v[120:123]\n"
"v_cvt_pk_bf16_f32 v42, v56, v57\n"
"v_cvt_pk_bf16_f32 v43, v58, v59\n"
"v_mfma_f32_16x16x32_bf16 v[120:123], a[216:219], v[176:179], v[120:123]\n"
"ds_write_b128 v236, v[40:43] offset:14336\n"
"v_mfma_f32_16x16x32_bf16 v[116:119], a[216:219], v[168:171], v[116:119]\n"
"v_fma_f32 v52, v52, v242, -v209\n"
"v_fma_f32 v53, v53, v242, -v209\n"
"v_fma_f32 v54, v54, v242, -v209\n"
"v_mfma_f32_16x16x32_bf16 v[116:119], a[220:223], v[172:175], v[116:119]\n"
"v_fma_f32 v55, v55, v242, -v209\n"
"v_cvt_pk_bf16_f32 v36, v36, v37\n"
"v_cvt_pk_bf16_f32 v37, v38, v39\n"
"v_mfma_f32_16x16x32_bf16 v[112:115], a[220:223], v[164:167], v[112:115]\n"
"v_cvt_pk_bf16_f32 v38, v52, v53\n"
"v_cvt_pk_bf16_f32 v39, v54, v55\n"
"v_mfma_f32_16x16x32_bf16 v[112:115], a[216:219], v[160:163], v[112:115]\n"
"ds_write_b128 v236, v[36:39] offset:13312\n"
"v_fma_f32 v48, v48, v241, -v208\n"
"v_fma_f32 v49, v49, v241, -v208\n"
"v_fma_f32 v50, v50, v241, -v208\n"
"v_fma_f32 v51, v51, v241, -v208\n"
"v_cvt_pk_bf16_f32 v32, v32, v33\n"
"v_cvt_pk_bf16_f32 v33, v34, v35\n"
"v_cvt_pk_bf16_f32 v34, v48, v49\n"
"v_cvt_pk_bf16_f32 v35, v50, v51\n"
"ds_write_b128 v236, v[32:35] offset:12288\n"
"v_accvgpr_read_b32 v228, a84\n"
"v_accvgpr_read_b32 v229, a85\n"
"v_accvgpr_read_b32 v230, a86\n"
"v_accvgpr_read_b32 v231, a87\n"
"v_accvgpr_read_b32 v232, a88\n"
"v_accvgpr_read_b32 v233, a89\n"
"v_accvgpr_read_b32 v234, a90\n"
"v_accvgpr_read_b32 v235, a91\n"
"s_waitcnt lgkmcnt(0)\n"
"s_barrier\n"
"ds_read_b32 v176, v250 offset:4096\n"
"ds_read_b128 a[0:3], v237 offset:8192\n"
"ds_read_b128 a[16:19], v237 offset:12288\n"
"ds_read_b128 a[4:7], v237 offset:9216\n"
"ds_read_b128 a[20:23], v237 offset:13312\n"
"ds_read_b128 a[8:11], v237 offset:10240\n"
"ds_read_b128 a[24:27], v237 offset:14336\n"
"ds_read_b128 a[12:15], v237 offset:11264\n"
"ds_read_b128 a[28:31], v237 offset:15360\n"
"v_mfma_f32_16x16x32_bf16 v[128:131], a[224:227], v[0:3], 0\n"
"ds_read_b128 a[32:35], v238 offset:8192\n"
"ds_read_b128 a[48:51], v238 offset:12288\n"
"v_fma_f32 v64, v64, v241, -v208\n"
"v_mfma_f32_16x16x32_bf16 v[128:131], a[228:231], v[32:35], v[128:131]\n"
"ds_read_b128 a[36:39], v238 offset:9216\n"
"v_fma_f32 v65, v65, v241, -v208\n"
"v_fma_f32 v66, v66, v241, -v208\n"
"v_mfma_f32_16x16x32_bf16 v[132:135], a[224:227], v[4:7], 0\n"
"ds_read_b128 a[52:55], v238 offset:13312\n"
"ds_read_b128 a[40:43], v238 offset:10240\n"
"v_fma_f32 v67, v67, v241, -v208\n"
"v_mfma_f32_16x16x32_bf16 v[132:135], a[228:231], v[36:39], v[132:135]\n"
"ds_read_b128 a[56:59], v238 offset:14336\n"
"v_fma_f32 v68, v68, v242, -v209\n"
"v_fma_f32 v69, v69, v242, -v209\n"
"v_mfma_f32_16x16x32_bf16 v[136:139], a[224:227], v[8:11], 0\n"
"ds_read_b128 a[44:47], v238 offset:11264\n"
"ds_read_b128 a[60:63], v238 offset:15360\n"
"v_fma_f32 v70, v70, v242, -v209\n"
"v_mfma_f32_16x16x32_bf16 v[136:139], a[228:231], v[40:43], v[136:139]\n"
"ds_read_b128 a[64:67], v239 offset:8192\n"
"v_fma_f32 v71, v71, v242, -v209\n"
"v_fma_f32 v72, v72, v243, -v210\n"
"v_mfma_f32_16x16x32_bf16 v[140:143], a[224:227], v[12:15], 0\n"
"ds_read_b128 a[80:83], v239 offset:12288\n"
"ds_read_b128 a[68:71], v239 offset:9216\n"
"v_fma_f32 v73, v73, v243, -v210\n"
"v_mfma_f32_16x16x32_bf16 v[140:143], a[228:231], v[44:47], v[140:143]\n"
"s_waitcnt lgkmcnt(11)\n"
"ds_read_b128 a[84:87], v239 offset:13312\n"
"v_fma_f32 v74, v74, v243, -v210\n"
"v_fma_f32 v75, v75, v243, -v210\n"
"v_mfma_f32_16x16x32_bf16 v[128:131], a[232:235], a[0:3], v[128:131]\n"
"s_add_u32 s80, s69, 65536\n"
"ds_read_b128 a[72:75], v239 offset:10240\n"
"ds_read_b128 a[88:91], v239 offset:14336\n"
"v_fma_f32 v76, v76, v244, -v211\n"
"v_mfma_f32_16x16x32_bf16 v[128:131], a[236:239], a[16:19], v[128:131]\n"
"s_add_u32 m0, s46, 65536\n"
"ds_read_b128 a[76:79], v239 offset:11264\n"
"v_fma_f32 v77, v77, v244, -v211\n"
"v_fma_f32 v78, v78, v244, -v211\n"
"v_mfma_f32_16x16x32_bf16 v[132:135], a[232:235], a[4:7], v[132:135]\n"
"buffer_load_dwordx4 v254, s[64:67], s80 offen lds\n"
"v_mfma_f32_16x16x32_bf16 v[132:135], a[236:239], a[20:23], v[132:135]\n"
"s_addk_i32 s80, 8192\n"
"ds_read_b128 a[92:95], v239 offset:15360\n"
"v_fma_f32 v79, v79, v244, -v211\n"
"v_fma_f32 v80, v80, v241, -v208\n"
"v_mfma_f32_16x16x32_bf16 v[136:139], a[232:235], a[8:11], v[136:139]\n"
"s_addk_i32 m0, 1024\n"
"ds_read_b64_tr_b16 a[192:193], v246 offset:0\n"
"ds_read_b64_tr_b16 a[194:195], v246 offset:2048\n"
"v_fma_f32 v81, v81, v241, -v208\n"
"v_mfma_f32_16x16x32_bf16 v[136:139], a[236:239], a[24:27], v[136:139]\n"
"buffer_load_dwordx4 v254, s[64:67], s80 offen lds\n"
"v_mfma_f32_16x16x32_bf16 v[140:143], a[232:235], a[12:15], v[140:143]\n"
"s_addk_i32 s80, 8192\n"
"ds_read_b64_tr_b16 a[196:197], v246 offset:4096\n"
"v_fma_f32 v82, v82, v241, -v208\n"
"v_fma_f32 v83, v83, v241, -v208\n"
"v_mfma_f32_16x16x32_bf16 v[140:143], a[236:239], a[28:31], v[140:143]\n"
"s_waitcnt lgkmcnt(11)\n"
"s_addk_i32 m0, 1024\n"
"ds_read_b64_tr_b16 a[198:199], v246 offset:6144\n"
"ds_read_b64_tr_b16 a[200:201], v247 offset:0\n"
"v_mfma_f32_16x16x32_bf16 v[128:131], a[240:243], a[32:35], v[128:131]\n"
"buffer_load_dwordx4 v254, s[64:67], s80 offen lds\n"
"v_mfma_f32_16x16x32_bf16 v[128:131], a[244:247], a[48:51], v[128:131]\n"
"s_addk_i32 s80, 8192\n"
"ds_read_b64_tr_b16 a[202:203], v247 offset:2048\n"
"v_cvt_pk_bf16_f32 v64, v64, v65\n"
"v_cvt_pk_bf16_f32 v65, v66, v67\n"
"v_mfma_f32_16x16x32_bf16 v[132:135], a[240:243], a[36:39], v[132:135]\n"
"s_addk_i32 m0, 1024\n"
"ds_read_b64_tr_b16 a[204:205], v247 offset:4096\n"
"ds_read_b64_tr_b16 a[206:207], v247 offset:6144\n"
"v_cvt_pk_bf16_f32 v66, v80, v81\n"
"v_mfma_f32_16x16x32_bf16 v[132:135], a[244:247], a[52:55], v[132:135]\n"
"buffer_load_dwordx4 v254, s[64:67], s80 offen lds\n"
"v_mfma_f32_16x16x32_bf16 v[136:139], a[240:243], a[40:43], v[136:139]\n"
"s_addk_i32 s80, 8192\n"
"ds_read_b64_tr_b16 a[208:209], v248 offset:0\n"
"v_cvt_pk_bf16_f32 v67, v82, v83\n"
"v_fma_f32 v84, v84, v242, -v209\n"
"v_mfma_f32_16x16x32_bf16 v[136:139], a[244:247], a[56:59], v[136:139]\n"
"s_addk_i32 m0, 1024\n"
"ds_read_b64_tr_b16 a[210:211], v248 offset:2048\n"
"ds_read_b64_tr_b16 a[212:213], v248 offset:4096\n"
"v_fma_f32 v85, v85, v242, -v209\n"
"v_mfma_f32_16x16x32_bf16 v[140:143], a[240:243], a[44:47], v[140:143]\n"
"buffer_load_dwordx4 v254, s[64:67], s80 offen lds\n"
"v_mfma_f32_16x16x32_bf16 v[140:143], a[244:247], a[60:63], v[140:143]\n"
"s_waitcnt lgkmcnt(11)\n"
"s_addk_i32 s80, 8192\n"
"ds_read_b64_tr_b16 a[214:215], v248 offset:6144\n"
"v_fma_f32 v86, v86, v242, -v209\n"
"v_mfma_f32_16x16x32_bf16 v[128:131], a[248:251], a[64:67], v[128:131]\n"
"s_addk_i32 m0, 1024\n"
"ds_read_b64_tr_b16 a[216:217], v249 offset:0\n"
"ds_read_b64_tr_b16 a[218:219], v249 offset:2048\n"
"v_fma_f32 v87, v87, v242, -v209\n"
"v_mfma_f32_16x16x32_bf16 v[128:131], a[252:255], a[80:83], v[128:131]\n"
"buffer_load_dwordx4 v254, s[64:67], s80 offen lds\n"
"v_mfma_f32_16x16x32_bf16 v[132:135], a[248:251], a[68:71], v[132:135]\n"
"s_addk_i32 s80, 8192\n"
"ds_read_b64_tr_b16 a[220:221], v249 offset:4096\n"
"v_cvt_pk_bf16_f32 v68, v68, v69\n"
"v_cvt_pk_bf16_f32 v69, v70, v71\n"
"v_mfma_f32_16x16x32_bf16 v[132:135], a[252:255], a[84:87], v[132:135]\n"
"s_addk_i32 m0, 1024\n"
"ds_read_b64_tr_b16 a[222:223], v249 offset:6144\n"
"v_cvt_pk_bf16_f32 v70, v84, v85\n"
"v_cvt_pk_bf16_f32 v71, v86, v87\n"
"v_mfma_f32_16x16x32_bf16 v[136:139], a[248:251], a[72:75], v[136:139]\n"
"buffer_load_dwordx4 v254, s[64:67], s80 offen lds\n"
"v_mfma_f32_16x16x32_bf16 v[136:139], a[252:255], a[88:91], v[136:139]\n"
"s_addk_i32 s80, 8192\n"
"v_fma_f32 v88, v88, v243, -v210\n"
"v_fma_f32 v89, v89, v243, -v210\n"
"v_fma_f32 v90, v90, v243, -v210\n"
"v_mfma_f32_16x16x32_bf16 v[140:143], a[248:251], a[76:79], v[140:143]\n"
"s_addk_i32 m0, 1024\n"
"v_fma_f32 v91, v91, v243, -v210\n"
"v_cvt_pk_bf16_f32 v72, v72, v73\n"
"v_cvt_pk_bf16_f32 v73, v74, v75\n"
"v_mfma_f32_16x16x32_bf16 v[140:143], a[252:255], a[92:95], v[140:143]\n"
"buffer_load_dwordx4 v254, s[64:67], s80 offen lds\n"
"s_waitcnt vmcnt(16)\n"
"s_waitcnt lgkmcnt(0)\n"
"v_mfma_f32_16x16x32_bf16 v[144:147], a[192:195], v[0:3], 0\n"
"s_barrier\n"
"s_add_u32 s80, s70, 65536\n"
"ds_read_b64_tr_b16 a[224:225], v250 offset:8192\n"
"v_cvt_pk_bf16_f32 v74, v88, v89\n"
"v_mfma_f32_16x16x32_bf16 v[144:147], a[196:199], v[32:35], v[144:147]\n"
"s_add_u32 m0, s81, 0\n"
"ds_read_b64_tr_b16 a[226:227], v250 offset:10240\n"
"v_cvt_pk_bf16_f32 v75, v90, v91\n"
"v_fma_f32 v92, v92, v244, -v211\n"
"v_mfma_f32_16x16x32_bf16 v[144:147], a[200:203], a[0:3], v[144:147]\n"
"buffer_load_dwordx4 v254, s[64:67], s80 offen lds\n"
"v_mfma_f32_16x16x32_bf16 v[144:147], a[204:207], a[16:19], v[144:147]\n"
"s_addk_i32 s80, 8192\n"
"ds_read_b64_tr_b16 a[228:229], v250 offset:12288\n"
"v_fma_f32 v93, v93, v244, -v211\n"
"v_fma_f32 v94, v94, v244, -v211\n"
"v_mfma_f32_16x16x32_bf16 v[144:147], a[208:211], a[32:35], v[144:147]\n"
"s_addk_i32 m0, 1024\n"
"ds_read_b64_tr_b16 a[230:231], v250 offset:14336\n"
"v_fma_f32 v95, v95, v244, -v211\n"
"v_cvt_pk_bf16_f32 v76, v76, v77\n"
"v_mfma_f32_16x16x32_bf16 v[144:147], a[212:215], a[48:51], v[144:147]\n"
"buffer_load_dwordx4 v254, s[64:67], s80 offen lds\n"
"v_mfma_f32_16x16x32_bf16 v[144:147], a[216:219], a[64:67], v[144:147]\n"
"v_cvt_pk_bf16_f32 v77, v78, v79\n"
"v_cvt_pk_bf16_f32 v78, v92, v93\n"
"v_cvt_pk_bf16_f32 v79, v94, v95\n"
"v_mfma_f32_16x16x32_bf16 v[144:147], a[220:223], a[80:83], v[144:147]\n"
"s_addk_i32 s80, 8192\n"
"ds_read_b64_tr_b16 a[232:233], v251 offset:8192\n"
"v_fma_f32 v96, v96, v241, -v208\n"
"v_fma_f32 v97, v97, v241, -v208\n"
"v_mfma_f32_16x16x32_bf16 v[148:151], a[220:223], a[84:87], 0\n"
"s_addk_i32 m0, 1024\n"
"ds_read_b64_tr_b16 a[234:235], v251 offset:10240\n"
"v_fma_f32 v98, v98, v241, -v208\n"
"v_fma_f32 v99, v99, v241, -v208\n"
"v_mfma_f32_16x16x32_bf16 v[148:151], a[216:219], a[68:71], v[148:151]\n"
"buffer_load_dwordx4 v254, s[64:67], s80 offen lds\n"
"v_mfma_f32_16x16x32_bf16 v[148:151], a[212:215], a[52:55], v[148:151]\n"
"ds_read_b64_tr_b16 a[236:237], v251 offset:12288\n"
"v_fma_f32 v100, v100, v242, -v209\n"
"v_fma_f32 v101, v101, v242, -v209\n"
"v_mfma_f32_16x16x32_bf16 v[148:151], a[208:211], a[36:39], v[148:151]\n"
"s_addk_i32 s80, 8192\n"
"v_fma_f32 v102, v102, v242, -v209\n"
"v_fma_f32 v103, v103, v242, -v209\n"
"v_fma_f32 v104, v104, v243, -v210\n"
"v_mfma_f32_16x16x32_bf16 v[148:151], a[204:207], a[20:23], v[148:151]\n"
"s_addk_i32 m0, 1024\n"
"ds_read_b64_tr_b16 a[238:239], v251 offset:14336\n"
"v_fma_f32 v105, v105, v243, -v210\n"
"v_fma_f32 v106, v106, v243, -v210\n"
"v_mfma_f32_16x16x32_bf16 v[148:151], a[200:203], a[4:7], v[148:151]\n"
"buffer_load_dwordx4 v254, s[64:67], s80 offen lds\n"
"v_mfma_f32_16x16x32_bf16 v[148:151], a[196:199], v[36:39], v[148:151]\n"
"ds_read_b64_tr_b16 a[240:241], v252 offset:8192\n"
"v_fma_f32 v107, v107, v243, -v210\n"
"v_fma_f32 v108, v108, v244, -v211\n"
"v_mfma_f32_16x16x32_bf16 v[148:151], a[192:195], v[4:7], v[148:151]\n"
"s_addk_i32 s80, 8192\n"
"ds_read_b64_tr_b16 a[242:243], v252 offset:10240\n"
"v_fma_f32 v109, v109, v244, -v211\n"
"v_fma_f32 v110, v110, v244, -v211\n"
"v_mfma_f32_16x16x32_bf16 v[152:155], a[192:195], v[8:11], 0\n"
"s_addk_i32 m0, 1024\n"
"v_fma_f32 v111, v111, v244, -v211\n"
"v_mfma_f32_16x16x32_bf16 v[152:155], a[196:199], v[40:43], v[152:155]\n"
"buffer_load_dwordx4 v254, s[64:67], s80 offen lds\n"
"v_mfma_f32_16x16x32_bf16 v[152:155], a[200:203], a[8:11], v[152:155]\n"
"s_addk_i32 s80, 8192\n"
"ds_read_b64_tr_b16 a[244:245], v252 offset:12288\n"
"ds_write_b128 v236, v[64:67] offset:8192\n"
"v_mfma_f32_16x16x32_bf16 v[152:155], a[204:207], a[24:27], v[152:155]\n"
"s_addk_i32 m0, 1024\n"
"ds_read_b64_tr_b16 a[246:247], v252 offset:14336\n"
"v_accvgpr_read_b32 v160, a96\n"
"v_accvgpr_read_b32 v161, a97\n"
"v_mfma_f32_16x16x32_bf16 v[152:155], a[208:211], a[40:43], v[152:155]\n"
"buffer_load_dwordx4 v254, s[64:67], s80 offen lds\n"
"v_mfma_f32_16x16x32_bf16 v[152:155], a[212:215], a[56:59], v[152:155]\n"
"ds_read_b64_tr_b16 a[248:249], v253 offset:8192\n"
"v_accvgpr_read_b32 v162, a98\n"
"v_accvgpr_read_b32 v163, a99\n"
"v_mfma_f32_16x16x32_bf16 v[152:155], a[216:219], a[72:75], v[152:155]\n"
"s_addk_i32 s80, 8192\n"
"ds_write_b128 v236, v[68:71] offset:9216\n"
"v_mfma_f32_16x16x32_bf16 v[152:155], a[220:223], a[88:91], v[152:155]\n"
"s_addk_i32 m0, 1024\n"
"ds_read_b64_tr_b16 a[250:251], v253 offset:10240\n"
"v_accvgpr_read_b32 v164, a100\n"
"v_accvgpr_read_b32 v165, a101\n"
"v_mfma_f32_16x16x32_bf16 v[156:159], a[220:223], a[92:95], 0\n"
"buffer_load_dwordx4 v254, s[64:67], s80 offen lds\n"
"v_mfma_f32_16x16x32_bf16 v[156:159], a[216:219], a[76:79], v[156:159]\n"
"ds_read_b64_tr_b16 a[252:253], v253 offset:12288\n"
"v_accvgpr_read_b32 v166, a102\n"
"v_accvgpr_read_b32 v167, a103\n"
"v_mfma_f32_16x16x32_bf16 v[156:159], a[212:215], a[60:63], v[156:159]\n"
"s_addk_i32 s80, 8192\n"
"ds_read_b64_tr_b16 a[254:255], v253 offset:14336\n"
"ds_write_b128 v236, v[72:75] offset:10240\n"
"v_mfma_f32_16x16x32_bf16 v[156:159], a[208:211], a[44:47], v[156:159]\n"
"s_addk_i32 m0, 1024\n"
"v_accvgpr_read_b32 v168, a104\n"
"v_accvgpr_read_b32 v169, a105\n"
"v_accvgpr_read_b32 v170, a106\n"
"v_mfma_f32_16x16x32_bf16 v[156:159], a[204:207], a[28:31], v[156:159]\n"
"buffer_load_dwordx4 v254, s[64:67], s80 offen lds\n"
"v_mfma_f32_16x16x32_bf16 v[156:159], a[200:203], a[12:15], v[156:159]\n"
"v_accvgpr_read_b32 v171, a107\n"
"v_mfma_f32_16x16x32_bf16 v[156:159], a[196:199], v[44:47], v[156:159]\n"
"ds_write_b128 v236, v[76:79] offset:11264\n"
"v_mfma_f32_16x16x32_bf16 v[156:159], a[192:195], v[12:15], v[156:159]\n"
"v_accvgpr_read_b32 v172, a108\n"
"v_accvgpr_read_b32 v173, a109\n"
"v_accvgpr_read_b32 v174, a110\n"
"v_accvgpr_read_b32 v175, a111\n"
"v_fma_f32 v112, v112, v241, -v208\n"
"v_fma_f32 v113, v113, v241, -v208\n"
"v_fma_f32 v114, v114, v241, -v208\n"
"v_fma_f32 v115, v115, v241, -v208\n"
"v_cvt_pk_bf16_f32 v96, v96, v97\n"
"v_cvt_pk_bf16_f32 v97, v98, v99\n"
"v_cvt_pk_bf16_f32 v98, v112, v113\n"
"v_cvt_pk_bf16_f32 v99, v114, v115\n"
"ds_write_b128 v236, v[96:99] offset:12288\n"
"v_fma_f32 v116, v116, v242, -v209\n"
"v_fma_f32 v117, v117, v242, -v209\n"
"v_fma_f32 v118, v118, v242, -v209\n"
"v_fma_f32 v119, v119, v242, -v209\n"
"v_cvt_pk_bf16_f32 v100, v100, v101\n"
"v_cvt_pk_bf16_f32 v101, v102, v103\n"
"v_cvt_pk_bf16_f32 v102, v116, v117\n"
"v_cvt_pk_bf16_f32 v103, v118, v119\n"
"ds_write_b128 v236, v[100:103] offset:13312\n"
"v_fma_f32 v120, v120, v243, -v210\n"
"v_fma_f32 v121, v121, v243, -v210\n"
"v_fma_f32 v122, v122, v243, -v210\n"
"v_fma_f32 v123, v123, v243, -v210\n"
"v_cvt_pk_bf16_f32 v104, v104, v105\n"
"v_cvt_pk_bf16_f32 v105, v106, v107\n"
"v_cvt_pk_bf16_f32 v106, v120, v121\n"
"v_cvt_pk_bf16_f32 v107, v122, v123\n"
"ds_write_b128 v236, v[104:107] offset:14336\n"
"v_fma_f32 v124, v124, v244, -v211\n"
"v_fma_f32 v125, v125, v244, -v211\n"
"v_fma_f32 v126, v126, v244, -v211\n"
"v_fma_f32 v127, v127, v244, -v211\n"
"v_cvt_pk_bf16_f32 v108, v108, v109\n"
"v_cvt_pk_bf16_f32 v109, v110, v111\n"
"v_cvt_pk_bf16_f32 v110, v124, v125\n"
"v_cvt_pk_bf16_f32 v111, v126, v127\n"
"ds_write_b128 v236, v[108:111] offset:15360\n"
"v_readfirstlane_b32 s82, v176\n"
"s_nop 0\n"
"s_cmp_lg_u32 s82, 0\n"
"s_cbranch_scc1 .LFALL_%=\n"
"s_waitcnt vmcnt(16)\n"
"s_waitcnt lgkmcnt(0)\n"
"v_mfma_f32_16x16x32_bf16 v[192:195], a[224:227], v[0:3], 0\n"
"s_barrier\n"
"s_add_u32 s80, s71, 0\n"
"ds_read_b64_tr_b16 a[192:193], v224 offset:0\n"
"ds_read_b64_tr_b16 a[194:195], v224 offset:2048\n"
"v_mfma_f32_16x16x32_bf16 v[192:195], a[228:231], v[32:35], v[192:195]\n"
"s_add_u32 m0, s46, 73728\n"
"ds_read_b64_tr_b16 a[196:197], v224 offset:4096\n"
"ds_read_b64_tr_b16 a[198:199], v224 offset:6144\n"
"v_mfma_f32_16x16x32_bf16 v[192:195], a[232:235], a[0:3], v[192:195]\n"
"buffer_load_dwordx4 v254, s[64:67], s80 offen lds\n"
"v_mfma_f32_16x16x32_bf16 v[192:195], a[236:239], a[16:19], v[192:195]\n"
"s_addk_i32 s80, 8192\n"
"ds_read_b64_tr_b16 a[200:201], v225 offset:0\n"
"ds_read_b64_tr_b16 a[202:203], v225 offset:2048\n"
"v_mfma_f32_16x16x32_bf16 v[192:195], a[240:243], a[32:35], v[192:195]\n"
"s_addk_i32 m0, 1024\n"
"ds_read_b64_tr_b16 a[204:205], v225 offset:4096\n"
"ds_read_b64_tr_b16 a[206:207], v225 offset:6144\n"
"v_mfma_f32_16x16x32_bf16 v[192:195], a[244:247], a[48:51], v[192:195]\n"
"buffer_load_dwordx4 v254, s[64:67], s80 offen lds\n"
"v_mfma_f32_16x16x32_bf16 v[192:195], a[248:251], a[64:67], v[192:195]\n"
"s_mov_b64 s[56:57], s[20:21]\n"
"ds_read_b64_tr_b16 a[208:209], v226 offset:0\n"
"ds_read_b64_tr_b16 a[210:211], v226 offset:2048\n"
"v_mfma_f32_16x16x32_bf16 v[192:195], a[252:255], a[80:83], v[192:195]\n"
"s_addk_i32 s80, 8192\n"
"ds_read_b64_tr_b16 a[212:213], v226 offset:4096\n"
"ds_read_b64_tr_b16 a[214:215], v226 offset:6144\n"
"v_mfma_f32_16x16x32_bf16 v[196:199], a[252:255], a[84:87], 0\n"
"s_addk_i32 m0, 1024\n"
"ds_read_b64_tr_b16 a[216:217], v227 offset:0\n"
"ds_read_b64_tr_b16 a[218:219], v227 offset:2048\n"
"v_mfma_f32_16x16x32_bf16 v[196:199], a[248:251], a[68:71], v[196:199]\n"
"buffer_load_dwordx4 v254, s[64:67], s80 offen lds\n"
"v_mfma_f32_16x16x32_bf16 v[196:199], a[244:247], a[52:55], v[196:199]\n"
"s_mov_b32 s58, 20971520\n"
"ds_read_b64_tr_b16 a[220:221], v227 offset:4096\n"
"ds_read_b64_tr_b16 a[222:223], v227 offset:6144\n"
"v_mfma_f32_16x16x32_bf16 v[196:199], a[240:243], a[36:39], v[196:199]\n"
"s_addk_i32 s80, 8192\n"
"ds_read_b128 a[96:99], v237 offset:8192\n"
"ds_read_b128 a[112:115], v237 offset:12288\n"
"v_mfma_f32_16x16x32_bf16 v[196:199], a[236:239], a[20:23], v[196:199]\n"
"s_addk_i32 m0, 1024\n"
"ds_read_b128 a[128:131], v238 offset:8192\n"
"ds_read_b128 a[144:147], v238 offset:12288\n"
"v_mfma_f32_16x16x32_bf16 v[196:199], a[232:235], a[4:7], v[196:199]\n"
"buffer_load_dwordx4 v254, s[64:67], s80 offen lds\n"
"v_mfma_f32_16x16x32_bf16 v[196:199], a[228:231], v[36:39], v[196:199]\n"
"s_mov_b32 s59, 0x20000\n"
"ds_read_b128 a[160:163], v239 offset:8192\n"
"s_mul_i32 s85, s79, 610304\n"
"v_mfma_f32_16x16x32_bf16 v[196:199], a[224:227], v[4:7], v[196:199]\n"
"s_addk_i32 s80, 8192\n"
"ds_read_b128 a[176:179], v239 offset:12288\n"
"ds_read_b128 a[100:103], v237 offset:9216\n"
"v_mfma_f32_16x16x32_bf16 v[200:203], a[224:227], v[8:11], 0\n"
"s_addk_i32 m0, 1024\n"
"ds_read_b128 a[116:119], v237 offset:13312\n"
"ds_read_b128 a[132:135], v238 offset:9216\n"
"v_mfma_f32_16x16x32_bf16 v[200:203], a[228:231], v[40:43], v[200:203]\n"
"buffer_load_dwordx4 v254, s[64:67], s80 offen lds\n"
"v_mfma_f32_16x16x32_bf16 v[200:203], a[232:235], a[8:11], v[200:203]\n"
"s_addk_i32 s80, 8192\n"
"ds_read_b128 a[148:151], v238 offset:13312\n"
"ds_read_b128 a[164:167], v239 offset:9216\n"
"v_mfma_f32_16x16x32_bf16 v[200:203], a[236:239], a[24:27], v[200:203]\n"
"s_addk_i32 m0, 1024\n"
"ds_read_b128 a[180:183], v239 offset:13312\n"
"ds_read_b128 a[104:107], v237 offset:10240\n"
"v_mfma_f32_16x16x32_bf16 v[200:203], a[240:243], a[40:43], v[200:203]\n"
"buffer_load_dwordx4 v254, s[64:67], s80 offen lds\n"
"v_mfma_f32_16x16x32_bf16 v[200:203], a[244:247], a[56:59], v[200:203]\n"
"s_mul_i32 s83, s77, 18432\n"
"ds_read_b128 a[120:123], v237 offset:14336\n"
"ds_read_b128 a[136:139], v238 offset:10240\n"
"v_mfma_f32_16x16x32_bf16 v[200:203], a[248:251], a[72:75], v[200:203]\n"
"s_addk_i32 s80, 8192\n"
"ds_read_b128 a[152:155], v238 offset:14336\n"
"ds_read_b128 a[168:171], v239 offset:10240\n"
"v_mfma_f32_16x16x32_bf16 v[200:203], a[252:255], a[88:91], v[200:203]\n"
"s_addk_i32 m0, 1024\n"
"ds_read_b128 a[184:187], v239 offset:14336\n"
"ds_read_b128 a[108:111], v237 offset:11264\n"
"v_mfma_f32_16x16x32_bf16 v[204:207], a[252:255], a[92:95], 0\n"
"buffer_load_dwordx4 v254, s[64:67], s80 offen lds\n"
"v_mfma_f32_16x16x32_bf16 v[204:207], a[248:251], a[76:79], v[204:207]\n"
"s_add_u32 s85, s85, s83\n"
"ds_read_b128 a[124:127], v237 offset:15360\n"
"ds_read_b128 a[140:143], v238 offset:11264\n"
"v_mfma_f32_16x16x32_bf16 v[204:207], a[244:247], a[60:63], v[204:207]\n"
"s_addk_i32 s80, 8192\n"
"ds_read_b128 a[156:159], v238 offset:15360\n"
"ds_read_b128 a[172:175], v239 offset:11264\n"
"v_mfma_f32_16x16x32_bf16 v[204:207], a[240:243], a[44:47], v[204:207]\n"
"s_addk_i32 m0, 1024\n"
"ds_read_b128 a[188:191], v239 offset:15360\n"
"s_mov_b64 s[72:73], s[18:19]\n"
"v_mfma_f32_16x16x32_bf16 v[204:207], a[236:239], a[28:31], v[204:207]\n"
"buffer_load_dwordx4 v254, s[64:67], s80 offen lds\n"
"v_mfma_f32_16x16x32_bf16 v[204:207], a[232:235], a[12:15], v[204:207]\n"
"s_mov_b32 s74, 2097152\n"
"s_mov_b32 s75, 0x20000\n"
"s_mov_b32 s95, 0\n"
"v_mfma_f32_16x16x32_bf16 v[204:207], a[228:231], v[44:47], v[204:207]\n"
"v_bfe_u32 v240, v255, 4, 5\n"
"v_lshrrev_b32_e32 v241, 9, v255\n"
"v_mfma_f32_16x16x32_bf16 v[204:207], a[224:227], v[12:15], v[204:207]\n"
"v_lshlrev_b32_e32 v241, 4, v241\n"
"v_lshl_add_u32 v240, v240, 10, v241\n"
"s_waitcnt vmcnt(16)\n"
"s_waitcnt lgkmcnt(0)\n"
"v_mfma_f32_16x16x32_bf16 v[128:131], a[192:195], v[64:67], v[128:131]\n"
"s_barrier\n"
"s_add_u32 s80, s71, 65536\n"
"ds_read_b64_tr_b16 a[224:225], v250 offset:0\n"
"v_mfma_f32_16x16x32_bf16 v[128:131], a[196:199], v[96:99], v[128:131]\n"
"s_add_u32 m0, s46, 0\n"
"ds_read_b64_tr_b16 a[226:227], v250 offset:2048\n"
"v_mov_b32_e32 v241, 0x3f80\n"
"v_mfma_f32_16x16x32_bf16 v[128:131], a[200:203], a[96:99], v[128:131]\n"
"buffer_load_dwordx4 v254, s[64:67], s80 offen lds\n"
"v_mfma_f32_16x16x32_bf16 v[128:131], a[204:207], a[112:115], v[128:131]\n"
"s_addk_i32 s80, 8192\n"
"ds_read_b64_tr_b16 a[228:229], v250 offset:4096\n"
"v_mov_b32_e32 v242, 0x3f800000\n"
"v_mfma_f32_16x16x32_bf16 v[128:131], a[208:211], a[128:131], v[128:131]\n"
"s_addk_i32 m0, 1024\n"
"ds_read_b64_tr_b16 a[230:231], v250 offset:6144\n"
"v_mfma_f32_16x16x32_bf16 v[128:131], a[212:215], a[144:147], v[128:131]\n"
"buffer_load_dwordx4 v254, s[64:67], s80 offen lds\n"
"v_mfma_f32_16x16x32_bf16 v[128:131], a[216:219], a[160:163], v[128:131]\n"
"s_addk_i32 s80, 8192\n"
"ds_read_b64_tr_b16 a[232:233], v251 offset:0\n"
"v_mfma_f32_16x16x32_bf16 v[128:131], a[220:223], a[176:179], v[128:131]\n"
"s_addk_i32 m0, 1024\n"
"ds_read_b64_tr_b16 a[234:235], v251 offset:2048\n"
"v_mfma_f32_16x16x32_bf16 v[132:135], a[220:223], a[180:183], v[132:135]\n"
"buffer_load_dwordx4 v254, s[64:67], s80 offen lds\n"
"v_mfma_f32_16x16x32_bf16 v[132:135], a[216:219], a[164:167], v[132:135]\n"
"s_addk_i32 s80, 8192\n"
"v_permlane16_swap_b32 v228, v230\n"
"v_mfma_f32_16x16x32_bf16 v[132:135], a[212:215], a[148:151], v[132:135]\n"
"s_addk_i32 m0, 1024\n"
"ds_read_b64_tr_b16 a[236:237], v251 offset:4096\n"
"v_mfma_f32_16x16x32_bf16 v[132:135], a[208:211], a[132:135], v[132:135]\n"
"buffer_load_dwordx4 v254, s[64:67], s80 offen lds\n"
"v_mfma_f32_16x16x32_bf16 v[132:135], a[204:207], a[116:119], v[132:135]\n"
"s_addk_i32 s80, 8192\n"
"ds_read_b64_tr_b16 a[238:239], v251 offset:6144\n"
"v_mfma_f32_16x16x32_bf16 v[132:135], a[200:203], a[100:103], v[132:135]\n"
"s_addk_i32 m0, 1024\n"
"ds_read_b64_tr_b16 a[240:241], v252 offset:0\n"
"v_mfma_f32_16x16x32_bf16 v[132:135], a[196:199], v[100:103], v[132:135]\n"
"buffer_load_dwordx4 v254, s[64:67], s80 offen lds\n"
"v_mfma_f32_16x16x32_bf16 v[132:135], a[192:195], v[68:71], v[132:135]\n"
"s_addk_i32 s80, 8192\n"
"ds_read_b64_tr_b16 a[242:243], v252 offset:2048\n"
"v_mfma_f32_16x16x32_bf16 v[136:139], a[192:195], v[72:75], v[136:139]\n"
"s_addk_i32 m0, 1024\n"
"ds_read_b64_tr_b16 a[244:245], v252 offset:4096\n"
"v_mfma_f32_16x16x32_bf16 v[136:139], a[196:199], v[104:107], v[136:139]\n"
"buffer_load_dwordx4 v254, s[64:67], s80 offen lds\n"
"v_mfma_f32_16x16x32_bf16 v[136:139], a[200:203], a[104:107], v[136:139]\n"
"s_addk_i32 s80, 8192\n"
"v_permlane16_swap_b32 v229, v231\n"
"v_mfma_f32_16x16x32_bf16 v[136:139], a[204:207], a[120:123], v[136:139]\n"
"s_addk_i32 m0, 1024\n"
"ds_read_b64_tr_b16 a[246:247], v252 offset:6144\n"
"v_mfma_f32_16x16x32_bf16 v[136:139], a[208:211], a[136:139], v[136:139]\n"
"s_and_b32 s68, s68, 0x380\n"
"buffer_load_dwordx4 v254, s[64:67], s80 offen lds\n"
"v_mfma_f32_16x16x32_bf16 v[136:139], a[212:215], a[152:155], v[136:139]\n"
"s_addk_i32 s80, 8192\n"
"ds_read_b64_tr_b16 a[248:249], v253 offset:0\n"
"v_mfma_f32_16x16x32_bf16 v[136:139], a[216:219], a[168:171], v[136:139]\n"
"s_addk_i32 m0, 1024\n"
"ds_read_b64_tr_b16 a[250:251], v253 offset:2048\n"
"v_mfma_f32_16x16x32_bf16 v[136:139], a[220:223], a[184:187], v[136:139]\n"
"s_mul_i32 s68, s68, 592\n"
".LPG16_%=:\n"
"s_cbranch_execz .LPGR_%=\n"
"buffer_load_dwordx4 v254, s[64:67], s80 offen lds\n"
"v_mfma_f32_16x16x32_bf16 v[140:143], a[220:223], a[188:191], v[140:143]\n"
"s_add_u32 s80, s87, 0\n"
"ds_read_b64_tr_b16 a[252:253], v253 offset:4096\n"
"v_mfma_f32_16x16x32_bf16 v[140:143], a[216:219], a[172:175], v[140:143]\n"
"s_add_u32 m0, s46, 8192\n"
"ds_read_b64_tr_b16 a[254:255], v253 offset:6144\n"
"v_mfma_f32_16x16x32_bf16 v[140:143], a[212:215], a[156:159], v[140:143]\n"
"s_add_u32 s68, s68, 0\n"
"buffer_load_dwordx4 v254, s[64:67], s80 offen lds\n"
"v_mfma_f32_16x16x32_bf16 v[140:143], a[208:211], a[140:143], v[140:143]\n"
"s_addk_i32 s80, 8192\n"
"v_permlane16_swap_b32 v232, v234\n"
"v_mfma_f32_16x16x32_bf16 v[140:143], a[204:207], a[124:127], v[140:143]\n"
"s_addk_i32 m0, 1024\n"
"v_permlane16_swap_b32 v233, v235\n"
"v_mfma_f32_16x16x32_bf16 v[140:143], a[200:203], a[108:111], v[140:143]\n"
"s_add_u32 s68, s68, s85\n"
"buffer_load_dwordx4 v254, s[64:67], s80 offen lds\n"
"v_mfma_f32_16x16x32_bf16 v[140:143], a[196:199], v[108:111], v[140:143]\n"
"v_mul_f32_e32 v176, 0xc13504f3, v160\n"
"v_mul_f32_e32 v177, 0xc13504f3, v161\n"
"v_mfma_f32_16x16x32_bf16 v[140:143], a[192:195], v[76:79], v[140:143]\n"
"v_exp_f32_e32 v176, v176\n"
"s_waitcnt vmcnt(18)\n"
"s_waitcnt lgkmcnt(0)\n"
"v_mfma_f32_16x16x32_bf16 v[144:147], a[224:227], v[64:67], v[144:147]\n"
"s_barrier\n"
"s_addk_i32 s80, 8192\n"
"ds_read_b64_tr_b16 a[192:193], v246 offset:0\n"
"v_mfma_f32_16x16x32_bf16 v[144:147], a[228:231], v[96:99], v[144:147]\n"
"s_addk_i32 m0, 1024\n"
"ds_read_b64_tr_b16 a[194:195], v246 offset:2048\n"
"v_cvt_pk_bf16_f32 v128, v128, v129\n"
"v_mfma_f32_16x16x32_bf16 v[144:147], a[232:235], a[96:99], v[144:147]\n"
"buffer_load_dwordx4 v254, s[64:67], s80 offen lds\n"
"v_mfma_f32_16x16x32_bf16 v[144:147], a[236:239], a[112:115], v[144:147]\n"
"ds_read_b64_tr_b16 a[196:197], v246 offset:4096\n"
"v_cvt_pk_bf16_f32 v129, v130, v131\n"
"v_mfma_f32_16x16x32_bf16 v[144:147], a[240:243], a[128:131], v[144:147]\n"
"v_cvt_pk_bf16_f32 v130, v132, v133\n"
"v_cvt_pk_bf16_f32 v131, v134, v135\n"
"v_mfma_f32_16x16x32_bf16 v[144:147], a[244:247], a[144:147], v[144:147]\n"
"s_addk_i32 s80, 8192\n"
"ds_read_b64_tr_b16 a[198:199], v246 offset:6144\n"
"v_cvt_pk_bf16_f32 v136, v136, v137\n"
"v_mfma_f32_16x16x32_bf16 v[144:147], a[248:251], a[160:163], v[144:147]\n"
"s_addk_i32 m0, 1024\n"
"ds_read_b64_tr_b16 a[200:201], v247 offset:0\n"
"buffer_store_dwordx4 v[128:131], v255, s[56:59], s68 offen offset:0 sc1\n"
"v_mfma_f32_16x16x32_bf16 v[144:147], a[252:255], a[176:179], v[144:147]\n"
"buffer_load_dwordx4 v254, s[64:67], s80 offen lds\n"
"v_mfma_f32_16x16x32_bf16 v[148:151], a[252:255], a[180:183], v[148:151]\n"
"ds_read_b64_tr_b16 a[202:203], v247 offset:2048\n"
"v_cvt_pk_bf16_f32 v137, v138, v139\n"
"v_mfma_f32_16x16x32_bf16 v[148:151], a[248:251], a[164:167], v[148:151]\n"
"v_cvt_pk_bf16_f32 v138, v140, v141\n"
"v_cvt_pk_bf16_f32 v139, v142, v143\n"
"v_mfma_f32_16x16x32_bf16 v[148:151], a[244:247], a[148:151], v[148:151]\n"
"s_addk_i32 s80, 8192\n"
"ds_read_b64_tr_b16 a[204:205], v247 offset:4096\n"
"v_mul_f32_e32 v178, 0xc13504f3, v162\n"
"v_mfma_f32_16x16x32_bf16 v[148:151], a[240:243], a[132:135], v[148:151]\n"
"s_addk_i32 m0, 1024\n"
"ds_read_b64_tr_b16 a[206:207], v247 offset:6144\n"
"v_mfma_f32_16x16x32_bf16 v[148:151], a[236:239], a[116:119], v[148:151]\n"
"buffer_load_dwordx4 v254, s[64:67], s80 offen lds\n"
"v_mfma_f32_16x16x32_bf16 v[148:151], a[232:235], a[100:103], v[148:151]\n"
"v_exp_f32_e32 v177, v177\n"
"v_mfma_f32_16x16x32_bf16 v[148:151], a[228:231], v[100:103], v[148:151]\n"
"ds_read_b64_tr_b16 a[208:209], v248 offset:0\n"
"v_add_f32_e32 v176, 1.0, v176\n"
"v_mfma_f32_16x16x32_bf16 v[148:151], a[224:227], v[68:71], v[148:151]\n"
"s_addk_i32 s80, 8192\n"
"ds_read_b64_tr_b16 a[210:211], v248 offset:2048\n"
"v_mul_f32_e32 v179, 0xc13504f3, v163\n"
"v_mfma_f32_16x16x32_bf16 v[152:155], a[224:227], v[72:75], v[152:155]\n"
"s_addk_i32 m0, 1024\n"
"ds_read_b64_tr_b16 a[212:213], v248 offset:4096\n"
"v_mfma_f32_16x16x32_bf16 v[152:155], a[228:231], v[104:107], v[152:155]\n"
"buffer_load_dwordx4 v254, s[64:67], s80 offen lds\n"
"v_mfma_f32_16x16x32_bf16 v[152:155], a[232:235], a[104:107], v[152:155]\n"
"v_exp_f32_e32 v178, v178\n"
"v_mfma_f32_16x16x32_bf16 v[152:155], a[236:239], a[120:123], v[152:155]\n"
"ds_read_b64_tr_b16 a[214:215], v248 offset:6144\n"
"buffer_store_dwordx4 v[136:139], v255, s[56:59], s68 offen offset:1024 sc1\n"
"v_mfma_f32_16x16x32_bf16 v[152:155], a[240:243], a[136:139], v[152:155]\n"
"s_addk_i32 s80, 8192\n"
"s_and_b32 s69, s69, 0x380\n"
"ds_read_b64_tr_b16 a[216:217], v249 offset:0\n"
"v_mfma_f32_16x16x32_bf16 v[152:155], a[244:247], a[152:155], v[152:155]\n"
"s_addk_i32 m0, 1024\n"
"ds_read_b64_tr_b16 a[218:219], v249 offset:2048\n"
"v_add_f32_e32 v177, 1.0, v177\n"
"v_mfma_f32_16x16x32_bf16 v[152:155], a[248:251], a[168:171], v[152:155]\n"
"buffer_load_dwordx4 v254, s[64:67], s80 offen lds\n"
"v_mfma_f32_16x16x32_bf16 v[152:155], a[252:255], a[184:187], v[152:155]\n"
"s_mul_i32 s69, s69, 592\n"
"v_rcp_f32_e32 v176, v176\n"
"v_mfma_f32_16x16x32_bf16 v[156:159], a[252:255], a[188:191], v[156:159]\n"
"ds_read_b64_tr_b16 a[220:221], v249 offset:4096\n"
"v_mul_f32_e32 v180, 0xc13504f3, v164\n"
"v_mfma_f32_16x16x32_bf16 v[156:159], a[248:251], a[172:175], v[156:159]\n"
"s_addk_i32 s80, 8192\n"
"ds_read_b64_tr_b16 a[222:223], v249 offset:6144\n"
"v_mfma_f32_16x16x32_bf16 v[156:159], a[244:247], a[156:159], v[156:159]\n"
"s_addk_i32 m0, 1024\n"
"s_add_u32 s69, s69, 2048\n"
"v_mfma_f32_16x16x32_bf16 v[156:159], a[240:243], a[140:143], v[156:159]\n"
"buffer_load_dwordx4 v254, s[64:67], s80 offen lds\n"
"v_mfma_f32_16x16x32_bf16 v[156:159], a[236:239], a[124:127], v[156:159]\n"
"v_exp_f32_e32 v179, v179\n"
"v_mfma_f32_16x16x32_bf16 v[156:159], a[232:235], a[108:111], v[156:159]\n"
"s_add_u32 s69, s69, s85\n"
"v_add_f32_e32 v178, 1.0, v178\n"
"v_mfma_f32_16x16x32_bf16 v[156:159], a[228:231], v[108:111], v[156:159]\n"
"v_rcp_f32_e32 v177, v177\n"
"v_mfma_f32_16x16x32_bf16 v[156:159], a[224:227], v[76:79], v[156:159]\n"
"v_mul_f32_e32 v160, v176, v160\n"
"v_mul_f32_e32 v181, 0xc13504f3, v165\n"
"s_waitcnt vmcnt(18)\n"
"s_waitcnt lgkmcnt(0)\n"
"v_mfma_f32_16x16x32_bf16 v[192:195], a[192:195], v[64:67], v[192:195]\n"
"s_barrier\n"
"s_add_u32 s80, s87, 65536\n"
"ds_read_b64_tr_b16 a[224:225], v250 offset:8192\n"
"v_mfma_f32_16x16x32_bf16 v[192:195], a[196:199], v[96:99], v[192:195]\n"
"s_add_u32 m0, s46, 65536\n"
"ds_read_b64_tr_b16 a[226:227], v250 offset:10240\n"
"v_cvt_pk_bf16_f32 v144, v144, v145\n"
"v_mfma_f32_16x16x32_bf16 v[192:195], a[200:203], a[96:99], v[192:195]\n"
"buffer_load_dwordx4 v254, s[64:67], s80 offen lds\n"
"v_mfma_f32_16x16x32_bf16 v[192:195], a[204:207], a[112:115], v[192:195]\n"
"ds_read_b64_tr_b16 a[228:229], v250 offset:12288\n"
"v_cvt_pk_bf16_f32 v145, v146, v147\n"
"v_mfma_f32_16x16x32_bf16 v[192:195], a[208:211], a[128:131], v[192:195]\n"
"v_cvt_pk_bf16_f32 v146, v148, v149\n"
"v_cvt_pk_bf16_f32 v147, v150, v151\n"
"v_mfma_f32_16x16x32_bf16 v[192:195], a[212:215], a[144:147], v[192:195]\n"
"s_addk_i32 s80, 8192\n"
"ds_read_b64_tr_b16 a[230:231], v250 offset:14336\n"
"v_cvt_pk_bf16_f32 v152, v152, v153\n"
"v_mfma_f32_16x16x32_bf16 v[192:195], a[216:219], a[160:163], v[192:195]\n"
"s_addk_i32 m0, 1024\n"
"ds_read_b64_tr_b16 a[232:233], v251 offset:8192\n"
"buffer_store_dwordx4 v[144:147], v255, s[56:59], s69 offen offset:0 sc1\n"
"v_mfma_f32_16x16x32_bf16 v[192:195], a[220:223], a[176:179], v[192:195]\n"
"buffer_load_dwordx4 v254, s[64:67], s80 offen lds\n"
"v_mfma_f32_16x16x32_bf16 v[196:199], a[220:223], a[180:183], v[196:199]\n"
"ds_read_b64_tr_b16 a[234:235], v251 offset:10240\n"
"v_cvt_pk_bf16_f32 v153, v154, v155\n"
"v_mfma_f32_16x16x32_bf16 v[196:199], a[216:219], a[164:167], v[196:199]\n"
"v_cvt_pk_bf16_f32 v154, v156, v157\n"
"v_cvt_pk_bf16_f32 v155, v158, v159\n"
"v_mfma_f32_16x16x32_bf16 v[196:199], a[212:215], a[148:151], v[196:199]\n"
"s_addk_i32 s80, 8192\n"
"ds_read_b64_tr_b16 a[236:237], v251 offset:12288\n"
"v_mfma_f32_16x16x32_bf16 v[196:199], a[208:211], a[132:135], v[196:199]\n"
"s_addk_i32 m0, 1024\n"
"ds_read_b64_tr_b16 a[238:239], v251 offset:14336\n"
"v_mfma_f32_16x16x32_bf16 v[196:199], a[204:207], a[116:119], v[196:199]\n"
"buffer_load_dwordx4 v254, s[64:67], s80 offen lds\n"
"v_mfma_f32_16x16x32_bf16 v[196:199], a[200:203], a[100:103], v[196:199]\n"
"v_exp_f32_e32 v180, v180\n"
"v_mfma_f32_16x16x32_bf16 v[196:199], a[196:199], v[100:103], v[196:199]\n"
"ds_read_b64_tr_b16 a[240:241], v252 offset:8192\n"
"v_add_f32_e32 v179, 1.0, v179\n"
"v_mfma_f32_16x16x32_bf16 v[196:199], a[192:195], v[68:71], v[196:199]\n"
"s_addk_i32 s80, 8192\n"
"ds_read_b64_tr_b16 a[242:243], v252 offset:10240\n"
"v_mfma_f32_16x16x32_bf16 v[200:203], a[192:195], v[72:75], v[200:203]\n"
"s_addk_i32 m0, 1024\n"
"ds_read_b64_tr_b16 a[244:245], v252 offset:12288\n"
"v_mfma_f32_16x16x32_bf16 v[200:203], a[196:199], v[104:107], v[200:203]\n"
"buffer_load_dwordx4 v254, s[64:67], s80 offen lds\n"
"v_mfma_f32_16x16x32_bf16 v[200:203], a[200:203], a[104:107], v[200:203]\n"
"v_rcp_f32_e32 v178, v178\n"
"v_mfma_f32_16x16x32_bf16 v[200:203], a[204:207], a[120:123], v[200:203]\n"
"ds_read_b64_tr_b16 a[246:247], v252 offset:14336\n"
"buffer_store_dwordx4 v[152:155], v255, s[56:59], s69 offen offset:1024 sc1\n"
"v_mfma_f32_16x16x32_bf16 v[200:203], a[208:211], a[136:139], v[200:203]\n"
"s_addk_i32 s80, 8192\n"
"s_and_b32 s70, s70, 0x380\n"
"ds_read_b64_tr_b16 a[248:249], v253 offset:8192\n"
"v_mfma_f32_16x16x32_bf16 v[200:203], a[212:215], a[152:155], v[200:203]\n"
"s_addk_i32 m0, 1024\n"
"ds_read_b64_tr_b16 a[250:251], v253 offset:10240\n"
"v_mul_f32_e32 v161, v177, v161\n"
"v_mfma_f32_16x16x32_bf16 v[200:203], a[216:219], a[168:171], v[200:203]\n"
"buffer_load_dwordx4 v254, s[64:67], s80 offen lds\n"
"v_mfma_f32_16x16x32_bf16 v[200:203], a[220:223], a[184:187], v[200:203]\n"
"s_mul_i32 s70, s70, 592\n"
"v_mul_f32_e32 v182, 0xc13504f3, v166\n"
"v_mfma_f32_16x16x32_bf16 v[204:207], a[220:223], a[188:191], v[204:207]\n"
"ds_read_b64_tr_b16 a[252:253], v253 offset:12288\n"
"v_mfma_f32_16x16x32_bf16 v[204:207], a[216:219], a[172:175], v[204:207]\n"
"s_addk_i32 s80, 8192\n"
"ds_read_b64_tr_b16 a[254:255], v253 offset:14336\n"
"v_mfma_f32_16x16x32_bf16 v[204:207], a[212:215], a[156:159], v[204:207]\n"
"s_addk_i32 m0, 1024\n"
"s_add_u32 s70, s70, 4096\n"
"v_mfma_f32_16x16x32_bf16 v[204:207], a[208:211], a[140:143], v[204:207]\n"
"buffer_load_dwordx4 v254, s[64:67], s80 offen lds\n"
"v_mfma_f32_16x16x32_bf16 v[204:207], a[204:207], a[124:127], v[204:207]\n"
"v_exp_f32_e32 v181, v181\n"
"v_mfma_f32_16x16x32_bf16 v[204:207], a[200:203], a[108:111], v[204:207]\n"
"s_add_u32 s70, s70, s85\n"
"v_add_f32_e32 v180, 1.0, v180\n"
"v_mfma_f32_16x16x32_bf16 v[204:207], a[196:199], v[108:111], v[204:207]\n"
"v_rcp_f32_e32 v179, v179\n"
"v_mfma_f32_16x16x32_bf16 v[204:207], a[192:195], v[76:79], v[204:207]\n"
"v_mul_f32_e32 v162, v178, v162\n"
"v_mul_f32_e32 v183, 0xc13504f3, v167\n"
"s_waitcnt vmcnt(18)\n"
"s_waitcnt lgkmcnt(0)\n"
"v_mfma_f32_16x16x32_bf16 v[208:211], a[224:227], v[0:3], 0\n"
"s_barrier\n"
"s_addk_i32 s80, 8192\n"
"ds_read_b64_tr_b16 a[192:193], v224 offset:0\n"
"v_mfma_f32_16x16x32_bf16 v[208:211], a[228:231], v[32:35], v[208:211]\n"
"s_addk_i32 m0, 1024\n"
"ds_read_b64_tr_b16 a[194:195], v224 offset:2048\n"
"v_cvt_pk_bf16_f32 v192, v192, v193\n"
"v_mfma_f32_16x16x32_bf16 v[208:211], a[232:235], a[0:3], v[208:211]\n"
"buffer_load_dwordx4 v254, s[64:67], s80 offen lds\n"
"v_mfma_f32_16x16x32_bf16 v[208:211], a[236:239], a[16:19], v[208:211]\n"
"ds_read_b64_tr_b16 a[196:197], v224 offset:4096\n"
"v_cvt_pk_bf16_f32 v193, v194, v195\n"
"v_mfma_f32_16x16x32_bf16 v[208:211], a[240:243], a[32:35], v[208:211]\n"
"v_cvt_pk_bf16_f32 v194, v196, v197\n"
"v_cvt_pk_bf16_f32 v195, v198, v199\n"
"v_mfma_f32_16x16x32_bf16 v[208:211], a[244:247], a[48:51], v[208:211]\n"
"s_addk_i32 s80, 8192\n"
"ds_read_b64_tr_b16 a[198:199], v224 offset:6144\n"
"v_cvt_pk_bf16_f32 v200, v200, v201\n"
"v_mfma_f32_16x16x32_bf16 v[208:211], a[248:251], a[64:67], v[208:211]\n"
"s_addk_i32 m0, 1024\n"
"ds_read_b64_tr_b16 a[200:201], v225 offset:0\n"
"buffer_store_dwordx4 v[192:195], v255, s[56:59], s70 offen offset:0 sc1\n"
"v_mfma_f32_16x16x32_bf16 v[208:211], a[252:255], a[80:83], v[208:211]\n"
"buffer_load_dwordx4 v254, s[64:67], s80 offen lds\n"
"v_mfma_f32_16x16x32_bf16 v[212:215], a[252:255], a[84:87], 0\n"
"ds_read_b64_tr_b16 a[202:203], v225 offset:2048\n"
"v_cvt_pk_bf16_f32 v201, v202, v203\n"
"v_mfma_f32_16x16x32_bf16 v[212:215], a[248:251], a[68:71], v[212:215]\n"
"v_cvt_pk_bf16_f32 v202, v204, v205\n"
"v_cvt_pk_bf16_f32 v203, v206, v207\n"
"v_mfma_f32_16x16x32_bf16 v[212:215], a[244:247], a[52:55], v[212:215]\n"
"s_add_u32 s80, s88, 0\n"
"ds_read_b64_tr_b16 a[204:205], v225 offset:4096\n"
"v_mfma_f32_16x16x32_bf16 v[212:215], a[240:243], a[36:39], v[212:215]\n"
"s_add_u32 m0, s81, 0\n"
"ds_read_b64_tr_b16 a[206:207], v225 offset:6144\n"
"v_mfma_f32_16x16x32_bf16 v[212:215], a[236:239], a[20:23], v[212:215]\n"
"buffer_load_dwordx4 v254, s[64:67], s80 offen lds\n"
"v_mfma_f32_16x16x32_bf16 v[212:215], a[232:235], a[4:7], v[212:215]\n"
"v_exp_f32_e32 v182, v182\n"
"v_mfma_f32_16x16x32_bf16 v[212:215], a[228:231], v[36:39], v[212:215]\n"
"ds_read_b64_tr_b16 a[208:209], v226 offset:0\n"
"v_add_f32_e32 v181, 1.0, v181\n"
"v_mfma_f32_16x16x32_bf16 v[212:215], a[224:227], v[4:7], v[212:215]\n"
"s_addk_i32 s80, 8192\n"
"ds_read_b64_tr_b16 a[210:211], v226 offset:2048\n"
"v_mfma_f32_16x16x32_bf16 v[216:219], a[224:227], v[8:11], 0\n"
"s_addk_i32 m0, 1024\n"
"ds_read_b64_tr_b16 a[212:213], v226 offset:4096\n"
"v_mfma_f32_16x16x32_bf16 v[216:219], a[228:231], v[40:43], v[216:219]\n"
"buffer_load_dwordx4 v254, s[64:67], s80 offen lds\n"
"v_mfma_f32_16x16x32_bf16 v[216:219], a[232:235], a[8:11], v[216:219]\n"
"v_rcp_f32_e32 v180, v180\n"
"v_mfma_f32_16x16x32_bf16 v[216:219], a[236:239], a[24:27], v[216:219]\n"
"ds_read_b64_tr_b16 a[214:215], v226 offset:6144\n"
"buffer_store_dwordx4 v[200:203], v255, s[56:59], s70 offen offset:1024 sc1\n"
"v_mfma_f32_16x16x32_bf16 v[216:219], a[240:243], a[40:43], v[216:219]\n"
"s_addk_i32 s80, 8192\n"
"ds_read_b64_tr_b16 a[216:217], v227 offset:0\n"
"v_mul_f32_e32 v163, v179, v163\n"
"v_mfma_f32_16x16x32_bf16 v[216:219], a[244:247], a[56:59], v[216:219]\n"
"s_addk_i32 m0, 1024\n"
"ds_read_b64_tr_b16 a[218:219], v227 offset:2048\n"
"v_mul_f32_e32 v184, 0xc13504f3, v168\n"
"v_mfma_f32_16x16x32_bf16 v[216:219], a[248:251], a[72:75], v[216:219]\n"
"buffer_load_dwordx4 v254, s[64:67], s80 offen lds\n"
"v_mfma_f32_16x16x32_bf16 v[216:219], a[252:255], a[88:91], v[216:219]\n"
"v_exp_f32_e32 v183, v183\n"
"v_mfma_f32_16x16x32_bf16 v[220:223], a[252:255], a[92:95], 0\n"
"ds_read_b64_tr_b16 a[220:221], v227 offset:4096\n"
"v_add_f32_e32 v182, 1.0, v182\n"
"v_mfma_f32_16x16x32_bf16 v[220:223], a[248:251], a[76:79], v[220:223]\n"
"s_addk_i32 s80, 8192\n"
"ds_read_b64_tr_b16 a[222:223], v227 offset:6144\n"
"v_mfma_f32_16x16x32_bf16 v[220:223], a[244:247], a[60:63], v[220:223]\n"
"s_addk_i32 m0, 1024\n"
"v_rcp_f32_e32 v181, v181\n"
"v_mfma_f32_16x16x32_bf16 v[220:223], a[240:243], a[44:47], v[220:223]\n"
"buffer_load_dwordx4 v254, s[64:67], s80 offen lds\n"
"v_mfma_f32_16x16x32_bf16 v[220:223], a[236:239], a[28:31], v[220:223]\n"
"v_mul_f32_e32 v164, v180, v164\n"
"v_mul_f32_e32 v185, 0xc13504f3, v169\n"
"v_mfma_f32_16x16x32_bf16 v[220:223], a[232:235], a[12:15], v[220:223]\n"
"v_exp_f32_e32 v184, v184\n"
"v_mfma_f32_16x16x32_bf16 v[220:223], a[228:231], v[44:47], v[220:223]\n"
"v_add_f32_e32 v183, 1.0, v183\n"
"v_mfma_f32_16x16x32_bf16 v[220:223], a[224:227], v[12:15], v[220:223]\n"
"v_rcp_f32_e32 v182, v182\n"
"s_waitcnt vmcnt(16)\n"
"s_waitcnt lgkmcnt(0)\n"
"v_mfma_f32_16x16x32_bf16 v[208:211], a[192:195], v[64:67], v[208:211]\n"
"s_barrier\n"
"s_addk_i32 s80, 8192\n"
"ds_read_b64_tr_b16 a[224:225], v224 offset:8192\n"
"v_mfma_f32_16x16x32_bf16 v[208:211], a[196:199], v[96:99], v[208:211]\n"
"s_addk_i32 m0, 1024\n"
"ds_read_b64_tr_b16 a[226:227], v224 offset:10240\n"
"v_mul_f32_e32 v165, v181, v165\n"
"v_mfma_f32_16x16x32_bf16 v[208:211], a[200:203], a[96:99], v[208:211]\n"
"buffer_load_dwordx4 v254, s[64:67], s80 offen lds\n"
"v_mfma_f32_16x16x32_bf16 v[208:211], a[204:207], a[112:115], v[208:211]\n"
"s_addk_i32 s80, 8192\n"
"ds_read_b64_tr_b16 a[228:229], v224 offset:12288\n"
"v_mul_f32_e32 v186, 0xc13504f3, v170\n"
"v_mfma_f32_16x16x32_bf16 v[208:211], a[208:211], a[128:131], v[208:211]\n"
"s_addk_i32 m0, 1024\n"
"ds_read_b64_tr_b16 a[230:231], v224 offset:14336\n"
"v_mfma_f32_16x16x32_bf16 v[208:211], a[212:215], a[144:147], v[208:211]\n"
"buffer_load_dwordx4 v254, s[64:67], s80 offen lds\n"
"v_mfma_f32_16x16x32_bf16 v[208:211], a[216:219], a[160:163], v[208:211]\n"
"s_addk_i32 s80, 8192\n"
"v_exp_f32_e32 v185, v185\n"
"v_mfma_f32_16x16x32_bf16 v[208:211], a[220:223], a[176:179], v[208:211]\n"
"s_addk_i32 m0, 1024\n"
"ds_read_b64_tr_b16 a[232:233], v225 offset:8192\n"
"v_add_f32_e32 v184, 1.0, v184\n"
"v_mfma_f32_16x16x32_bf16 v[212:215], a[220:223], a[180:183], v[212:215]\n"
"buffer_load_dwordx4 v254, s[64:67], s80 offen lds\n"
"v_mfma_f32_16x16x32_bf16 v[212:215], a[216:219], a[164:167], v[212:215]\n"
"ds_read_b64_tr_b16 a[234:235], v225 offset:10240\n"
"v_mfma_f32_16x16x32_bf16 v[212:215], a[212:215], a[148:151], v[212:215]\n"
"s_addk_i32 s80, 8192\n"
"ds_read_b64_tr_b16 a[236:237], v225 offset:12288\n"
"v_mfma_f32_16x16x32_bf16 v[212:215], a[208:211], a[132:135], v[212:215]\n"
"s_addk_i32 m0, 1024\n"
"ds_read_b64_tr_b16 a[238:239], v225 offset:14336\n"
"v_mfma_f32_16x16x32_bf16 v[212:215], a[204:207], a[116:119], v[212:215]\n"
"buffer_load_dwordx4 v254, s[64:67], s80 offen lds\n"
"v_mfma_f32_16x16x32_bf16 v[212:215], a[200:203], a[100:103], v[212:215]\n"
"s_add_u32 s80, s88, 65536\n"
"v_rcp_f32_e32 v183, v183\n"
"v_mfma_f32_16x16x32_bf16 v[212:215], a[196:199], v[100:103], v[212:215]\n"
"s_add_u32 m0, s46, 73728\n"
"ds_read_b64_tr_b16 a[240:241], v226 offset:8192\n"
"v_mul_f32_e32 v166, v182, v166\n"
"v_mfma_f32_16x16x32_bf16 v[212:215], a[192:195], v[68:71], v[212:215]\n"
"buffer_load_dwordx4 v254, s[64:67], s80 offen lds\n"
"v_mfma_f32_16x16x32_bf16 v[216:219], a[192:195], v[72:75], v[216:219]\n"
"s_addk_i32 s80, 8192\n"
"ds_read_b64_tr_b16 a[242:243], v226 offset:10240\n"
"v_mul_f32_e32 v187, 0xc13504f3, v171\n"
"v_mfma_f32_16x16x32_bf16 v[216:219], a[196:199], v[104:107], v[216:219]\n"
"s_addk_i32 m0, 1024\n"
"ds_read_b64_tr_b16 a[244:245], v226 offset:12288\n"
"v_mfma_f32_16x16x32_bf16 v[216:219], a[200:203], a[104:107], v[216:219]\n"
"buffer_load_dwordx4 v254, s[64:67], s80 offen lds\n"
"v_mfma_f32_16x16x32_bf16 v[216:219], a[204:207], a[120:123], v[216:219]\n"
"ds_read_b64_tr_b16 a[246:247], v226 offset:14336\n"
"v_mfma_f32_16x16x32_bf16 v[216:219], a[208:211], a[136:139], v[216:219]\n"
"s_addk_i32 s80, 8192\n"
"s_and_b32 s71, s71, 0x380\n"
"v_mfma_f32_16x16x32_bf16 v[216:219], a[212:215], a[152:155], v[216:219]\n"
"s_addk_i32 m0, 1024\n"
"ds_read_b64_tr_b16 a[248:249], v227 offset:8192\n"
"v_mfma_f32_16x16x32_bf16 v[216:219], a[216:219], a[168:171], v[216:219]\n"
"buffer_load_dwordx4 v254, s[64:67], s80 offen lds\n"
"v_mfma_f32_16x16x32_bf16 v[216:219], a[220:223], a[184:187], v[216:219]\n"
"s_addk_i32 s80, 8192\n"
"s_mul_i32 s71, s71, 592\n"
"ds_read_b64_tr_b16 a[250:251], v227 offset:10240\n"
"v_mfma_f32_16x16x32_bf16 v[220:223], a[220:223], a[188:191], v[220:223]\n"
"s_addk_i32 m0, 1024\n"
"ds_read_b64_tr_b16 a[252:253], v227 offset:12288\n"
"v_mfma_f32_16x16x32_bf16 v[220:223], a[216:219], a[172:175], v[220:223]\n"
"buffer_load_dwordx4 v254, s[64:67], s80 offen lds\n"
"v_mfma_f32_16x16x32_bf16 v[220:223], a[212:215], a[156:159], v[220:223]\n"
"s_addk_i32 s80, 8192\n"
"s_add_u32 s71, s71, 6144\n"
"ds_read_b64_tr_b16 a[254:255], v227 offset:14336\n"
"v_mfma_f32_16x16x32_bf16 v[220:223], a[208:211], a[140:143], v[220:223]\n"
"s_addk_i32 m0, 1024\n"
"v_exp_f32_e32 v186, v186\n"
"v_mfma_f32_16x16x32_bf16 v[220:223], a[204:207], a[124:127], v[220:223]\n"
"buffer_load_dwordx4 v254, s[64:67], s80 offen lds\n"
"v_mfma_f32_16x16x32_bf16 v[220:223], a[200:203], a[108:111], v[220:223]\n"
"s_add_u32 s71, s71, s85\n"
"v_add_f32_e32 v185, 1.0, v185\n"
"v_mfma_f32_16x16x32_bf16 v[220:223], a[196:199], v[108:111], v[220:223]\n"
"v_rcp_f32_e32 v184, v184\n"
"v_mfma_f32_16x16x32_bf16 v[220:223], a[192:195], v[76:79], v[220:223]\n"
"v_mul_f32_e32 v167, v183, v167\n"
"v_mul_f32_e32 v188, 0xc13504f3, v172\n"
"s_waitcnt vmcnt(14)\n"
"s_waitcnt lgkmcnt(0)\n"
"v_mfma_f32_16x16x32_bf16 v[16:19], a[224:227], v[0:3], 0\n"
"s_barrier\n"
"s_addk_i32 s80, 8192\n"
"ds_read_b64_tr_b16 a[192:193], v250 offset:0\n"
"v_mfma_f32_16x16x32_bf16 v[16:19], a[228:231], v[32:35], v[16:19]\n"
"s_addk_i32 m0, 1024\n"
"ds_read_b64_tr_b16 a[194:195], v250 offset:2048\n"
"v_cvt_pk_bf16_f32 v208, v208, v209\n"
"v_mfma_f32_16x16x32_bf16 v[16:19], a[232:235], a[0:3], v[16:19]\n"
"buffer_load_dwordx4 v254, s[64:67], s80 offen lds\n"
"v_mfma_f32_16x16x32_bf16 v[16:19], a[236:239], a[16:19], v[16:19]\n"
"ds_read_b64_tr_b16 a[196:197], v250 offset:4096\n"
"v_cvt_pk_bf16_f32 v209, v210, v211\n"
"v_mfma_f32_16x16x32_bf16 v[16:19], a[240:243], a[32:35], v[16:19]\n"
"v_cvt_pk_bf16_f32 v210, v212, v213\n"
"v_cvt_pk_bf16_f32 v211, v214, v215\n"
"v_mfma_f32_16x16x32_bf16 v[16:19], a[244:247], a[48:51], v[16:19]\n"
"s_addk_i32 s80, 8192\n"
"ds_read_b64_tr_b16 a[198:199], v250 offset:6144\n"
"v_cvt_pk_bf16_f32 v216, v216, v217\n"
"v_mfma_f32_16x16x32_bf16 v[16:19], a[248:251], a[64:67], v[16:19]\n"
"s_addk_i32 m0, 1024\n"
"ds_read_b64_tr_b16 a[200:201], v251 offset:0\n"
"buffer_store_dwordx4 v[208:211], v255, s[56:59], s71 offen offset:0 sc1\n"
"v_mfma_f32_16x16x32_bf16 v[16:19], a[252:255], a[80:83], v[16:19]\n"
"buffer_load_dwordx4 v254, s[64:67], s80 offen lds\n"
"v_mfma_f32_16x16x32_bf16 v[20:23], a[252:255], a[84:87], 0\n"
"ds_read_b64_tr_b16 a[202:203], v251 offset:2048\n"
"v_cvt_pk_bf16_f32 v217, v218, v219\n"
"v_mfma_f32_16x16x32_bf16 v[20:23], a[248:251], a[68:71], v[20:23]\n"
"v_cvt_pk_bf16_f32 v218, v220, v221\n"
"v_cvt_pk_bf16_f32 v219, v222, v223\n"
"v_mfma_f32_16x16x32_bf16 v[20:23], a[244:247], a[52:55], v[20:23]\n"
"s_addk_i32 s80, 8192\n"
"ds_read_b64_tr_b16 a[204:205], v251 offset:4096\n"
"v_mfma_f32_16x16x32_bf16 v[20:23], a[240:243], a[36:39], v[20:23]\n"
"s_addk_i32 m0, 1024\n"
"ds_read_b64_tr_b16 a[206:207], v251 offset:6144\n"
"v_mfma_f32_16x16x32_bf16 v[20:23], a[236:239], a[20:23], v[20:23]\n"
"buffer_load_dwordx4 v254, s[64:67], s80 offen lds\n"
"v_mfma_f32_16x16x32_bf16 v[20:23], a[232:235], a[4:7], v[20:23]\n"
"v_exp_f32_e32 v187, v187\n"
"v_mfma_f32_16x16x32_bf16 v[20:23], a[228:231], v[36:39], v[20:23]\n"
"ds_read_b64_tr_b16 a[208:209], v252 offset:0\n"
"v_add_f32_e32 v186, 1.0, v186\n"
"v_mfma_f32_16x16x32_bf16 v[20:23], a[224:227], v[4:7], v[20:23]\n"
"s_add_u32 s80, s90, 0\n"
"ds_read_b64_tr_b16 a[210:211], v252 offset:2048\n"
"v_mfma_f32_16x16x32_bf16 v[24:27], a[224:227], v[8:11], 0\n"
"s_add_u32 m0, s46, 0\n"
"ds_read_b64_tr_b16 a[212:213], v252 offset:4096\n"
"v_mfma_f32_16x16x32_bf16 v[24:27], a[228:231], v[40:43], v[24:27]\n"
"buffer_load_dwordx4 v254, s[64:67], s80 offen lds\n"
"v_mfma_f32_16x16x32_bf16 v[24:27], a[232:235], a[8:11], v[24:27]\n"
"v_rcp_f32_e32 v185, v185\n"
"v_mfma_f32_16x16x32_bf16 v[24:27], a[236:239], a[24:27], v[24:27]\n"
"ds_read_b64_tr_b16 a[214:215], v252 offset:6144\n"
"buffer_store_dwordx4 v[216:219], v255, s[56:59], s71 offen offset:1024 sc1\n"
"v_mfma_f32_16x16x32_bf16 v[24:27], a[240:243], a[40:43], v[24:27]\n"
"s_addk_i32 s80, 8192\n"
"ds_read_b64_tr_b16 a[216:217], v253 offset:0\n"
"v_mul_f32_e32 v168, v184, v168\n"
"v_mfma_f32_16x16x32_bf16 v[24:27], a[244:247], a[56:59], v[24:27]\n"
"s_addk_i32 m0, 1024\n"
"ds_read_b64_tr_b16 a[218:219], v253 offset:2048\n"
"v_mul_f32_e32 v189, 0xc13504f3, v173\n"
"v_mfma_f32_16x16x32_bf16 v[24:27], a[248:251], a[72:75], v[24:27]\n"
"buffer_load_dwordx4 v254, s[64:67], s80 offen lds\n"
"v_mfma_f32_16x16x32_bf16 v[24:27], a[252:255], a[88:91], v[24:27]\n"
"v_exp_f32_e32 v188, v188\n"
"v_mfma_f32_16x16x32_bf16 v[28:31], a[252:255], a[92:95], 0\n"
"ds_read_b64_tr_b16 a[220:221], v253 offset:4096\n"
"v_add_f32_e32 v187, 1.0, v187\n"
"v_mfma_f32_16x16x32_bf16 v[28:31], a[248:251], a[76:79], v[28:31]\n"
"s_addk_i32 s80, 8192\n"
"ds_read_b64_tr_b16 a[222:223], v253 offset:6144\n"
"v_mfma_f32_16x16x32_bf16 v[28:31], a[244:247], a[60:63], v[28:31]\n"
"s_addk_i32 m0, 1024\n"
"v_rcp_f32_e32 v186, v186\n"
"v_mfma_f32_16x16x32_bf16 v[28:31], a[240:243], a[44:47], v[28:31]\n"
"buffer_load_dwordx4 v254, s[64:67], s80 offen lds\n"
"v_mfma_f32_16x16x32_bf16 v[28:31], a[236:239], a[28:31], v[28:31]\n"
"v_mul_f32_e32 v169, v185, v169\n"
"v_mul_f32_e32 v190, 0xc13504f3, v174\n"
"v_mfma_f32_16x16x32_bf16 v[28:31], a[232:235], a[12:15], v[28:31]\n"
"v_exp_f32_e32 v189, v189\n"
"v_mfma_f32_16x16x32_bf16 v[28:31], a[228:231], v[44:47], v[28:31]\n"
"v_add_f32_e32 v188, 1.0, v188\n"
"v_mfma_f32_16x16x32_bf16 v[28:31], a[224:227], v[12:15], v[28:31]\n"
"v_rcp_f32_e32 v187, v187\n"
"s_waitcnt vmcnt(13)\n"
"s_waitcnt lgkmcnt(0)\n"
"v_mfma_f32_16x16x32_bf16 v[16:19], a[192:195], v[64:67], v[16:19]\n"
"s_barrier\n"
"s_addk_i32 s80, 8192\n"
"ds_read_b64_tr_b16 a[224:225], v246 offset:0\n"
"v_mfma_f32_16x16x32_bf16 v[16:19], a[196:199], v[96:99], v[16:19]\n"
"s_addk_i32 m0, 1024\n"
"ds_read_b64_tr_b16 a[226:227], v246 offset:2048\n"
"v_mul_f32_e32 v170, v186, v170\n"
"v_mfma_f32_16x16x32_bf16 v[16:19], a[200:203], a[96:99], v[16:19]\n"
"buffer_load_dwordx4 v254, s[64:67], s80 offen lds\n"
"v_mfma_f32_16x16x32_bf16 v[16:19], a[204:207], a[112:115], v[16:19]\n"
"s_addk_i32 s80, 8192\n"
"ds_read_b64_tr_b16 a[228:229], v246 offset:4096\n"
"v_mul_f32_e32 v191, 0xc13504f3, v175\n"
"v_mfma_f32_16x16x32_bf16 v[16:19], a[208:211], a[128:131], v[16:19]\n"
"s_addk_i32 m0, 1024\n"
"ds_read_b64_tr_b16 a[230:231], v246 offset:6144\n"
"v_mfma_f32_16x16x32_bf16 v[16:19], a[212:215], a[144:147], v[16:19]\n"
"buffer_load_dwordx4 v254, s[64:67], s80 offen lds\n"
"v_mfma_f32_16x16x32_bf16 v[16:19], a[216:219], a[160:163], v[16:19]\n"
"s_addk_i32 s80, 8192\n"
"v_exp_f32_e32 v190, v190\n"
"v_mfma_f32_16x16x32_bf16 v[16:19], a[220:223], a[176:179], v[16:19]\n"
"s_addk_i32 m0, 1024\n"
"ds_read_b64_tr_b16 a[232:233], v247 offset:0\n"
"v_add_f32_e32 v189, 1.0, v189\n"
"v_mfma_f32_16x16x32_bf16 v[20:23], a[220:223], a[180:183], v[20:23]\n"
"buffer_load_dwordx4 v254, s[64:67], s80 offen lds\n"
"v_mfma_f32_16x16x32_bf16 v[20:23], a[216:219], a[164:167], v[20:23]\n"
"ds_read_b64_tr_b16 a[234:235], v247 offset:2048\n"
"v_mfma_f32_16x16x32_bf16 v[20:23], a[212:215], a[148:151], v[20:23]\n"
"s_addk_i32 s80, 8192\n"
"ds_read_b64_tr_b16 a[236:237], v247 offset:4096\n"
"v_mfma_f32_16x16x32_bf16 v[20:23], a[208:211], a[132:135], v[20:23]\n"
"s_addk_i32 m0, 1024\n"
"ds_read_b64_tr_b16 a[238:239], v247 offset:6144\n"
"v_mfma_f32_16x16x32_bf16 v[20:23], a[204:207], a[116:119], v[20:23]\n"
"buffer_load_dwordx4 v254, s[64:67], s80 offen lds\n"
"v_mfma_f32_16x16x32_bf16 v[20:23], a[200:203], a[100:103], v[20:23]\n"
"s_addk_i32 s80, 8192\n"
"v_rcp_f32_e32 v188, v188\n"
"v_mfma_f32_16x16x32_bf16 v[20:23], a[196:199], v[100:103], v[20:23]\n"
"s_addk_i32 m0, 1024\n"
"ds_read_b64_tr_b16 a[240:241], v248 offset:0\n"
"v_mul_f32_e32 v171, v187, v171\n"
"v_mfma_f32_16x16x32_bf16 v[20:23], a[192:195], v[68:71], v[20:23]\n"
"buffer_load_dwordx4 v254, s[64:67], s80 offen lds\n"
"v_mfma_f32_16x16x32_bf16 v[24:27], a[192:195], v[72:75], v[24:27]\n"
"s_add_u32 s80, s90, 65536\n"
"ds_read_b64_tr_b16 a[242:243], v248 offset:2048\n"
"v_mfma_f32_16x16x32_bf16 v[24:27], a[196:199], v[104:107], v[24:27]\n"
"s_add_u32 m0, s46, 8192\n"
"ds_read_b64_tr_b16 a[244:245], v248 offset:4096\n"
"v_mfma_f32_16x16x32_bf16 v[24:27], a[200:203], a[104:107], v[24:27]\n"
"buffer_load_dwordx4 v254, s[64:67], s80 offen lds\n"
"v_mfma_f32_16x16x32_bf16 v[24:27], a[204:207], a[120:123], v[24:27]\n"
"ds_read_b64_tr_b16 a[246:247], v248 offset:6144\n"
"v_mfma_f32_16x16x32_bf16 v[24:27], a[208:211], a[136:139], v[24:27]\n"
"s_addk_i32 s80, 8192\n"
"s_and_b32 s87, s87, 0x380\n"
"v_mfma_f32_16x16x32_bf16 v[24:27], a[212:215], a[152:155], v[24:27]\n"
"s_addk_i32 m0, 1024\n"
"ds_read_b64_tr_b16 a[248:249], v249 offset:0\n"
"v_mfma_f32_16x16x32_bf16 v[24:27], a[216:219], a[168:171], v[24:27]\n"
"buffer_load_dwordx4 v254, s[64:67], s80 offen lds\n"
"v_mfma_f32_16x16x32_bf16 v[24:27], a[220:223], a[184:187], v[24:27]\n"
"s_addk_i32 s80, 8192\n"
"s_mul_i32 s87, s87, 592\n"
"ds_read_b64_tr_b16 a[250:251], v249 offset:2048\n"
"v_mfma_f32_16x16x32_bf16 v[28:31], a[220:223], a[188:191], v[28:31]\n"
"s_addk_i32 m0, 1024\n"
"ds_read_b64_tr_b16 a[252:253], v249 offset:4096\n"
"v_mfma_f32_16x16x32_bf16 v[28:31], a[216:219], a[172:175], v[28:31]\n"
"buffer_load_dwordx4 v254, s[64:67], s80 offen lds\n"
"v_mfma_f32_16x16x32_bf16 v[28:31], a[212:215], a[156:159], v[28:31]\n"
"s_addk_i32 s80, 8192\n"
"s_add_u32 s87, s87, 8192\n"
"ds_read_b64_tr_b16 a[254:255], v249 offset:6144\n"
"v_mfma_f32_16x16x32_bf16 v[28:31], a[208:211], a[140:143], v[28:31]\n"
"s_addk_i32 m0, 1024\n"
"v_exp_f32_e32 v191, v191\n"
"v_mfma_f32_16x16x32_bf16 v[28:31], a[204:207], a[124:127], v[28:31]\n"
"buffer_load_dwordx4 v254, s[64:67], s80 offen lds\n"
"v_mfma_f32_16x16x32_bf16 v[28:31], a[200:203], a[108:111], v[28:31]\n"
"s_add_u32 s87, s87, s85\n"
"v_add_f32_e32 v190, 1.0, v190\n"
"v_mfma_f32_16x16x32_bf16 v[28:31], a[196:199], v[108:111], v[28:31]\n"
"v_rcp_f32_e32 v189, v189\n"
"v_mfma_f32_16x16x32_bf16 v[28:31], a[192:195], v[76:79], v[28:31]\n"
"v_mul_f32_e32 v172, v188, v172\n"
"v_add_f32_e32 v191, 1.0, v191\n"
"s_waitcnt vmcnt(13)\n"
"s_waitcnt lgkmcnt(0)\n"
"v_mfma_f32_16x16x32_bf16 v[48:51], a[224:227], v[0:3], 0\n"
"s_barrier\n"
"s_addk_i32 s80, 8192\n"
"ds_read_b64_tr_b16 a[192:193], v250 offset:8192\n"
"v_mfma_f32_16x16x32_bf16 v[48:51], a[228:231], v[32:35], v[48:51]\n"
"s_addk_i32 m0, 1024\n"
"ds_read_b64_tr_b16 a[194:195], v250 offset:10240\n"
"v_cvt_pk_bf16_f32 v16, v16, v17\n"
"v_mfma_f32_16x16x32_bf16 v[48:51], a[232:235], a[0:3], v[48:51]\n"
"buffer_load_dwordx4 v254, s[64:67], s80 offen lds\n"
"v_mfma_f32_16x16x32_bf16 v[48:51], a[236:239], a[16:19], v[48:51]\n"
".LPG17_%=:\n"
"s_cbranch_execz .LPGR_%=\n"
"ds_read_b64_tr_b16 a[196:197], v250 offset:12288\n"
"v_cvt_pk_bf16_f32 v17, v18, v19\n"
"v_mfma_f32_16x16x32_bf16 v[48:51], a[240:243], a[32:35], v[48:51]\n"
"v_cvt_pk_bf16_f32 v18, v20, v21\n"
"v_cvt_pk_bf16_f32 v19, v22, v23\n"
"v_mfma_f32_16x16x32_bf16 v[48:51], a[244:247], a[48:51], v[48:51]\n"
"s_addk_i32 s80, 8192\n"
"ds_read_b64_tr_b16 a[198:199], v250 offset:14336\n"
"v_cvt_pk_bf16_f32 v24, v24, v25\n"
"v_mfma_f32_16x16x32_bf16 v[48:51], a[248:251], a[64:67], v[48:51]\n"
"s_addk_i32 m0, 1024\n"
"ds_read_b64_tr_b16 a[200:201], v251 offset:8192\n"
"buffer_store_dwordx4 v[16:19], v255, s[56:59], s87 offen offset:0 sc1\n"
"v_mfma_f32_16x16x32_bf16 v[48:51], a[252:255], a[80:83], v[48:51]\n"
"buffer_load_dwordx4 v254, s[64:67], s80 offen lds\n"
"v_mfma_f32_16x16x32_bf16 v[52:55], a[252:255], a[84:87], 0\n"
"ds_read_b64_tr_b16 a[202:203], v251 offset:10240\n"
"v_cvt_pk_bf16_f32 v25, v26, v27\n"
"v_mfma_f32_16x16x32_bf16 v[52:55], a[248:251], a[68:71], v[52:55]\n"
"v_cvt_pk_bf16_f32 v26, v28, v29\n"
"v_cvt_pk_bf16_f32 v27, v30, v31\n"
"v_mfma_f32_16x16x32_bf16 v[52:55], a[244:247], a[52:55], v[52:55]\n"
"s_addk_i32 s80, 8192\n"
"ds_read_b64_tr_b16 a[204:205], v251 offset:12288\n"
"v_mfma_f32_16x16x32_bf16 v[52:55], a[240:243], a[36:39], v[52:55]\n"
"s_addk_i32 m0, 1024\n"
"ds_read_b64_tr_b16 a[206:207], v251 offset:14336\n"
"v_mfma_f32_16x16x32_bf16 v[52:55], a[236:239], a[20:23], v[52:55]\n"
"buffer_load_dwordx4 v254, s[64:67], s80 offen lds\n"
"v_mfma_f32_16x16x32_bf16 v[52:55], a[232:235], a[4:7], v[52:55]\n"
"v_rcp_f32_e32 v190, v190\n"
"v_mfma_f32_16x16x32_bf16 v[52:55], a[228:231], v[36:39], v[52:55]\n"
"ds_read_b64_tr_b16 a[208:209], v252 offset:8192\n"
"v_mul_f32_e32 v173, v189, v173\n"
"v_mfma_f32_16x16x32_bf16 v[52:55], a[224:227], v[4:7], v[52:55]\n"
"s_addk_i32 s80, 8192\n"
"ds_read_b64_tr_b16 a[210:211], v252 offset:10240\n"
"v_mfma_f32_16x16x32_bf16 v[56:59], a[224:227], v[8:11], 0\n"
"s_addk_i32 m0, 1024\n"
"ds_read_b64_tr_b16 a[212:213], v252 offset:12288\n"
"v_mfma_f32_16x16x32_bf16 v[56:59], a[228:231], v[40:43], v[56:59]\n"
"buffer_load_dwordx4 v254, s[64:67], s80 offen lds\n"
"v_mfma_f32_16x16x32_bf16 v[56:59], a[232:235], a[8:11], v[56:59]\n"
"v_rcp_f32_e32 v191, v191\n"
"v_mfma_f32_16x16x32_bf16 v[56:59], a[236:239], a[24:27], v[56:59]\n"
"ds_read_b64_tr_b16 a[214:215], v252 offset:14336\n"
"buffer_store_dwordx4 v[24:27], v255, s[56:59], s87 offen offset:1024 sc1\n"
"v_mfma_f32_16x16x32_bf16 v[56:59], a[240:243], a[40:43], v[56:59]\n"
"s_add_u32 s80, s91, 0\n"
"ds_read_b64_tr_b16 a[216:217], v253 offset:8192\n"
"v_mul_f32_e32 v174, v190, v174\n"
"v_mfma_f32_16x16x32_bf16 v[56:59], a[244:247], a[56:59], v[56:59]\n"
"s_add_u32 m0, s46, 65536\n"
"ds_read_b64_tr_b16 a[218:219], v253 offset:10240\n"
"v_mul_f32_e32 v175, v191, v175\n"
"v_mfma_f32_16x16x32_bf16 v[56:59], a[248:251], a[72:75], v[56:59]\n"
"buffer_load_dwordx4 v254, s[64:67], s80 offen lds\n"
"v_mfma_f32_16x16x32_bf16 v[56:59], a[252:255], a[88:91], v[56:59]\n"
"v_mfma_f32_16x16x32_bf16 v[60:63], a[252:255], a[92:95], 0\n"
"ds_read_b64_tr_b16 a[220:221], v253 offset:12288\n"
"v_mfma_f32_16x16x32_bf16 v[60:63], a[248:251], a[76:79], v[60:63]\n"
"s_addk_i32 s80, 8192\n"
"ds_read_b64_tr_b16 a[222:223], v253 offset:14336\n"
"v_mfma_f32_16x16x32_bf16 v[60:63], a[244:247], a[60:63], v[60:63]\n"
"s_addk_i32 m0, 1024\n"
"v_mfma_f32_16x16x32_bf16 v[60:63], a[240:243], a[44:47], v[60:63]\n"
"buffer_load_dwordx4 v254, s[64:67], s80 offen lds\n"
"v_mfma_f32_16x16x32_bf16 v[60:63], a[236:239], a[28:31], v[60:63]\n"
"v_mfma_f32_16x16x32_bf16 v[60:63], a[232:235], a[12:15], v[60:63]\n"
"v_mfma_f32_16x16x32_bf16 v[60:63], a[228:231], v[44:47], v[60:63]\n"
"v_mfma_f32_16x16x32_bf16 v[60:63], a[224:227], v[12:15], v[60:63]\n"
"s_waitcnt vmcnt(12)\n"
"s_waitcnt lgkmcnt(0)\n"
"v_mfma_f32_16x16x32_bf16 v[48:51], a[192:195], v[64:67], v[48:51]\n"
"s_barrier\n"
"s_addk_i32 s80, 8192\n"
"ds_read_b64_tr_b16 a[224:225], v224 offset:0\n"
"v_mfma_f32_16x16x32_bf16 v[48:51], a[196:199], v[96:99], v[48:51]\n"
"s_addk_i32 m0, 1024\n"
"ds_read_b64_tr_b16 a[226:227], v224 offset:2048\n"
"v_mfma_f32_16x16x32_bf16 v[48:51], a[200:203], a[96:99], v[48:51]\n"
"buffer_load_dwordx4 v254, s[64:67], s80 offen lds\n"
"v_mfma_f32_16x16x32_bf16 v[48:51], a[204:207], a[112:115], v[48:51]\n"
"s_addk_i32 s80, 8192\n"
"ds_read_b64_tr_b16 a[228:229], v224 offset:4096\n"
"v_mfma_f32_16x16x32_bf16 v[48:51], a[208:211], a[128:131], v[48:51]\n"
"s_addk_i32 m0, 1024\n"
"ds_read_b64_tr_b16 a[230:231], v224 offset:6144\n"
"v_mfma_f32_16x16x32_bf16 v[48:51], a[212:215], a[144:147], v[48:51]\n"
"buffer_load_dwordx4 v254, s[64:67], s80 offen lds\n"
"v_mfma_f32_16x16x32_bf16 v[48:51], a[216:219], a[160:163], v[48:51]\n"
"v_mfma_f32_16x16x32_bf16 v[48:51], a[220:223], a[176:179], v[48:51]\n"
"s_addk_i32 s80, 8192\n"
"ds_read_b64_tr_b16 a[232:233], v225 offset:0\n"
"v_mfma_f32_16x16x32_bf16 v[52:55], a[220:223], a[180:183], v[52:55]\n"
"s_addk_i32 m0, 1024\n"
"ds_read_b64_tr_b16 a[234:235], v225 offset:2048\n"
"v_mfma_f32_16x16x32_bf16 v[52:55], a[216:219], a[164:167], v[52:55]\n"
"buffer_load_dwordx4 v254, s[64:67], s80 offen lds\n"
"v_mfma_f32_16x16x32_bf16 v[52:55], a[212:215], a[148:151], v[52:55]\n"
"ds_read_b64_tr_b16 a[236:237], v225 offset:4096\n"
"v_mfma_f32_16x16x32_bf16 v[52:55], a[208:211], a[132:135], v[52:55]\n"
"s_addk_i32 s80, 8192\n"
"v_mfma_f32_16x16x32_bf16 v[52:55], a[204:207], a[116:119], v[52:55]\n"
"s_addk_i32 m0, 1024\n"
"ds_read_b64_tr_b16 a[238:239], v225 offset:6144\n"
"v_mfma_f32_16x16x32_bf16 v[52:55], a[200:203], a[100:103], v[52:55]\n"
"buffer_load_dwordx4 v254, s[64:67], s80 offen lds\n"
"v_mfma_f32_16x16x32_bf16 v[52:55], a[196:199], v[100:103], v[52:55]\n"
"ds_read_b64_tr_b16 a[240:241], v226 offset:0\n"
"v_mfma_f32_16x16x32_bf16 v[52:55], a[192:195], v[68:71], v[52:55]\n"
"s_addk_i32 s80, 8192\n"
"ds_read_b64_tr_b16 a[242:243], v226 offset:2048\n"
"v_mfma_f32_16x16x32_bf16 v[56:59], a[192:195], v[72:75], v[56:59]\n"
"s_addk_i32 m0, 1024\n"
"v_mfma_f32_16x16x32_bf16 v[56:59], a[196:199], v[104:107], v[56:59]\n"
"buffer_load_dwordx4 v254, s[64:67], s80 offen lds\n"
"v_mfma_f32_16x16x32_bf16 v[56:59], a[200:203], a[104:107], v[56:59]\n"
"s_addk_i32 s80, 8192\n"
"ds_read_b64_tr_b16 a[244:245], v226 offset:4096\n"
"v_mfma_f32_16x16x32_bf16 v[56:59], a[204:207], a[120:123], v[56:59]\n"
"s_addk_i32 m0, 1024\n"
"ds_read_b64_tr_b16 a[246:247], v226 offset:6144\n"
"v_mfma_f32_16x16x32_bf16 v[56:59], a[208:211], a[136:139], v[56:59]\n"
"s_and_b32 s88, s88, 0x380\n"
"buffer_load_dwordx4 v254, s[64:67], s80 offen lds\n"
"v_mfma_f32_16x16x32_bf16 v[56:59], a[212:215], a[152:155], v[56:59]\n"
"ds_read_b64_tr_b16 a[248:249], v227 offset:0\n"
"v_mfma_f32_16x16x32_bf16 v[56:59], a[216:219], a[168:171], v[56:59]\n"
"s_add_u32 s80, s91, 65536\n"
"v_mfma_f32_16x16x32_bf16 v[56:59], a[220:223], a[184:187], v[56:59]\n"
"s_add_u32 m0, s81, 0\n"
"s_mul_i32 s88, s88, 592\n"
"ds_read_b64_tr_b16 a[250:251], v227 offset:2048\n"
"v_mfma_f32_16x16x32_bf16 v[60:63], a[220:223], a[188:191], v[60:63]\n"
"buffer_load_dwordx4 v254, s[64:67], s80 offen lds\n"
"v_mfma_f32_16x16x32_bf16 v[60:63], a[216:219], a[172:175], v[60:63]\n"
"ds_read_b64_tr_b16 a[252:253], v227 offset:4096\n"
"v_mfma_f32_16x16x32_bf16 v[60:63], a[212:215], a[156:159], v[60:63]\n"
"s_addk_i32 s80, 8192\n"
"s_add_u32 s88, s88, 10240\n"
"ds_read_b64_tr_b16 a[254:255], v227 offset:6144\n"
"v_mfma_f32_16x16x32_bf16 v[60:63], a[208:211], a[140:143], v[60:63]\n"
"s_addk_i32 m0, 1024\n"
"v_mfma_f32_16x16x32_bf16 v[60:63], a[204:207], a[124:127], v[60:63]\n"
"buffer_load_dwordx4 v254, s[64:67], s80 offen lds\n"
"v_mfma_f32_16x16x32_bf16 v[60:63], a[200:203], a[108:111], v[60:63]\n"
"s_add_u32 s88, s88, s85\n"
"v_mfma_f32_16x16x32_bf16 v[60:63], a[196:199], v[108:111], v[60:63]\n"
"v_mfma_f32_16x16x32_bf16 v[60:63], a[192:195], v[76:79], v[60:63]\n"
"s_waitcnt vmcnt(11)\n"
"s_waitcnt lgkmcnt(0)\n"
"v_mfma_f32_16x16x32_bf16 v[80:83], a[224:227], v[0:3], 0\n"
"s_barrier\n"
"s_addk_i32 s80, 8192\n"
"ds_read_b64_tr_b16 a[192:193], v224 offset:8192\n"
"v_mfma_f32_16x16x32_bf16 v[80:83], a[228:231], v[32:35], v[80:83]\n"
"s_addk_i32 m0, 1024\n"
"ds_read_b64_tr_b16 a[194:195], v224 offset:10240\n"
"v_cvt_pk_bf16_f32 v48, v48, v49\n"
"v_mfma_f32_16x16x32_bf16 v[80:83], a[232:235], a[0:3], v[80:83]\n"
"buffer_load_dwordx4 v254, s[64:67], s80 offen lds\n"
"v_mfma_f32_16x16x32_bf16 v[80:83], a[236:239], a[16:19], v[80:83]\n"
"ds_read_b64_tr_b16 a[196:197], v224 offset:12288\n"
"v_cvt_pk_bf16_f32 v49, v50, v51\n"
"v_mfma_f32_16x16x32_bf16 v[80:83], a[240:243], a[32:35], v[80:83]\n"
"v_cvt_pk_bf16_f32 v50, v52, v53\n"
"v_cvt_pk_bf16_f32 v51, v54, v55\n"
"v_mfma_f32_16x16x32_bf16 v[80:83], a[244:247], a[48:51], v[80:83]\n"
"s_addk_i32 s80, 8192\n"
"ds_read_b64_tr_b16 a[198:199], v224 offset:14336\n"
"v_cvt_pk_bf16_f32 v56, v56, v57\n"
"v_mfma_f32_16x16x32_bf16 v[80:83], a[248:251], a[64:67], v[80:83]\n"
"s_addk_i32 m0, 1024\n"
"ds_read_b64_tr_b16 a[200:201], v225 offset:8192\n"
"buffer_store_dwordx4 v[48:51], v255, s[56:59], s88 offen offset:0 sc1\n"
"v_mfma_f32_16x16x32_bf16 v[80:83], a[252:255], a[80:83], v[80:83]\n"
"buffer_load_dwordx4 v254, s[64:67], s80 offen lds\n"
"v_mfma_f32_16x16x32_bf16 v[84:87], a[252:255], a[84:87], 0\n"
"ds_read_b64_tr_b16 a[202:203], v225 offset:10240\n"
"v_cvt_pk_bf16_f32 v57, v58, v59\n"
"v_mfma_f32_16x16x32_bf16 v[84:87], a[248:251], a[68:71], v[84:87]\n"
"v_cvt_pk_bf16_f32 v58, v60, v61\n"
"v_cvt_pk_bf16_f32 v59, v62, v63\n"
"v_mfma_f32_16x16x32_bf16 v[84:87], a[244:247], a[52:55], v[84:87]\n"
"s_addk_i32 s80, 8192\n"
"ds_read_b64_tr_b16 a[204:205], v225 offset:12288\n"
"v_mfma_f32_16x16x32_bf16 v[84:87], a[240:243], a[36:39], v[84:87]\n"
"s_addk_i32 m0, 1024\n"
"ds_read_b64_tr_b16 a[206:207], v225 offset:14336\n"
"v_mfma_f32_16x16x32_bf16 v[84:87], a[236:239], a[20:23], v[84:87]\n"
"buffer_load_dwordx4 v254, s[64:67], s80 offen lds\n"
"v_mfma_f32_16x16x32_bf16 v[84:87], a[232:235], a[4:7], v[84:87]\n"
"v_mfma_f32_16x16x32_bf16 v[84:87], a[228:231], v[36:39], v[84:87]\n"
"ds_read_b64_tr_b16 a[208:209], v226 offset:8192\n"
"v_mfma_f32_16x16x32_bf16 v[84:87], a[224:227], v[4:7], v[84:87]\n"
"s_addk_i32 s80, 8192\n"
"ds_read_b64_tr_b16 a[210:211], v226 offset:10240\n"
"v_mfma_f32_16x16x32_bf16 v[88:91], a[224:227], v[8:11], 0\n"
"s_addk_i32 m0, 1024\n"
"ds_read_b64_tr_b16 a[212:213], v226 offset:12288\n"
"v_mfma_f32_16x16x32_bf16 v[88:91], a[228:231], v[40:43], v[88:91]\n"
"buffer_load_dwordx4 v254, s[64:67], s80 offen lds\n"
"v_mfma_f32_16x16x32_bf16 v[88:91], a[232:235], a[8:11], v[88:91]\n"
"v_mfma_f32_16x16x32_bf16 v[88:91], a[236:239], a[24:27], v[88:91]\n"
"ds_read_b64_tr_b16 a[214:215], v226 offset:14336\n"
"buffer_store_dwordx4 v[56:59], v255, s[56:59], s88 offen offset:1024 sc1\n"
"v_mfma_f32_16x16x32_bf16 v[88:91], a[240:243], a[40:43], v[88:91]\n"
"s_addk_i32 s80, 8192\n"
"ds_read_b64_tr_b16 a[216:217], v227 offset:8192\n"
"v_mfma_f32_16x16x32_bf16 v[88:91], a[244:247], a[56:59], v[88:91]\n"
"s_addk_i32 m0, 1024\n"
"ds_read_b64_tr_b16 a[218:219], v227 offset:10240\n"
"v_mfma_f32_16x16x32_bf16 v[88:91], a[248:251], a[72:75], v[88:91]\n"
"buffer_load_dwordx4 v254, s[64:67], s80 offen lds\n"
"v_mfma_f32_16x16x32_bf16 v[88:91], a[252:255], a[88:91], v[88:91]\n"
"v_mfma_f32_16x16x32_bf16 v[92:95], a[252:255], a[92:95], 0\n"
"ds_read_b64_tr_b16 a[220:221], v227 offset:12288\n"
"v_mfma_f32_16x16x32_bf16 v[92:95], a[248:251], a[76:79], v[92:95]\n"
"s_addk_i32 s80, 8192\n"
"ds_read_b64_tr_b16 a[222:223], v227 offset:14336\n"
"v_mfma_f32_16x16x32_bf16 v[92:95], a[244:247], a[60:63], v[92:95]\n"
"s_addk_i32 m0, 1024\n"
"v_mfma_f32_16x16x32_bf16 v[92:95], a[240:243], a[44:47], v[92:95]\n"
"buffer_load_dwordx4 v254, s[64:67], s80 offen lds\n"
"v_mfma_f32_16x16x32_bf16 v[92:95], a[236:239], a[28:31], v[92:95]\n"
"v_mfma_f32_16x16x32_bf16 v[92:95], a[232:235], a[12:15], v[92:95]\n"
"v_mfma_f32_16x16x32_bf16 v[92:95], a[228:231], v[44:47], v[92:95]\n"
"v_mfma_f32_16x16x32_bf16 v[92:95], a[224:227], v[12:15], v[92:95]\n"
"s_waitcnt vmcnt(10)\n"
"s_waitcnt lgkmcnt(0)\n"
"v_mfma_f32_16x16x32_bf16 v[80:83], a[192:195], v[64:67], v[80:83]\n"
"s_barrier\n"
"ds_read_b64_tr_b16 a[224:225], v250 offset:0\n"
"v_lshlrev_b32_e32 v16, 16, v228\n"
"v_mfma_f32_16x16x32_bf16 v[80:83], a[196:199], v[96:99], v[80:83]\n"
"s_add_u32 s94, s22, 0\n"
"ds_read_b64_tr_b16 a[226:227], v250 offset:2048\n"
"buffer_load_dwordx4 v[132:135], v255, s[52:55], s94 offen offset:0 sc1\n"
"v_mfma_f32_16x16x32_bf16 v[80:83], a[200:203], a[96:99], v[80:83]\n"
"v_and_b32_e32 v17, 0xffff0000, v228\n"
"v_lshlrev_b32_e32 v18, 16, v229\n"
"v_mfma_f32_16x16x32_bf16 v[80:83], a[204:207], a[112:115], v[80:83]\n"
"ds_read_b64_tr_b16 a[228:229], v250 offset:4096\n"
"v_and_b32_e32 v19, 0xffff0000, v229\n"
"v_mfma_f32_16x16x32_bf16 v[80:83], a[208:211], a[128:131], v[80:83]\n"
"v_lshlrev_b32_e32 v20, 16, v230\n"
"v_and_b32_e32 v21, 0xffff0000, v230\n"
"v_mfma_f32_16x16x32_bf16 v[80:83], a[212:215], a[144:147], v[80:83]\n"
"ds_read_b64_tr_b16 a[230:231], v250 offset:6144\n"
"v_lshlrev_b32_e32 v22, 16, v231\n"
"v_mfma_f32_16x16x32_bf16 v[80:83], a[216:219], a[160:163], v[80:83]\n"
"buffer_load_dwordx4 v[140:143], v255, s[52:55], s94 offen offset:1024 sc1\n"
"v_and_b32_e32 v23, 0xffff0000, v231\n"
"v_mfma_f32_16x16x32_bf16 v[80:83], a[220:223], a[176:179], v[80:83]\n"
"ds_read_b64_tr_b16 a[232:233], v251 offset:0\n"
"v_lshlrev_b32_e32 v24, 16, v232\n"
"v_mfma_f32_16x16x32_bf16 v[84:87], a[220:223], a[180:183], v[84:87]\n"
"ds_read_b64_tr_b16 a[234:235], v251 offset:2048\n"
"v_and_b32_e32 v25, 0xffff0000, v232\n"
"v_mfma_f32_16x16x32_bf16 v[84:87], a[216:219], a[164:167], v[84:87]\n"
"v_lshlrev_b32_e32 v26, 16, v233\n"
"v_and_b32_e32 v27, 0xffff0000, v233\n"
"v_mfma_f32_16x16x32_bf16 v[84:87], a[212:215], a[148:151], v[84:87]\n"
"ds_read_b64_tr_b16 a[236:237], v251 offset:4096\n"
"v_lshlrev_b32_e32 v28, 16, v234\n"
"v_mfma_f32_16x16x32_bf16 v[84:87], a[208:211], a[132:135], v[84:87]\n"
"s_add_u32 s94, s22, 2048\n"
"buffer_load_dwordx4 v[148:151], v255, s[52:55], s94 offen offset:0 sc1\n"
"v_and_b32_e32 v29, 0xffff0000, v234\n"
"v_mfma_f32_16x16x32_bf16 v[84:87], a[204:207], a[116:119], v[84:87]\n"
"ds_read_b64_tr_b16 a[238:239], v251 offset:6144\n"
"v_lshlrev_b32_e32 v30, 16, v235\n"
"v_mfma_f32_16x16x32_bf16 v[84:87], a[200:203], a[100:103], v[84:87]\n"
"v_and_b32_e32 v31, 0xffff0000, v235\n"
"v_mfma_f32_16x16x32_bf16 v[84:87], a[196:199], v[100:103], v[84:87]\n"
"ds_read_b64_tr_b16 a[240:241], v252 offset:0\n"
"v_mfma_f32_16x16x32_bf16 v[84:87], a[192:195], v[68:71], v[84:87]\n"
"ds_read_b64_tr_b16 a[242:243], v252 offset:2048\n"
"v_mfma_f32_16x16x32_bf16 v[88:91], a[192:195], v[72:75], v[88:91]\n"
"buffer_load_dwordx4 v[156:159], v255, s[52:55], s94 offen offset:1024 sc1\n"
"v_mfma_f32_16x16x32_bf16 v[88:91], a[196:199], v[104:107], v[88:91]\n"
"ds_read_b64_tr_b16 a[244:245], v252 offset:4096\n"
"v_mfma_f32_16x16x32_bf16 v[88:91], a[200:203], a[104:107], v[88:91]\n"
"v_mfma_f32_16x16x32_bf16 v[88:91], a[204:207], a[120:123], v[88:91]\n"
"ds_read_b64_tr_b16 a[246:247], v252 offset:6144\n"
"v_mfma_f32_16x16x32_bf16 v[88:91], a[208:211], a[136:139], v[88:91]\n"
"s_and_b32 s90, s90, 0x380\n"
"v_mfma_f32_16x16x32_bf16 v[88:91], a[212:215], a[152:155], v[88:91]\n"
"s_add_u32 s94, s22, 4096\n"
"ds_read_b64_tr_b16 a[248:249], v253 offset:0\n"
"buffer_load_dwordx4 v[196:199], v255, s[52:55], s94 offen offset:0 sc1\n"
"v_mfma_f32_16x16x32_bf16 v[88:91], a[216:219], a[168:171], v[88:91]\n"
"ds_read_b64_tr_b16 a[250:251], v253 offset:2048\n"
"v_mfma_f32_16x16x32_bf16 v[88:91], a[220:223], a[184:187], v[88:91]\n"
"s_mul_i32 s90, s90, 592\n"
"v_mfma_f32_16x16x32_bf16 v[92:95], a[220:223], a[188:191], v[92:95]\n"
"ds_read_b64_tr_b16 a[252:253], v253 offset:4096\n"
"v_mfma_f32_16x16x32_bf16 v[92:95], a[216:219], a[172:175], v[92:95]\n"
"v_mfma_f32_16x16x32_bf16 v[92:95], a[212:215], a[156:159], v[92:95]\n"
"s_add_u32 s90, s90, 12288\n"
"ds_read_b64_tr_b16 a[254:255], v253 offset:6144\n"
"buffer_load_dwordx4 v[204:207], v255, s[52:55], s94 offen offset:1024 sc1\n"
"v_mfma_f32_16x16x32_bf16 v[92:95], a[208:211], a[140:143], v[92:95]\n"
"v_mfma_f32_16x16x32_bf16 v[92:95], a[204:207], a[124:127], v[92:95]\n"
"v_mfma_f32_16x16x32_bf16 v[92:95], a[200:203], a[108:111], v[92:95]\n"
"s_add_u32 s90, s90, s85\n"
"v_mfma_f32_16x16x32_bf16 v[92:95], a[196:199], v[108:111], v[92:95]\n"
"v_mfma_f32_16x16x32_bf16 v[92:95], a[192:195], v[76:79], v[92:95]\n"
"s_waitcnt vmcnt(6)\n"
"s_waitcnt lgkmcnt(0)\n"
"v_mfma_f32_16x16x32_bf16 v[112:115], a[224:227], v[0:3], 0\n"
"s_barrier\n"
"ds_read_b64_tr_b16 a[192:193], v246 offset:0\n"
"v_mfma_f32_16x16x32_bf16 v[112:115], a[228:231], v[32:35], v[112:115]\n"
"ds_read_b64_tr_b16 a[194:195], v246 offset:2048\n"
"v_cvt_pk_bf16_f32 v80, v80, v81\n"
"v_mfma_f32_16x16x32_bf16 v[112:115], a[232:235], a[0:3], v[112:115]\n"
"v_cvt_pk_bf16_f32 v81, v82, v83\n"
"v_cvt_pk_bf16_f32 v82, v84, v85\n"
"v_mfma_f32_16x16x32_bf16 v[112:115], a[236:239], a[16:19], v[112:115]\n"
"ds_read_b64_tr_b16 a[196:197], v246 offset:4096\n"
"v_cvt_pk_bf16_f32 v83, v86, v87\n"
"v_mfma_f32_16x16x32_bf16 v[112:115], a[240:243], a[32:35], v[112:115]\n"
"v_cvt_pk_bf16_f32 v88, v88, v89\n"
"v_cvt_pk_bf16_f32 v89, v90, v91\n"
"v_mfma_f32_16x16x32_bf16 v[112:115], a[244:247], a[48:51], v[112:115]\n"
"ds_read_b64_tr_b16 a[198:199], v246 offset:6144\n"
"v_cvt_pk_bf16_f32 v90, v92, v93\n"
"v_mfma_f32_16x16x32_bf16 v[112:115], a[248:251], a[64:67], v[112:115]\n"
"buffer_store_dwordx4 v[80:83], v255, s[56:59], s90 offen offset:0 sc1\n"
"v_cvt_pk_bf16_f32 v91, v94, v95\n"
"v_mfma_f32_16x16x32_bf16 v[112:115], a[252:255], a[80:83], v[112:115]\n"
"ds_read_b64_tr_b16 a[200:201], v247 offset:0\n"
"v_mfma_f32_16x16x32_bf16 v[116:119], a[252:255], a[84:87], 0\n"
"ds_read_b64_tr_b16 a[202:203], v247 offset:2048\n"
"v_mfma_f32_16x16x32_bf16 v[116:119], a[248:251], a[68:71], v[116:119]\n"
"v_mfma_f32_16x16x32_bf16 v[116:119], a[244:247], a[52:55], v[116:119]\n"
"ds_read_b64_tr_b16 a[204:205], v247 offset:4096\n"
"buffer_store_dwordx4 v[88:91], v255, s[56:59], s90 offen offset:1024 sc1\n"
"v_mfma_f32_16x16x32_bf16 v[116:119], a[240:243], a[36:39], v[116:119]\n"
"v_mfma_f32_16x16x32_bf16 v[116:119], a[236:239], a[20:23], v[116:119]\n"
"ds_read_b64_tr_b16 a[206:207], v247 offset:6144\n"
"v_mfma_f32_16x16x32_bf16 v[116:119], a[232:235], a[4:7], v[116:119]\n"
"v_mfma_f32_16x16x32_bf16 v[116:119], a[228:231], v[36:39], v[116:119]\n"
"s_add_u32 s94, s22, 6144\n"
"ds_read_b64_tr_b16 a[208:209], v248 offset:0\n"
"buffer_load_dwordx4 v[212:215], v255, s[52:55], s94 offen offset:0 sc1\n"
"v_mfma_f32_16x16x32_bf16 v[116:119], a[224:227], v[4:7], v[116:119]\n"
"ds_read_b64_tr_b16 a[210:211], v248 offset:2048\n"
"v_mfma_f32_16x16x32_bf16 v[120:123], a[224:227], v[8:11], 0\n"
"v_mfma_f32_16x16x32_bf16 v[120:123], a[228:231], v[40:43], v[120:123]\n"
"ds_read_b64_tr_b16 a[212:213], v248 offset:4096\n"
"v_mfma_f32_16x16x32_bf16 v[120:123], a[232:235], a[8:11], v[120:123]\n"
"v_mfma_f32_16x16x32_bf16 v[120:123], a[236:239], a[24:27], v[120:123]\n"
"ds_read_b64_tr_b16 a[214:215], v248 offset:6144\n"
"buffer_load_dwordx4 v[220:223], v255, s[52:55], s94 offen offset:1024 sc1\n"
"v_mfma_f32_16x16x32_bf16 v[120:123], a[240:243], a[40:43], v[120:123]\n"
"v_mfma_f32_16x16x32_bf16 v[120:123], a[244:247], a[56:59], v[120:123]\n"
"ds_read_b64_tr_b16 a[216:217], v249 offset:0\n"
"v_mfma_f32_16x16x32_bf16 v[120:123], a[248:251], a[72:75], v[120:123]\n"
"ds_read_b64_tr_b16 a[218:219], v249 offset:2048\n"
"v_mfma_f32_16x16x32_bf16 v[120:123], a[252:255], a[88:91], v[120:123]\n"
"s_add_u32 s94, s22, 8192\n"
"buffer_load_dwordx4 v[128:131], v255, s[52:55], s94 offen offset:0 sc1\n"
"v_mfma_f32_16x16x32_bf16 v[124:127], a[252:255], a[92:95], 0\n"
"ds_read_b64_tr_b16 a[220:221], v249 offset:4096\n"
"v_mfma_f32_16x16x32_bf16 v[124:127], a[248:251], a[76:79], v[124:127]\n"
"v_mfma_f32_16x16x32_bf16 v[124:127], a[244:247], a[60:63], v[124:127]\n"
"ds_read_b64_tr_b16 a[222:223], v249 offset:6144\n"
"v_mfma_f32_16x16x32_bf16 v[124:127], a[240:243], a[44:47], v[124:127]\n"
"buffer_load_dwordx4 v[136:139], v255, s[52:55], s94 offen offset:1024 sc1\n"
"v_mfma_f32_16x16x32_bf16 v[124:127], a[236:239], a[28:31], v[124:127]\n"
"v_mfma_f32_16x16x32_bf16 v[124:127], a[232:235], a[12:15], v[124:127]\n"
"v_mfma_f32_16x16x32_bf16 v[124:127], a[228:231], v[44:47], v[124:127]\n"
"v_mfma_f32_16x16x32_bf16 v[124:127], a[224:227], v[12:15], v[124:127]\n"
"s_waitcnt lgkmcnt(0)\n"
"v_mfma_f32_16x16x32_bf16 v[112:115], a[192:195], v[64:67], v[112:115]\n"
"s_waitcnt vmcnt(10)\n"
"v_lshlrev_b32_e32 v176, 16, v132\n"
"v_and_b32_e32 v177, 0xffff0000, v132\n"
"v_mfma_f32_16x16x32_bf16 v[112:115], a[196:199], v[96:99], v[112:115]\n"
"s_add_u32 s94, s22, 10240\n"
"buffer_load_dwordx4 v[144:147], v255, s[52:55], s94 offen offset:0 sc1\n"
"v_lshlrev_b32_e32 v178, 16, v133\n"
"v_mfma_f32_16x16x32_bf16 v[112:115], a[200:203], a[96:99], v[112:115]\n"
"v_and_b32_e32 v179, 0xffff0000, v133\n"
"v_lshlrev_b32_e32 v180, 16, v134\n"
"v_mfma_f32_16x16x32_bf16 v[112:115], a[204:207], a[112:115], v[112:115]\n"
"v_and_b32_e32 v181, 0xffff0000, v134\n"
"v_lshlrev_b32_e32 v182, 16, v135\n"
"v_mfma_f32_16x16x32_bf16 v[112:115], a[208:211], a[128:131], v[112:115]\n"
"v_and_b32_e32 v183, 0xffff0000, v135\n"
"v_lshlrev_b32_e32 v184, 16, v140\n"
"v_mfma_f32_16x16x32_bf16 v[112:115], a[212:215], a[144:147], v[112:115]\n"
"v_and_b32_e32 v185, 0xffff0000, v140\n"
"v_lshlrev_b32_e32 v186, 16, v141\n"
"v_mfma_f32_16x16x32_bf16 v[112:115], a[216:219], a[160:163], v[112:115]\n"
"v_and_b32_e32 v187, 0xffff0000, v141\n"
"v_lshlrev_b32_e32 v188, 16, v142\n"
"v_mfma_f32_16x16x32_bf16 v[112:115], a[220:223], a[176:179], v[112:115]\n"
"v_and_b32_e32 v189, 0xffff0000, v142\n"
"v_lshlrev_b32_e32 v190, 16, v143\n"
"v_mfma_f32_16x16x32_bf16 v[116:119], a[220:223], a[180:183], v[116:119]\n"
"v_and_b32_e32 v191, 0xffff0000, v143\n"
"s_waitcnt vmcnt(9)\n"
"v_mfma_f32_16x16x32_bf16 v[116:119], a[216:219], a[164:167], v[116:119]\n"
"v_lshlrev_b32_e32 v243, 16, v148\n"
"v_and_b32_e32 v244, 0xffff0000, v148\n"
"v_mfma_f32_16x16x32_bf16 v[116:119], a[212:215], a[148:151], v[116:119]\n"
"v_add_f32_e32 v176, v243, v176\n"
"v_add_f32_e32 v177, v244, v177\n"
"v_mfma_f32_16x16x32_bf16 v[116:119], a[208:211], a[132:135], v[116:119]\n"
"v_lshlrev_b32_e32 v243, 16, v149\n"
"v_and_b32_e32 v244, 0xffff0000, v149\n"
"v_mfma_f32_16x16x32_bf16 v[116:119], a[204:207], a[116:119], v[116:119]\n"
"v_add_f32_e32 v178, v243, v178\n"
"v_add_f32_e32 v179, v244, v179\n"
"v_mfma_f32_16x16x32_bf16 v[116:119], a[200:203], a[100:103], v[116:119]\n"
"v_lshlrev_b32_e32 v243, 16, v150\n"
"v_and_b32_e32 v244, 0xffff0000, v150\n"
"v_mfma_f32_16x16x32_bf16 v[116:119], a[196:199], v[100:103], v[116:119]\n"
"v_add_f32_e32 v180, v243, v180\n"
"v_add_f32_e32 v181, v244, v181\n"
"v_mfma_f32_16x16x32_bf16 v[116:119], a[192:195], v[68:71], v[116:119]\n"
"v_lshlrev_b32_e32 v243, 16, v151\n"
"v_and_b32_e32 v244, 0xffff0000, v151\n"
"v_mfma_f32_16x16x32_bf16 v[120:123], a[192:195], v[72:75], v[120:123]\n"
"buffer_load_dwordx4 v[152:155], v255, s[52:55], s94 offen offset:1024 sc1\n"
"v_add_f32_e32 v182, v243, v182\n"
"v_mfma_f32_16x16x32_bf16 v[120:123], a[196:199], v[104:107], v[120:123]\n"
"v_add_f32_e32 v183, v244, v183\n"
"v_lshlrev_b32_e32 v243, 16, v156\n"
"v_mfma_f32_16x16x32_bf16 v[120:123], a[200:203], a[104:107], v[120:123]\n"
"v_and_b32_e32 v244, 0xffff0000, v156\n"
"v_add_f32_e32 v184, v243, v184\n"
"v_mfma_f32_16x16x32_bf16 v[120:123], a[204:207], a[120:123], v[120:123]\n"
"v_add_f32_e32 v185, v244, v185\n"
"v_lshlrev_b32_e32 v243, 16, v157\n"
"v_mfma_f32_16x16x32_bf16 v[120:123], a[208:211], a[136:139], v[120:123]\n"
"v_and_b32_e32 v244, 0xffff0000, v157\n"
"v_add_f32_e32 v186, v243, v186\n"
"v_mfma_f32_16x16x32_bf16 v[120:123], a[212:215], a[152:155], v[120:123]\n"
"v_add_f32_e32 v187, v244, v187\n"
"v_lshlrev_b32_e32 v243, 16, v158\n"
"v_mfma_f32_16x16x32_bf16 v[120:123], a[216:219], a[168:171], v[120:123]\n"
"v_and_b32_e32 v244, 0xffff0000, v158\n"
"v_add_f32_e32 v188, v243, v188\n"
"v_mfma_f32_16x16x32_bf16 v[120:123], a[220:223], a[184:187], v[120:123]\n"
"v_add_f32_e32 v189, v244, v189\n"
"v_lshlrev_b32_e32 v243, 16, v159\n"
"v_mfma_f32_16x16x32_bf16 v[124:127], a[220:223], a[188:191], v[124:127]\n"
"v_and_b32_e32 v244, 0xffff0000, v159\n"
"v_add_f32_e32 v190, v243, v190\n"
"v_mfma_f32_16x16x32_bf16 v[124:127], a[216:219], a[172:175], v[124:127]\n"
"v_add_f32_e32 v191, v244, v191\n"
"v_mfma_f32_16x16x32_bf16 v[124:127], a[212:215], a[156:159], v[124:127]\n"
"v_mfma_f32_16x16x32_bf16 v[124:127], a[208:211], a[140:143], v[124:127]\n"
"v_mfma_f32_16x16x32_bf16 v[124:127], a[204:207], a[124:127], v[124:127]\n"
"v_mfma_f32_16x16x32_bf16 v[124:127], a[200:203], a[108:111], v[124:127]\n"
"v_mfma_f32_16x16x32_bf16 v[124:127], a[196:199], v[108:111], v[124:127]\n"
"v_mfma_f32_16x16x32_bf16 v[124:127], a[192:195], v[76:79], v[124:127]\n"
"s_add_u32 s94, s22, 12288\n"
"buffer_load_dwordx4 v[192:195], v255, s[52:55], s94 offen offset:0 sc1\n"
"buffer_load_dwordx4 v[200:203], v255, s[52:55], s94 offen offset:1024 sc1\n"
"v_fma_f32 v16, v112, v160, v16\n"
"v_fma_f32 v17, v113, v161, v17\n"
"v_fma_f32 v18, v114, v162, v18\n"
"v_fma_f32 v19, v115, v163, v19\n"
"v_fma_f32 v20, v116, v164, v20\n"
"v_fma_f32 v21, v117, v165, v21\n"
"v_fma_f32 v22, v118, v166, v22\n"
"v_fma_f32 v23, v119, v167, v23\n"
"v_fma_f32 v24, v120, v168, v24\n"
"v_fma_f32 v25, v121, v169, v25\n"
"v_fma_f32 v26, v122, v170, v26\n"
"v_fma_f32 v27, v123, v171, v27\n"
"v_fma_f32 v28, v124, v172, v28\n"
"v_fma_f32 v29, v125, v173, v29\n"
"v_fma_f32 v30, v126, v174, v30\n"
"v_fma_f32 v31, v127, v175, v31\n"
"s_waitcnt vmcnt(10)\n"
"v_dot2_f32_bf16 v176, v196, v241, v176\n"
"v_dot2_f32_bf16 v177, v196, v242, v177\n"
"v_dot2_f32_bf16 v178, v197, v241, v178\n"
"v_dot2_f32_bf16 v179, v197, v242, v179\n"
"v_dot2_f32_bf16 v180, v198, v241, v180\n"
"v_dot2_f32_bf16 v181, v198, v242, v181\n"
"v_dot2_f32_bf16 v182, v199, v241, v182\n"
"v_dot2_f32_bf16 v183, v199, v242, v183\n"
"v_dot2_f32_bf16 v184, v204, v241, v184\n"
"v_dot2_f32_bf16 v185, v204, v242, v185\n"
"v_dot2_f32_bf16 v186, v205, v241, v186\n"
"v_dot2_f32_bf16 v187, v205, v242, v187\n"
"v_dot2_f32_bf16 v188, v206, v241, v188\n"
"v_dot2_f32_bf16 v189, v206, v242, v189\n"
"v_dot2_f32_bf16 v190, v207, v241, v190\n"
"v_dot2_f32_bf16 v191, v207, v242, v191\n"
"s_waitcnt vmcnt(6)\n"
"v_dot2_f32_bf16 v176, v212, v241, v176\n"
"v_dot2_f32_bf16 v177, v212, v242, v177\n"
"v_dot2_f32_bf16 v178, v213, v241, v178\n"
"v_dot2_f32_bf16 v179, v213, v242, v179\n"
"v_dot2_f32_bf16 v180, v214, v241, v180\n"
"v_dot2_f32_bf16 v181, v214, v242, v181\n"
"v_dot2_f32_bf16 v182, v215, v241, v182\n"
"v_dot2_f32_bf16 v183, v215, v242, v183\n"
"v_dot2_f32_bf16 v184, v220, v241, v184\n"
"v_dot2_f32_bf16 v185, v220, v242, v185\n"
"v_dot2_f32_bf16 v186, v221, v241, v186\n"
"v_dot2_f32_bf16 v187, v221, v242, v187\n"
"v_dot2_f32_bf16 v188, v222, v241, v188\n"
"v_dot2_f32_bf16 v189, v222, v242, v189\n"
"v_dot2_f32_bf16 v190, v223, v241, v190\n"
"v_dot2_f32_bf16 v191, v223, v242, v191\n"
"s_waitcnt vmcnt(4)\n"
"v_dot2_f32_bf16 v176, v128, v241, v176\n"
"v_dot2_f32_bf16 v177, v128, v242, v177\n"
"v_dot2_f32_bf16 v178, v129, v241, v178\n"
"v_dot2_f32_bf16 v179, v129, v242, v179\n"
"v_dot2_f32_bf16 v180, v130, v241, v180\n"
"v_dot2_f32_bf16 v181, v130, v242, v181\n"
"v_dot2_f32_bf16 v182, v131, v241, v182\n"
"v_dot2_f32_bf16 v183, v131, v242, v183\n"
"v_dot2_f32_bf16 v184, v136, v241, v184\n"
"v_dot2_f32_bf16 v185, v136, v242, v185\n"
"v_dot2_f32_bf16 v186, v137, v241, v186\n"
"v_dot2_f32_bf16 v187, v137, v242, v187\n"
"v_dot2_f32_bf16 v188, v138, v241, v188\n"
"v_dot2_f32_bf16 v189, v138, v242, v189\n"
"v_dot2_f32_bf16 v190, v139, v241, v190\n"
"v_dot2_f32_bf16 v191, v139, v242, v191\n"
"s_waitcnt vmcnt(2)\n"
"v_dot2_f32_bf16 v176, v144, v241, v176\n"
"v_dot2_f32_bf16 v177, v144, v242, v177\n"
"v_dot2_f32_bf16 v178, v145, v241, v178\n"
"v_dot2_f32_bf16 v179, v145, v242, v179\n"
"v_dot2_f32_bf16 v180, v146, v241, v180\n"
"v_dot2_f32_bf16 v181, v146, v242, v181\n"
"v_dot2_f32_bf16 v182, v147, v241, v182\n"
"v_dot2_f32_bf16 v183, v147, v242, v183\n"
"v_dot2_f32_bf16 v184, v152, v241, v184\n"
"v_dot2_f32_bf16 v185, v152, v242, v185\n"
"v_dot2_f32_bf16 v186, v153, v241, v186\n"
"v_dot2_f32_bf16 v187, v153, v242, v187\n"
"v_dot2_f32_bf16 v188, v154, v241, v188\n"
"v_dot2_f32_bf16 v189, v154, v242, v189\n"
"v_dot2_f32_bf16 v190, v155, v241, v190\n"
"v_dot2_f32_bf16 v191, v155, v242, v191\n"
"s_waitcnt vmcnt(0)\n"
"v_dot2_f32_bf16 v176, v192, v241, v176\n"
"v_dot2_f32_bf16 v177, v192, v242, v177\n"
"v_dot2_f32_bf16 v178, v193, v241, v178\n"
"v_dot2_f32_bf16 v179, v193, v242, v179\n"
"v_dot2_f32_bf16 v180, v194, v241, v180\n"
"v_dot2_f32_bf16 v181, v194, v242, v181\n"
"v_dot2_f32_bf16 v182, v195, v241, v182\n"
"v_dot2_f32_bf16 v183, v195, v242, v183\n"
"v_dot2_f32_bf16 v184, v200, v241, v184\n"
"v_dot2_f32_bf16 v185, v200, v242, v185\n"
"v_dot2_f32_bf16 v186, v201, v241, v186\n"
"v_dot2_f32_bf16 v187, v201, v242, v187\n"
"v_dot2_f32_bf16 v188, v202, v241, v188\n"
".LPG18_%=:\n"
"s_cbranch_execz .LPGR_%=\n"
"v_dot2_f32_bf16 v189, v202, v242, v189\n"
"v_dot2_f32_bf16 v190, v203, v241, v190\n"
"v_dot2_f32_bf16 v191, v203, v242, v191\n"
".LF_%=:\n"
"s_nop 2\n"
"v_fma_f32 v48, v176, v160, v16\n"
"v_fma_f32 v49, v177, v161, v17\n"
"v_fma_f32 v50, v178, v162, v18\n"
"v_fma_f32 v51, v179, v163, v19\n"
"v_fma_f32 v52, v180, v164, v20\n"
"v_fma_f32 v53, v181, v165, v21\n"
"v_fma_f32 v54, v182, v166, v22\n"
"v_fma_f32 v55, v183, v167, v23\n"
"v_fma_f32 v56, v184, v168, v24\n"
"v_fma_f32 v57, v185, v169, v25\n"
"v_fma_f32 v58, v186, v170, v26\n"
"v_fma_f32 v59, v187, v171, v27\n"
"v_fma_f32 v60, v188, v172, v28\n"
"v_fma_f32 v61, v189, v173, v29\n"
"v_fma_f32 v62, v190, v174, v30\n"
"v_fma_f32 v63, v191, v175, v31\n"
"v_add_f32_e32 v176, v176, v178\n"
"v_add_f32_e32 v180, v180, v182\n"
"v_add_f32_e32 v184, v184, v186\n"
"v_add_f32_e32 v188, v188, v190\n"
"v_add_f32_e32 v176, v176, v180\n"
"v_add_f32_e32 v184, v184, v188\n"
"v_cmp_u_f32_e32 vcc, v176, v184\n"
"v_cvt_pk_bf16_f32 v228, v48, v49\n"
"v_cvt_pk_bf16_f32 v229, v50, v51\n"
"v_cvt_pk_bf16_f32 v230, v52, v53\n"
"v_cvt_pk_bf16_f32 v231, v54, v55\n"
"v_cvt_pk_bf16_f32 v232, v56, v57\n"
"v_cvt_pk_bf16_f32 v233, v58, v59\n"
"v_cvt_pk_bf16_f32 v234, v60, v61\n"
"v_cvt_pk_bf16_f32 v235, v62, v63\n"
"v_permlane16_swap_b32 v228, v230\n"
"v_permlane16_swap_b32 v229, v231\n"
"v_permlane16_swap_b32 v232, v234\n"
"v_permlane16_swap_b32 v233, v235\n"
"s_cbranch_vccnz .LR_%=\n"
".LD_%=:\n"
"buffer_store_dwordx4 v[228:231], v240, s[72:75], s34 offen\n"
"s_add_u32 s34, s34, 32768\n"
"buffer_store_dwordx4 v[232:235], v240, s[72:75], s34 offen\n"
"s_add_u32 s34, s34, 32768\n"
"s_endpgm\n"
".LR_%=:\n"
"s_cmp_lg_u32 s95, 0\n"
"s_cbranch_scc1 .LD_%=\n"
"v_mov_b32_e32 v243, 2139193217\n"
"s_mov_b32 s95, 1\n"
"s_waitcnt vmcnt(14)\n"
".LR0_%=:\n"
"v_cmpx_ne_u32_e32 vcc, v243, v132\n"
"v_cmpx_ne_u32_e32 vcc, v243, v133\n"
"v_cmpx_ne_u32_e32 vcc, v243, v134\n"
"v_cmpx_ne_u32_e32 vcc, v243, v135\n"
"v_cmpx_ne_u32_e32 vcc, v243, v140\n"
"v_cmpx_ne_u32_e32 vcc, v243, v141\n"
"v_cmpx_ne_u32_e32 vcc, v243, v142\n"
"v_cmpx_ne_u32_e32 vcc, v243, v143\n"
"s_nop 3\n"
"s_cmp_eq_u64 exec, -1\n"
"s_mov_b64 exec, -1\n"
"s_cbranch_scc1 .LS0_%=\n"
"s_add_u32 s95, s95, 1\n"
"s_cmp_gt_u32 s95, 0x100000\n"
"s_cbranch_scc1 .LS0_%=\n"
"s_add_u32 s94, s22, 0\n"
"buffer_load_dwordx4 v[132:135], v255, s[52:55], s94 offen offset:0 sc1\n"
"buffer_load_dwordx4 v[140:143], v255, s[52:55], s94 offen offset:1024 sc1\n"
"s_waitcnt vmcnt(0)\n"
"s_branch .LR0_%=\n"
".LS0_%=:\n"
"v_dot2_f32_bf16 v176, v132, v241, 0\n"
"v_dot2_f32_bf16 v177, v132, v242, 0\n"
"v_dot2_f32_bf16 v178, v133, v241, 0\n"
"v_dot2_f32_bf16 v179, v133, v242, 0\n"
"v_dot2_f32_bf16 v180, v134, v241, 0\n"
"v_dot2_f32_bf16 v181, v134, v242, 0\n"
"v_dot2_f32_bf16 v182, v135, v241, 0\n"
"v_dot2_f32_bf16 v183, v135, v242, 0\n"
"v_dot2_f32_bf16 v184, v140, v241, 0\n"
"v_dot2_f32_bf16 v185, v140, v242, 0\n"
"v_dot2_f32_bf16 v186, v141, v241, 0\n"
"v_dot2_f32_bf16 v187, v141, v242, 0\n"
"v_dot2_f32_bf16 v188, v142, v241, 0\n"
"v_dot2_f32_bf16 v189, v142, v242, 0\n"
"v_dot2_f32_bf16 v190, v143, v241, 0\n"
"v_dot2_f32_bf16 v191, v143, v242, 0\n"
"s_waitcnt vmcnt(12)\n"
".LR1_%=:\n"
"v_cmpx_ne_u32_e32 vcc, v243, v148\n"
"v_cmpx_ne_u32_e32 vcc, v243, v149\n"
"v_cmpx_ne_u32_e32 vcc, v243, v150\n"
"v_cmpx_ne_u32_e32 vcc, v243, v151\n"
"v_cmpx_ne_u32_e32 vcc, v243, v156\n"
"v_cmpx_ne_u32_e32 vcc, v243, v157\n"
"v_cmpx_ne_u32_e32 vcc, v243, v158\n"
"v_cmpx_ne_u32_e32 vcc, v243, v159\n"
"s_nop 3\n"
"s_cmp_eq_u64 exec, -1\n"
"s_mov_b64 exec, -1\n"
"s_cbranch_scc1 .LS1_%=\n"
"s_add_u32 s95, s95, 1\n"
"s_cmp_gt_u32 s95, 0x100000\n"
"s_cbranch_scc1 .LS1_%=\n"
"s_add_u32 s94, s22, 2048\n"
"buffer_load_dwordx4 v[148:151], v255, s[52:55], s94 offen offset:0 sc1\n"
"buffer_load_dwordx4 v[156:159], v255, s[52:55], s94 offen offset:1024 sc1\n"
"s_add_u32 s94, s22, 4096\n"
"buffer_load_dwordx4 v[196:199], v255, s[52:55], s94 offen offset:0 sc1\n"
"buffer_load_dwordx4 v[204:207], v255, s[52:55], s94 offen offset:1024 sc1\n"
"s_waitcnt vmcnt(0)\n"
"s_branch .LR1_%=\n"
".LS1_%=:\n"
"v_dot2_f32_bf16 v176, v148, v241, v176\n"
"v_dot2_f32_bf16 v177, v148, v242, v177\n"
"v_dot2_f32_bf16 v178, v149, v241, v178\n"
"v_dot2_f32_bf16 v179, v149, v242, v179\n"
"v_dot2_f32_bf16 v180, v150, v241, v180\n"
"v_dot2_f32_bf16 v181, v150, v242, v181\n"
"v_dot2_f32_bf16 v182, v151, v241, v182\n"
"v_dot2_f32_bf16 v183, v151, v242, v183\n"
"v_dot2_f32_bf16 v184, v156, v241, v184\n"
"v_dot2_f32_bf16 v185, v156, v242, v185\n"
"v_dot2_f32_bf16 v186, v157, v241, v186\n"
"v_dot2_f32_bf16 v187, v157, v242, v187\n"
"v_dot2_f32_bf16 v188, v158, v241, v188\n"
"v_dot2_f32_bf16 v189, v158, v242, v189\n"
"v_dot2_f32_bf16 v190, v159, v241, v190\n"
"v_dot2_f32_bf16 v191, v159, v242, v191\n"
"s_waitcnt vmcnt(10)\n"
".LR2_%=:\n"
"v_cmpx_ne_u32_e32 vcc, v243, v196\n"
"v_cmpx_ne_u32_e32 vcc, v243, v197\n"
"v_cmpx_ne_u32_e32 vcc, v243, v198\n"
"v_cmpx_ne_u32_e32 vcc, v243, v199\n"
"v_cmpx_ne_u32_e32 vcc, v243, v204\n"
"v_cmpx_ne_u32_e32 vcc, v243, v205\n"
"v_cmpx_ne_u32_e32 vcc, v243, v206\n"
"v_cmpx_ne_u32_e32 vcc, v243, v207\n"
"s_nop 3\n"
"s_cmp_eq_u64 exec, -1\n"
"s_mov_b64 exec, -1\n"
"s_cbranch_scc1 .LS2_%=\n"
"s_add_u32 s95, s95, 1\n"
"s_cmp_gt_u32 s95, 0x100000\n"
"s_cbranch_scc1 .LS2_%=\n"
"s_add_u32 s94, s22, 4096\n"
"buffer_load_dwordx4 v[196:199], v255, s[52:55], s94 offen offset:0 sc1\n"
"buffer_load_dwordx4 v[204:207], v255, s[52:55], s94 offen offset:1024 sc1\n"
"s_waitcnt vmcnt(0)\n"
"s_branch .LR2_%=\n"
".LS2_%=:\n"
"v_dot2_f32_bf16 v176, v196, v241, v176\n"
"v_dot2_f32_bf16 v177, v196, v242, v177\n"
"v_dot2_f32_bf16 v178, v197, v241, v178\n"
"v_dot2_f32_bf16 v179, v197, v242, v179\n"
"v_dot2_f32_bf16 v180, v198, v241, v180\n"
"v_dot2_f32_bf16 v181, v198, v242, v181\n"
"v_dot2_f32_bf16 v182, v199, v241, v182\n"
"v_dot2_f32_bf16 v183, v199, v242, v183\n"
"v_dot2_f32_bf16 v184, v204, v241, v184\n"
"v_dot2_f32_bf16 v185, v204, v242, v185\n"
"v_dot2_f32_bf16 v186, v205, v241, v186\n"
"v_dot2_f32_bf16 v187, v205, v242, v187\n"
"v_dot2_f32_bf16 v188, v206, v241, v188\n"
"v_dot2_f32_bf16 v189, v206, v242, v189\n"
"v_dot2_f32_bf16 v190, v207, v241, v190\n"
"v_dot2_f32_bf16 v191, v207, v242, v191\n"
"s_waitcnt vmcnt(6)\n"
".LR3_%=:\n"
"v_cmpx_ne_u32_e32 vcc, v243, v212\n"
"v_cmpx_ne_u32_e32 vcc, v243, v213\n"
"v_cmpx_ne_u32_e32 vcc, v243, v214\n"
"v_cmpx_ne_u32_e32 vcc, v243, v215\n"
"v_cmpx_ne_u32_e32 vcc, v243, v220\n"
"v_cmpx_ne_u32_e32 vcc, v243, v221\n"
"v_cmpx_ne_u32_e32 vcc, v243, v222\n"
"v_cmpx_ne_u32_e32 vcc, v243, v223\n"
"s_nop 3\n"
"s_cmp_eq_u64 exec, -1\n"
"s_mov_b64 exec, -1\n"
"s_cbranch_scc1 .LS3_%=\n"
"s_add_u32 s95, s95, 1\n"
"s_cmp_gt_u32 s95, 0x100000\n"
"s_cbranch_scc1 .LS3_%=\n"
"s_add_u32 s94, s22, 6144\n"
"buffer_load_dwordx4 v[212:215], v255, s[52:55], s94 offen offset:0 sc1\n"
"buffer_load_dwordx4 v[220:223], v255, s[52:55], s94 offen offset:1024 sc1\n"
"s_add_u32 s94, s22, 8192\n"
"buffer_load_dwordx4 v[128:131], v255, s[52:55], s94 offen offset:0 sc1\n"
"buffer_load_dwordx4 v[136:139], v255, s[52:55], s94 offen offset:1024 sc1\n"
"s_waitcnt vmcnt(0)\n"
"s_branch .LR3_%=\n"
".LS3_%=:\n"
"v_dot2_f32_bf16 v176, v212, v241, v176\n"
"v_dot2_f32_bf16 v177, v212, v242, v177\n"
"v_dot2_f32_bf16 v178, v213, v241, v178\n"
"v_dot2_f32_bf16 v179, v213, v242, v179\n"
"v_dot2_f32_bf16 v180, v214, v241, v180\n"
"v_dot2_f32_bf16 v181, v214, v242, v181\n"
"v_dot2_f32_bf16 v182, v215, v241, v182\n"
"v_dot2_f32_bf16 v183, v215, v242, v183\n"
"v_dot2_f32_bf16 v184, v220, v241, v184\n"
"v_dot2_f32_bf16 v185, v220, v242, v185\n"
"v_dot2_f32_bf16 v186, v221, v241, v186\n"
"v_dot2_f32_bf16 v187, v221, v242, v187\n"
"v_dot2_f32_bf16 v188, v222, v241, v188\n"
"v_dot2_f32_bf16 v189, v222, v242, v189\n"
"v_dot2_f32_bf16 v190, v223, v241, v190\n"
"v_dot2_f32_bf16 v191, v223, v242, v191\n"
"s_waitcnt vmcnt(4)\n"
".LR4_%=:\n"
"v_cmpx_ne_u32_e32 vcc, v243, v128\n"
"v_cmpx_ne_u32_e32 vcc, v243, v129\n"
"v_cmpx_ne_u32_e32 vcc, v243, v130\n"
"v_cmpx_ne_u32_e32 vcc, v243, v131\n"
"v_cmpx_ne_u32_e32 vcc, v243, v136\n"
"v_cmpx_ne_u32_e32 vcc, v243, v137\n"
"v_cmpx_ne_u32_e32 vcc, v243, v138\n"
"v_cmpx_ne_u32_e32 vcc, v243, v139\n"
"s_nop 3\n"
"s_cmp_eq_u64 exec, -1\n"
"s_mov_b64 exec, -1\n"
"s_cbranch_scc1 .LS4_%=\n"
"s_add_u32 s95, s95, 1\n"
"s_cmp_gt_u32 s95, 0x100000\n"
"s_cbranch_scc1 .LS4_%=\n"
"s_add_u32 s94, s22, 8192\n"
"buffer_load_dwordx4 v[128:131], v255, s[52:55], s94 offen offset:0 sc1\n"
"buffer_load_dwordx4 v[136:139], v255, s[52:55], s94 offen offset:1024 sc1\n"
"s_waitcnt vmcnt(0)\n"
"s_branch .LR4_%=\n"
".LS4_%=:\n"
"v_dot2_f32_bf16 v176, v128, v241, v176\n"
"v_dot2_f32_bf16 v177, v128, v242, v177\n"
"v_dot2_f32_bf16 v178, v129, v241, v178\n"
"v_dot2_f32_bf16 v179, v129, v242, v179\n"
"v_dot2_f32_bf16 v180, v130, v241, v180\n"
"v_dot2_f32_bf16 v181, v130, v242, v181\n"
"v_dot2_f32_bf16 v182, v131, v241, v182\n"
"v_dot2_f32_bf16 v183, v131, v242, v183\n"
"v_dot2_f32_bf16 v184, v136, v241, v184\n"
"v_dot2_f32_bf16 v185, v136, v242, v185\n"
"v_dot2_f32_bf16 v186, v137, v241, v186\n"
"v_dot2_f32_bf16 v187, v137, v242, v187\n"
"v_dot2_f32_bf16 v188, v138, v241, v188\n"
"v_dot2_f32_bf16 v189, v138, v242, v189\n"
"v_dot2_f32_bf16 v190, v139, v241, v190\n"
"v_dot2_f32_bf16 v191, v139, v242, v191\n"
"s_waitcnt vmcnt(2)\n"
".LR5_%=:\n"
"v_cmpx_ne_u32_e32 vcc, v243, v144\n"
"v_cmpx_ne_u32_e32 vcc, v243, v145\n"
"v_cmpx_ne_u32_e32 vcc, v243, v146\n"
"v_cmpx_ne_u32_e32 vcc, v243, v147\n"
"v_cmpx_ne_u32_e32 vcc, v243, v152\n"
"v_cmpx_ne_u32_e32 vcc, v243, v153\n"
"v_cmpx_ne_u32_e32 vcc, v243, v154\n"
"v_cmpx_ne_u32_e32 vcc, v243, v155\n"
"s_nop 3\n"
"s_cmp_eq_u64 exec, -1\n"
"s_mov_b64 exec, -1\n"
"s_cbranch_scc1 .LS5_%=\n"
"s_add_u32 s95, s95, 1\n"
"s_cmp_gt_u32 s95, 0x100000\n"
"s_cbranch_scc1 .LS5_%=\n"
"s_add_u32 s94, s22, 10240\n"
"buffer_load_dwordx4 v[144:147], v255, s[52:55], s94 offen offset:0 sc1\n"
"buffer_load_dwordx4 v[152:155], v255, s[52:55], s94 offen offset:1024 sc1\n"
"s_add_u32 s94, s22, 12288\n"
"buffer_load_dwordx4 v[192:195], v255, s[52:55], s94 offen offset:0 sc1\n"
"buffer_load_dwordx4 v[200:203], v255, s[52:55], s94 offen offset:1024 sc1\n"
"s_waitcnt vmcnt(0)\n"
"s_branch .LR5_%=\n"
".LS5_%=:\n"
"v_dot2_f32_bf16 v176, v144, v241, v176\n"
"v_dot2_f32_bf16 v177, v144, v242, v177\n"
"v_dot2_f32_bf16 v178, v145, v241, v178\n"
"v_dot2_f32_bf16 v179, v145, v242, v179\n"
"v_dot2_f32_bf16 v180, v146, v241, v180\n"
"v_dot2_f32_bf16 v181, v146, v242, v181\n"
"v_dot2_f32_bf16 v182, v147, v241, v182\n"
"v_dot2_f32_bf16 v183, v147, v242, v183\n"
"v_dot2_f32_bf16 v184, v152, v241, v184\n"
"v_dot2_f32_bf16 v185, v152, v242, v185\n"
"v_dot2_f32_bf16 v186, v153, v241, v186\n"
"v_dot2_f32_bf16 v187, v153, v242, v187\n"
"v_dot2_f32_bf16 v188, v154, v241, v188\n"
"v_dot2_f32_bf16 v189, v154, v242, v189\n"
"v_dot2_f32_bf16 v190, v155, v241, v190\n"
"v_dot2_f32_bf16 v191, v155, v242, v191\n"
"s_waitcnt vmcnt(0)\n"
".LR6_%=:\n"
"v_cmpx_ne_u32_e32 vcc, v243, v192\n"
"v_cmpx_ne_u32_e32 vcc, v243, v193\n"
"v_cmpx_ne_u32_e32 vcc, v243, v194\n"
"v_cmpx_ne_u32_e32 vcc, v243, v195\n"
"v_cmpx_ne_u32_e32 vcc, v243, v200\n"
"v_cmpx_ne_u32_e32 vcc, v243, v201\n"
"v_cmpx_ne_u32_e32 vcc, v243, v202\n"
"v_cmpx_ne_u32_e32 vcc, v243, v203\n"
"s_nop 3\n"
"s_cmp_eq_u64 exec, -1\n"
"s_mov_b64 exec, -1\n"
"s_cbranch_scc1 .LS6_%=\n"
"s_add_u32 s95, s95, 1\n"
"s_cmp_gt_u32 s95, 0x100000\n"
"s_cbranch_scc1 .LS6_%=\n"
"s_add_u32 s94, s22, 12288\n"
"buffer_load_dwordx4 v[192:195], v255, s[52:55], s94 offen offset:0 sc1\n"
"buffer_load_dwordx4 v[200:203], v255, s[52:55], s94 offen offset:1024 sc1\n"
"s_waitcnt vmcnt(0)\n"
"s_branch .LR6_%=\n"
".LS6_%=:\n"
"v_dot2_f32_bf16 v176, v192, v241, v176\n"
"v_dot2_f32_bf16 v177, v192, v242, v177\n"
"v_dot2_f32_bf16 v178, v193, v241, v178\n"
"v_dot2_f32_bf16 v179, v193, v242, v179\n"
"v_dot2_f32_bf16 v180, v194, v241, v180\n"
"v_dot2_f32_bf16 v181, v194, v242, v181\n"
"v_dot2_f32_bf16 v182, v195, v241, v182\n"
"v_dot2_f32_bf16 v183, v195, v242, v183\n"
"v_dot2_f32_bf16 v184, v200, v241, v184\n"
"v_dot2_f32_bf16 v185, v200, v242, v185\n"
"v_dot2_f32_bf16 v186, v201, v241, v186\n"
"v_dot2_f32_bf16 v187, v201, v242, v187\n"
"v_dot2_f32_bf16 v188, v202, v241, v188\n"
"v_dot2_f32_bf16 v189, v202, v242, v189\n"
"v_dot2_f32_bf16 v190, v203, v241, v190\n"
"v_dot2_f32_bf16 v191, v203, v242, v191\n"
"s_branch .LF_%=\n"
".LHS_%=:\n"
"s_mov_b32 s95, 0\n"
".LHS1_%=:\n"
"buffer_load_dword v250, v246, s[24:27], s30 offen sc1\n"
"s_waitcnt vmcnt(0)\n"
"buffer_store_dword v250, v247, s[24:27], s29 offen sc1\n"
"s_sleep 8\n"
"buffer_load_dword v249, v246, s[24:27], s28 offen sc1\n"
"s_waitcnt vmcnt(0)\n"
"s_add_u32 s95, s95, 1\n"
"s_cmp_gt_u32 s95, 0x40000\n"
"s_cbranch_scc1 .LHSOK_%=\n"
"v_readlane_b32 s82, v249, 0\n"
"v_readlane_b32 s83, v249, 1\n"
"s_nop 0\n"
"s_cmp_eq_u64 s[82:83], s[32:33]\n"
"s_cbranch_scc0 .LHS1_%=\n"
"s_branch .LHSOK_%=\n"
".LFALL_%=:\n"
"s_waitcnt vmcnt(0) lgkmcnt(0)\n"
"v_lshrrev_b32_e32 v0, 8, v255\n"
"v_bfe_u32 v1, v255, 4, 4\n"
"v_xor_b32_e32 v224, v0, v1\n"
"s_mul_i32 s83, s89, 128\n"
"v_lshlrev_b32_e32 v224, 4, v224\n"
"v_lshl_add_u32 v224, v1, 8, v224\n"
"v_xor_b32_e32 v224, s83, v224\n"
"v_xor_b32_e32 v225, 64, v224\n"
"v_xor_b32_e32 v226, 128, v224\n"
"v_xor_b32_e32 v227, 192, v224\n"
"v_add_u32_e32 v2, 0, v0\n"
"v_xor_b32_e32 v3, v2, v1\n"
"v_lshlrev_b32_e32 v3, 4, v3\n"
"v_lshl_add_u32 v236, v0, 8, v3\n"
"v_lshlrev_b32_e32 v3, 1, v2\n"
"v_xor_b32_e32 v3, v3, v1\n"
"v_lshlrev_b32_e32 v3, 4, v3\n"
"v_lshl_add_u32 v240, v0, 8, v3\n"
"v_add_u32_e32 v2, 4, v0\n"
"v_xor_b32_e32 v3, v2, v1\n"
"v_lshlrev_b32_e32 v3, 4, v3\n"
"v_lshl_add_u32 v237, v0, 8, v3\n"
"v_lshlrev_b32_e32 v3, 1, v2\n"
"v_xor_b32_e32 v3, v3, v1\n"
"v_lshlrev_b32_e32 v3, 4, v3\n"
"v_lshl_add_u32 v241, v0, 8, v3\n"
"v_add_u32_e32 v2, 8, v0\n"
"v_xor_b32_e32 v3, v2, v1\n"
"v_lshlrev_b32_e32 v3, 4, v3\n"
"v_lshl_add_u32 v238, v0, 8, v3\n"
"v_add_u32_e32 v2, 12, v0\n"
"v_xor_b32_e32 v3, v2, v1\n"
"v_lshlrev_b32_e32 v3, 4, v3\n"
"v_lshl_add_u32 v239, v0, 8, v3\n"
"v_lshrrev_b32_e32 v0, 6, v255\n"
"v_bfe_u32 v228, v255, 4, 2\n"
"v_lshlrev_b32_e32 v228, 3, v228\n"
"v_lshl_add_u32 v228, v0, 8, v228\n"
"v_and_b32_e32 v0, 7, v0\n"
"v_lshl_add_u32 v228, v0, 5, v228\n"
"v_add_u32_e32 v228, 65536, v228\n"
"v_xor_b32_e32 v229, 32, v228\n"
"v_xor_b32_e32 v230, 64, v228\n"
"v_xor_b32_e32 v231, 96, v228\n"
"v_xor_b32_e32 v232, 128, v228\n"
"v_xor_b32_e32 v233, 160, v228\n"
"v_xor_b32_e32 v234, 192, v228\n"
"v_xor_b32_e32 v235, 224, v228\n"
"v_mov_b32_e32 v128, 0xff800000\n"
"v_mov_b32_e32 v130, 0xff800000\n"
"v_mov_b32_e32 v132, 0xff800000\n"
"v_mov_b32_e32 v134, 0xff800000\n"
"v_writelane_b32 v243, s32, 0\n"
"v_writelane_b32 v243, s33, 1\n"
"s_mul_i32 s44, s78, 524288\n"
"s_mul_i32 s83, s77, 4096\n"
"s_add_u32 s44, s44, s83\n"
"s_add_u32 s44, s44, -16384\n"
"s_mov_b32 s45, s44\n"
"s_mov_b64 s[68:69], s[20:21]\n"
"s_mov_b32 s70, 0\n"
"s_mov_b32 s71, 0x20000\n"
"s_mov_b32 s47, 32\n"
"s_mov_b32 s92, -1\n"
"s_mov_b32 s54, 0\n"
"s_mov_b32 s70, 0\n"
"s_mov_b32 s74, 0\n"
"s_add_u32 s30, s28, 0x40000\n"
".LFL_%=:\n"
"s_barrier\n"
"s_add_u32 m0, s46, 0\n"
"s_add_u32 s44, s44, 16384\n"
"buffer_load_dwordx4 v236, s[36:39], s44 offen offset:0 lds\n"
"buffer_load_dwordx4 v237, s[36:39], s44 offen offset:1024 lds\n"
"buffer_load_dwordx4 v238, s[36:39], s44 offen offset:2048 lds\n"
"buffer_load_dwordx4 v239, s[36:39], s44 offen offset:3072 lds\n"
"s_waitcnt vmcnt(0)\n"
"s_barrier\n"
"ds_read_b128 a[224:227], v224 offset:0\n"
"ds_read_b128 a[228:231], v225 offset:0\n"
"ds_read_b128 a[232:235], v226 offset:0\n"
"ds_read_b128 a[236:239], v227 offset:0\n"
"s_waitcnt lgkmcnt(0)\n"
"v_mfma_f32_16x16x32_bf16 v[192:195], a[224:227], a[128:131], 0\n"
"v_mfma_f32_16x16x32_bf16 v[192:195], a[228:231], a[132:135], v[192:195]\n"
"v_mfma_f32_16x16x32_bf16 v[192:195], a[232:235], a[136:139], v[192:195]\n"
"v_mfma_f32_16x16x32_bf16 v[192:195], a[236:239], a[140:143], v[192:195]\n"
"v_mfma_f32_16x16x32_bf16 v[196:199], a[224:227], a[144:147], 0\n"
"v_mfma_f32_16x16x32_bf16 v[196:199], a[228:231], a[148:151], v[196:199]\n"
"v_mfma_f32_16x16x32_bf16 v[196:199], a[232:235], a[152:155], v[196:199]\n"
"v_mfma_f32_16x16x32_bf16 v[196:199], a[236:239], a[156:159], v[196:199]\n"
"v_mfma_f32_16x16x32_bf16 v[200:203], a[224:227], a[160:163], 0\n"
"v_mfma_f32_16x16x32_bf16 v[200:203], a[228:231], a[164:167], v[200:203]\n"
"v_mfma_f32_16x16x32_bf16 v[200:203], a[232:235], a[168:171], v[200:203]\n"
"v_mfma_f32_16x16x32_bf16 v[200:203], a[236:239], a[172:175], v[200:203]\n"
"v_mfma_f32_16x16x32_bf16 v[204:207], a[224:227], a[176:179], 0\n"
"v_mfma_f32_16x16x32_bf16 v[204:207], a[228:231], a[180:183], v[204:207]\n"
"v_mfma_f32_16x16x32_bf16 v[204:207], a[232:235], a[184:187], v[204:207]\n"
"v_mfma_f32_16x16x32_bf16 v[204:207], a[236:239], a[188:191], v[204:207]\n"
"v_max3_f32 v128, v128, v192, v193\n"
"v_max3_f32 v128, v128, v194, v195\n"
"v_max3_f32 v130, v130, v196, v197\n"
"v_max3_f32 v130, v130, v198, v199\n"
"v_max3_f32 v132, v132, v200, v201\n"
"v_max3_f32 v132, v132, v202, v203\n"
"s_nop 1\n"
"v_max3_f32 v134, v134, v204, v205\n"
"v_max3_f32 v134, v134, v206, v207\n"
"ds_read_b128 a[240:243], v224 offset:16384\n"
"ds_read_b128 a[244:247], v225 offset:16384\n"
"ds_read_b128 a[248:251], v226 offset:16384\n"
"ds_read_b128 a[252:255], v227 offset:16384\n"
"s_waitcnt lgkmcnt(0)\n"
"v_mfma_f32_16x16x32_bf16 v[192:195], a[240:243], a[128:131], 0\n"
"v_mfma_f32_16x16x32_bf16 v[192:195], a[244:247], a[132:135], v[192:195]\n"
"v_mfma_f32_16x16x32_bf16 v[192:195], a[248:251], a[136:139], v[192:195]\n"
"v_mfma_f32_16x16x32_bf16 v[192:195], a[252:255], a[140:143], v[192:195]\n"
"v_mfma_f32_16x16x32_bf16 v[196:199], a[240:243], a[144:147], 0\n"
"v_mfma_f32_16x16x32_bf16 v[196:199], a[244:247], a[148:151], v[196:199]\n"
"v_mfma_f32_16x16x32_bf16 v[196:199], a[248:251], a[152:155], v[196:199]\n"
"v_mfma_f32_16x16x32_bf16 v[196:199], a[252:255], a[156:159], v[196:199]\n"
"v_mfma_f32_16x16x32_bf16 v[200:203], a[240:243], a[160:163], 0\n"
"v_mfma_f32_16x16x32_bf16 v[200:203], a[244:247], a[164:167], v[200:203]\n"
"v_mfma_f32_16x16x32_bf16 v[200:203], a[248:251], a[168:171], v[200:203]\n"
"v_mfma_f32_16x16x32_bf16 v[200:203], a[252:255], a[172:175], v[200:203]\n"
"v_mfma_f32_16x16x32_bf16 v[204:207], a[240:243], a[176:179], 0\n"
"v_mfma_f32_16x16x32_bf16 v[204:207], a[244:247], a[180:183], v[204:207]\n"
"v_mfma_f32_16x16x32_bf16 v[204:207], a[248:251], a[184:187], v[204:207]\n"
"v_mfma_f32_16x16x32_bf16 v[204:207], a[252:255], a[188:191], v[204:207]\n"
"v_max3_f32 v128, v128, v192, v193\n"
"v_max3_f32 v128, v128, v194, v195\n"
"v_max3_f32 v130, v130, v196, v197\n"
"v_max3_f32 v130, v130, v198, v199\n"
"v_max3_f32 v132, v132, v200, v201\n"
"v_max3_f32 v132, v132, v202, v203\n"
"s_nop 1\n"
"v_max3_f32 v134, v134, v204, v205\n"
"v_max3_f32 v134, v134, v206, v207\n"
"ds_read_b128 a[224:227], v224 offset:32768\n"
"ds_read_b128 a[228:231], v225 offset:32768\n"
"ds_read_b128 a[232:235], v226 offset:32768\n"
"ds_read_b128 a[236:239], v227 offset:32768\n"
"s_waitcnt lgkmcnt(0)\n"
"v_mfma_f32_16x16x32_bf16 v[192:195], a[224:227], a[128:131], 0\n"
"v_mfma_f32_16x16x32_bf16 v[192:195], a[228:231], a[132:135], v[192:195]\n"
"v_mfma_f32_16x16x32_bf16 v[192:195], a[232:235], a[136:139], v[192:195]\n"
"v_mfma_f32_16x16x32_bf16 v[192:195], a[236:239], a[140:143], v[192:195]\n"
"v_mfma_f32_16x16x32_bf16 v[196:199], a[224:227], a[144:147], 0\n"
"v_mfma_f32_16x16x32_bf16 v[196:199], a[228:231], a[148:151], v[196:199]\n"
"v_mfma_f32_16x16x32_bf16 v[196:199], a[232:235], a[152:155], v[196:199]\n"
"v_mfma_f32_16x16x32_bf16 v[196:199], a[236:239], a[156:159], v[196:199]\n"
"v_mfma_f32_16x16x32_bf16 v[200:203], a[224:227], a[160:163], 0\n"
"v_mfma_f32_16x16x32_bf16 v[200:203], a[228:231], a[164:167], v[200:203]\n"
"v_mfma_f32_16x16x32_bf16 v[200:203], a[232:235], a[168:171], v[200:203]\n"
"v_mfma_f32_16x16x32_bf16 v[200:203], a[236:239], a[172:175], v[200:203]\n"
"v_mfma_f32_16x16x32_bf16 v[204:207], a[224:227], a[176:179], 0\n"
"v_mfma_f32_16x16x32_bf16 v[204:207], a[228:231], a[180:183], v[204:207]\n"
"v_mfma_f32_16x16x32_bf16 v[204:207], a[232:235], a[184:187], v[204:207]\n"
"v_mfma_f32_16x16x32_bf16 v[204:207], a[236:239], a[188:191], v[204:207]\n"
"v_max3_f32 v128, v128, v192, v193\n"
"v_max3_f32 v128, v128, v194, v195\n"
"v_max3_f32 v130, v130, v196, v197\n"
"v_max3_f32 v130, v130, v198, v199\n"
"v_max3_f32 v132, v132, v200, v201\n"
"v_max3_f32 v132, v132, v202, v203\n"
"s_nop 1\n"
"v_max3_f32 v134, v134, v204, v205\n"
"v_max3_f32 v134, v134, v206, v207\n"
"ds_read_b128 a[240:243], v224 offset:49152\n"
"ds_read_b128 a[244:247], v225 offset:49152\n"
"ds_read_b128 a[248:251], v226 offset:49152\n"
"ds_read_b128 a[252:255], v227 offset:49152\n"
"s_waitcnt lgkmcnt(0)\n"
"v_mfma_f32_16x16x32_bf16 v[192:195], a[240:243], a[128:131], 0\n"
"v_mfma_f32_16x16x32_bf16 v[192:195], a[244:247], a[132:135], v[192:195]\n"
"v_mfma_f32_16x16x32_bf16 v[192:195], a[248:251], a[136:139], v[192:195]\n"
"v_mfma_f32_16x16x32_bf16 v[192:195], a[252:255], a[140:143], v[192:195]\n"
"v_mfma_f32_16x16x32_bf16 v[196:199], a[240:243], a[144:147], 0\n"
"v_mfma_f32_16x16x32_bf16 v[196:199], a[244:247], a[148:151], v[196:199]\n"
"v_mfma_f32_16x16x32_bf16 v[196:199], a[248:251], a[152:155], v[196:199]\n"
"v_mfma_f32_16x16x32_bf16 v[196:199], a[252:255], a[156:159], v[196:199]\n"
"v_mfma_f32_16x16x32_bf16 v[200:203], a[240:243], a[160:163], 0\n"
"v_mfma_f32_16x16x32_bf16 v[200:203], a[244:247], a[164:167], v[200:203]\n"
"v_mfma_f32_16x16x32_bf16 v[200:203], a[248:251], a[168:171], v[200:203]\n"
"v_mfma_f32_16x16x32_bf16 v[200:203], a[252:255], a[172:175], v[200:203]\n"
"v_mfma_f32_16x16x32_bf16 v[204:207], a[240:243], a[176:179], 0\n"
"v_mfma_f32_16x16x32_bf16 v[204:207], a[244:247], a[180:183], v[204:207]\n"
"v_mfma_f32_16x16x32_bf16 v[204:207], a[248:251], a[184:187], v[204:207]\n"
"v_mfma_f32_16x16x32_bf16 v[204:207], a[252:255], a[188:191], v[204:207]\n"
"v_max3_f32 v128, v128, v192, v193\n"
"v_max3_f32 v128, v128, v194, v195\n"
"v_max3_f32 v130, v130, v196, v197\n"
"v_max3_f32 v130, v130, v198, v199\n"
"v_max3_f32 v132, v132, v200, v201\n"
"v_max3_f32 v132, v132, v202, v203\n"
"s_nop 1\n"
"v_max3_f32 v134, v134, v204, v205\n"
"v_max3_f32 v134, v134, v206, v207\n"
"s_sub_u32 s47, s47, 1\n"
"s_cmp_lg_u32 s47, 0\n"
"s_cbranch_scc1 .LFL_%=\n"
"s_barrier\n"
"s_mul_i32 s44, s78, 524288\n"
"s_mul_i32 s83, s77, 4096\n"
"s_add_u32 s44, s44, s83\n"
"s_add_u32 s44, s44, -16384\n"
"s_mov_b32 s45, s44\n"
"s_add_u32 m0, s46, 0\n"
"s_add_u32 s44, s44, 16384\n"
"buffer_load_dwordx4 v236, s[36:39], s44 offen offset:0 lds\n"
"buffer_load_dwordx4 v237, s[36:39], s44 offen offset:1024 lds\n"
"buffer_load_dwordx4 v238, s[36:39], s44 offen offset:2048 lds\n"
"buffer_load_dwordx4 v239, s[36:39], s44 offen offset:3072 lds\n"
"s_add_u32 m0, s46, 4096\n"
"s_add_u32 s44, s44, 16384\n"
"buffer_load_dwordx4 v236, s[36:39], s44 offen offset:0 lds\n"
"buffer_load_dwordx4 v237, s[36:39], s44 offen offset:1024 lds\n"
"buffer_load_dwordx4 v238, s[36:39], s44 offen offset:2048 lds\n"
"buffer_load_dwordx4 v239, s[36:39], s44 offen offset:3072 lds\n"
"s_add_u32 m0, s46, 8192\n"
"s_add_u32 s44, s44, 16384\n"
"buffer_load_dwordx4 v236, s[36:39], s44 offen offset:0 lds\n"
"buffer_load_dwordx4 v237, s[36:39], s44 offen offset:1024 lds\n"
"buffer_load_dwordx4 v238, s[36:39], s44 offen offset:2048 lds\n"
"buffer_load_dwordx4 v239, s[36:39], s44 offen offset:3072 lds\n"
"s_add_u32 m0, s46, 12288\n"
"s_add_u32 s44, s44, 16384\n"
"buffer_load_dwordx4 v236, s[36:39], s44 offen offset:0 lds\n"
"buffer_load_dwordx4 v237, s[36:39], s44 offen offset:1024 lds\n"
"buffer_load_dwordx4 v238, s[36:39], s44 offen offset:2048 lds\n"
"buffer_load_dwordx4 v239, s[36:39], s44 offen offset:3072 lds\n"
"s_add_u32 m0, s46, 69632\n"
"s_add_u32 s45, s45, 16384\n"
"buffer_load_dwordx4 v240, s[40:43], s45 offen offset:0 lds\n"
"buffer_load_dwordx4 v241, s[40:43], s45 offen offset:1024 lds\n"
"buffer_load_dwordx4 v240, s[40:43], s45 offen offset:2048 lds\n"
"buffer_load_dwordx4 v241, s[40:43], s45 offen offset:3072 lds\n"
"s_add_u32 m0, s46, 73728\n"
"s_add_u32 s45, s45, 16384\n"
"buffer_load_dwordx4 v240, s[40:43], s45 offen offset:0 lds\n"
"buffer_load_dwordx4 v241, s[40:43], s45 offen offset:1024 lds\n"
"buffer_load_dwordx4 v240, s[40:43], s45 offen offset:2048 lds\n"
"buffer_load_dwordx4 v241, s[40:43], s45 offen offset:3072 lds\n"
"s_add_u32 m0, s46, 77824\n"
"s_add_u32 s45, s45, 16384\n"
"buffer_load_dwordx4 v240, s[40:43], s45 offen offset:0 lds\n"
"buffer_load_dwordx4 v241, s[40:43], s45 offen offset:1024 lds\n"
"buffer_load_dwordx4 v240, s[40:43], s45 offen offset:2048 lds\n"
"buffer_load_dwordx4 v241, s[40:43], s45 offen offset:3072 lds\n"
"v_mov_b32_e32 v129, v128\n"
"v_mov_b32_e32 v131, v130\n"
"v_mov_b32_e32 v133, v132\n"
"v_mov_b32_e32 v135, v134\n"
"v_permlane32_swap_b32 v128, v129\n"
"v_permlane32_swap_b32 v130, v131\n"
"v_permlane32_swap_b32 v132, v133\n"
"v_permlane32_swap_b32 v134, v135\n"
"v_max_f32_e32 v128, v128, v129\n"
"v_max_f32_e32 v130, v130, v131\n"
"v_max_f32_e32 v132, v132, v133\n"
"v_max_f32_e32 v134, v134, v135\n"
"v_mov_b32_e32 v129, v128\n"
"v_mov_b32_e32 v131, v130\n"
"v_mov_b32_e32 v133, v132\n"
"v_mov_b32_e32 v135, v134\n"
"v_permlane16_swap_b32 v128, v129\n"
"v_permlane16_swap_b32 v130, v131\n"
"v_permlane16_swap_b32 v132, v133\n"
"v_permlane16_swap_b32 v134, v135\n"
"v_max_f32_e32 v128, v128, v129\n"
"v_max_f32_e32 v130, v130, v131\n"
"v_max_f32_e32 v132, v132, v133\n"
"v_max_f32_e32 v134, v134, v135\n"
"v_sub_f32_e32 v208, 0, v128\n"
"v_sub_f32_e32 v212, 0, v130\n"
"v_sub_f32_e32 v216, 0, v132\n"
"v_sub_f32_e32 v220, 0, v134\n"
"v_sub_f32_e32 v209, 0, v128\n"
"v_sub_f32_e32 v213, 0, v130\n"
"v_sub_f32_e32 v217, 0, v132\n"
"v_sub_f32_e32 v221, 0, v134\n"
"v_sub_f32_e32 v210, 0, v128\n"
"v_sub_f32_e32 v214, 0, v130\n"
"v_sub_f32_e32 v218, 0, v132\n"
"v_sub_f32_e32 v222, 0, v134\n"
"v_sub_f32_e32 v211, 0, v128\n"
"v_sub_f32_e32 v215, 0, v130\n"
"v_sub_f32_e32 v219, 0, v132\n"
"v_sub_f32_e32 v223, 0, v134\n"
"s_waitcnt vmcnt(0)\n"
"s_barrier\n"
"ds_read_b128 a[224:227], v224 offset:0\n"
"ds_read_b128 a[228:231], v225 offset:0\n"
"ds_read_b128 a[232:235], v226 offset:0\n"
"ds_read_b128 a[236:239], v227 offset:0\n"
"s_waitcnt lgkmcnt(0)\n"
"v_mfma_f32_16x16x32_bf16 v[192:195], a[224:227], a[128:131], v[208:211]\n"
"v_mfma_f32_16x16x32_bf16 v[192:195], a[228:231], a[132:135], v[192:195]\n"
"v_mfma_f32_16x16x32_bf16 v[192:195], a[232:235], a[136:139], v[192:195]\n"
"v_mfma_f32_16x16x32_bf16 v[192:195], a[236:239], a[140:143], v[192:195]\n"
"v_mfma_f32_16x16x32_bf16 v[196:199], a[224:227], a[144:147], v[212:215]\n"
"v_mfma_f32_16x16x32_bf16 v[196:199], a[228:231], a[148:151], v[196:199]\n"
"v_mfma_f32_16x16x32_bf16 v[196:199], a[232:235], a[152:155], v[196:199]\n"
"v_mfma_f32_16x16x32_bf16 v[196:199], a[236:239], a[156:159], v[196:199]\n"
"v_mfma_f32_16x16x32_bf16 v[200:203], a[224:227], a[160:163], v[216:219]\n"
"v_mfma_f32_16x16x32_bf16 v[200:203], a[228:231], a[164:167], v[200:203]\n"
"v_mfma_f32_16x16x32_bf16 v[200:203], a[232:235], a[168:171], v[200:203]\n"
"v_mfma_f32_16x16x32_bf16 v[200:203], a[236:239], a[172:175], v[200:203]\n"
"v_mfma_f32_16x16x32_bf16 v[204:207], a[224:227], a[176:179], v[220:223]\n"
"v_mfma_f32_16x16x32_bf16 v[204:207], a[228:231], a[180:183], v[204:207]\n"
"v_mfma_f32_16x16x32_bf16 v[204:207], a[232:235], a[184:187], v[204:207]\n"
"v_mfma_f32_16x16x32_bf16 v[204:207], a[236:239], a[188:191], v[204:207]\n"
"s_nop 7\n"
"s_branch .LATT_%=\n"
".LLATEEND_%=:\n"
".LOTH_%=:\n"
"s_cbranch_execz .LPGR_%=\n"
"s_cmp_eq_u32 s47, 2\n"
"s_cbranch_scc1 .LIDLE2_%=\n"
"s_mov_b32 s100, 57344\n"
"s_mov_b32 s101, 43008\n"
"s_waitcnt lgkmcnt(6)\n"
"v_mfma_f32_16x16x32_bf16 v[0:3], v[64:67], a[0:3], 0\n"
"s_waitcnt vmcnt(9)\n"
"ds_read_b64_tr_b16 v[96:97], v224 offset:0\n"
"ds_read_b128 a[32:35], v230 offset:2048\n"
"v_mfma_f32_16x16x32_bf16 v[0:3], v[68:71], a[16:19], v[0:3]\n"
"s_add_u32 s80, s84, 688128\n"
"s_waitcnt lgkmcnt(6)\n"
"ds_read_b128 a[36:39], v230 offset:18432\n"
"v_mfma_f32_16x16x32_bf16 v[4:7], v[68:71], a[20:23], 0\n"
"s_add_u32 m0, s46, 0\n"
"ds_read_b64_tr_b16 v[98:99], v224 offset:2048\n"
"ds_read_b128 a[40:43], v230 offset:34816\n"
"v_mfma_f32_16x16x32_bf16 v[4:7], v[64:67], a[4:7], v[4:7]\n"
"s_waitcnt lgkmcnt(7)\n"
"buffer_load_dwordx4 v228, s[52:55], s80 offen lds\n"
"v_mfma_f32_16x16x32_bf16 v[8:11], v[64:67], a[8:11], 0\n"
"s_add_u32 s80, s80, s100\n"
"ds_read_b64_tr_b16 v[100:101], v224 offset:4096\n"
"ds_read_b128 a[44:47], v230 offset:51200\n"
"v_mfma_f32_16x16x32_bf16 v[8:11], v[68:71], a[24:27], v[8:11]\n"
"s_addk_i32 m0, 1024\n"
"s_waitcnt lgkmcnt(7)\n"
"ds_read_b128 a[48:51], v230 offset:3072\n"
"v_mfma_f32_16x16x32_bf16 v[12:15], v[68:71], a[28:31], 0\n"
"ds_read_b64_tr_b16 v[102:103], v224 offset:6144\n"
"ds_read_b128 a[52:55], v230 offset:19456\n"
"v_mfma_f32_16x16x32_bf16 v[12:15], v[64:67], a[12:15], v[12:15]\n"
"s_waitcnt lgkmcnt(14)\n"
"buffer_load_dwordx4 v228, s[52:55], s80 offen lds\n"
"v_mfma_f32_16x16x32_bf16 v[28:31], v[72:75], a[12:15], 0\n"
"s_sub_u32 s80, s80, s101\n"
"ds_read_b64_tr_b16 v[104:105], v225 offset:0\n"
"ds_read_b128 a[56:59], v230 offset:35840\n"
"v_mfma_f32_16x16x32_bf16 v[28:31], v[76:79], a[28:31], v[28:31]\n"
"s_addk_i32 m0, 1024\n"
"ds_read_b128 a[60:63], v230 offset:52224\n"
"ds_read_b128 a[64:67], v230 offset:4096\n"
"v_mfma_f32_16x16x32_bf16 v[24:27], v[76:79], a[24:27], 0\n"
"ds_read_b64_tr_b16 v[106:107], v225 offset:2048\n"
"ds_read_b128 a[68:71], v230 offset:20480\n"
"v_mfma_f32_16x16x32_bf16 v[24:27], v[72:75], a[8:11], v[24:27]\n"
"buffer_load_dwordx4 v228, s[52:55], s80 offen lds\n"
"v_mfma_f32_16x16x32_bf16 v[20:23], v[72:75], a[4:7], 0\n"
"s_add_u32 s80, s80, s100\n"
"ds_read_b64_tr_b16 v[108:109], v225 offset:4096\n"
"ds_read_b128 a[72:75], v230 offset:36864\n"
"v_mfma_f32_16x16x32_bf16 v[20:23], v[76:79], a[20:23], v[20:23]\n"
"s_addk_i32 m0, 1024\n"
"ds_read_b128 a[76:79], v230 offset:53248\n"
"ds_read_b128 a[80:83], v230 offset:5120\n"
"v_mfma_f32_16x16x32_bf16 v[16:19], v[76:79], a[16:19], 0\n"
"ds_read_b64_tr_b16 v[110:111], v225 offset:6144\n"
"ds_read_b128 a[84:87], v230 offset:21504\n"
"v_mfma_f32_16x16x32_bf16 v[16:19], v[72:75], a[0:3], v[16:19]\n"
"s_waitcnt lgkmcnt(14)\n"
"buffer_load_dwordx4 v228, s[52:55], s80 offen lds\n"
"v_mfma_f32_16x16x32_bf16 v[32:35], v[80:83], a[0:3], 0\n"
"s_add_u32 s80, s80, s101\n"
"ds_read_b64_tr_b16 v[112:113], v226 offset:0\n"
"ds_read_b128 a[88:91], v230 offset:37888\n"
"v_mfma_f32_16x16x32_bf16 v[32:35], v[84:87], a[16:19], v[32:35]\n"
"s_addk_i32 m0, 1024\n"
"ds_read_b128 a[92:95], v230 offset:54272\n"
"ds_read_b128 a[96:99], v230 offset:6144\n"
"v_mfma_f32_16x16x32_bf16 v[36:39], v[84:87], a[20:23], 0\n"
"ds_read_b64_tr_b16 v[114:115], v226 offset:2048\n"
"ds_read_b128 a[100:103], v230 offset:22528\n"
"v_mfma_f32_16x16x32_bf16 v[36:39], v[80:83], a[4:7], v[36:39]\n"
"buffer_load_dwordx4 v228, s[52:55], s80 offen lds\n"
"v_mfma_f32_16x16x32_bf16 v[40:43], v[80:83], a[8:11], 0\n"
"s_add_u32 s80, s80, s100\n"
"ds_read_b64_tr_b16 v[116:117], v226 offset:4096\n"
"ds_read_b128 a[104:107], v230 offset:38912\n"
"v_mfma_f32_16x16x32_bf16 v[40:43], v[84:87], a[24:27], v[40:43]\n"
"s_addk_i32 m0, 1024\n"
"ds_read_b128 a[108:111], v230 offset:55296\n"
"ds_read_b128 a[112:115], v230 offset:7168\n"
"v_mfma_f32_16x16x32_bf16 v[44:47], v[84:87], a[28:31], 0\n"
"ds_read_b64_tr_b16 v[118:119], v226 offset:6144\n"
"ds_read_b128 a[116:119], v230 offset:23552\n"
"v_mfma_f32_16x16x32_bf16 v[44:47], v[80:83], a[12:15], v[44:47]\n"
"s_waitcnt lgkmcnt(14)\n"
"buffer_load_dwordx4 v228, s[52:55], s80 offen lds\n"
"v_mfma_f32_16x16x32_bf16 v[60:63], v[88:91], a[12:15], 0\n"
"s_sub_u32 s80, s80, s101\n"
"ds_read_b64_tr_b16 v[120:121], v227 offset:0\n"
"ds_read_b128 a[120:123], v230 offset:39936\n"
"v_mfma_f32_16x16x32_bf16 v[60:63], v[92:95], a[28:31], v[60:63]\n"
"s_addk_i32 m0, 1024\n"
"ds_read_b128 a[124:127], v230 offset:56320\n"
"ds_read_b128 a[128:131], v230 offset:8192\n"
"v_mfma_f32_16x16x32_bf16 v[56:59], v[92:95], a[24:27], 0\n"
"ds_read_b64_tr_b16 v[122:123], v227 offset:2048\n"
"ds_read_b128 a[132:135], v230 offset:24576\n"
"v_mfma_f32_16x16x32_bf16 v[56:59], v[88:91], a[8:11], v[56:59]\n"
"buffer_load_dwordx4 v228, s[52:55], s80 offen lds\n"
"v_mfma_f32_16x16x32_bf16 v[52:55], v[88:91], a[4:7], 0\n"
"s_add_u32 s80, s80, s100\n"
"ds_read_b64_tr_b16 v[124:125], v227 offset:4096\n"
"ds_read_b128 a[136:139], v230 offset:40960\n"
"v_mfma_f32_16x16x32_bf16 v[52:55], v[92:95], a[20:23], v[52:55]\n"
"s_addk_i32 m0, 1024\n"
"ds_read_b128 a[140:143], v230 offset:57344\n"
"ds_read_b128 a[144:147], v230 offset:9216\n"
"v_mfma_f32_16x16x32_bf16 v[48:51], v[92:95], a[16:19], 0\n"
"ds_read_b64_tr_b16 v[126:127], v227 offset:6144\n"
"ds_read_b128 a[148:151], v230 offset:25600\n"
"v_mfma_f32_16x16x32_bf16 v[48:51], v[88:91], a[0:3], v[48:51]\n"
"s_waitcnt lgkmcnt(14)\n"
"buffer_load_dwordx4 v228, s[52:55], s80 offen lds\n"
"v_mfma_f32_16x16x32_bf16 v[0:3], v[96:99], a[32:35], v[0:3]\n"
"s_add_u32 s80, s84, 917504\n"
"s_waitcnt lgkmcnt(1)\n"
"ds_read_b128 a[152:155], v230 offset:41984\n"
"v_mfma_f32_16x16x32_bf16 v[0:3], v[100:103], a[48:51], v[0:3]\n"
"s_add_u32 m0, s81, 0\n"
"ds_read_b128 a[156:159], v230 offset:58368\n"
"ds_read_b128 a[160:163], v230 offset:10240\n"
"v_mfma_f32_16x16x32_bf16 v[4:7], v[100:103], a[52:55], v[4:7]\n"
"ds_read_b128 a[164:167], v230 offset:26624\n"
"ds_read_b128 a[168:171], v230 offset:43008\n"
"v_mfma_f32_16x16x32_bf16 v[4:7], v[96:99], a[36:39], v[4:7]\n"
"buffer_load_dwordx4 v228, s[52:55], s80 offen lds\n"
"v_mfma_f32_16x16x32_bf16 v[8:11], v[96:99], a[40:43], v[8:11]\n"
"s_add_u32 s80, s80, s100\n"
"ds_read_b128 a[172:175], v230 offset:59392\n"
"ds_read_b128 a[176:179], v230 offset:11264\n"
"v_mfma_f32_16x16x32_bf16 v[8:11], v[100:103], a[56:59], v[8:11]\n"
"s_addk_i32 m0, 1024\n"
"ds_read_b128 a[180:183], v230 offset:27648\n"
"ds_read_b128 a[184:187], v230 offset:44032\n"
"v_mfma_f32_16x16x32_bf16 v[12:15], v[100:103], a[60:63], v[12:15]\n"
"ds_read_b128 a[188:191], v230 offset:60416\n"
"ds_read_b128 a[192:195], v230 offset:12288\n"
"v_mfma_f32_16x16x32_bf16 v[12:15], v[96:99], a[44:47], v[12:15]\n"
"s_waitcnt lgkmcnt(14)\n"
"buffer_load_dwordx4 v228, s[52:55], s80 offen lds\n"
"v_mfma_f32_16x16x32_bf16 v[28:31], v[104:107], a[44:47], v[28:31]\n"
"s_sub_u32 s80, s80, s101\n"
"ds_read_b128 a[196:199], v230 offset:28672\n"
"ds_read_b128 a[200:203], v230 offset:45056\n"
"v_mfma_f32_16x16x32_bf16 v[28:31], v[108:111], a[60:63], v[28:31]\n"
"s_addk_i32 m0, 1024\n"
"ds_read_b128 a[204:207], v230 offset:61440\n"
"ds_read_b128 a[208:211], v230 offset:13312\n"
"v_mfma_f32_16x16x32_bf16 v[24:27], v[108:111], a[56:59], v[24:27]\n"
"ds_read_b128 a[212:215], v230 offset:29696\n"
"ds_read_b128 a[216:219], v230 offset:46080\n"
"v_mfma_f32_16x16x32_bf16 v[24:27], v[104:107], a[40:43], v[24:27]\n"
"buffer_load_dwordx4 v228, s[52:55], s80 offen lds\n"
"v_mfma_f32_16x16x32_bf16 v[20:23], v[104:107], a[36:39], v[20:23]\n"
"s_add_u32 s80, s80, s100\n"
"ds_read_b128 a[220:223], v230 offset:62464\n"
"ds_read_b128 a[224:227], v230 offset:14336\n"
"v_mfma_f32_16x16x32_bf16 v[20:23], v[108:111], a[52:55], v[20:23]\n"
"s_addk_i32 m0, 1024\n"
"ds_read_b128 a[228:231], v230 offset:30720\n"
"ds_read_b128 a[232:235], v230 offset:47104\n"
"v_mfma_f32_16x16x32_bf16 v[16:19], v[108:111], a[48:51], v[16:19]\n"
"ds_read_b128 a[236:239], v230 offset:63488\n"
"ds_read_b128 a[240:243], v230 offset:15360\n"
"v_mfma_f32_16x16x32_bf16 v[16:19], v[104:107], a[32:35], v[16:19]\n"
"s_waitcnt lgkmcnt(14)\n"
"buffer_load_dwordx4 v228, s[52:55], s80 offen lds\n"
"v_mfma_f32_16x16x32_bf16 v[32:35], v[112:115], a[32:35], v[32:35]\n"
"s_waitcnt vmcnt(13)\n"
"ds_read_b64_tr_b16 v[128:129], v246 offset:8192\n"
"ds_read_b128 a[244:247], v230 offset:31744\n"
"v_mfma_f32_16x16x32_bf16 v[32:35], v[116:119], a[48:51], v[32:35]\n"
"s_add_u32 s80, s80, s101\n"
"ds_read_b128 a[248:251], v230 offset:48128\n"
"ds_read_b128 a[252:255], v230 offset:64512\n"
"v_mfma_f32_16x16x32_bf16 v[36:39], v[116:119], a[52:55], v[36:39]\n"
"s_addk_i32 m0, 1024\n"
"ds_read_b64_tr_b16 v[130:131], v246 offset:10240\n"
"v_mfma_f32_16x16x32_bf16 v[36:39], v[112:115], a[36:39], v[36:39]\n"
"buffer_load_dwordx4 v228, s[52:55], s80 offen lds\n"
"v_mfma_f32_16x16x32_bf16 v[40:43], v[112:115], a[40:43], v[40:43]\n"
"s_add_u32 s80, s80, s100\n"
"ds_read_b64_tr_b16 v[132:133], v246 offset:12288\n"
"v_mfma_f32_16x16x32_bf16 v[40:43], v[116:119], a[56:59], v[40:43]\n"
"s_addk_i32 m0, 1024\n"
"v_mfma_f32_16x16x32_bf16 v[44:47], v[116:119], a[60:63], v[44:47]\n"
"ds_read_b64_tr_b16 v[134:135], v246 offset:14336\n"
"v_mfma_f32_16x16x32_bf16 v[44:47], v[112:115], a[44:47], v[44:47]\n"
"s_waitcnt lgkmcnt(14)\n"
"buffer_load_dwordx4 v228, s[52:55], s80 offen lds\n"
"v_mfma_f32_16x16x32_bf16 v[60:63], v[120:123], a[44:47], v[60:63]\n"
"s_sub_u32 s80, s80, s101\n"
"ds_read_b64_tr_b16 v[136:137], v247 offset:8192\n"
"v_mfma_f32_16x16x32_bf16 v[60:63], v[124:127], a[60:63], v[60:63]\n"
"s_addk_i32 m0, 1024\n"
"v_mfma_f32_16x16x32_bf16 v[56:59], v[124:127], a[56:59], v[56:59]\n"
"ds_read_b64_tr_b16 v[138:139], v247 offset:10240\n"
"v_mfma_f32_16x16x32_bf16 v[56:59], v[120:123], a[40:43], v[56:59]\n"
"buffer_load_dwordx4 v228, s[52:55], s80 offen lds\n"
"v_mfma_f32_16x16x32_bf16 v[52:55], v[120:123], a[36:39], v[52:55]\n"
"s_add_u32 s80, s80, s100\n"
"ds_read_b64_tr_b16 v[140:141], v247 offset:12288\n"
"v_mfma_f32_16x16x32_bf16 v[52:55], v[124:127], a[52:55], v[52:55]\n"
"s_addk_i32 m0, 1024\n"
"v_mfma_f32_16x16x32_bf16 v[48:51], v[124:127], a[48:51], v[48:51]\n"
"ds_read_b64_tr_b16 v[142:143], v247 offset:14336\n"
"v_mfma_f32_16x16x32_bf16 v[48:51], v[120:123], a[32:35], v[48:51]\n"
"buffer_load_dwordx4 v228, s[52:55], s80 offen lds\n"
"s_waitcnt lgkmcnt(0)\n"
"s_barrier\n"
"s_cmp_lt_u32 s77, 2\n"
"s_cselect_b64 s[72:73], s[6:7], s[8:9]\n"
"s_and_b32 s94, s77, 1\n"
"s_mul_i32 s83, s78, 524288\n"
"s_mul_i32 s95, s35, 4096\n"
"s_add_u32 s83, s83, s95\n"
"s_mul_i32 s95, s94, 2048\n"
"s_add_u32 s83, s83, s95\n"
"s_add_u32 s72, s72, s83\n"
"s_addc_u32 s73, s73, 0\n"
"s_mov_b32 s74, 460800\n"
"s_mov_b32 s75, 0x20000\n"
"s_mov_b32 s76, -65536\n"
"v_mfma_f32_16x16x32_bf16 v[0:3], v[128:131], a[64:67], v[0:3]\n"
"s_add_u32 s80, s84, 1146880\n"
"ds_read_b64_tr_b16 v[144:145], v248 offset:8192\n"
"s_mov_b64 s[60:61], s[16:17]\n"
"v_mfma_f32_16x16x32_bf16 v[0:3], v[132:135], a[80:83], v[0:3]\n"
"s_add_u32 m0, s46, 65536\n"
"s_mov_b32 s62, 1835008\n"
"v_mfma_f32_16x16x32_bf16 v[4:7], v[132:135], a[84:87], v[4:7]\n"
"ds_read_b64_tr_b16 v[146:147], v248 offset:10240\n"
"v_mfma_f32_16x16x32_bf16 v[4:7], v[128:131], a[68:71], v[4:7]\n"
"buffer_load_dwordx4 v228, s[52:55], s80 offen lds\n"
"v_mfma_f32_16x16x32_bf16 v[8:11], v[128:131], a[72:75], v[8:11]\n"
"s_add_u32 s80, s80, s100\n"
"ds_read_b64_tr_b16 v[148:149], v248 offset:12288\n"
"s_mov_b32 s63, 0x20000\n"
"v_mfma_f32_16x16x32_bf16 v[8:11], v[132:135], a[88:91], v[8:11]\n"
"s_addk_i32 m0, 1024\n"
"s_mul_i32 s34, s35, 16384\n"
"v_mfma_f32_16x16x32_bf16 v[12:15], v[132:135], a[92:95], v[12:15]\n"
"s_mul_i32 s83, s77, 4096\n"
"ds_read_b64_tr_b16 v[150:151], v248 offset:14336\n"
"v_mfma_f32_16x16x32_bf16 v[12:15], v[128:131], a[76:79], v[12:15]\n"
"buffer_load_dwordx4 v228, s[52:55], s80 offen lds\n"
"v_mfma_f32_16x16x32_bf16 v[28:31], v[136:139], a[76:79], v[28:31]\n"
"s_sub_u32 s80, s80, s101\n"
"ds_read_b64_tr_b16 v[152:153], v249 offset:8192\n"
"s_add_u32 s34, s34, s83\n"
"v_mfma_f32_16x16x32_bf16 v[28:31], v[140:143], a[92:95], v[28:31]\n"
"s_addk_i32 m0, 1024\n"
"v_mfma_f32_16x16x32_bf16 v[24:27], v[140:143], a[88:91], v[24:27]\n"
"s_mul_i32 s83, s23, 32768\n"
"ds_read_b64_tr_b16 v[154:155], v249 offset:10240\n"
"v_mfma_f32_16x16x32_bf16 v[24:27], v[136:139], a[72:75], v[24:27]\n"
"buffer_load_dwordx4 v228, s[52:55], s80 offen lds\n"
"v_mfma_f32_16x16x32_bf16 v[20:23], v[136:139], a[68:71], v[20:23]\n"
"s_add_u32 s80, s80, s100\n"
"ds_read_b64_tr_b16 v[156:157], v249 offset:12288\n"
"s_add_u32 s34, s34, s83\n"
"v_mfma_f32_16x16x32_bf16 v[20:23], v[140:143], a[84:87], v[20:23]\n"
"s_addk_i32 m0, 1024\n"
"v_lshlrev_b32_e32 v235, 2, v255\n"
"v_mfma_f32_16x16x32_bf16 v[16:19], v[140:143], a[80:83], v[16:19]\n"
"ds_read_b64_tr_b16 v[158:159], v249 offset:14336\n"
"v_mfma_f32_16x16x32_bf16 v[16:19], v[136:139], a[64:67], v[16:19]\n"
"s_waitcnt lgkmcnt(4)\n"
"buffer_load_dwordx4 v228, s[52:55], s80 offen lds\n"
"v_mfma_f32_16x16x32_bf16 v[32:35], v[144:147], a[64:67], v[32:35]\n"
"s_waitcnt vmcnt(12)\n"
"ds_read_b64_tr_b16 v[128:129], v246 offset:0\n"
"v_lshlrev_b32_e32 v234, 1, v255\n"
"v_mfma_f32_16x16x32_bf16 v[32:35], v[148:151], a[80:83], v[32:35]\n"
"s_add_u32 s80, s80, s101\n"
"v_add_u32_e32 v250, 65536, v246\n"
"v_mfma_f32_16x16x32_bf16 v[36:39], v[148:151], a[84:87], v[36:39]\n"
"s_addk_i32 m0, 1024\n"
"ds_read_b64_tr_b16 v[130:131], v246 offset:2048\n"
"v_add_u32_e32 v251, 65536, v247\n"
"v_mfma_f32_16x16x32_bf16 v[36:39], v[144:147], a[68:71], v[36:39]\n"
"buffer_load_dwordx4 v228, s[52:55], s80 offen lds\n"
"v_mfma_f32_16x16x32_bf16 v[40:43], v[144:147], a[72:75], v[40:43]\n"
"s_add_u32 s80, s80, s100\n"
"ds_read_b64_tr_b16 v[132:133], v246 offset:4096\n"
"v_add_u32_e32 v252, 65536, v248\n"
"v_mfma_f32_16x16x32_bf16 v[40:43], v[148:151], a[88:91], v[40:43]\n"
"s_addk_i32 m0, 1024\n"
"v_add_u32_e32 v253, 65536, v249\n"
"v_mfma_f32_16x16x32_bf16 v[44:47], v[148:151], a[92:95], v[44:47]\n"
"ds_read_b64_tr_b16 v[134:135], v246 offset:6144\n"
"v_mfma_f32_16x16x32_bf16 v[44:47], v[144:147], a[76:79], v[44:47]\n"
"s_waitcnt lgkmcnt(4)\n"
"buffer_load_dwordx4 v228, s[52:55], s80 offen lds\n"
"v_mfma_f32_16x16x32_bf16 v[60:63], v[152:155], a[76:79], v[60:63]\n"
"s_sub_u32 s80, s80, s101\n"
"ds_read_b64_tr_b16 v[136:137], v247 offset:0\n"
"s_and_b32 s89, s79, 1\n"
"v_mfma_f32_16x16x32_bf16 v[60:63], v[156:159], a[92:95], v[60:63]\n"
"s_addk_i32 m0, 1024\n"
"s_mul_i32 s85, s78, 1024\n"
"v_mfma_f32_16x16x32_bf16 v[56:59], v[156:159], a[88:91], v[56:59]\n"
"s_mul_i32 s83, s77, 256\n"
"ds_read_b64_tr_b16 v[138:139], v247 offset:2048\n"
"v_mfma_f32_16x16x32_bf16 v[56:59], v[152:155], a[72:75], v[56:59]\n"
"buffer_load_dwordx4 v228, s[52:55], s80 offen lds\n"
"v_mfma_f32_16x16x32_bf16 v[52:55], v[152:155], a[68:71], v[52:55]\n"
"s_add_u32 s80, s80, s100\n"
"ds_read_b64_tr_b16 v[140:141], v247 offset:4096\n"
"s_add_u32 s85, s85, s83\n"
"v_mfma_f32_16x16x32_bf16 v[52:55], v[156:159], a[84:87], v[52:55]\n"
"s_addk_i32 m0, 1024\n"
"v_mfma_f32_16x16x32_bf16 v[48:51], v[156:159], a[80:83], v[48:51]\n"
"s_mul_i32 s83, s89, 128\n"
"ds_read_b64_tr_b16 v[142:143], v247 offset:6144\n"
"v_mfma_f32_16x16x32_bf16 v[48:51], v[152:155], a[64:67], v[48:51]\n"
"s_waitcnt lgkmcnt(4)\n"
"buffer_load_dwordx4 v228, s[52:55], s80 offen lds\n"
"v_mfma_f32_16x16x32_bf16 v[0:3], v[128:131], a[96:99], v[0:3]\n"
"s_add_u32 s80, s84, 1376256\n"
"ds_read_b64_tr_b16 v[144:145], v248 offset:0\n"
"s_add_u32 s85, s85, s83\n"
"v_mfma_f32_16x16x32_bf16 v[0:3], v[132:135], a[112:115], v[0:3]\n"
"s_add_u32 m0, s46, 8192\n"
"s_xor_b32 s86, s85, 128\n"
"v_mfma_f32_16x16x32_bf16 v[4:7], v[132:135], a[116:119], v[4:7]\n"
"ds_read_b64_tr_b16 v[146:147], v248 offset:2048\n"
"v_mfma_f32_16x16x32_bf16 v[4:7], v[128:131], a[100:103], v[4:7]\n"
"buffer_load_dwordx4 v228, s[52:55], s80 offen lds\n"
"v_mfma_f32_16x16x32_bf16 v[8:11], v[128:131], a[104:107], v[8:11]\n"
"s_add_u32 s80, s80, s100\n"
"ds_read_b64_tr_b16 v[148:149], v248 offset:4096\n"
"s_mov_b64 s[24:25], s[20:21]\n"
"v_mfma_f32_16x16x32_bf16 v[8:11], v[132:135], a[120:123], v[8:11]\n"
"s_addk_i32 m0, 1024\n"
"s_mov_b32 s26, 50855936\n"
"v_mfma_f32_16x16x32_bf16 v[12:15], v[132:135], a[124:127], v[12:15]\n"
"s_mov_b32 s27, 0x20000\n"
"ds_read_b64_tr_b16 v[150:151], v248 offset:6144\n"
"v_mfma_f32_16x16x32_bf16 v[12:15], v[128:131], a[108:111], v[12:15]\n"
"s_waitcnt lgkmcnt(4)\n"
"buffer_load_dwordx4 v228, s[52:55], s80 offen lds\n"
"v_mfma_f32_16x16x32_bf16 v[28:31], v[136:139], a[108:111], v[28:31]\n"
"s_sub_u32 s80, s80, s101\n"
"ds_read_b64_tr_b16 v[152:153], v249 offset:0\n"
"s_mul_i32 s28, s79, 8192\n"
"v_mfma_f32_16x16x32_bf16 v[28:31], v[140:143], a[124:127], v[28:31]\n"
"s_addk_i32 m0, 1024\n"
"v_mfma_f32_16x16x32_bf16 v[24:27], v[140:143], a[120:123], v[24:27]\n"
"s_mul_i32 s83, s78, 1024\n"
"ds_read_b64_tr_b16 v[154:155], v249 offset:2048\n"
"v_mfma_f32_16x16x32_bf16 v[24:27], v[136:139], a[104:107], v[24:27]\n"
"buffer_load_dwordx4 v228, s[52:55], s80 offen lds\n"
"v_mfma_f32_16x16x32_bf16 v[20:23], v[136:139], a[100:103], v[20:23]\n"
"s_add_u32 s80, s80, s100\n"
"ds_read_b64_tr_b16 v[156:157], v249 offset:4096\n"
"s_add_u32 s28, s28, s83\n"
"v_mfma_f32_16x16x32_bf16 v[20:23], v[140:143], a[116:119], v[20:23]\n"
"s_addk_i32 m0, 1024\n"
"s_mul_i32 s83, s77, 256\n"
"v_mfma_f32_16x16x32_bf16 v[16:19], v[140:143], a[112:115], v[16:19]\n"
"ds_read_b64_tr_b16 v[158:159], v249 offset:6144\n"
"v_mfma_f32_16x16x32_bf16 v[16:19], v[136:139], a[96:99], v[16:19]\n"
"s_waitcnt lgkmcnt(4)\n"
"buffer_load_dwordx4 v228, s[52:55], s80 offen lds\n"
"v_mfma_f32_16x16x32_bf16 v[32:35], v[144:147], a[96:99], v[32:35]\n"
"s_waitcnt vmcnt(12)\n"
"ds_read_b64_tr_b16 v[128:129], v224 offset:0\n"
"s_add_u32 s28, s28, s83\n"
"v_mfma_f32_16x16x32_bf16 v[32:35], v[148:151], a[112:115], v[32:35]\n"
"s_add_u32 s80, s80, s101\n"
"s_add_u32 s28, s28, 50331648\n"
"v_mfma_f32_16x16x32_bf16 v[36:39], v[148:151], a[116:119], v[36:39]\n"
"s_addk_i32 m0, 1024\n"
"ds_read_b64_tr_b16 v[130:131], v224 offset:2048\n"
"s_mul_i32 s29, s79, 8192\n"
"v_mfma_f32_16x16x32_bf16 v[36:39], v[144:147], a[100:103], v[36:39]\n"
"buffer_load_dwordx4 v228, s[52:55], s80 offen lds\n"
"v_mfma_f32_16x16x32_bf16 v[40:43], v[144:147], a[104:107], v[40:43]\n"
"s_add_u32 s80, s80, s100\n"
"ds_read_b64_tr_b16 v[132:133], v224 offset:4096\n"
"s_mul_i32 s83, s77, 2048\n"
"v_mfma_f32_16x16x32_bf16 v[40:43], v[148:151], a[120:123], v[40:43]\n"
"s_addk_i32 m0, 1024\n"
"s_add_u32 s29, s29, s83\n"
"v_mfma_f32_16x16x32_bf16 v[44:47], v[148:151], a[124:127], v[44:47]\n"
"ds_read_b64_tr_b16 v[134:135], v224 offset:6144\n"
"v_mfma_f32_16x16x32_bf16 v[44:47], v[144:147], a[108:111], v[44:47]\n"
"s_waitcnt lgkmcnt(4)\n"
"buffer_load_dwordx4 v228, s[52:55], s80 offen lds\n"
"v_mfma_f32_16x16x32_bf16 v[60:63], v[152:155], a[108:111], v[60:63]\n"
"s_sub_u32 s80, s80, s101\n"
"ds_read_b64_tr_b16 v[136:137], v225 offset:0\n"
"s_mul_i32 s83, s78, 128\n"
"v_mfma_f32_16x16x32_bf16 v[60:63], v[156:159], a[124:127], v[60:63]\n"
"s_addk_i32 m0, 1024\n"
"s_add_u32 s29, s29, s83\n"
"v_mfma_f32_16x16x32_bf16 v[56:59], v[156:159], a[120:123], v[56:59]\n"
"s_add_u32 s29, s29, 50331648\n"
"ds_read_b64_tr_b16 v[138:139], v225 offset:2048\n"
"v_mfma_f32_16x16x32_bf16 v[56:59], v[152:155], a[104:107], v[56:59]\n"
"buffer_load_dwordx4 v228, s[52:55], s80 offen lds\n"
"v_mfma_f32_16x16x32_bf16 v[52:55], v[152:155], a[100:103], v[52:55]\n"
"s_add_u32 s80, s80, s100\n"
"ds_read_b64_tr_b16 v[140:141], v225 offset:4096\n"
"s_add_u32 s30, s28, 0x40000\n"
"v_mfma_f32_16x16x32_bf16 v[52:55], v[156:159], a[116:119], v[52:55]\n"
"s_addk_i32 m0, 1024\n"
"v_mfma_f32_16x16x32_bf16 v[48:51], v[156:159], a[112:115], v[48:51]\n"
"s_add_u32 s31, s29, 0x40000\n"
"ds_read_b64_tr_b16 v[142:143], v225 offset:6144\n"
"v_mfma_f32_16x16x32_bf16 v[48:51], v[152:155], a[96:99], v[48:51]\n"
"s_waitcnt lgkmcnt(4)\n"
"buffer_load_dwordx4 v228, s[52:55], s80 offen lds\n"
"v_mfma_f32_16x16x32_bf16 v[0:3], v[128:131], a[128:131], v[0:3]\n"
"s_add_u32 s80, s84, 1605632\n"
"ds_read_b64_tr_b16 v[144:145], v226 offset:0\n"
"v_mov_b32_e32 v243, 1\n"
"v_mfma_f32_16x16x32_bf16 v[0:3], v[132:135], a[144:147], v[0:3]\n"
"s_add_u32 m0, s46, 73728\n"
"v_add_u32_e32 v233, s81, v255\n"
"v_mfma_f32_16x16x32_bf16 v[4:7], v[132:135], a[148:151], v[4:7]\n"
"ds_read_b64_tr_b16 v[146:147], v226 offset:2048\n"
"v_mfma_f32_16x16x32_bf16 v[4:7], v[128:131], a[132:135], v[4:7]\n"
"buffer_load_dwordx4 v228, s[52:55], s80 offen lds\n"
"v_mfma_f32_16x16x32_bf16 v[8:11], v[128:131], a[136:139], v[8:11]\n"
"s_add_u32 s80, s80, s100\n"
"ds_read_b64_tr_b16 v[148:149], v226 offset:4096\n"
"v_add_u32_e32 v232, 131072, v255\n"
"v_mfma_f32_16x16x32_bf16 v[8:11], v[132:135], a[152:155], v[8:11]\n"
"s_addk_i32 m0, 1024\n"
"v_add_u32_e32 v204, 0, v202\n"
"v_mfma_f32_16x16x32_bf16 v[12:15], v[132:135], a[156:159], v[12:15]\n"
"ds_read_b64_tr_b16 v[150:151], v226 offset:6144\n"
"v_xor_b32_e32 v205, v204, v201\n"
"v_mfma_f32_16x16x32_bf16 v[12:15], v[128:131], a[140:143], v[12:15]\n"
"s_waitcnt lgkmcnt(4)\n"
"buffer_load_dwordx4 v228, s[52:55], s80 offen lds\n"
"v_mfma_f32_16x16x32_bf16 v[28:31], v[136:139], a[140:143], v[28:31]\n"
"s_sub_u32 s80, s80, s101\n"
"ds_read_b64_tr_b16 v[152:153], v227 offset:0\n"
"v_lshlrev_b32_e32 v205, 4, v205\n"
"v_mfma_f32_16x16x32_bf16 v[28:31], v[140:143], a[156:159], v[28:31]\n"
"s_addk_i32 m0, 1024\n"
"v_mfma_f32_16x16x32_bf16 v[24:27], v[140:143], a[152:155], v[24:27]\n"
"ds_read_b64_tr_b16 v[154:155], v227 offset:2048\n"
"v_lshl_add_u32 v236, v202, 8, v205\n"
"v_mfma_f32_16x16x32_bf16 v[24:27], v[136:139], a[136:139], v[24:27]\n"
"buffer_load_dwordx4 v228, s[52:55], s80 offen lds\n"
"v_mfma_f32_16x16x32_bf16 v[20:23], v[136:139], a[132:135], v[20:23]\n"
"s_add_u32 s80, s80, s100\n"
"ds_read_b64_tr_b16 v[156:157], v227 offset:4096\n"
"v_lshlrev_b32_e32 v205, 1, v204\n"
"v_mfma_f32_16x16x32_bf16 v[20:23], v[140:143], a[148:151], v[20:23]\n"
"s_addk_i32 m0, 1024\n"
"v_xor_b32_e32 v205, v205, v201\n"
"v_mfma_f32_16x16x32_bf16 v[16:19], v[140:143], a[144:147], v[16:19]\n"
"ds_read_b64_tr_b16 v[158:159], v227 offset:6144\n"
"v_mfma_f32_16x16x32_bf16 v[16:19], v[136:139], a[128:131], v[16:19]\n"
"s_waitcnt lgkmcnt(4)\n"
"buffer_load_dwordx4 v228, s[52:55], s80 offen lds\n"
"v_mfma_f32_16x16x32_bf16 v[32:35], v[144:147], a[128:131], v[32:35]\n"
"s_waitcnt vmcnt(12)\n"
"ds_read_b64_tr_b16 v[128:129], v250 offset:0\n"
"v_lshlrev_b32_e32 v205, 4, v205\n"
"v_mfma_f32_16x16x32_bf16 v[32:35], v[148:151], a[144:147], v[32:35]\n"
"s_add_u32 s80, s80, s101\n"
"v_lshl_add_u32 v240, v202, 8, v205\n"
"v_mfma_f32_16x16x32_bf16 v[36:39], v[148:151], a[148:151], v[36:39]\n"
"s_addk_i32 m0, 1024\n"
"ds_read_b64_tr_b16 v[130:131], v250 offset:2048\n"
"v_add_u32_e32 v204, 4, v202\n"
"v_mfma_f32_16x16x32_bf16 v[36:39], v[144:147], a[132:135], v[36:39]\n"
"buffer_load_dwordx4 v228, s[52:55], s80 offen lds\n"
"v_mfma_f32_16x16x32_bf16 v[40:43], v[144:147], a[136:139], v[40:43]\n"
"s_add_u32 s80, s80, s100\n"
"ds_read_b64_tr_b16 v[132:133], v250 offset:4096\n"
"v_xor_b32_e32 v205, v204, v201\n"
"v_mfma_f32_16x16x32_bf16 v[40:43], v[148:151], a[152:155], v[40:43]\n"
"s_addk_i32 m0, 1024\n"
"v_lshlrev_b32_e32 v205, 4, v205\n"
"v_mfma_f32_16x16x32_bf16 v[44:47], v[148:151], a[156:159], v[44:47]\n"
"ds_read_b64_tr_b16 v[134:135], v250 offset:6144\n"
"v_mfma_f32_16x16x32_bf16 v[44:47], v[144:147], a[140:143], v[44:47]\n"
"s_waitcnt lgkmcnt(4)\n"
"buffer_load_dwordx4 v228, s[52:55], s80 offen lds\n"
"v_mfma_f32_16x16x32_bf16 v[60:63], v[152:155], a[140:143], v[60:63]\n"
"s_sub_u32 s80, s80, s101\n"
"ds_read_b64_tr_b16 v[136:137], v251 offset:0\n"
"v_lshl_add_u32 v237, v202, 8, v205\n"
"v_mfma_f32_16x16x32_bf16 v[60:63], v[156:159], a[156:159], v[60:63]\n"
"s_addk_i32 m0, 1024\n"
"v_lshlrev_b32_e32 v205, 1, v204\n"
"v_mfma_f32_16x16x32_bf16 v[56:59], v[156:159], a[152:155], v[56:59]\n"
"ds_read_b64_tr_b16 v[138:139], v251 offset:2048\n"
"v_xor_b32_e32 v205, v205, v201\n"
"v_mfma_f32_16x16x32_bf16 v[56:59], v[152:155], a[136:139], v[56:59]\n"
"buffer_load_dwordx4 v228, s[52:55], s80 offen lds\n"
"v_mfma_f32_16x16x32_bf16 v[52:55], v[152:155], a[132:135], v[52:55]\n"
"s_add_u32 s80, s80, s100\n"
"ds_read_b64_tr_b16 v[140:141], v251 offset:4096\n"
"v_lshlrev_b32_e32 v205, 4, v205\n"
"v_mfma_f32_16x16x32_bf16 v[52:55], v[156:159], a[148:151], v[52:55]\n"
"s_addk_i32 m0, 1024\n"
"v_mfma_f32_16x16x32_bf16 v[48:51], v[156:159], a[144:147], v[48:51]\n"
"ds_read_b64_tr_b16 v[142:143], v251 offset:6144\n"
"v_lshl_add_u32 v241, v202, 8, v205\n"
"v_mfma_f32_16x16x32_bf16 v[48:51], v[152:155], a[128:131], v[48:51]\n"
"s_waitcnt lgkmcnt(4)\n"
"buffer_load_dwordx4 v228, s[52:55], s80 offen lds\n"
"s_mov_b32 s100, 131072\n"
"s_mov_b32 s101, 98304\n"
"v_mfma_f32_16x16x32_bf16 v[0:3], v[128:131], a[160:163], v[0:3]\n"
"s_add_u32 s80, s85, 0\n"
"ds_read_b64_tr_b16 v[144:145], v252 offset:0\n"
"v_add_u32_e32 v204, 8, v202\n"
"v_mfma_f32_16x16x32_bf16 v[0:3], v[132:135], a[176:179], v[0:3]\n"
"s_add_u32 m0, s46, 0\n"
"v_xor_b32_e32 v205, v204, v201\n"
"v_mfma_f32_16x16x32_bf16 v[4:7], v[132:135], a[180:183], v[4:7]\n"
"ds_read_b64_tr_b16 v[146:147], v252 offset:2048\n"
"v_mfma_f32_16x16x32_bf16 v[4:7], v[128:131], a[164:167], v[4:7]\n"
"buffer_load_dwordx4 v229, s[56:59], s80 offen lds\n"
"v_mfma_f32_16x16x32_bf16 v[8:11], v[128:131], a[168:171], v[8:11]\n"
"s_add_u32 s80, s80, s100\n"
"ds_read_b64_tr_b16 v[148:149], v252 offset:4096\n"
"v_lshlrev_b32_e32 v205, 4, v205\n"
"v_mfma_f32_16x16x32_bf16 v[8:11], v[132:135], a[184:187], v[8:11]\n"
"s_addk_i32 m0, 1024\n"
"v_lshl_add_u32 v238, v202, 8, v205\n"
"v_mfma_f32_16x16x32_bf16 v[12:15], v[132:135], a[188:191], v[12:15]\n"
"ds_read_b64_tr_b16 v[150:151], v252 offset:6144\n"
"v_add_u32_e32 v204, 12, v202\n"
"v_mfma_f32_16x16x32_bf16 v[12:15], v[128:131], a[172:175], v[12:15]\n"
"s_waitcnt lgkmcnt(4)\n"
"buffer_load_dwordx4 v229, s[56:59], s80 offen lds\n"
"v_mfma_f32_16x16x32_bf16 v[28:31], v[136:139], a[172:175], v[28:31]\n"
"s_sub_u32 s80, s80, s101\n"
"ds_read_b64_tr_b16 v[152:153], v253 offset:0\n"
"v_xor_b32_e32 v205, v204, v201\n"
"v_mfma_f32_16x16x32_bf16 v[28:31], v[140:143], a[188:191], v[28:31]\n"
"s_addk_i32 m0, 1024\n"
"v_mfma_f32_16x16x32_bf16 v[24:27], v[140:143], a[184:187], v[24:27]\n"
"ds_read_b64_tr_b16 v[154:155], v253 offset:2048\n"
"v_lshlrev_b32_e32 v205, 4, v205\n"
"v_mfma_f32_16x16x32_bf16 v[24:27], v[136:139], a[168:171], v[24:27]\n"
"buffer_load_dwordx4 v229, s[56:59], s80 offen lds\n"
"v_mfma_f32_16x16x32_bf16 v[20:23], v[136:139], a[164:167], v[20:23]\n"
"s_add_u32 s80, s80, s100\n"
"ds_read_b64_tr_b16 v[156:157], v253 offset:4096\n"
"v_lshl_add_u32 v239, v202, 8, v205\n"
"v_mfma_f32_16x16x32_bf16 v[20:23], v[140:143], a[180:183], v[20:23]\n"
"s_addk_i32 m0, 1024\n"
"v_bfe_u32 v196, v200, 3, 2\n"
"v_mfma_f32_16x16x32_bf16 v[16:19], v[140:143], a[176:179], v[16:19]\n"
"ds_read_b64_tr_b16 v[158:159], v253 offset:6144\n"
"v_mfma_f32_16x16x32_bf16 v[16:19], v[136:139], a[160:163], v[16:19]\n"
"s_waitcnt lgkmcnt(4)\n"
"buffer_load_dwordx4 v229, s[56:59], s80 offen lds\n"
"v_mfma_f32_16x16x32_bf16 v[32:35], v[144:147], a[160:163], v[32:35]\n"
"s_waitcnt vmcnt(12)\n"
"ds_read_b64_tr_b16 v[128:129], v246 offset:8192\n"
"v_lshlrev_b32_e32 v196, 1, v196\n"
"v_mfma_f32_16x16x32_bf16 v[32:35], v[148:151], a[176:179], v[32:35]\n"
"s_add_u32 s80, s80, s101\n"
"v_and_b32_e32 v197, 3, v200\n"
"v_mfma_f32_16x16x32_bf16 v[36:39], v[148:151], a[180:183], v[36:39]\n"
"s_addk_i32 m0, 1024\n"
"ds_read_b64_tr_b16 v[130:131], v246 offset:10240\n"
"v_xor_b32_e32 v196, v196, v197\n"
"v_mfma_f32_16x16x32_bf16 v[36:39], v[144:147], a[164:167], v[36:39]\n"
"buffer_load_dwordx4 v229, s[56:59], s80 offen lds\n"
"v_mfma_f32_16x16x32_bf16 v[40:43], v[144:147], a[168:171], v[40:43]\n"
"s_add_u32 s80, s80, s100\n"
"ds_read_b64_tr_b16 v[132:133], v246 offset:12288\n"
"v_lshlrev_b32_e32 v196, 4, v196\n"
"v_mfma_f32_16x16x32_bf16 v[40:43], v[148:151], a[184:187], v[40:43]\n"
"s_addk_i32 m0, 1024\n"
"v_lshrrev_b32_e32 v197, 2, v200\n"
"v_mfma_f32_16x16x32_bf16 v[44:47], v[148:151], a[188:191], v[44:47]\n"
".LPG20_%=:\n"
"s_cbranch_execz .LPGR_%=\n"
"ds_read_b64_tr_b16 v[134:135], v246 offset:14336\n"
"v_mfma_f32_16x16x32_bf16 v[44:47], v[144:147], a[172:175], v[44:47]\n"
"s_waitcnt lgkmcnt(4)\n"
"buffer_load_dwordx4 v229, s[56:59], s80 offen lds\n"
"v_mfma_f32_16x16x32_bf16 v[60:63], v[152:155], a[172:175], v[60:63]\n"
"s_sub_u32 s80, s80, s101\n"
"ds_read_b64_tr_b16 v[136:137], v247 offset:8192\n"
"v_lshl_add_u32 v197, v197, 7, v196\n"
"v_mfma_f32_16x16x32_bf16 v[60:63], v[156:159], a[188:191], v[60:63]\n"
"s_addk_i32 m0, 1024\n"
"v_add_u32_e32 v196, s46, v197\n"
"v_mfma_f32_16x16x32_bf16 v[56:59], v[156:159], a[184:187], v[56:59]\n"
"ds_read_b64_tr_b16 v[138:139], v247 offset:10240\n"
"v_add_u32_e32 v206, s81, v197\n"
"v_mfma_f32_16x16x32_bf16 v[56:59], v[152:155], a[168:171], v[56:59]\n"
"buffer_load_dwordx4 v229, s[56:59], s80 offen lds\n"
"v_mfma_f32_16x16x32_bf16 v[52:55], v[152:155], a[164:167], v[52:55]\n"
"s_add_u32 s80, s80, s100\n"
"ds_read_b64_tr_b16 v[140:141], v247 offset:12288\n"
"v_xor_b32_e32 v197, 64, v196\n"
"v_mfma_f32_16x16x32_bf16 v[52:55], v[156:159], a[180:183], v[52:55]\n"
"s_addk_i32 m0, 1024\n"
"v_mfma_f32_16x16x32_bf16 v[48:51], v[156:159], a[176:179], v[48:51]\n"
"ds_read_b64_tr_b16 v[142:143], v247 offset:14336\n"
"v_add_u32_e32 v198, 65536, v196\n"
"v_mfma_f32_16x16x32_bf16 v[48:51], v[152:155], a[160:163], v[48:51]\n"
"s_waitcnt lgkmcnt(4)\n"
"buffer_load_dwordx4 v229, s[56:59], s80 offen lds\n"
"v_mfma_f32_16x16x32_bf16 v[0:3], v[128:131], a[192:195], v[0:3]\n"
"s_add_u32 s80, s85, 524288\n"
"ds_read_b64_tr_b16 v[144:145], v248 offset:8192\n"
"v_add_u32_e32 v199, 65536, v197\n"
"v_mfma_f32_16x16x32_bf16 v[0:3], v[132:135], a[208:211], v[0:3]\n"
"s_add_u32 m0, s46, 65536\n"
"v_xor_b32_e32 v207, 64, v206\n"
"v_mfma_f32_16x16x32_bf16 v[4:7], v[132:135], a[212:215], v[4:7]\n"
"ds_read_b64_tr_b16 v[146:147], v248 offset:10240\n"
"v_mfma_f32_16x16x32_bf16 v[4:7], v[128:131], a[196:199], v[4:7]\n"
"buffer_load_dwordx4 v229, s[56:59], s80 offen lds\n"
"v_mfma_f32_16x16x32_bf16 v[8:11], v[128:131], a[200:203], v[8:11]\n"
"s_add_u32 s80, s80, s100\n"
"ds_read_b64_tr_b16 v[148:149], v248 offset:12288\n"
"s_mul_i32 s87, s23, 32768\n"
"v_mfma_f32_16x16x32_bf16 v[8:11], v[132:135], a[216:219], v[8:11]\n"
"s_addk_i32 m0, 1024\n"
"s_mul_i32 s83, s77, 256\n"
"v_mfma_f32_16x16x32_bf16 v[12:15], v[132:135], a[220:223], v[12:15]\n"
"s_add_u32 s87, s87, s83\n"
"ds_read_b64_tr_b16 v[150:151], v248 offset:14336\n"
"v_mfma_f32_16x16x32_bf16 v[12:15], v[128:131], a[204:207], v[12:15]\n"
"s_waitcnt lgkmcnt(4)\n"
"buffer_load_dwordx4 v229, s[56:59], s80 offen lds\n"
"v_mfma_f32_16x16x32_bf16 v[28:31], v[136:139], a[204:207], v[28:31]\n"
"s_sub_u32 s80, s80, s101\n"
"ds_read_b64_tr_b16 v[152:153], v249 offset:8192\n"
"s_mul_i32 s83, s89, 128\n"
"v_mfma_f32_16x16x32_bf16 v[28:31], v[140:143], a[220:223], v[28:31]\n"
"s_addk_i32 m0, 1024\n"
"v_mfma_f32_16x16x32_bf16 v[24:27], v[140:143], a[216:219], v[24:27]\n"
"s_add_u32 s87, s87, s83\n"
"ds_read_b64_tr_b16 v[154:155], v249 offset:10240\n"
"v_mfma_f32_16x16x32_bf16 v[24:27], v[136:139], a[200:203], v[24:27]\n"
"buffer_load_dwordx4 v229, s[56:59], s80 offen lds\n"
"v_mfma_f32_16x16x32_bf16 v[20:23], v[136:139], a[196:199], v[20:23]\n"
"s_add_u32 s80, s80, s100\n"
"ds_read_b64_tr_b16 v[156:157], v249 offset:12288\n"
"s_xor_b32 s88, s87, 128\n"
"v_mfma_f32_16x16x32_bf16 v[20:23], v[140:143], a[212:215], v[20:23]\n"
"s_addk_i32 m0, 1024\n"
"s_mov_b64 s[36:37], s[6:7]\n"
"v_mfma_f32_16x16x32_bf16 v[16:19], v[140:143], a[208:211], v[16:19]\n"
"ds_read_b64_tr_b16 v[158:159], v249 offset:14336\n"
"v_mfma_f32_16x16x32_bf16 v[16:19], v[136:139], a[192:195], v[16:19]\n"
"s_waitcnt lgkmcnt(4)\n"
"buffer_load_dwordx4 v229, s[56:59], s80 offen lds\n"
"v_mfma_f32_16x16x32_bf16 v[32:35], v[144:147], a[192:195], v[32:35]\n"
"s_waitcnt vmcnt(12)\n"
"ds_read_b64_tr_b16 v[128:129], v250 offset:8192\n"
"s_mov_b32 s38, 4194304\n"
"v_mfma_f32_16x16x32_bf16 v[32:35], v[148:151], a[208:211], v[32:35]\n"
"s_add_u32 s80, s80, s101\n"
"s_mov_b32 s39, 0x20000\n"
"v_mfma_f32_16x16x32_bf16 v[36:39], v[148:151], a[212:215], v[36:39]\n"
"s_addk_i32 m0, 1024\n"
"ds_read_b64_tr_b16 v[130:131], v250 offset:10240\n"
"s_mov_b64 s[40:41], s[8:9]\n"
"v_mfma_f32_16x16x32_bf16 v[36:39], v[144:147], a[196:199], v[36:39]\n"
"buffer_load_dwordx4 v229, s[56:59], s80 offen lds\n"
"v_mfma_f32_16x16x32_bf16 v[40:43], v[144:147], a[200:203], v[40:43]\n"
"s_add_u32 s80, s80, s100\n"
"ds_read_b64_tr_b16 v[132:133], v250 offset:12288\n"
"s_mov_b32 s42, 4194304\n"
"v_mfma_f32_16x16x32_bf16 v[40:43], v[148:151], a[216:219], v[40:43]\n"
"s_addk_i32 m0, 1024\n"
"s_mov_b32 s43, 0x20000\n"
"v_mfma_f32_16x16x32_bf16 v[44:47], v[148:151], a[220:223], v[44:47]\n"
"ds_read_b64_tr_b16 v[134:135], v250 offset:14336\n"
"v_mfma_f32_16x16x32_bf16 v[44:47], v[144:147], a[204:207], v[44:47]\n"
"s_waitcnt lgkmcnt(4)\n"
"buffer_load_dwordx4 v229, s[56:59], s80 offen lds\n"
"v_mfma_f32_16x16x32_bf16 v[60:63], v[152:155], a[204:207], v[60:63]\n"
"s_sub_u32 s80, s80, s101\n"
"ds_read_b64_tr_b16 v[136:137], v251 offset:8192\n"
"s_mul_i32 s44, s78, 524288\n"
"v_mfma_f32_16x16x32_bf16 v[60:63], v[156:159], a[220:223], v[60:63]\n"
"s_addk_i32 m0, 1024\n"
"s_mul_i32 s83, s77, 4096\n"
"v_mfma_f32_16x16x32_bf16 v[56:59], v[156:159], a[216:219], v[56:59]\n"
"s_add_u32 s44, s44, s83\n"
"ds_read_b64_tr_b16 v[138:139], v251 offset:10240\n"
"v_mfma_f32_16x16x32_bf16 v[56:59], v[152:155], a[200:203], v[56:59]\n"
"buffer_load_dwordx4 v229, s[56:59], s80 offen lds\n"
"v_mfma_f32_16x16x32_bf16 v[52:55], v[152:155], a[196:199], v[52:55]\n"
"s_add_u32 s80, s80, s100\n"
"ds_read_b64_tr_b16 v[140:141], v251 offset:12288\n"
"s_add_u32 s44, s44, -16384\n"
"v_mfma_f32_16x16x32_bf16 v[52:55], v[156:159], a[212:215], v[52:55]\n"
"s_addk_i32 m0, 1024\n"
"v_mfma_f32_16x16x32_bf16 v[48:51], v[156:159], a[208:211], v[48:51]\n"
"s_mov_b32 s45, s44\n"
"ds_read_b64_tr_b16 v[142:143], v251 offset:14336\n"
"v_mfma_f32_16x16x32_bf16 v[48:51], v[152:155], a[192:195], v[48:51]\n"
"s_waitcnt lgkmcnt(4)\n"
"buffer_load_dwordx4 v229, s[56:59], s80 offen lds\n"
"v_mfma_f32_16x16x32_bf16 v[0:3], v[128:131], a[224:227], v[0:3]\n"
"s_add_u32 s80, s85, 1048576\n"
"ds_read_b64_tr_b16 v[144:145], v252 offset:8192\n"
"s_mov_b64 s[64:65], s[12:13]\n"
"v_mfma_f32_16x16x32_bf16 v[0:3], v[132:135], a[240:243], v[0:3]\n"
"s_add_u32 m0, s46, 8192\n"
"s_mov_b32 s66, 4194304\n"
"v_mfma_f32_16x16x32_bf16 v[4:7], v[132:135], a[244:247], v[4:7]\n"
"ds_read_b64_tr_b16 v[146:147], v252 offset:10240\n"
"v_mfma_f32_16x16x32_bf16 v[4:7], v[128:131], a[228:231], v[4:7]\n"
"buffer_load_dwordx4 v229, s[56:59], s80 offen lds\n"
"v_mfma_f32_16x16x32_bf16 v[8:11], v[128:131], a[232:235], v[8:11]\n"
"s_add_u32 s80, s80, s100\n"
"ds_read_b64_tr_b16 v[148:149], v252 offset:12288\n"
"s_mov_b32 s67, 0x20000\n"
"v_mfma_f32_16x16x32_bf16 v[8:11], v[132:135], a[248:251], v[8:11]\n"
"s_addk_i32 m0, 1024\n"
"s_mov_b64 s[68:69], s[20:21]\n"
"v_mfma_f32_16x16x32_bf16 v[12:15], v[132:135], a[252:255], v[12:15]\n"
"s_mov_b32 s70, 41943040\n"
"ds_read_b64_tr_b16 v[150:151], v252 offset:14336\n"
"v_mfma_f32_16x16x32_bf16 v[12:15], v[128:131], a[236:239], v[12:15]\n"
"s_waitcnt lgkmcnt(4)\n"
"buffer_load_dwordx4 v229, s[56:59], s80 offen lds\n"
"v_mfma_f32_16x16x32_bf16 v[28:31], v[136:139], a[236:239], v[28:31]\n"
"s_sub_u32 s80, s80, s101\n"
"ds_read_b64_tr_b16 v[152:153], v253 offset:8192\n"
"s_mov_b32 s71, 0x20000\n"
"v_mfma_f32_16x16x32_bf16 v[28:31], v[140:143], a[252:255], v[28:31]\n"
"s_addk_i32 m0, 1024\n"
"v_mfma_f32_16x16x32_bf16 v[24:27], v[140:143], a[248:251], v[24:27]\n"
"s_mul_i32 s90, s79, 610304\n"
"ds_read_b64_tr_b16 v[154:155], v253 offset:10240\n"
"v_mfma_f32_16x16x32_bf16 v[24:27], v[136:139], a[232:235], v[24:27]\n"
"buffer_load_dwordx4 v229, s[56:59], s80 offen lds\n"
"v_mfma_f32_16x16x32_bf16 v[20:23], v[136:139], a[228:231], v[20:23]\n"
"s_add_u32 s80, s80, s100\n"
"ds_read_b64_tr_b16 v[156:157], v253 offset:12288\n"
"s_mul_i32 s83, s77, 151552\n"
"v_mfma_f32_16x16x32_bf16 v[20:23], v[140:143], a[244:247], v[20:23]\n"
"s_addk_i32 m0, 1024\n"
"s_add_u32 s90, s90, s83\n"
"v_mfma_f32_16x16x32_bf16 v[16:19], v[140:143], a[240:243], v[16:19]\n"
"ds_read_b64_tr_b16 v[158:159], v253 offset:14336\n"
"v_mfma_f32_16x16x32_bf16 v[16:19], v[136:139], a[224:227], v[16:19]\n"
"s_waitcnt lgkmcnt(4)\n"
"buffer_load_dwordx4 v229, s[56:59], s80 offen lds\n"
"v_mfma_f32_16x16x32_bf16 v[32:35], v[144:147], a[224:227], v[32:35]\n"
"s_waitcnt vmcnt(12)\n"
"ds_read_b64_tr_b16 v[128:129], v196 offset:0\n"
"s_mul_i32 s83, s78, 2048\n"
"v_mfma_f32_16x16x32_bf16 v[32:35], v[148:151], a[240:243], v[32:35]\n"
"s_add_u32 s80, s80, s101\n"
"s_add_u32 s90, s90, s83\n"
"v_mfma_f32_16x16x32_bf16 v[36:39], v[148:151], a[244:247], v[36:39]\n"
"s_addk_i32 m0, 1024\n"
"ds_read_b64_tr_b16 v[130:131], v196 offset:2048\n"
"s_mul_i32 s83, s89, 75776\n"
"v_mfma_f32_16x16x32_bf16 v[36:39], v[144:147], a[228:231], v[36:39]\n"
"buffer_load_dwordx4 v229, s[56:59], s80 offen lds\n"
"v_mfma_f32_16x16x32_bf16 v[40:43], v[144:147], a[232:235], v[40:43]\n"
"s_add_u32 s80, s80, s100\n"
"ds_read_b64_tr_b16 v[132:133], v196 offset:4096\n"
"s_add_u32 s90, s90, s83\n"
"v_mfma_f32_16x16x32_bf16 v[40:43], v[148:151], a[248:251], v[40:43]\n"
"s_addk_i32 m0, 1024\n"
"s_add_u32 s90, s90, 20971520\n"
"v_mfma_f32_16x16x32_bf16 v[44:47], v[148:151], a[252:255], v[44:47]\n"
"ds_read_b64_tr_b16 v[134:135], v196 offset:6144\n"
"v_mfma_f32_16x16x32_bf16 v[44:47], v[144:147], a[236:239], v[44:47]\n"
"s_waitcnt lgkmcnt(4)\n"
"buffer_load_dwordx4 v229, s[56:59], s80 offen lds\n"
"v_mfma_f32_16x16x32_bf16 v[60:63], v[152:155], a[236:239], v[60:63]\n"
"s_sub_u32 s80, s80, s101\n"
"ds_read_b64_tr_b16 v[136:137], v196 offset:8\n"
"s_mul_i32 s91, s89, -151552\n"
"v_mfma_f32_16x16x32_bf16 v[60:63], v[156:159], a[252:255], v[60:63]\n"
"s_addk_i32 m0, 1024\n"
"s_mul_i32 s83, s90, 1\n"
"v_mfma_f32_16x16x32_bf16 v[56:59], v[156:159], a[248:251], v[56:59]\n"
"s_add_u32 s91, s91, s83\n"
"ds_read_b64_tr_b16 v[138:139], v196 offset:2056\n"
"v_mfma_f32_16x16x32_bf16 v[56:59], v[152:155], a[232:235], v[56:59]\n"
"buffer_load_dwordx4 v229, s[56:59], s80 offen lds\n"
"v_mfma_f32_16x16x32_bf16 v[52:55], v[152:155], a[228:231], v[52:55]\n"
"s_add_u32 s80, s80, s100\n"
"ds_read_b64_tr_b16 v[140:141], v196 offset:4104\n"
"s_add_u32 s91, s91, 75776\n"
"v_mfma_f32_16x16x32_bf16 v[52:55], v[156:159], a[244:247], v[52:55]\n"
"s_addk_i32 m0, 1024\n"
"v_mfma_f32_16x16x32_bf16 v[48:51], v[156:159], a[240:243], v[48:51]\n"
"s_mov_b32 s92, 0\n"
"ds_read_b64_tr_b16 v[142:143], v196 offset:6152\n"
"v_mfma_f32_16x16x32_bf16 v[48:51], v[152:155], a[224:227], v[48:51]\n"
"s_waitcnt lgkmcnt(4)\n"
"buffer_load_dwordx4 v229, s[56:59], s80 offen lds\n"
"s_getpc_b64 s[52:53]\n"
".LPC4_%=:\n"
"s_sub_u32 s52, s52, .LPC4_%=-.LSTART_%=\n"
"s_subb_u32 s53, s53, 0\n"
"v_mfma_f32_16x16x32_bf16 v[64:67], v[128:131], a[0:3], 0\n"
"s_add_u32 s80, s85, 1572864\n"
"ds_read_b64_tr_b16 v[144:145], v197 offset:0\n"
"s_mov_b32 s54, .LEND_%=-.LSTART_%=\n"
"v_mfma_f32_16x16x32_bf16 v[64:67], v[132:135], a[16:19], v[64:67]\n"
"s_add_u32 m0, s46, 73728\n"
"s_lshr_b32 s95, %1, 3\n"
"v_mfma_f32_16x16x32_bf16 v[68:71], v[132:135], a[20:23], 0\n"
"ds_read_b64_tr_b16 v[146:147], v197 offset:2048\n"
"v_mfma_f32_16x16x32_bf16 v[68:71], v[128:131], a[4:7], v[68:71]\n"
"buffer_load_dwordx4 v229, s[56:59], s80 offen lds\n"
"v_mfma_f32_16x16x32_bf16 v[72:75], v[128:131], a[8:11], 0\n"
"s_add_u32 s80, s80, s100\n"
"ds_read_b64_tr_b16 v[148:149], v197 offset:4096\n"
"s_lshl_b32 s95, s95, 2\n"
"v_mfma_f32_16x16x32_bf16 v[72:75], v[132:135], a[24:27], v[72:75]\n"
"s_addk_i32 m0, 1024\n"
"s_add_u32 s95, s95, s77\n"
"v_mfma_f32_16x16x32_bf16 v[76:79], v[132:135], a[28:31], 0\n"
"s_lshl_b32 s95, s95, 10\n"
"ds_read_b64_tr_b16 v[150:151], v197 offset:6144\n"
"v_mfma_f32_16x16x32_bf16 v[76:79], v[128:131], a[12:15], v[76:79]\n"
"s_waitcnt lgkmcnt(4)\n"
"buffer_load_dwordx4 v229, s[56:59], s80 offen lds\n"
"v_mfma_f32_16x16x32_bf16 v[92:95], v[136:139], a[12:15], 0\n"
"s_sub_u32 s80, s80, s101\n"
"ds_read_b64_tr_b16 v[152:153], v197 offset:8\n"
"v_max_f32_e32 v0, 0, v0\n"
"v_mfma_f32_16x16x32_bf16 v[92:95], v[140:143], a[28:31], v[92:95]\n"
"s_addk_i32 m0, 1024\n"
"v_mfma_f32_16x16x32_bf16 v[88:91], v[140:143], a[24:27], 0\n"
"ds_read_b64_tr_b16 v[154:155], v197 offset:2056\n"
"v_max_f32_e32 v1, 0, v1\n"
"v_mfma_f32_16x16x32_bf16 v[88:91], v[136:139], a[8:11], v[88:91]\n"
"buffer_load_dwordx4 v229, s[56:59], s80 offen lds\n"
"v_mfma_f32_16x16x32_bf16 v[84:87], v[136:139], a[4:7], 0\n"
"s_add_u32 s80, s80, s100\n"
"ds_read_b64_tr_b16 v[156:157], v197 offset:4104\n"
"v_max_f32_e32 v2, 0, v2\n"
"v_mfma_f32_16x16x32_bf16 v[84:87], v[140:143], a[20:23], v[84:87]\n"
"s_addk_i32 m0, 1024\n"
"v_max_f32_e32 v3, 0, v3\n"
"v_mfma_f32_16x16x32_bf16 v[80:83], v[140:143], a[16:19], 0\n"
"ds_read_b64_tr_b16 v[158:159], v197 offset:6152\n"
"v_mfma_f32_16x16x32_bf16 v[80:83], v[136:139], a[0:3], v[80:83]\n"
"s_waitcnt lgkmcnt(4)\n"
"buffer_load_dwordx4 v229, s[56:59], s80 offen lds\n"
"v_mfma_f32_16x16x32_bf16 v[96:99], v[144:147], a[0:3], 0\n"
"s_waitcnt vmcnt(12)\n"
"ds_read_b64_tr_b16 v[128:129], v198 offset:0\n"
"v_max_f32_e32 v16, 0, v16\n"
"v_mfma_f32_16x16x32_bf16 v[96:99], v[148:151], a[16:19], v[96:99]\n"
"s_add_u32 s80, s80, s101\n"
"v_max_f32_e32 v17, 0, v17\n"
"v_mfma_f32_16x16x32_bf16 v[100:103], v[148:151], a[20:23], 0\n"
"s_addk_i32 m0, 1024\n"
"ds_read_b64_tr_b16 v[130:131], v198 offset:2048\n"
"v_max_f32_e32 v18, 0, v18\n"
"v_mfma_f32_16x16x32_bf16 v[100:103], v[144:147], a[4:7], v[100:103]\n"
"buffer_load_dwordx4 v229, s[56:59], s80 offen lds\n"
"v_mfma_f32_16x16x32_bf16 v[104:107], v[144:147], a[8:11], 0\n"
"s_add_u32 s80, s80, s100\n"
"ds_read_b64_tr_b16 v[132:133], v198 offset:4096\n"
"v_max_f32_e32 v19, 0, v19\n"
"v_mfma_f32_16x16x32_bf16 v[104:107], v[148:151], a[24:27], v[104:107]\n"
"s_addk_i32 m0, 1024\n"
"v_cvt_pk_bf16_f32 v0, v0, v1\n"
"v_mfma_f32_16x16x32_bf16 v[108:111], v[148:151], a[28:31], 0\n"
"ds_read_b64_tr_b16 v[134:135], v198 offset:6144\n"
"v_mfma_f32_16x16x32_bf16 v[108:111], v[144:147], a[12:15], v[108:111]\n"
"s_waitcnt lgkmcnt(4)\n"
"buffer_load_dwordx4 v229, s[56:59], s80 offen lds\n"
"v_mfma_f32_16x16x32_bf16 v[124:127], v[152:155], a[12:15], 0\n"
"s_sub_u32 s80, s80, s101\n"
"ds_read_b64_tr_b16 v[136:137], v198 offset:8\n"
"v_cvt_pk_bf16_f32 v1, v2, v3\n"
"v_mfma_f32_16x16x32_bf16 v[124:127], v[156:159], a[28:31], v[124:127]\n"
"s_addk_i32 m0, 1024\n"
"v_cvt_pk_bf16_f32 v2, v16, v17\n"
"v_mfma_f32_16x16x32_bf16 v[120:123], v[156:159], a[24:27], 0\n"
"ds_read_b64_tr_b16 v[138:139], v198 offset:2056\n"
"v_cvt_pk_bf16_f32 v3, v18, v19\n"
"v_mfma_f32_16x16x32_bf16 v[120:123], v[152:155], a[8:11], v[120:123]\n"
"buffer_load_dwordx4 v229, s[56:59], s80 offen lds\n"
"v_mfma_f32_16x16x32_bf16 v[116:119], v[152:155], a[4:7], 0\n"
"s_add_u32 s80, s80, s100\n"
"ds_read_b64_tr_b16 v[140:141], v198 offset:4104\n"
"v_max_f32_e32 v4, 0, v4\n"
"v_mfma_f32_16x16x32_bf16 v[116:119], v[156:159], a[20:23], v[116:119]\n"
"s_addk_i32 m0, 1024\n"
"v_mfma_f32_16x16x32_bf16 v[112:115], v[156:159], a[16:19], 0\n"
"ds_read_b64_tr_b16 v[142:143], v198 offset:6152\n"
"v_max_f32_e32 v5, 0, v5\n"
"v_mfma_f32_16x16x32_bf16 v[112:115], v[152:155], a[0:3], v[112:115]\n"
"s_waitcnt lgkmcnt(4)\n"
"buffer_load_dwordx4 v229, s[56:59], s80 offen lds\n"
"v_mfma_f32_16x16x32_bf16 v[64:67], v[128:131], a[32:35], v[64:67]\n"
"s_add_u32 s80, s85, 2097152\n"
"ds_read_b64_tr_b16 v[144:145], v199 offset:0\n"
"v_max_f32_e32 v6, 0, v6\n"
"v_mfma_f32_16x16x32_bf16 v[64:67], v[132:135], a[48:51], v[64:67]\n"
"s_add_u32 m0, s46, 0\n"
"v_max_f32_e32 v7, 0, v7\n"
"v_mfma_f32_16x16x32_bf16 v[68:71], v[132:135], a[52:55], v[68:71]\n"
"ds_read_b64_tr_b16 v[146:147], v199 offset:2048\n"
"v_mfma_f32_16x16x32_bf16 v[68:71], v[128:131], a[36:39], v[68:71]\n"
"buffer_load_dwordx4 v229, s[56:59], s80 offen lds\n"
"v_mfma_f32_16x16x32_bf16 v[72:75], v[128:131], a[40:43], v[72:75]\n"
"s_add_u32 s80, s80, s100\n"
"ds_read_b64_tr_b16 v[148:149], v199 offset:4096\n"
"v_max_f32_e32 v20, 0, v20\n"
"v_mfma_f32_16x16x32_bf16 v[72:75], v[132:135], a[56:59], v[72:75]\n"
"s_addk_i32 m0, 1024\n"
"v_max_f32_e32 v21, 0, v21\n"
"v_mfma_f32_16x16x32_bf16 v[76:79], v[132:135], a[60:63], v[76:79]\n"
"ds_read_b64_tr_b16 v[150:151], v199 offset:6144\n"
"v_max_f32_e32 v22, 0, v22\n"
"v_mfma_f32_16x16x32_bf16 v[76:79], v[128:131], a[44:47], v[76:79]\n"
"s_waitcnt lgkmcnt(4)\n"
"buffer_load_dwordx4 v229, s[56:59], s80 offen lds\n"
"v_mfma_f32_16x16x32_bf16 v[92:95], v[136:139], a[44:47], v[92:95]\n"
"s_sub_u32 s80, s80, s101\n"
"ds_read_b64_tr_b16 v[152:153], v199 offset:8\n"
"v_max_f32_e32 v23, 0, v23\n"
"v_mfma_f32_16x16x32_bf16 v[92:95], v[140:143], a[60:63], v[92:95]\n"
"s_addk_i32 m0, 1024\n"
"v_mfma_f32_16x16x32_bf16 v[88:91], v[140:143], a[56:59], v[88:91]\n"
"ds_read_b64_tr_b16 v[154:155], v199 offset:2056\n"
"v_cvt_pk_bf16_f32 v4, v4, v5\n"
"v_mfma_f32_16x16x32_bf16 v[88:91], v[136:139], a[40:43], v[88:91]\n"
"buffer_load_dwordx4 v229, s[56:59], s80 offen lds\n"
"v_mfma_f32_16x16x32_bf16 v[84:87], v[136:139], a[36:39], v[84:87]\n"
"s_add_u32 s80, s80, s100\n"
"ds_read_b64_tr_b16 v[156:157], v199 offset:4104\n"
"v_cvt_pk_bf16_f32 v5, v6, v7\n"
"v_mfma_f32_16x16x32_bf16 v[84:87], v[140:143], a[52:55], v[84:87]\n"
"s_addk_i32 m0, 1024\n"
"v_cvt_pk_bf16_f32 v6, v20, v21\n"
"v_mfma_f32_16x16x32_bf16 v[80:83], v[140:143], a[48:51], v[80:83]\n"
"ds_read_b64_tr_b16 v[158:159], v199 offset:6152\n"
"v_mfma_f32_16x16x32_bf16 v[80:83], v[136:139], a[32:35], v[80:83]\n"
"s_waitcnt lgkmcnt(4)\n"
"buffer_load_dwordx4 v229, s[56:59], s80 offen lds\n"
"v_mfma_f32_16x16x32_bf16 v[96:99], v[144:147], a[32:35], v[96:99]\n"
"s_waitcnt vmcnt(12)\n"
"ds_read_b64_tr_b16 v[128:129], v196 offset:8192\n"
"v_cvt_pk_bf16_f32 v7, v22, v23\n"
"v_mfma_f32_16x16x32_bf16 v[96:99], v[148:151], a[48:51], v[96:99]\n"
"s_add_u32 s80, s80, s101\n"
"v_max_f32_e32 v8, 0, v8\n"
"v_mfma_f32_16x16x32_bf16 v[100:103], v[148:151], a[52:55], v[100:103]\n"
"s_addk_i32 m0, 1024\n"
"ds_read_b64_tr_b16 v[130:131], v196 offset:10240\n"
"v_max_f32_e32 v9, 0, v9\n"
"v_mfma_f32_16x16x32_bf16 v[100:103], v[144:147], a[36:39], v[100:103]\n"
"buffer_load_dwordx4 v229, s[56:59], s80 offen lds\n"
"v_mfma_f32_16x16x32_bf16 v[104:107], v[144:147], a[40:43], v[104:107]\n"
"s_add_u32 s80, s80, s100\n"
"ds_read_b64_tr_b16 v[132:133], v196 offset:12288\n"
"v_max_f32_e32 v10, 0, v10\n"
"v_mfma_f32_16x16x32_bf16 v[104:107], v[148:151], a[56:59], v[104:107]\n"
"s_addk_i32 m0, 1024\n"
"v_max_f32_e32 v11, 0, v11\n"
"v_mfma_f32_16x16x32_bf16 v[108:111], v[148:151], a[60:63], v[108:111]\n"
"ds_read_b64_tr_b16 v[134:135], v196 offset:14336\n"
"v_mfma_f32_16x16x32_bf16 v[108:111], v[144:147], a[44:47], v[108:111]\n"
"s_waitcnt lgkmcnt(4)\n"
"buffer_load_dwordx4 v229, s[56:59], s80 offen lds\n"
"v_mfma_f32_16x16x32_bf16 v[124:127], v[152:155], a[44:47], v[124:127]\n"
"s_sub_u32 s80, s80, s101\n"
"ds_read_b64_tr_b16 v[136:137], v196 offset:8200\n"
"v_max_f32_e32 v24, 0, v24\n"
"v_mfma_f32_16x16x32_bf16 v[124:127], v[156:159], a[60:63], v[124:127]\n"
"s_addk_i32 m0, 1024\n"
"v_max_f32_e32 v25, 0, v25\n"
"v_mfma_f32_16x16x32_bf16 v[120:123], v[156:159], a[56:59], v[120:123]\n"
"ds_read_b64_tr_b16 v[138:139], v196 offset:10248\n"
"v_max_f32_e32 v26, 0, v26\n"
"v_mfma_f32_16x16x32_bf16 v[120:123], v[152:155], a[40:43], v[120:123]\n"
"buffer_load_dwordx4 v229, s[56:59], s80 offen lds\n"
"v_mfma_f32_16x16x32_bf16 v[116:119], v[152:155], a[36:39], v[116:119]\n"
"s_add_u32 s80, s80, s100\n"
"ds_read_b64_tr_b16 v[140:141], v196 offset:12296\n"
"v_max_f32_e32 v27, 0, v27\n"
"v_mfma_f32_16x16x32_bf16 v[116:119], v[156:159], a[52:55], v[116:119]\n"
"s_addk_i32 m0, 1024\n"
"v_mfma_f32_16x16x32_bf16 v[112:115], v[156:159], a[48:51], v[112:115]\n"
"ds_read_b64_tr_b16 v[142:143], v196 offset:14344\n"
"v_cvt_pk_bf16_f32 v8, v8, v9\n"
"v_mfma_f32_16x16x32_bf16 v[112:115], v[152:155], a[32:35], v[112:115]\n"
"s_waitcnt lgkmcnt(4)\n"
"buffer_load_dwordx4 v229, s[56:59], s80 offen lds\n"
"v_mfma_f32_16x16x32_bf16 v[64:67], v[128:131], a[64:67], v[64:67]\n"
"s_add_u32 s80, s85, 2621440\n"
"ds_read_b64_tr_b16 v[144:145], v197 offset:8192\n"
"v_cvt_pk_bf16_f32 v9, v10, v11\n"
"v_mfma_f32_16x16x32_bf16 v[64:67], v[132:135], a[80:83], v[64:67]\n"
"s_add_u32 m0, s46, 65536\n"
"v_cvt_pk_bf16_f32 v10, v24, v25\n"
"v_mfma_f32_16x16x32_bf16 v[68:71], v[132:135], a[84:87], v[68:71]\n"
"ds_read_b64_tr_b16 v[146:147], v197 offset:10240\n"
"v_mfma_f32_16x16x32_bf16 v[68:71], v[128:131], a[68:71], v[68:71]\n"
"buffer_load_dwordx4 v229, s[56:59], s80 offen lds\n"
"v_mfma_f32_16x16x32_bf16 v[72:75], v[128:131], a[72:75], v[72:75]\n"
"s_add_u32 s80, s80, s100\n"
"ds_read_b64_tr_b16 v[148:149], v197 offset:12288\n"
"v_cvt_pk_bf16_f32 v11, v26, v27\n"
"v_mfma_f32_16x16x32_bf16 v[72:75], v[132:135], a[88:91], v[72:75]\n"
"s_addk_i32 m0, 1024\n"
"v_max_f32_e32 v12, 0, v12\n"
"v_mfma_f32_16x16x32_bf16 v[76:79], v[132:135], a[92:95], v[76:79]\n"
"ds_read_b64_tr_b16 v[150:151], v197 offset:14336\n"
"v_max_f32_e32 v13, 0, v13\n"
"v_mfma_f32_16x16x32_bf16 v[76:79], v[128:131], a[76:79], v[76:79]\n"
"s_waitcnt lgkmcnt(4)\n"
"buffer_load_dwordx4 v229, s[56:59], s80 offen lds\n"
"v_mfma_f32_16x16x32_bf16 v[92:95], v[136:139], a[76:79], v[92:95]\n"
"s_sub_u32 s80, s80, s101\n"
"ds_read_b64_tr_b16 v[152:153], v197 offset:8200\n"
"v_max_f32_e32 v14, 0, v14\n"
"v_mfma_f32_16x16x32_bf16 v[92:95], v[140:143], a[92:95], v[92:95]\n"
"s_addk_i32 m0, 1024\n"
"v_mfma_f32_16x16x32_bf16 v[88:91], v[140:143], a[88:91], v[88:91]\n"
"ds_read_b64_tr_b16 v[154:155], v197 offset:10248\n"
"v_max_f32_e32 v15, 0, v15\n"
"v_mfma_f32_16x16x32_bf16 v[88:91], v[136:139], a[72:75], v[88:91]\n"
"buffer_load_dwordx4 v229, s[56:59], s80 offen lds\n"
"v_mfma_f32_16x16x32_bf16 v[84:87], v[136:139], a[68:71], v[84:87]\n"
"s_add_u32 s80, s80, s100\n"
"ds_read_b64_tr_b16 v[156:157], v197 offset:12296\n"
"v_max_f32_e32 v28, 0, v28\n"
"v_mfma_f32_16x16x32_bf16 v[84:87], v[140:143], a[84:87], v[84:87]\n"
"s_addk_i32 m0, 1024\n"
"v_max_f32_e32 v29, 0, v29\n"
"v_mfma_f32_16x16x32_bf16 v[80:83], v[140:143], a[80:83], v[80:83]\n"
"ds_read_b64_tr_b16 v[158:159], v197 offset:14344\n"
"v_mfma_f32_16x16x32_bf16 v[80:83], v[136:139], a[64:67], v[80:83]\n"
"s_waitcnt lgkmcnt(4)\n"
"buffer_load_dwordx4 v229, s[56:59], s80 offen lds\n"
"v_mfma_f32_16x16x32_bf16 v[96:99], v[144:147], a[64:67], v[96:99]\n"
"s_waitcnt vmcnt(12)\n"
"ds_read_b64_tr_b16 v[128:129], v198 offset:8192\n"
"v_max_f32_e32 v30, 0, v30\n"
"v_mfma_f32_16x16x32_bf16 v[96:99], v[148:151], a[80:83], v[96:99]\n"
"s_add_u32 s80, s80, s101\n"
"v_max_f32_e32 v31, 0, v31\n"
"v_mfma_f32_16x16x32_bf16 v[100:103], v[148:151], a[84:87], v[100:103]\n"
"s_addk_i32 m0, 1024\n"
"ds_read_b64_tr_b16 v[130:131], v198 offset:10240\n"
"v_cvt_pk_bf16_f32 v12, v12, v13\n"
"v_mfma_f32_16x16x32_bf16 v[100:103], v[144:147], a[68:71], v[100:103]\n"
"buffer_load_dwordx4 v229, s[56:59], s80 offen lds\n"
"v_mfma_f32_16x16x32_bf16 v[104:107], v[144:147], a[72:75], v[104:107]\n"
"s_add_u32 s80, s80, s100\n"
"ds_read_b64_tr_b16 v[132:133], v198 offset:12288\n"
"v_cvt_pk_bf16_f32 v13, v14, v15\n"
"v_mfma_f32_16x16x32_bf16 v[104:107], v[148:151], a[88:91], v[104:107]\n"
"s_addk_i32 m0, 1024\n"
"v_cvt_pk_bf16_f32 v14, v28, v29\n"
"v_mfma_f32_16x16x32_bf16 v[108:111], v[148:151], a[92:95], v[108:111]\n"
"ds_read_b64_tr_b16 v[134:135], v198 offset:14336\n"
"v_mfma_f32_16x16x32_bf16 v[108:111], v[144:147], a[76:79], v[108:111]\n"
"s_waitcnt lgkmcnt(4)\n"
"buffer_load_dwordx4 v229, s[56:59], s80 offen lds\n"
"v_mfma_f32_16x16x32_bf16 v[124:127], v[152:155], a[76:79], v[124:127]\n"
"s_sub_u32 s80, s80, s101\n"
"ds_read_b64_tr_b16 v[136:137], v198 offset:8200\n"
"v_cvt_pk_bf16_f32 v15, v30, v31\n"
"v_mfma_f32_16x16x32_bf16 v[124:127], v[156:159], a[92:95], v[124:127]\n"
"s_addk_i32 m0, 1024\n"
"v_max_f32_e32 v32, 0, v32\n"
"v_mfma_f32_16x16x32_bf16 v[120:123], v[156:159], a[88:91], v[120:123]\n"
"ds_read_b64_tr_b16 v[138:139], v198 offset:10248\n"
"v_max_f32_e32 v33, 0, v33\n"
"v_mfma_f32_16x16x32_bf16 v[120:123], v[152:155], a[72:75], v[120:123]\n"
"buffer_load_dwordx4 v229, s[56:59], s80 offen lds\n"
"v_mfma_f32_16x16x32_bf16 v[116:119], v[152:155], a[68:71], v[116:119]\n"
"s_add_u32 s80, s80, s100\n"
"ds_read_b64_tr_b16 v[140:141], v198 offset:12296\n"
"v_max_f32_e32 v34, 0, v34\n"
"v_mfma_f32_16x16x32_bf16 v[116:119], v[156:159], a[84:87], v[116:119]\n"
"s_addk_i32 m0, 1024\n"
"v_mfma_f32_16x16x32_bf16 v[112:115], v[156:159], a[80:83], v[112:115]\n"
"ds_read_b64_tr_b16 v[142:143], v198 offset:14344\n"
"v_max_f32_e32 v35, 0, v35\n"
"v_mfma_f32_16x16x32_bf16 v[112:115], v[152:155], a[64:67], v[112:115]\n"
"s_waitcnt lgkmcnt(4)\n"
"buffer_load_dwordx4 v229, s[56:59], s80 offen lds\n"
"v_mfma_f32_16x16x32_bf16 v[64:67], v[128:131], a[96:99], v[64:67]\n"
"s_add_u32 s80, s85, 3145728\n"
"ds_read_b64_tr_b16 v[144:145], v199 offset:8192\n"
"v_max_f32_e32 v48, 0, v48\n"
"v_mfma_f32_16x16x32_bf16 v[64:67], v[132:135], a[112:115], v[64:67]\n"
"s_add_u32 m0, s46, 8192\n"
"v_max_f32_e32 v49, 0, v49\n"
"v_mfma_f32_16x16x32_bf16 v[68:71], v[132:135], a[116:119], v[68:71]\n"
"ds_read_b64_tr_b16 v[146:147], v199 offset:10240\n"
"v_mfma_f32_16x16x32_bf16 v[68:71], v[128:131], a[100:103], v[68:71]\n"
"buffer_load_dwordx4 v229, s[56:59], s80 offen lds\n"
"v_mfma_f32_16x16x32_bf16 v[72:75], v[128:131], a[104:107], v[72:75]\n"
"s_add_u32 s80, s80, s100\n"
"ds_read_b64_tr_b16 v[148:149], v199 offset:12288\n"
"v_max_f32_e32 v50, 0, v50\n"
"v_mfma_f32_16x16x32_bf16 v[72:75], v[132:135], a[120:123], v[72:75]\n"
"s_addk_i32 m0, 1024\n"
"v_max_f32_e32 v51, 0, v51\n"
"v_mfma_f32_16x16x32_bf16 v[76:79], v[132:135], a[124:127], v[76:79]\n"
"ds_read_b64_tr_b16 v[150:151], v199 offset:14336\n"
"v_mfma_f32_16x16x32_bf16 v[76:79], v[128:131], a[108:111], v[76:79]\n"
"s_waitcnt lgkmcnt(4)\n"
"buffer_load_dwordx4 v229, s[56:59], s80 offen lds\n"
"v_mfma_f32_16x16x32_bf16 v[92:95], v[136:139], a[108:111], v[92:95]\n"
"s_sub_u32 s80, s80, s101\n"
"ds_read_b64_tr_b16 v[152:153], v199 offset:8200\n"
"v_cvt_pk_bf16_f32 v32, v32, v33\n"
"v_mfma_f32_16x16x32_bf16 v[92:95], v[140:143], a[124:127], v[92:95]\n"
"s_addk_i32 m0, 1024\n"
"v_cvt_pk_bf16_f32 v33, v34, v35\n"
"v_mfma_f32_16x16x32_bf16 v[88:91], v[140:143], a[120:123], v[88:91]\n"
"ds_read_b64_tr_b16 v[154:155], v199 offset:10248\n"
"v_cvt_pk_bf16_f32 v34, v48, v49\n"
"v_mfma_f32_16x16x32_bf16 v[88:91], v[136:139], a[104:107], v[88:91]\n"
"buffer_load_dwordx4 v229, s[56:59], s80 offen lds\n"
"v_mfma_f32_16x16x32_bf16 v[84:87], v[136:139], a[100:103], v[84:87]\n"
"s_add_u32 s80, s80, s100\n"
"ds_read_b64_tr_b16 v[156:157], v199 offset:12296\n"
"v_cvt_pk_bf16_f32 v35, v50, v51\n"
"v_mfma_f32_16x16x32_bf16 v[84:87], v[140:143], a[116:119], v[84:87]\n"
"s_addk_i32 m0, 1024\n"
"v_mfma_f32_16x16x32_bf16 v[80:83], v[140:143], a[112:115], v[80:83]\n"
"ds_read_b64_tr_b16 v[158:159], v199 offset:14344\n"
"v_max_f32_e32 v36, 0, v36\n"
"v_mfma_f32_16x16x32_bf16 v[80:83], v[136:139], a[96:99], v[80:83]\n"
"s_waitcnt lgkmcnt(4)\n"
"buffer_load_dwordx4 v229, s[56:59], s80 offen lds\n"
"v_mfma_f32_16x16x32_bf16 v[96:99], v[144:147], a[96:99], v[96:99]\n"
"s_waitcnt vmcnt(12)\n"
"ds_read_b64_tr_b16 v[128:129], v196 offset:0\n"
"v_max_f32_e32 v37, 0, v37\n"
"v_mfma_f32_16x16x32_bf16 v[96:99], v[148:151], a[112:115], v[96:99]\n"
"s_add_u32 s80, s80, s101\n"
"v_max_f32_e32 v38, 0, v38\n"
"v_mfma_f32_16x16x32_bf16 v[100:103], v[148:151], a[116:119], v[100:103]\n"
"s_addk_i32 m0, 1024\n"
"ds_read_b64_tr_b16 v[130:131], v196 offset:2048\n"
"v_mfma_f32_16x16x32_bf16 v[100:103], v[144:147], a[100:103], v[100:103]\n"
"buffer_load_dwordx4 v229, s[56:59], s80 offen lds\n"
"v_mfma_f32_16x16x32_bf16 v[104:107], v[144:147], a[104:107], v[104:107]\n"
"s_add_u32 s80, s80, s100\n"
"ds_read_b64_tr_b16 v[132:133], v196 offset:4096\n"
"v_max_f32_e32 v39, 0, v39\n"
"v_mfma_f32_16x16x32_bf16 v[104:107], v[148:151], a[120:123], v[104:107]\n"
"s_addk_i32 m0, 1024\n"
"v_max_f32_e32 v52, 0, v52\n"
"v_mfma_f32_16x16x32_bf16 v[108:111], v[148:151], a[124:127], v[108:111]\n"
"ds_read_b64_tr_b16 v[134:135], v196 offset:6144\n"
"v_mfma_f32_16x16x32_bf16 v[108:111], v[144:147], a[108:111], v[108:111]\n"
"s_waitcnt lgkmcnt(4)\n"
"buffer_load_dwordx4 v229, s[56:59], s80 offen lds\n"
"v_mfma_f32_16x16x32_bf16 v[124:127], v[152:155], a[108:111], v[124:127]\n"
"s_sub_u32 s80, s80, s101\n"
"ds_read_b64_tr_b16 v[136:137], v196 offset:8\n"
"v_max_f32_e32 v53, 0, v53\n"
"v_mfma_f32_16x16x32_bf16 v[124:127], v[156:159], a[124:127], v[124:127]\n"
"s_addk_i32 m0, 1024\n"
"v_max_f32_e32 v54, 0, v54\n"
"v_mfma_f32_16x16x32_bf16 v[120:123], v[156:159], a[120:123], v[120:123]\n"
"ds_read_b64_tr_b16 v[138:139], v196 offset:2056\n"
"v_max_f32_e32 v55, 0, v55\n"
"v_mfma_f32_16x16x32_bf16 v[120:123], v[152:155], a[104:107], v[120:123]\n"
"buffer_load_dwordx4 v229, s[56:59], s80 offen lds\n"
"v_mfma_f32_16x16x32_bf16 v[116:119], v[152:155], a[100:103], v[116:119]\n"
"s_add_u32 s80, s80, s100\n"
"ds_read_b64_tr_b16 v[140:141], v196 offset:4104\n"
"v_cvt_pk_bf16_f32 v36, v36, v37\n"
"v_mfma_f32_16x16x32_bf16 v[116:119], v[156:159], a[116:119], v[116:119]\n"
"s_addk_i32 m0, 1024\n"
"v_mfma_f32_16x16x32_bf16 v[112:115], v[156:159], a[112:115], v[112:115]\n"
"ds_read_b64_tr_b16 v[142:143], v196 offset:6152\n"
"v_cvt_pk_bf16_f32 v37, v38, v39\n"
"v_mfma_f32_16x16x32_bf16 v[112:115], v[152:155], a[96:99], v[112:115]\n"
"s_waitcnt lgkmcnt(4)\n"
"buffer_load_dwordx4 v229, s[56:59], s80 offen lds\n"
"v_mfma_f32_16x16x32_bf16 v[64:67], v[128:131], a[128:131], v[64:67]\n"
"s_add_u32 s80, s85, 3670016\n"
"ds_read_b64_tr_b16 v[144:145], v197 offset:0\n"
"v_cvt_pk_bf16_f32 v38, v52, v53\n"
"v_mfma_f32_16x16x32_bf16 v[64:67], v[132:135], a[144:147], v[64:67]\n"
"s_add_u32 m0, s46, 73728\n"
"v_cvt_pk_bf16_f32 v39, v54, v55\n"
"v_mfma_f32_16x16x32_bf16 v[68:71], v[132:135], a[148:151], v[68:71]\n"
".LPG21_%=:\n"
"s_cbranch_execz .LPGR_%=\n"
"ds_read_b64_tr_b16 v[146:147], v197 offset:2048\n"
"v_mfma_f32_16x16x32_bf16 v[68:71], v[128:131], a[132:135], v[68:71]\n"
"buffer_load_dwordx4 v229, s[56:59], s80 offen lds\n"
"v_mfma_f32_16x16x32_bf16 v[72:75], v[128:131], a[136:139], v[72:75]\n"
"s_add_u32 s80, s80, s100\n"
"ds_read_b64_tr_b16 v[148:149], v197 offset:4096\n"
"v_max_f32_e32 v40, 0, v40\n"
"v_mfma_f32_16x16x32_bf16 v[72:75], v[132:135], a[152:155], v[72:75]\n"
"s_addk_i32 m0, 1024\n"
"v_max_f32_e32 v41, 0, v41\n"
"v_mfma_f32_16x16x32_bf16 v[76:79], v[132:135], a[156:159], v[76:79]\n"
"ds_read_b64_tr_b16 v[150:151], v197 offset:6144\n"
"v_mfma_f32_16x16x32_bf16 v[76:79], v[128:131], a[140:143], v[76:79]\n"
"s_waitcnt lgkmcnt(4)\n"
"buffer_load_dwordx4 v229, s[56:59], s80 offen lds\n"
"v_mfma_f32_16x16x32_bf16 v[92:95], v[136:139], a[140:143], v[92:95]\n"
"s_sub_u32 s80, s80, s101\n"
"ds_read_b64_tr_b16 v[152:153], v197 offset:8\n"
"v_max_f32_e32 v42, 0, v42\n"
"v_mfma_f32_16x16x32_bf16 v[92:95], v[140:143], a[156:159], v[92:95]\n"
"s_addk_i32 m0, 1024\n"
"v_max_f32_e32 v43, 0, v43\n"
"v_mfma_f32_16x16x32_bf16 v[88:91], v[140:143], a[152:155], v[88:91]\n"
"ds_read_b64_tr_b16 v[154:155], v197 offset:2056\n"
"v_max_f32_e32 v56, 0, v56\n"
"v_mfma_f32_16x16x32_bf16 v[88:91], v[136:139], a[136:139], v[88:91]\n"
"buffer_load_dwordx4 v229, s[56:59], s80 offen lds\n"
"v_mfma_f32_16x16x32_bf16 v[84:87], v[136:139], a[132:135], v[84:87]\n"
"s_add_u32 s80, s80, s100\n"
"ds_read_b64_tr_b16 v[156:157], v197 offset:4104\n"
"v_max_f32_e32 v57, 0, v57\n"
"v_mfma_f32_16x16x32_bf16 v[84:87], v[140:143], a[148:151], v[84:87]\n"
"s_addk_i32 m0, 1024\n"
"v_mfma_f32_16x16x32_bf16 v[80:83], v[140:143], a[144:147], v[80:83]\n"
"ds_read_b64_tr_b16 v[158:159], v197 offset:6152\n"
"v_max_f32_e32 v58, 0, v58\n"
"v_mfma_f32_16x16x32_bf16 v[80:83], v[136:139], a[128:131], v[80:83]\n"
"s_waitcnt lgkmcnt(4)\n"
"buffer_load_dwordx4 v229, s[56:59], s80 offen lds\n"
"v_mfma_f32_16x16x32_bf16 v[96:99], v[144:147], a[128:131], v[96:99]\n"
"s_waitcnt vmcnt(12)\n"
"ds_read_b64_tr_b16 v[128:129], v198 offset:0\n"
"v_max_f32_e32 v59, 0, v59\n"
"v_mfma_f32_16x16x32_bf16 v[96:99], v[148:151], a[144:147], v[96:99]\n"
"s_add_u32 s80, s80, s101\n"
"v_cvt_pk_bf16_f32 v40, v40, v41\n"
"v_mfma_f32_16x16x32_bf16 v[100:103], v[148:151], a[148:151], v[100:103]\n"
"s_addk_i32 m0, 1024\n"
"ds_read_b64_tr_b16 v[130:131], v198 offset:2048\n"
"v_mfma_f32_16x16x32_bf16 v[100:103], v[144:147], a[132:135], v[100:103]\n"
"buffer_load_dwordx4 v229, s[56:59], s80 offen lds\n"
"v_mfma_f32_16x16x32_bf16 v[104:107], v[144:147], a[136:139], v[104:107]\n"
"s_add_u32 s80, s80, s100\n"
"ds_read_b64_tr_b16 v[132:133], v198 offset:4096\n"
"v_cvt_pk_bf16_f32 v41, v42, v43\n"
"v_mfma_f32_16x16x32_bf16 v[104:107], v[148:151], a[152:155], v[104:107]\n"
"s_addk_i32 m0, 1024\n"
"v_cvt_pk_bf16_f32 v42, v56, v57\n"
"v_mfma_f32_16x16x32_bf16 v[108:111], v[148:151], a[156:159], v[108:111]\n"
"ds_read_b64_tr_b16 v[134:135], v198 offset:6144\n"
"v_mfma_f32_16x16x32_bf16 v[108:111], v[144:147], a[140:143], v[108:111]\n"
"s_waitcnt lgkmcnt(4)\n"
"buffer_load_dwordx4 v229, s[56:59], s80 offen lds\n"
"v_mfma_f32_16x16x32_bf16 v[124:127], v[152:155], a[140:143], v[124:127]\n"
"s_sub_u32 s80, s80, s101\n"
"ds_read_b64_tr_b16 v[136:137], v198 offset:8\n"
"v_cvt_pk_bf16_f32 v43, v58, v59\n"
"v_mfma_f32_16x16x32_bf16 v[124:127], v[156:159], a[156:159], v[124:127]\n"
"s_addk_i32 m0, 1024\n"
"v_max_f32_e32 v44, 0, v44\n"
"v_mfma_f32_16x16x32_bf16 v[120:123], v[156:159], a[152:155], v[120:123]\n"
"ds_read_b64_tr_b16 v[138:139], v198 offset:2056\n"
"v_max_f32_e32 v45, 0, v45\n"
"v_mfma_f32_16x16x32_bf16 v[120:123], v[152:155], a[136:139], v[120:123]\n"
"buffer_load_dwordx4 v229, s[56:59], s80 offen lds\n"
"v_mfma_f32_16x16x32_bf16 v[116:119], v[152:155], a[132:135], v[116:119]\n"
"s_add_u32 s80, s80, s100\n"
"ds_read_b64_tr_b16 v[140:141], v198 offset:4104\n"
"v_max_f32_e32 v46, 0, v46\n"
"v_mfma_f32_16x16x32_bf16 v[116:119], v[156:159], a[148:151], v[116:119]\n"
"s_addk_i32 m0, 1024\n"
"v_mfma_f32_16x16x32_bf16 v[112:115], v[156:159], a[144:147], v[112:115]\n"
"ds_read_b64_tr_b16 v[142:143], v198 offset:6152\n"
"v_max_f32_e32 v47, 0, v47\n"
"v_mfma_f32_16x16x32_bf16 v[112:115], v[152:155], a[128:131], v[112:115]\n"
"s_waitcnt lgkmcnt(4)\n"
"buffer_load_dwordx4 v229, s[56:59], s80 offen lds\n"
"v_mfma_f32_16x16x32_bf16 v[64:67], v[128:131], a[160:163], v[64:67]\n"
"s_add_u32 s80, s86, 0\n"
"s_add_u32 m0, s46, 0\n"
"ds_read_b64_tr_b16 v[144:145], v199 offset:0\n"
"v_mfma_f32_16x16x32_bf16 v[64:67], v[132:135], a[176:179], v[64:67]\n"
"buffer_load_dwordx4 v229, s[56:59], s80 offen lds\n"
"v_mfma_f32_16x16x32_bf16 v[68:71], v[132:135], a[180:183], v[68:71]\n"
"s_add_u32 s80, s80, s100\n"
"ds_read_b64_tr_b16 v[146:147], v199 offset:2048\n"
"v_max_f32_e32 v60, 0, v60\n"
"v_mfma_f32_16x16x32_bf16 v[68:71], v[128:131], a[164:167], v[68:71]\n"
"s_addk_i32 m0, 1024\n"
"v_max_f32_e32 v61, 0, v61\n"
"v_max_f32_e32 v62, 0, v62\n"
"v_mfma_f32_16x16x32_bf16 v[72:75], v[128:131], a[168:171], v[72:75]\n"
"ds_read_b64_tr_b16 v[148:149], v199 offset:4096\n"
"v_mfma_f32_16x16x32_bf16 v[72:75], v[132:135], a[184:187], v[72:75]\n"
"buffer_load_dwordx4 v229, s[56:59], s80 offen lds\n"
"v_mfma_f32_16x16x32_bf16 v[76:79], v[132:135], a[188:191], v[76:79]\n"
"s_sub_u32 s80, s80, s101\n"
"ds_read_b64_tr_b16 v[150:151], v199 offset:6144\n"
"v_max_f32_e32 v63, 0, v63\n"
"v_mfma_f32_16x16x32_bf16 v[76:79], v[128:131], a[172:175], v[76:79]\n"
"s_waitcnt lgkmcnt(4)\n"
"v_cvt_pk_bf16_f32 v44, v44, v45\n"
"v_mfma_f32_16x16x32_bf16 v[92:95], v[136:139], a[172:175], v[92:95]\n"
"s_addk_i32 m0, 1024\n"
"ds_read_b64_tr_b16 v[152:153], v199 offset:8\n"
"v_cvt_pk_bf16_f32 v45, v46, v47\n"
"v_mfma_f32_16x16x32_bf16 v[92:95], v[140:143], a[188:191], v[92:95]\n"
"buffer_load_dwordx4 v229, s[56:59], s80 offen lds\n"
"v_mfma_f32_16x16x32_bf16 v[88:91], v[140:143], a[184:187], v[88:91]\n"
"s_add_u32 s80, s80, s100\n"
"s_addk_i32 m0, 1024\n"
"ds_read_b64_tr_b16 v[154:155], v199 offset:2056\n"
"v_mfma_f32_16x16x32_bf16 v[88:91], v[136:139], a[168:171], v[88:91]\n"
"buffer_load_dwordx4 v229, s[56:59], s80 offen lds\n"
"v_mfma_f32_16x16x32_bf16 v[84:87], v[136:139], a[164:167], v[84:87]\n"
"s_add_u32 s80, s80, s101\n"
"ds_read_b64_tr_b16 v[156:157], v199 offset:4104\n"
"v_cvt_pk_bf16_f32 v46, v60, v61\n"
"v_mfma_f32_16x16x32_bf16 v[84:87], v[140:143], a[180:183], v[84:87]\n"
"s_addk_i32 m0, 1024\n"
"v_cvt_pk_bf16_f32 v47, v62, v63\n"
"v_mfma_f32_16x16x32_bf16 v[80:83], v[140:143], a[176:179], v[80:83]\n"
"ds_read_b64_tr_b16 v[158:159], v199 offset:6152\n"
"v_lshrrev_b32_e32 v204, 8, v255\n"
"v_mfma_f32_16x16x32_bf16 v[80:83], v[136:139], a[160:163], v[80:83]\n"
"s_waitcnt lgkmcnt(4)\n"
"buffer_load_dwordx4 v229, s[56:59], s80 offen lds\n"
"v_mfma_f32_16x16x32_bf16 v[96:99], v[144:147], a[160:163], v[96:99]\n"
"s_waitcnt vmcnt(13)\n"
"s_addk_i32 m0, 1024\n"
"ds_read_b64_tr_b16 v[128:129], v196 offset:8192\n"
"v_mfma_f32_16x16x32_bf16 v[96:99], v[148:151], a[176:179], v[96:99]\n"
"s_add_u32 s80, s80, s100\n"
"buffer_load_dwordx4 v229, s[56:59], s80 offen lds\n"
"v_mfma_f32_16x16x32_bf16 v[100:103], v[148:151], a[180:183], v[100:103]\n"
"s_sub_u32 s80, s80, s101\n"
"ds_read_b64_tr_b16 v[130:131], v196 offset:10240\n"
"v_bfe_u32 v205, v255, 4, 4\n"
"v_mfma_f32_16x16x32_bf16 v[100:103], v[144:147], a[164:167], v[100:103]\n"
"s_addk_i32 m0, 1024\n"
"v_xor_b32_e32 v224, v204, v205\n"
"s_mul_i32 s83, s89, 128\n"
"v_mfma_f32_16x16x32_bf16 v[104:107], v[144:147], a[168:171], v[104:107]\n"
"ds_read_b64_tr_b16 v[132:133], v196 offset:12288\n"
"v_mfma_f32_16x16x32_bf16 v[104:107], v[148:151], a[184:187], v[104:107]\n"
"buffer_load_dwordx4 v229, s[56:59], s80 offen lds\n"
"v_mfma_f32_16x16x32_bf16 v[108:111], v[148:151], a[188:191], v[108:111]\n"
"s_add_u32 s80, s80, s100\n"
"ds_read_b64_tr_b16 v[134:135], v196 offset:14336\n"
"v_lshlrev_b32_e32 v224, 4, v224\n"
"v_mfma_f32_16x16x32_bf16 v[108:111], v[144:147], a[172:175], v[108:111]\n"
"s_waitcnt lgkmcnt(4)\n"
"v_lshl_add_u32 v224, v205, 8, v224\n"
"v_mfma_f32_16x16x32_bf16 v[124:127], v[152:155], a[172:175], v[124:127]\n"
"s_addk_i32 m0, 1024\n"
"ds_read_b64_tr_b16 v[136:137], v196 offset:8200\n"
"v_xor_b32_e32 v224, s83, v224\n"
"v_mfma_f32_16x16x32_bf16 v[124:127], v[156:159], a[188:191], v[124:127]\n"
"buffer_load_dwordx4 v229, s[56:59], s80 offen lds\n"
"v_mfma_f32_16x16x32_bf16 v[120:123], v[156:159], a[184:187], v[120:123]\n"
"ds_read_b64_tr_b16 v[138:139], v196 offset:10248\n"
"v_xor_b32_e32 v225, 64, v224\n"
"v_mfma_f32_16x16x32_bf16 v[120:123], v[152:155], a[168:171], v[120:123]\n"
"buffer_load_dword v195, v235, s[60:63], s34 offen\n"
"v_mfma_f32_16x16x32_bf16 v[116:119], v[152:155], a[164:167], v[116:119]\n"
"s_add_u32 s76, s76, 65536\n"
"ds_read_b64_tr_b16 v[140:141], v196 offset:12296\n"
"v_xor_b32_e32 v226, 128, v224\n"
"v_mfma_f32_16x16x32_bf16 v[116:119], v[156:159], a[180:183], v[116:119]\n"
"v_mfma_f32_16x16x32_bf16 v[112:115], v[156:159], a[176:179], v[112:115]\n"
"ds_read_b64_tr_b16 v[142:143], v196 offset:14344\n"
"v_xor_b32_e32 v227, 192, v224\n"
"v_mfma_f32_16x16x32_bf16 v[112:115], v[152:155], a[160:163], v[112:115]\n"
"s_waitcnt lgkmcnt(4)\n"
"buffer_load_dword v193, v234, s[72:75], s76 offen\n"
"v_mfma_f32_16x16x32_bf16 v[64:67], v[128:131], a[192:195], v[64:67]\n"
"s_add_u32 s80, s86, 524288\n"
"s_add_u32 m0, s46, 65536\n"
"ds_read_b64_tr_b16 v[144:145], v197 offset:8192\n"
"v_mfma_f32_16x16x32_bf16 v[64:67], v[132:135], a[208:211], v[64:67]\n"
"buffer_load_dwordx4 v229, s[56:59], s80 offen lds\n"
"v_mfma_f32_16x16x32_bf16 v[68:71], v[132:135], a[212:215], v[68:71]\n"
"s_add_u32 s80, s80, s100\n"
"ds_read_b64_tr_b16 v[146:147], v197 offset:10240\n"
"ds_write_b128 v233, v[0:3] offset:0\n"
"v_mfma_f32_16x16x32_bf16 v[68:71], v[128:131], a[196:199], v[68:71]\n"
"s_addk_i32 m0, 1024\n"
"ds_write_b128 v233, v[4:7] offset:1024\n"
"v_mfma_f32_16x16x32_bf16 v[72:75], v[128:131], a[200:203], v[72:75]\n"
"ds_read_b64_tr_b16 v[148:149], v197 offset:12288\n"
"ds_write_b128 v233, v[8:11] offset:2048\n"
"v_mfma_f32_16x16x32_bf16 v[72:75], v[132:135], a[216:219], v[72:75]\n"
"buffer_load_dwordx4 v229, s[56:59], s80 offen lds\n"
"v_mfma_f32_16x16x32_bf16 v[76:79], v[132:135], a[220:223], v[76:79]\n"
"s_sub_u32 s80, s80, s101\n"
"ds_read_b64_tr_b16 v[150:151], v197 offset:14336\n"
"ds_write_b128 v233, v[12:15] offset:3072\n"
"v_mfma_f32_16x16x32_bf16 v[76:79], v[128:131], a[204:207], v[76:79]\n"
"s_waitcnt lgkmcnt(8)\n"
"ds_write_b128 v233, v[32:35] offset:4096\n"
"v_mfma_f32_16x16x32_bf16 v[92:95], v[136:139], a[204:207], v[92:95]\n"
"s_addk_i32 m0, 1024\n"
"ds_read_b64_tr_b16 v[152:153], v197 offset:8200\n"
"ds_write_b128 v233, v[36:39] offset:5120\n"
"v_mfma_f32_16x16x32_bf16 v[92:95], v[140:143], a[220:223], v[92:95]\n"
"buffer_load_dwordx4 v229, s[56:59], s80 offen lds\n"
"v_mfma_f32_16x16x32_bf16 v[88:91], v[140:143], a[216:219], v[88:91]\n"
"s_add_u32 s80, s80, s100\n"
"ds_read_b64_tr_b16 v[154:155], v197 offset:10248\n"
"ds_write_b128 v233, v[40:43] offset:6144\n"
"v_mfma_f32_16x16x32_bf16 v[88:91], v[136:139], a[200:203], v[88:91]\n"
"s_addk_i32 m0, 1024\n"
"ds_write_b128 v233, v[44:47] offset:7168\n"
"v_mfma_f32_16x16x32_bf16 v[84:87], v[136:139], a[196:199], v[84:87]\n"
"ds_read_b64_tr_b16 v[156:157], v197 offset:12296\n"
"v_mfma_f32_16x16x32_bf16 v[84:87], v[140:143], a[212:215], v[84:87]\n"
"buffer_load_dwordx4 v229, s[56:59], s80 offen lds\n"
"v_mfma_f32_16x16x32_bf16 v[80:83], v[140:143], a[208:211], v[80:83]\n"
"s_add_u32 s80, s80, s101\n"
"ds_read_b64_tr_b16 v[158:159], v197 offset:14344\n"
"v_mfma_f32_16x16x32_bf16 v[80:83], v[136:139], a[192:195], v[80:83]\n"
"s_waitcnt lgkmcnt(9)\n"
"v_mfma_f32_16x16x32_bf16 v[96:99], v[144:147], a[192:195], v[96:99]\n"
"s_waitcnt vmcnt(14)\n"
"s_addk_i32 m0, 1024\n"
"ds_read_b64_tr_b16 v[128:129], v198 offset:8192\n"
"v_mfma_f32_16x16x32_bf16 v[96:99], v[148:151], a[208:211], v[96:99]\n"
"buffer_load_dwordx4 v229, s[56:59], s80 offen lds\n"
"v_mfma_f32_16x16x32_bf16 v[100:103], v[148:151], a[212:215], v[100:103]\n"
"s_add_u32 s80, s80, s100\n"
"s_addk_i32 m0, 1024\n"
"ds_read_b64_tr_b16 v[130:131], v198 offset:10240\n"
"v_mfma_f32_16x16x32_bf16 v[100:103], v[144:147], a[196:199], v[100:103]\n"
"buffer_load_dwordx4 v229, s[56:59], s80 offen lds\n"
"v_mfma_f32_16x16x32_bf16 v[104:107], v[144:147], a[200:203], v[104:107]\n"
"s_sub_u32 s80, s80, s101\n"
"ds_read_b64_tr_b16 v[132:133], v198 offset:12288\n"
"v_mfma_f32_16x16x32_bf16 v[104:107], v[148:151], a[216:219], v[104:107]\n"
"s_addk_i32 m0, 1024\n"
"v_mfma_f32_16x16x32_bf16 v[108:111], v[148:151], a[220:223], v[108:111]\n"
"ds_read_b64_tr_b16 v[134:135], v198 offset:14336\n"
"v_mfma_f32_16x16x32_bf16 v[108:111], v[144:147], a[204:207], v[108:111]\n"
"s_waitcnt lgkmcnt(4)\n"
"buffer_load_dwordx4 v229, s[56:59], s80 offen lds\n"
"v_mfma_f32_16x16x32_bf16 v[124:127], v[152:155], a[204:207], v[124:127]\n"
"s_add_u32 s80, s80, s100\n"
"ds_read_b64_tr_b16 v[136:137], v198 offset:8200\n"
"v_mfma_f32_16x16x32_bf16 v[124:127], v[156:159], a[220:223], v[124:127]\n"
"s_addk_i32 m0, 1024\n"
"v_mfma_f32_16x16x32_bf16 v[120:123], v[156:159], a[216:219], v[120:123]\n"
"ds_read_b64_tr_b16 v[138:139], v198 offset:10248\n"
"v_mfma_f32_16x16x32_bf16 v[120:123], v[152:155], a[200:203], v[120:123]\n"
"buffer_load_dwordx4 v229, s[56:59], s80 offen lds\n"
"v_mfma_f32_16x16x32_bf16 v[116:119], v[152:155], a[196:199], v[116:119]\n"
"ds_read_b64_tr_b16 v[140:141], v198 offset:12296\n"
"v_mfma_f32_16x16x32_bf16 v[116:119], v[156:159], a[212:215], v[116:119]\n"
"v_mfma_f32_16x16x32_bf16 v[112:115], v[156:159], a[208:211], v[112:115]\n"
"ds_read_b64_tr_b16 v[142:143], v198 offset:14344\n"
"v_mfma_f32_16x16x32_bf16 v[112:115], v[152:155], a[192:195], v[112:115]\n"
"s_waitcnt lgkmcnt(4)\n"
"buffer_load_dword v194, v255, s[52:55], s95 offen\n"
"v_mfma_f32_16x16x32_bf16 v[64:67], v[128:131], a[224:227], v[64:67]\n"
"s_add_u32 s80, s86, 1048576\n"
"ds_read_b64_tr_b16 v[144:145], v199 offset:8192\n"
"v_mfma_f32_16x16x32_bf16 v[64:67], v[132:135], a[240:243], v[64:67]\n"
"s_add_u32 m0, s46, 8192\n"
"v_mfma_f32_16x16x32_bf16 v[68:71], v[132:135], a[244:247], v[68:71]\n"
"ds_read_b64_tr_b16 v[146:147], v199 offset:10240\n"
"v_mfma_f32_16x16x32_bf16 v[68:71], v[128:131], a[228:231], v[68:71]\n"
"buffer_load_dwordx4 v229, s[56:59], s80 offen lds\n"
"v_mfma_f32_16x16x32_bf16 v[72:75], v[128:131], a[232:235], v[72:75]\n"
"s_add_u32 s80, s80, s100\n"
"ds_read_b64_tr_b16 v[148:149], v199 offset:12288\n"
"v_mfma_f32_16x16x32_bf16 v[72:75], v[132:135], a[248:251], v[72:75]\n"
"s_addk_i32 m0, 1024\n"
"v_mfma_f32_16x16x32_bf16 v[76:79], v[132:135], a[252:255], v[76:79]\n"
"ds_read_b64_tr_b16 v[150:151], v199 offset:14336\n"
"v_mfma_f32_16x16x32_bf16 v[76:79], v[128:131], a[236:239], v[76:79]\n"
"s_waitcnt lgkmcnt(4)\n"
"buffer_load_dwordx4 v229, s[56:59], s80 offen lds\n"
"v_mfma_f32_16x16x32_bf16 v[92:95], v[136:139], a[236:239], v[92:95]\n"
"s_sub_u32 s80, s80, s101\n"
"ds_read_b64_tr_b16 v[152:153], v199 offset:8200\n"
"v_mfma_f32_16x16x32_bf16 v[92:95], v[140:143], a[252:255], v[92:95]\n"
"s_addk_i32 m0, 1024\n"
"v_mfma_f32_16x16x32_bf16 v[88:91], v[140:143], a[248:251], v[88:91]\n"
"ds_read_b64_tr_b16 v[154:155], v199 offset:10248\n"
"v_mfma_f32_16x16x32_bf16 v[88:91], v[136:139], a[232:235], v[88:91]\n"
"buffer_load_dwordx4 v229, s[56:59], s80 offen lds\n"
"v_mfma_f32_16x16x32_bf16 v[84:87], v[136:139], a[228:231], v[84:87]\n"
"s_add_u32 s80, s80, s100\n"
"ds_read_b64_tr_b16 v[156:157], v199 offset:12296\n"
"v_mfma_f32_16x16x32_bf16 v[84:87], v[140:143], a[244:247], v[84:87]\n"
"s_addk_i32 m0, 1024\n"
"v_mfma_f32_16x16x32_bf16 v[80:83], v[140:143], a[240:243], v[80:83]\n"
"ds_read_b64_tr_b16 v[158:159], v199 offset:14344\n"
"v_mfma_f32_16x16x32_bf16 v[80:83], v[136:139], a[224:227], v[80:83]\n"
"s_waitcnt lgkmcnt(4)\n"
"buffer_load_dwordx4 v229, s[56:59], s80 offen lds\n"
"v_mfma_f32_16x16x32_bf16 v[96:99], v[144:147], a[224:227], v[96:99]\n"
"s_waitcnt vmcnt(15)\n"
"ds_read_b64_tr_b16 v[128:129], v196 offset:0\n"
"v_mfma_f32_16x16x32_bf16 v[96:99], v[148:151], a[240:243], v[96:99]\n"
"s_add_u32 s80, s80, s101\n"
"v_mfma_f32_16x16x32_bf16 v[100:103], v[148:151], a[244:247], v[100:103]\n"
"s_addk_i32 m0, 1024\n"
"ds_read_b64_tr_b16 v[130:131], v196 offset:2048\n"
"v_mfma_f32_16x16x32_bf16 v[100:103], v[144:147], a[228:231], v[100:103]\n"
"buffer_load_dwordx4 v229, s[56:59], s80 offen lds\n"
"v_mfma_f32_16x16x32_bf16 v[104:107], v[144:147], a[232:235], v[104:107]\n"
"s_add_u32 s80, s80, s100\n"
"ds_read_b64_tr_b16 v[132:133], v196 offset:4096\n"
"v_mfma_f32_16x16x32_bf16 v[104:107], v[148:151], a[248:251], v[104:107]\n"
"s_addk_i32 m0, 1024\n"
"v_mfma_f32_16x16x32_bf16 v[108:111], v[148:151], a[252:255], v[108:111]\n"
"ds_read_b64_tr_b16 v[134:135], v196 offset:6144\n"
"v_mfma_f32_16x16x32_bf16 v[108:111], v[144:147], a[236:239], v[108:111]\n"
"s_waitcnt lgkmcnt(4)\n"
"buffer_load_dwordx4 v229, s[56:59], s80 offen lds\n"
"v_mfma_f32_16x16x32_bf16 v[124:127], v[152:155], a[236:239], v[124:127]\n"
"s_sub_u32 s80, s80, s101\n"
"ds_read_b64_tr_b16 v[136:137], v196 offset:8\n"
"v_mfma_f32_16x16x32_bf16 v[124:127], v[156:159], a[252:255], v[124:127]\n"
"s_addk_i32 m0, 1024\n"
"v_mfma_f32_16x16x32_bf16 v[120:123], v[156:159], a[248:251], v[120:123]\n"
"ds_read_b64_tr_b16 v[138:139], v196 offset:2056\n"
"v_mfma_f32_16x16x32_bf16 v[120:123], v[152:155], a[232:235], v[120:123]\n"
"buffer_load_dwordx4 v229, s[56:59], s80 offen lds\n"
"v_mfma_f32_16x16x32_bf16 v[116:119], v[152:155], a[228:231], v[116:119]\n"
"s_add_u32 s80, s80, s100\n"
"ds_read_b64_tr_b16 v[140:141], v196 offset:4104\n"
"v_mfma_f32_16x16x32_bf16 v[116:119], v[156:159], a[244:247], v[116:119]\n"
"s_addk_i32 m0, 1024\n"
"v_mfma_f32_16x16x32_bf16 v[112:115], v[156:159], a[240:243], v[112:115]\n"
"ds_read_b64_tr_b16 v[142:143], v196 offset:6152\n"
"v_mfma_f32_16x16x32_bf16 v[112:115], v[152:155], a[224:227], v[112:115]\n"
"s_waitcnt lgkmcnt(4)\n"
"buffer_load_dwordx4 v229, s[56:59], s80 offen lds\n"
"v_mfma_f32_16x16x32_bf16 v[0:3], v[128:131], a[0:3], 0\n"
"s_add_u32 s80, s86, 1572864\n"
"ds_read_b64_tr_b16 v[144:145], v197 offset:0\n"
"s_add_u32 s84, s78, s79\n"
"v_mfma_f32_16x16x32_bf16 v[0:3], v[132:135], a[16:19], v[0:3]\n"
"s_add_u32 m0, s46, 73728\n"
"v_mfma_f32_16x16x32_bf16 v[4:7], v[132:135], a[20:23], 0\n"
"ds_read_b64_tr_b16 v[146:147], v197 offset:2048\n"
"v_mfma_f32_16x16x32_bf16 v[4:7], v[128:131], a[4:7], v[4:7]\n"
"buffer_load_dwordx4 v229, s[56:59], s80 offen lds\n"
"v_mfma_f32_16x16x32_bf16 v[8:11], v[128:131], a[8:11], 0\n"
"s_add_u32 s80, s80, s100\n"
"ds_read_b64_tr_b16 v[148:149], v197 offset:4096\n"
"s_and_b32 s84, s84, 7\n"
"v_mfma_f32_16x16x32_bf16 v[8:11], v[132:135], a[24:27], v[8:11]\n"
"s_addk_i32 m0, 1024\n"
"v_mfma_f32_16x16x32_bf16 v[12:15], v[132:135], a[28:31], 0\n"
"s_mov_b64 s[52:53], s[20:21]\n"
"ds_read_b64_tr_b16 v[150:151], v197 offset:6144\n"
"v_mfma_f32_16x16x32_bf16 v[12:15], v[128:131], a[12:15], v[12:15]\n"
"s_waitcnt lgkmcnt(4)\n"
"buffer_load_dwordx4 v229, s[56:59], s80 offen lds\n"
"v_mfma_f32_16x16x32_bf16 v[28:31], v[136:139], a[12:15], 0\n"
"s_sub_u32 s80, s80, s101\n"
"ds_read_b64_tr_b16 v[152:153], v197 offset:8\n"
"v_mfma_f32_16x16x32_bf16 v[28:31], v[140:143], a[28:31], v[28:31]\n"
"s_addk_i32 m0, 1024\n"
"s_mov_b32 s54, 0\n"
"v_mfma_f32_16x16x32_bf16 v[24:27], v[140:143], a[24:27], 0\n"
"ds_read_b64_tr_b16 v[154:155], v197 offset:2056\n"
"v_mfma_f32_16x16x32_bf16 v[24:27], v[136:139], a[8:11], v[24:27]\n"
"buffer_load_dwordx4 v229, s[56:59], s80 offen lds\n"
"v_mfma_f32_16x16x32_bf16 v[20:23], v[136:139], a[4:7], 0\n"
"s_add_u32 s80, s80, s100\n"
"ds_read_b64_tr_b16 v[156:157], v197 offset:4104\n"
"s_mov_b32 s55, 0x20000\n"
"v_mfma_f32_16x16x32_bf16 v[20:23], v[140:143], a[20:23], v[20:23]\n"
"s_addk_i32 m0, 1024\n"
"v_mfma_f32_16x16x32_bf16 v[16:19], v[140:143], a[16:19], 0\n"
"ds_read_b64_tr_b16 v[158:159], v197 offset:6152\n"
"v_mfma_f32_16x16x32_bf16 v[16:19], v[136:139], a[0:3], v[16:19]\n"
"s_waitcnt lgkmcnt(4)\n"
"buffer_load_dwordx4 v229, s[56:59], s80 offen lds\n"
"v_mfma_f32_16x16x32_bf16 v[32:35], v[144:147], a[0:3], 0\n"
"s_waitcnt vmcnt(13)\n"
"ds_read_b64_tr_b16 v[128:129], v198 offset:0\n"
"s_mul_i32 s22, s79, 610304\n"
"v_mfma_f32_16x16x32_bf16 v[32:35], v[148:151], a[16:19], v[32:35]\n"
"s_add_u32 s80, s80, s101\n"
"v_mfma_f32_16x16x32_bf16 v[36:39], v[148:151], a[20:23], 0\n"
"s_addk_i32 m0, 1024\n"
"ds_read_b64_tr_b16 v[130:131], v198 offset:2048\n"
"v_mfma_f32_16x16x32_bf16 v[36:39], v[144:147], a[4:7], v[36:39]\n"
"buffer_load_dwordx4 v229, s[56:59], s80 offen lds\n"
"v_mfma_f32_16x16x32_bf16 v[40:43], v[144:147], a[8:11], 0\n"
"s_add_u32 s80, s80, s100\n"
"ds_read_b64_tr_b16 v[132:133], v198 offset:4096\n"
"s_mul_i32 s83, s84, 75776\n"
"v_mfma_f32_16x16x32_bf16 v[40:43], v[148:151], a[24:27], v[40:43]\n"
"s_addk_i32 m0, 1024\n"
"v_mfma_f32_16x16x32_bf16 v[44:47], v[148:151], a[28:31], 0\n"
"s_add_u32 s22, s22, s83\n"
"ds_read_b64_tr_b16 v[134:135], v198 offset:6144\n"
"v_mfma_f32_16x16x32_bf16 v[44:47], v[144:147], a[12:15], v[44:47]\n"
"s_waitcnt lgkmcnt(4)\n"
"buffer_load_dwordx4 v229, s[56:59], s80 offen lds\n"
"v_mfma_f32_16x16x32_bf16 v[60:63], v[152:155], a[12:15], 0\n"
"s_sub_u32 s80, s80, s101\n"
"ds_read_b64_tr_b16 v[136:137], v198 offset:8\n"
"v_mfma_f32_16x16x32_bf16 v[60:63], v[156:159], a[28:31], v[60:63]\n"
"s_addk_i32 m0, 1024\n"
"s_mul_i32 s83, s77, 18432\n"
"v_mfma_f32_16x16x32_bf16 v[56:59], v[156:159], a[24:27], 0\n"
"ds_read_b64_tr_b16 v[138:139], v198 offset:2056\n"
"v_mfma_f32_16x16x32_bf16 v[56:59], v[152:155], a[8:11], v[56:59]\n"
"buffer_load_dwordx4 v229, s[56:59], s80 offen lds\n"
"v_mfma_f32_16x16x32_bf16 v[52:55], v[152:155], a[4:7], 0\n"
"s_add_u32 s80, s80, s100\n"
"ds_read_b64_tr_b16 v[140:141], v198 offset:4104\n"
"s_add_u32 s22, s22, s83\n"
"v_mfma_f32_16x16x32_bf16 v[52:55], v[156:159], a[20:23], v[52:55]\n"
"s_addk_i32 m0, 1024\n"
"v_mfma_f32_16x16x32_bf16 v[48:51], v[156:159], a[16:19], 0\n"
"ds_read_b64_tr_b16 v[142:143], v198 offset:6152\n"
"v_mfma_f32_16x16x32_bf16 v[48:51], v[152:155], a[0:3], v[48:51]\n"
"s_waitcnt lgkmcnt(4)\n"
"buffer_load_dwordx4 v229, s[56:59], s80 offen lds\n"
"v_mfma_f32_16x16x32_bf16 v[0:3], v[128:131], a[32:35], v[0:3]\n"
"s_add_u32 s80, s86, 2097152\n"
"ds_read_b64_tr_b16 v[144:145], v199 offset:0\n"
"s_add_u32 s54, s22, 14336\n"
"v_mfma_f32_16x16x32_bf16 v[0:3], v[132:135], a[48:51], v[0:3]\n"
"s_add_u32 m0, s46, 0\n"
"v_mfma_f32_16x16x32_bf16 v[4:7], v[132:135], a[52:55], v[4:7]\n"
"ds_read_b64_tr_b16 v[146:147], v199 offset:2048\n"
"v_mfma_f32_16x16x32_bf16 v[4:7], v[128:131], a[36:39], v[4:7]\n"
"buffer_load_dwordx4 v229, s[56:59], s80 offen lds\n"
"v_mfma_f32_16x16x32_bf16 v[8:11], v[128:131], a[40:43], v[8:11]\n"
"s_add_u32 s80, s80, s100\n"
"ds_read_b64_tr_b16 v[148:149], v199 offset:4096\n"
"s_sub_u32 s34, s22, 1024\n"
"v_mfma_f32_16x16x32_bf16 v[8:11], v[132:135], a[56:59], v[8:11]\n"
"s_addk_i32 m0, 1024\n"
"v_mfma_f32_16x16x32_bf16 v[12:15], v[132:135], a[60:63], v[12:15]\n"
"s_mul_i32 s85, s78, 524288\n"
"ds_read_b64_tr_b16 v[150:151], v199 offset:6144\n"
"v_mfma_f32_16x16x32_bf16 v[12:15], v[128:131], a[44:47], v[12:15]\n"
"s_waitcnt lgkmcnt(4)\n"
"buffer_load_dwordx4 v229, s[56:59], s80 offen lds\n"
"v_mfma_f32_16x16x32_bf16 v[28:31], v[136:139], a[44:47], v[28:31]\n"
"s_sub_u32 s80, s80, s101\n"
"ds_read_b64_tr_b16 v[152:153], v199 offset:8\n"
"v_mfma_f32_16x16x32_bf16 v[28:31], v[140:143], a[60:63], v[28:31]\n"
"s_addk_i32 m0, 1024\n"
"s_mul_i32 s83, s77, 131072\n"
"v_mfma_f32_16x16x32_bf16 v[24:27], v[140:143], a[56:59], v[24:27]\n"
"ds_read_b64_tr_b16 v[154:155], v199 offset:2056\n"
"v_mfma_f32_16x16x32_bf16 v[24:27], v[136:139], a[40:43], v[24:27]\n"
"buffer_load_dwordx4 v229, s[56:59], s80 offen lds\n"
"v_mfma_f32_16x16x32_bf16 v[20:23], v[136:139], a[36:39], v[20:23]\n"
"s_add_u32 s80, s80, s100\n"
"ds_read_b64_tr_b16 v[156:157], v199 offset:4104\n"
"s_add_u32 s85, s85, s83\n"
"v_mfma_f32_16x16x32_bf16 v[20:23], v[140:143], a[52:55], v[20:23]\n"
"s_addk_i32 m0, 1024\n"
"v_mfma_f32_16x16x32_bf16 v[16:19], v[140:143], a[48:51], v[16:19]\n"
"ds_read_b64_tr_b16 v[158:159], v199 offset:6152\n"
"v_mfma_f32_16x16x32_bf16 v[16:19], v[136:139], a[32:35], v[16:19]\n"
"s_waitcnt lgkmcnt(4)\n"
"buffer_load_dwordx4 v229, s[56:59], s80 offen lds\n"
"v_mfma_f32_16x16x32_bf16 v[32:35], v[144:147], a[32:35], v[32:35]\n"
"s_waitcnt vmcnt(12)\n"
"ds_read_b64_tr_b16 v[128:129], v196 offset:8192\n"
"s_mul_i32 s83, s35, 8192\n"
"v_mfma_f32_16x16x32_bf16 v[32:35], v[148:151], a[48:51], v[32:35]\n"
"s_add_u32 s80, s80, s101\n"
"v_mfma_f32_16x16x32_bf16 v[36:39], v[148:151], a[52:55], v[36:39]\n"
"s_addk_i32 m0, 1024\n"
"ds_read_b64_tr_b16 v[130:131], v196 offset:10240\n"
"v_mfma_f32_16x16x32_bf16 v[36:39], v[144:147], a[36:39], v[36:39]\n"
"buffer_load_dwordx4 v229, s[56:59], s80 offen lds\n"
"v_mfma_f32_16x16x32_bf16 v[40:43], v[144:147], a[40:43], v[40:43]\n"
"s_add_u32 s80, s80, s100\n"
"ds_read_b64_tr_b16 v[132:133], v196 offset:12288\n"
"s_add_u32 s85, s85, s83\n"
"v_mfma_f32_16x16x32_bf16 v[40:43], v[148:151], a[56:59], v[40:43]\n"
"s_addk_i32 m0, 1024\n"
"v_mfma_f32_16x16x32_bf16 v[44:47], v[148:151], a[60:63], v[44:47]\n"
"s_add_u32 s85, s85, -4096\n"
"ds_read_b64_tr_b16 v[134:135], v196 offset:14336\n"
"v_mfma_f32_16x16x32_bf16 v[44:47], v[144:147], a[44:47], v[44:47]\n"
"s_waitcnt lgkmcnt(4)\n"
"buffer_load_dwordx4 v229, s[56:59], s80 offen lds\n"
"v_mfma_f32_16x16x32_bf16 v[60:63], v[152:155], a[44:47], v[60:63]\n"
"s_sub_u32 s80, s80, s101\n"
"ds_read_b64_tr_b16 v[136:137], v196 offset:8200\n"
"v_mfma_f32_16x16x32_bf16 v[60:63], v[156:159], a[60:63], v[60:63]\n"
"s_addk_i32 m0, 1024\n"
"v_lshrrev_b32_e32 v204, 6, v255\n"
"v_mfma_f32_16x16x32_bf16 v[56:59], v[156:159], a[56:59], v[56:59]\n"
"ds_read_b64_tr_b16 v[138:139], v196 offset:10248\n"
"v_mfma_f32_16x16x32_bf16 v[56:59], v[152:155], a[40:43], v[56:59]\n"
"buffer_load_dwordx4 v229, s[56:59], s80 offen lds\n"
"v_mfma_f32_16x16x32_bf16 v[52:55], v[152:155], a[36:39], v[52:55]\n"
"s_add_u32 s80, s80, s100\n"
"ds_read_b64_tr_b16 v[140:141], v196 offset:12296\n"
"v_bfe_u32 v228, v255, 4, 2\n"
"v_mfma_f32_16x16x32_bf16 v[52:55], v[156:159], a[52:55], v[52:55]\n"
"s_addk_i32 m0, 1024\n"
"v_mfma_f32_16x16x32_bf16 v[48:51], v[156:159], a[48:51], v[48:51]\n"
"ds_read_b64_tr_b16 v[142:143], v196 offset:14344\n"
"v_mfma_f32_16x16x32_bf16 v[48:51], v[152:155], a[32:35], v[48:51]\n"
"s_waitcnt lgkmcnt(4)\n"
"buffer_load_dwordx4 v229, s[56:59], s80 offen lds\n"
"v_mfma_f32_16x16x32_bf16 v[0:3], v[128:131], a[64:67], v[0:3]\n"
"s_add_u32 s80, s86, 2621440\n"
"ds_read_b64_tr_b16 v[144:145], v197 offset:8192\n"
"v_lshlrev_b32_e32 v228, 3, v228\n"
"v_mfma_f32_16x16x32_bf16 v[0:3], v[132:135], a[80:83], v[0:3]\n"
"s_add_u32 m0, s46, 65536\n"
"v_mfma_f32_16x16x32_bf16 v[4:7], v[132:135], a[84:87], v[4:7]\n"
"ds_read_b64_tr_b16 v[146:147], v197 offset:10240\n"
"v_mfma_f32_16x16x32_bf16 v[4:7], v[128:131], a[68:71], v[4:7]\n"
"buffer_load_dwordx4 v229, s[56:59], s80 offen lds\n"
"v_mfma_f32_16x16x32_bf16 v[8:11], v[128:131], a[72:75], v[8:11]\n"
"s_add_u32 s80, s80, s100\n"
"ds_read_b64_tr_b16 v[148:149], v197 offset:12288\n"
"v_lshl_add_u32 v228, v204, 8, v228\n"
"v_mfma_f32_16x16x32_bf16 v[8:11], v[132:135], a[88:91], v[8:11]\n"
"s_addk_i32 m0, 1024\n"
"v_mfma_f32_16x16x32_bf16 v[12:15], v[132:135], a[92:95], v[12:15]\n"
"ds_read_b64_tr_b16 v[150:151], v197 offset:14336\n"
"v_and_b32_e32 v204, 7, v204\n"
"v_mfma_f32_16x16x32_bf16 v[12:15], v[128:131], a[76:79], v[12:15]\n"
"s_waitcnt lgkmcnt(4)\n"
"buffer_load_dwordx4 v229, s[56:59], s80 offen lds\n"
"v_mfma_f32_16x16x32_bf16 v[28:31], v[136:139], a[76:79], v[28:31]\n"
"s_sub_u32 s80, s80, s101\n"
"ds_read_b64_tr_b16 v[152:153], v197 offset:8200\n"
"v_mfma_f32_16x16x32_bf16 v[28:31], v[140:143], a[92:95], v[28:31]\n"
"s_addk_i32 m0, 1024\n"
"v_lshl_add_u32 v228, v204, 5, v228\n"
"v_mfma_f32_16x16x32_bf16 v[24:27], v[140:143], a[88:91], v[24:27]\n"
"ds_read_b64_tr_b16 v[154:155], v197 offset:10248\n"
"v_mfma_f32_16x16x32_bf16 v[24:27], v[136:139], a[72:75], v[24:27]\n"
"buffer_load_dwordx4 v229, s[56:59], s80 offen lds\n"
"v_mfma_f32_16x16x32_bf16 v[20:23], v[136:139], a[68:71], v[20:23]\n"
"s_add_u32 s80, s80, s100\n"
"ds_read_b64_tr_b16 v[156:157], v197 offset:12296\n"
"v_add_u32_e32 v228, 65536, v228\n"
"v_mfma_f32_16x16x32_bf16 v[20:23], v[140:143], a[84:87], v[20:23]\n"
"s_addk_i32 m0, 1024\n"
"v_mfma_f32_16x16x32_bf16 v[16:19], v[140:143], a[80:83], v[16:19]\n"
"ds_read_b64_tr_b16 v[158:159], v197 offset:14344\n"
"v_mfma_f32_16x16x32_bf16 v[16:19], v[136:139], a[64:67], v[16:19]\n"
"s_waitcnt lgkmcnt(4)\n"
"buffer_load_dwordx4 v229, s[56:59], s80 offen lds\n"
"v_mfma_f32_16x16x32_bf16 v[32:35], v[144:147], a[64:67], v[32:35]\n"
"s_waitcnt vmcnt(12)\n"
"ds_read_b64_tr_b16 v[128:129], v198 offset:8192\n"
"v_cvt_pk_bf16_f32 v160, v64, v65\n"
"v_mfma_f32_16x16x32_bf16 v[32:35], v[148:151], a[80:83], v[32:35]\n"
"s_add_u32 s80, s80, s101\n"
"v_mfma_f32_16x16x32_bf16 v[36:39], v[148:151], a[84:87], v[36:39]\n"
"s_addk_i32 m0, 1024\n"
"ds_read_b64_tr_b16 v[130:131], v198 offset:10240\n"
"v_mfma_f32_16x16x32_bf16 v[36:39], v[144:147], a[68:71], v[36:39]\n"
"buffer_load_dwordx4 v229, s[56:59], s80 offen lds\n"
"v_mfma_f32_16x16x32_bf16 v[40:43], v[144:147], a[72:75], v[40:43]\n"
"s_add_u32 s80, s80, s100\n"
"ds_read_b64_tr_b16 v[132:133], v198 offset:12288\n"
"v_cvt_pk_bf16_f32 v161, v66, v67\n"
"v_mfma_f32_16x16x32_bf16 v[40:43], v[148:151], a[88:91], v[40:43]\n"
"s_addk_i32 m0, 1024\n"
"v_mfma_f32_16x16x32_bf16 v[44:47], v[148:151], a[92:95], v[44:47]\n"
"ds_read_b64_tr_b16 v[134:135], v198 offset:14336\n"
"v_cvt_pk_bf16_f32 v162, v80, v81\n"
"v_mfma_f32_16x16x32_bf16 v[44:47], v[144:147], a[76:79], v[44:47]\n"
"s_waitcnt lgkmcnt(4)\n"
"buffer_load_dwordx4 v229, s[56:59], s80 offen lds\n"
"v_mfma_f32_16x16x32_bf16 v[60:63], v[152:155], a[76:79], v[60:63]\n"
"s_sub_u32 s80, s80, s101\n"
"ds_read_b64_tr_b16 v[136:137], v198 offset:8200\n"
"v_mfma_f32_16x16x32_bf16 v[60:63], v[156:159], a[92:95], v[60:63]\n"
"s_addk_i32 m0, 1024\n"
"v_cvt_pk_bf16_f32 v163, v82, v83\n"
"v_mfma_f32_16x16x32_bf16 v[56:59], v[156:159], a[88:91], v[56:59]\n"
".LPG22_%=:\n"
"s_cbranch_execz .LPGR_%=\n"
"ds_read_b64_tr_b16 v[138:139], v198 offset:10248\n"
"v_mfma_f32_16x16x32_bf16 v[56:59], v[152:155], a[72:75], v[56:59]\n"
"buffer_load_dwordx4 v229, s[56:59], s80 offen lds\n"
"v_mfma_f32_16x16x32_bf16 v[52:55], v[152:155], a[68:71], v[52:55]\n"
"s_add_u32 s80, s80, s100\n"
"ds_read_b64_tr_b16 v[140:141], v198 offset:12296\n"
"v_cvt_pk_bf16_f32 v164, v68, v69\n"
"v_mfma_f32_16x16x32_bf16 v[52:55], v[156:159], a[84:87], v[52:55]\n"
"s_addk_i32 m0, 1024\n"
"v_mfma_f32_16x16x32_bf16 v[48:51], v[156:159], a[80:83], v[48:51]\n"
"ds_read_b64_tr_b16 v[142:143], v198 offset:14344\n"
"v_mfma_f32_16x16x32_bf16 v[48:51], v[152:155], a[64:67], v[48:51]\n"
"s_waitcnt lgkmcnt(4)\n"
"buffer_load_dwordx4 v229, s[56:59], s80 offen lds\n"
"v_mfma_f32_16x16x32_bf16 v[0:3], v[128:131], a[96:99], v[0:3]\n"
"s_add_u32 s80, s86, 3145728\n"
"ds_read_b64_tr_b16 v[144:145], v199 offset:8192\n"
"v_cvt_pk_bf16_f32 v165, v70, v71\n"
"v_mfma_f32_16x16x32_bf16 v[0:3], v[132:135], a[112:115], v[0:3]\n"
"s_add_u32 m0, s46, 8192\n"
"v_mfma_f32_16x16x32_bf16 v[4:7], v[132:135], a[116:119], v[4:7]\n"
"ds_read_b64_tr_b16 v[146:147], v199 offset:10240\n"
"v_mfma_f32_16x16x32_bf16 v[4:7], v[128:131], a[100:103], v[4:7]\n"
"buffer_load_dwordx4 v229, s[56:59], s80 offen lds\n"
"v_mfma_f32_16x16x32_bf16 v[8:11], v[128:131], a[104:107], v[8:11]\n"
"s_add_u32 s80, s80, s100\n"
"ds_read_b64_tr_b16 v[148:149], v199 offset:12288\n"
"v_cvt_pk_bf16_f32 v166, v84, v85\n"
"v_mfma_f32_16x16x32_bf16 v[8:11], v[132:135], a[120:123], v[8:11]\n"
"s_addk_i32 m0, 1024\n"
"v_mfma_f32_16x16x32_bf16 v[12:15], v[132:135], a[124:127], v[12:15]\n"
"ds_read_b64_tr_b16 v[150:151], v199 offset:14336\n"
"v_mfma_f32_16x16x32_bf16 v[12:15], v[128:131], a[108:111], v[12:15]\n"
"s_waitcnt lgkmcnt(4)\n"
"buffer_load_dwordx4 v229, s[56:59], s80 offen lds\n"
"v_mfma_f32_16x16x32_bf16 v[28:31], v[136:139], a[108:111], v[28:31]\n"
"s_sub_u32 s80, s80, s101\n"
"ds_read_b64_tr_b16 v[152:153], v199 offset:8200\n"
"v_cvt_pk_bf16_f32 v167, v86, v87\n"
"v_mfma_f32_16x16x32_bf16 v[28:31], v[140:143], a[124:127], v[28:31]\n"
"s_addk_i32 m0, 1024\n"
"v_mfma_f32_16x16x32_bf16 v[24:27], v[140:143], a[120:123], v[24:27]\n"
"ds_read_b64_tr_b16 v[154:155], v199 offset:10248\n"
"v_cvt_pk_bf16_f32 v168, v72, v73\n"
"v_mfma_f32_16x16x32_bf16 v[24:27], v[136:139], a[104:107], v[24:27]\n"
"buffer_load_dwordx4 v229, s[56:59], s80 offen lds\n"
"v_mfma_f32_16x16x32_bf16 v[20:23], v[136:139], a[100:103], v[20:23]\n"
"s_add_u32 s80, s80, s100\n"
"ds_read_b64_tr_b16 v[156:157], v199 offset:12296\n"
"v_mfma_f32_16x16x32_bf16 v[20:23], v[140:143], a[116:119], v[20:23]\n"
"s_addk_i32 m0, 1024\n"
"v_mfma_f32_16x16x32_bf16 v[16:19], v[140:143], a[112:115], v[16:19]\n"
"ds_read_b64_tr_b16 v[158:159], v199 offset:14344\n"
"v_cvt_pk_bf16_f32 v169, v74, v75\n"
"v_mfma_f32_16x16x32_bf16 v[16:19], v[136:139], a[96:99], v[16:19]\n"
"s_waitcnt lgkmcnt(4)\n"
"buffer_load_dwordx4 v229, s[56:59], s80 offen lds\n"
"v_mfma_f32_16x16x32_bf16 v[32:35], v[144:147], a[96:99], v[32:35]\n"
"s_waitcnt vmcnt(12)\n"
"ds_read_b64_tr_b16 v[128:129], v196 offset:0\n"
"v_mfma_f32_16x16x32_bf16 v[32:35], v[148:151], a[112:115], v[32:35]\n"
"s_add_u32 s80, s80, s101\n"
"v_cvt_pk_bf16_f32 v170, v88, v89\n"
"v_mfma_f32_16x16x32_bf16 v[36:39], v[148:151], a[116:119], v[36:39]\n"
"s_addk_i32 m0, 1024\n"
"ds_read_b64_tr_b16 v[130:131], v196 offset:2048\n"
"v_mfma_f32_16x16x32_bf16 v[36:39], v[144:147], a[100:103], v[36:39]\n"
"buffer_load_dwordx4 v229, s[56:59], s80 offen lds\n"
"v_mfma_f32_16x16x32_bf16 v[40:43], v[144:147], a[104:107], v[40:43]\n"
"s_add_u32 s80, s80, s100\n"
"ds_read_b64_tr_b16 v[132:133], v196 offset:4096\n"
"v_mfma_f32_16x16x32_bf16 v[40:43], v[148:151], a[120:123], v[40:43]\n"
"s_addk_i32 m0, 1024\n"
"v_cvt_pk_bf16_f32 v171, v90, v91\n"
"v_mfma_f32_16x16x32_bf16 v[44:47], v[148:151], a[124:127], v[44:47]\n"
"ds_read_b64_tr_b16 v[134:135], v196 offset:6144\n"
"v_mfma_f32_16x16x32_bf16 v[44:47], v[144:147], a[108:111], v[44:47]\n"
"s_waitcnt lgkmcnt(4)\n"
"buffer_load_dwordx4 v229, s[56:59], s80 offen lds\n"
"v_mfma_f32_16x16x32_bf16 v[60:63], v[152:155], a[108:111], v[60:63]\n"
"s_sub_u32 s80, s80, s101\n"
"ds_read_b64_tr_b16 v[136:137], v196 offset:8\n"
"v_cvt_pk_bf16_f32 v172, v76, v77\n"
"v_mfma_f32_16x16x32_bf16 v[60:63], v[156:159], a[124:127], v[60:63]\n"
"s_addk_i32 m0, 1024\n"
"v_mfma_f32_16x16x32_bf16 v[56:59], v[156:159], a[120:123], v[56:59]\n"
"ds_read_b64_tr_b16 v[138:139], v196 offset:2056\n"
"v_mfma_f32_16x16x32_bf16 v[56:59], v[152:155], a[104:107], v[56:59]\n"
"buffer_load_dwordx4 v229, s[56:59], s80 offen lds\n"
"v_mfma_f32_16x16x32_bf16 v[52:55], v[152:155], a[100:103], v[52:55]\n"
"s_add_u32 s80, s80, s100\n"
"ds_read_b64_tr_b16 v[140:141], v196 offset:4104\n"
"v_cvt_pk_bf16_f32 v173, v78, v79\n"
"v_mfma_f32_16x16x32_bf16 v[52:55], v[156:159], a[116:119], v[52:55]\n"
"s_addk_i32 m0, 1024\n"
"v_mfma_f32_16x16x32_bf16 v[48:51], v[156:159], a[112:115], v[48:51]\n"
"ds_read_b64_tr_b16 v[142:143], v196 offset:6152\n"
"v_mfma_f32_16x16x32_bf16 v[48:51], v[152:155], a[96:99], v[48:51]\n"
"buffer_load_dwordx4 v229, s[56:59], s80 offen lds\n"
"s_waitcnt lgkmcnt(0)\n"
"s_barrier\n"
"v_mfma_f32_16x16x32_bf16 v[0:3], v[128:131], a[128:131], v[0:3]\n"
"s_add_u32 s80, s86, 3670016\n"
"ds_read_b64_tr_b16 v[144:145], v197 offset:0\n"
"ds_read_b128 a[0:3], v232 offset:0\n"
"v_mfma_f32_16x16x32_bf16 v[0:3], v[132:135], a[144:147], v[0:3]\n"
"s_add_u32 m0, s46, 73728\n"
"v_cvt_pk_bf16_f32 v174, v92, v93\n"
"v_mfma_f32_16x16x32_bf16 v[4:7], v[132:135], a[148:151], v[4:7]\n"
"ds_read_b64_tr_b16 v[146:147], v197 offset:2048\n"
"ds_read_b128 a[4:7], v232 offset:1024\n"
"v_mfma_f32_16x16x32_bf16 v[4:7], v[128:131], a[132:135], v[4:7]\n"
"buffer_load_dwordx4 v229, s[56:59], s80 offen lds\n"
"v_mfma_f32_16x16x32_bf16 v[8:11], v[128:131], a[136:139], v[8:11]\n"
"s_add_u32 s80, s80, s100\n"
"ds_read_b64_tr_b16 v[148:149], v197 offset:4096\n"
"ds_read_b128 a[8:11], v232 offset:2048\n"
"v_mfma_f32_16x16x32_bf16 v[8:11], v[132:135], a[152:155], v[8:11]\n"
"s_addk_i32 m0, 1024\n"
"v_cvt_pk_bf16_f32 v175, v94, v95\n"
"v_mfma_f32_16x16x32_bf16 v[12:15], v[132:135], a[156:159], v[12:15]\n"
"ds_read_b64_tr_b16 v[150:151], v197 offset:6144\n"
"ds_read_b128 a[12:15], v232 offset:3072\n"
"v_mfma_f32_16x16x32_bf16 v[12:15], v[128:131], a[140:143], v[12:15]\n"
"buffer_load_dwordx4 v229, s[56:59], s80 offen lds\n"
"v_mfma_f32_16x16x32_bf16 v[28:31], v[136:139], a[140:143], v[28:31]\n"
"s_sub_u32 s80, s80, s101\n"
"ds_read_b64_tr_b16 v[152:153], v197 offset:8\n"
"ds_read_b128 a[16:19], v232 offset:4096\n"
"v_mfma_f32_16x16x32_bf16 v[28:31], v[140:143], a[156:159], v[28:31]\n"
"s_addk_i32 m0, 1024\n"
"v_cvt_pk_bf16_f32 v176, v96, v97\n"
"v_mfma_f32_16x16x32_bf16 v[24:27], v[140:143], a[152:155], v[24:27]\n"
"ds_read_b64_tr_b16 v[154:155], v197 offset:2056\n"
"ds_read_b128 a[20:23], v232 offset:5120\n"
"v_mfma_f32_16x16x32_bf16 v[24:27], v[136:139], a[136:139], v[24:27]\n"
"buffer_load_dwordx4 v229, s[56:59], s80 offen lds\n"
"v_mfma_f32_16x16x32_bf16 v[20:23], v[136:139], a[132:135], v[20:23]\n"
"s_add_u32 s80, s80, s100\n"
"ds_read_b64_tr_b16 v[156:157], v197 offset:4104\n"
"ds_read_b128 a[24:27], v232 offset:6144\n"
"v_mfma_f32_16x16x32_bf16 v[20:23], v[140:143], a[148:151], v[20:23]\n"
"s_addk_i32 m0, 1024\n"
"v_cvt_pk_bf16_f32 v177, v98, v99\n"
"v_mfma_f32_16x16x32_bf16 v[16:19], v[140:143], a[144:147], v[16:19]\n"
"ds_read_b64_tr_b16 v[158:159], v197 offset:6152\n"
"ds_read_b128 a[28:31], v232 offset:7168\n"
"v_mfma_f32_16x16x32_bf16 v[16:19], v[136:139], a[128:131], v[16:19]\n"
"s_waitcnt lgkmcnt(9)\n"
"buffer_load_dwordx4 v229, s[56:59], s80 offen lds\n"
"v_mfma_f32_16x16x32_bf16 v[32:35], v[144:147], a[128:131], v[32:35]\n"
"s_waitcnt vmcnt(12)\n"
"ds_read_b64_tr_b16 v[128:129], v198 offset:0\n"
"v_cvt_pk_bf16_f32 v178, v112, v113\n"
"v_mfma_f32_16x16x32_bf16 v[32:35], v[148:151], a[144:147], v[32:35]\n"
"s_add_u32 s80, s80, s101\n"
"v_cvt_pk_bf16_f32 v179, v114, v115\n"
"v_mfma_f32_16x16x32_bf16 v[36:39], v[148:151], a[148:151], v[36:39]\n"
"s_addk_i32 m0, 1024\n"
"ds_read_b64_tr_b16 v[130:131], v198 offset:2048\n"
"v_mfma_f32_16x16x32_bf16 v[36:39], v[144:147], a[132:135], v[36:39]\n"
"buffer_load_dwordx4 v229, s[56:59], s80 offen lds\n"
"v_mfma_f32_16x16x32_bf16 v[40:43], v[144:147], a[136:139], v[40:43]\n"
"s_add_u32 s80, s80, s100\n"
"ds_read_b64_tr_b16 v[132:133], v198 offset:4096\n"
"v_mfma_f32_16x16x32_bf16 v[40:43], v[148:151], a[152:155], v[40:43]\n"
"s_addk_i32 m0, 1024\n"
"v_cvt_pk_bf16_f32 v180, v100, v101\n"
"v_mfma_f32_16x16x32_bf16 v[44:47], v[148:151], a[156:159], v[44:47]\n"
"ds_read_b64_tr_b16 v[134:135], v198 offset:6144\n"
"v_mfma_f32_16x16x32_bf16 v[44:47], v[144:147], a[140:143], v[44:47]\n"
"s_waitcnt lgkmcnt(5)\n"
"buffer_load_dwordx4 v229, s[56:59], s80 offen lds\n"
"v_mfma_f32_16x16x32_bf16 v[60:63], v[152:155], a[140:143], v[60:63]\n"
"s_sub_u32 s80, s80, s101\n"
"ds_read_b64_tr_b16 v[136:137], v198 offset:8\n"
"v_cvt_pk_bf16_f32 v181, v102, v103\n"
"v_mfma_f32_16x16x32_bf16 v[60:63], v[156:159], a[156:159], v[60:63]\n"
"s_addk_i32 m0, 1024\n"
"v_mfma_f32_16x16x32_bf16 v[56:59], v[156:159], a[152:155], v[56:59]\n"
"ds_read_b64_tr_b16 v[138:139], v198 offset:2056\n"
"v_mfma_f32_16x16x32_bf16 v[56:59], v[152:155], a[136:139], v[56:59]\n"
"buffer_load_dwordx4 v229, s[56:59], s80 offen lds\n"
"v_mfma_f32_16x16x32_bf16 v[52:55], v[152:155], a[132:135], v[52:55]\n"
"s_add_u32 s80, s80, s100\n"
"ds_read_b64_tr_b16 v[140:141], v198 offset:4104\n"
"v_cvt_pk_bf16_f32 v182, v116, v117\n"
"v_mfma_f32_16x16x32_bf16 v[52:55], v[156:159], a[148:151], v[52:55]\n"
"s_addk_i32 m0, 1024\n"
"v_mfma_f32_16x16x32_bf16 v[48:51], v[156:159], a[144:147], v[48:51]\n"
"ds_read_b64_tr_b16 v[142:143], v198 offset:6152\n"
"v_mfma_f32_16x16x32_bf16 v[48:51], v[152:155], a[128:131], v[48:51]\n"
"s_waitcnt lgkmcnt(4)\n"
"buffer_load_dwordx4 v229, s[56:59], s80 offen lds\n"
"v_mfma_f32_16x16x32_bf16 v[0:3], v[128:131], a[160:163], v[0:3]\n"
"s_add_u32 s80, s87, 0\n"
"ds_read_b64_tr_b16 v[144:145], v199 offset:0\n"
"ds_read_b128 a[32:35], v232 offset:8192\n"
"v_mfma_f32_16x16x32_bf16 v[0:3], v[132:135], a[176:179], v[0:3]\n"
"s_add_u32 m0, s46, 0\n"
"v_cvt_pk_bf16_f32 v183, v118, v119\n"
"v_mfma_f32_16x16x32_bf16 v[4:7], v[132:135], a[180:183], v[4:7]\n"
"ds_read_b64_tr_b16 v[146:147], v199 offset:2048\n"
"ds_read_b128 a[36:39], v232 offset:9216\n"
"v_mfma_f32_16x16x32_bf16 v[4:7], v[128:131], a[164:167], v[4:7]\n"
"buffer_load_dwordx4 v254, s[60:63], s80 offen lds\n"
"v_mfma_f32_16x16x32_bf16 v[8:11], v[128:131], a[168:171], v[8:11]\n"
"s_addk_i32 s80, 8192\n"
"ds_read_b64_tr_b16 v[148:149], v199 offset:4096\n"
"ds_read_b128 a[40:43], v232 offset:10240\n"
"v_mfma_f32_16x16x32_bf16 v[8:11], v[132:135], a[184:187], v[8:11]\n"
"s_addk_i32 m0, 1024\n"
"v_cvt_pk_bf16_f32 v184, v104, v105\n"
"v_mfma_f32_16x16x32_bf16 v[12:15], v[132:135], a[188:191], v[12:15]\n"
"ds_read_b64_tr_b16 v[150:151], v199 offset:6144\n"
"ds_read_b128 a[44:47], v232 offset:11264\n"
"v_mfma_f32_16x16x32_bf16 v[12:15], v[128:131], a[172:175], v[12:15]\n"
"s_waitcnt lgkmcnt(8)\n"
"buffer_load_dwordx4 v254, s[60:63], s80 offen lds\n"
"v_mfma_f32_16x16x32_bf16 v[28:31], v[136:139], a[172:175], v[28:31]\n"
"s_addk_i32 s80, 8192\n"
"ds_read_b64_tr_b16 v[152:153], v199 offset:8\n"
"ds_read_b128 a[48:51], v232 offset:12288\n"
"v_mfma_f32_16x16x32_bf16 v[28:31], v[140:143], a[188:191], v[28:31]\n"
"s_addk_i32 m0, 1024\n"
"v_cvt_pk_bf16_f32 v185, v106, v107\n"
"v_mfma_f32_16x16x32_bf16 v[24:27], v[140:143], a[184:187], v[24:27]\n"
"ds_read_b64_tr_b16 v[154:155], v199 offset:2056\n"
"ds_read_b128 a[52:55], v232 offset:13312\n"
"v_mfma_f32_16x16x32_bf16 v[24:27], v[136:139], a[168:171], v[24:27]\n"
"buffer_load_dwordx4 v254, s[60:63], s80 offen lds\n"
"v_mfma_f32_16x16x32_bf16 v[20:23], v[136:139], a[164:167], v[20:23]\n"
"s_addk_i32 s80, 8192\n"
"ds_read_b64_tr_b16 v[156:157], v199 offset:4104\n"
"ds_read_b128 a[56:59], v232 offset:14336\n"
"v_mfma_f32_16x16x32_bf16 v[20:23], v[140:143], a[180:183], v[20:23]\n"
"s_addk_i32 m0, 1024\n"
"v_cvt_pk_bf16_f32 v186, v120, v121\n"
"v_mfma_f32_16x16x32_bf16 v[16:19], v[140:143], a[176:179], v[16:19]\n"
"ds_read_b64_tr_b16 v[158:159], v199 offset:6152\n"
"ds_read_b128 a[60:63], v232 offset:15360\n"
"v_mfma_f32_16x16x32_bf16 v[16:19], v[136:139], a[160:163], v[16:19]\n"
"s_waitcnt lgkmcnt(9)\n"
"buffer_load_dwordx4 v254, s[60:63], s80 offen lds\n"
"v_mfma_f32_16x16x32_bf16 v[32:35], v[144:147], a[160:163], v[32:35]\n"
"s_waitcnt vmcnt(12)\n"
"ds_read_b64_tr_b16 v[128:129], v196 offset:8192\n"
"v_cvt_pk_bf16_f32 v187, v122, v123\n"
"v_mfma_f32_16x16x32_bf16 v[32:35], v[148:151], a[176:179], v[32:35]\n"
"s_addk_i32 s80, 8192\n"
"v_cvt_pk_bf16_f32 v188, v108, v109\n"
"v_mfma_f32_16x16x32_bf16 v[36:39], v[148:151], a[180:183], v[36:39]\n"
"s_addk_i32 m0, 1024\n"
"ds_read_b64_tr_b16 v[130:131], v196 offset:10240\n"
"v_mfma_f32_16x16x32_bf16 v[36:39], v[144:147], a[164:167], v[36:39]\n"
"buffer_load_dwordx4 v254, s[60:63], s80 offen lds\n"
"v_mfma_f32_16x16x32_bf16 v[40:43], v[144:147], a[168:171], v[40:43]\n"
"s_addk_i32 s80, 8192\n"
"ds_read_b64_tr_b16 v[132:133], v196 offset:12288\n"
"v_mfma_f32_16x16x32_bf16 v[40:43], v[148:151], a[184:187], v[40:43]\n"
"s_addk_i32 m0, 1024\n"
"v_cvt_pk_bf16_f32 v189, v110, v111\n"
"v_mfma_f32_16x16x32_bf16 v[44:47], v[148:151], a[188:191], v[44:47]\n"
"ds_read_b64_tr_b16 v[134:135], v196 offset:14336\n"
"v_mfma_f32_16x16x32_bf16 v[44:47], v[144:147], a[172:175], v[44:47]\n"
"s_waitcnt lgkmcnt(5)\n"
"buffer_load_dwordx4 v254, s[60:63], s80 offen lds\n"
"v_mfma_f32_16x16x32_bf16 v[60:63], v[152:155], a[172:175], v[60:63]\n"
"s_addk_i32 s80, 8192\n"
"ds_read_b64_tr_b16 v[136:137], v196 offset:8200\n"
"v_cvt_pk_bf16_f32 v190, v124, v125\n"
"v_mfma_f32_16x16x32_bf16 v[60:63], v[156:159], a[188:191], v[60:63]\n"
"s_addk_i32 m0, 1024\n"
"v_mfma_f32_16x16x32_bf16 v[56:59], v[156:159], a[184:187], v[56:59]\n"
"ds_read_b64_tr_b16 v[138:139], v196 offset:10248\n"
"v_mfma_f32_16x16x32_bf16 v[56:59], v[152:155], a[168:171], v[56:59]\n"
"buffer_load_dwordx4 v254, s[60:63], s80 offen lds\n"
"v_mfma_f32_16x16x32_bf16 v[52:55], v[152:155], a[164:167], v[52:55]\n"
"s_addk_i32 s80, 8192\n"
"ds_read_b64_tr_b16 v[140:141], v196 offset:12296\n"
"v_cvt_pk_bf16_f32 v191, v126, v127\n"
"v_mfma_f32_16x16x32_bf16 v[52:55], v[156:159], a[180:183], v[52:55]\n"
"s_addk_i32 m0, 1024\n"
"v_mfma_f32_16x16x32_bf16 v[48:51], v[156:159], a[176:179], v[48:51]\n"
"ds_read_b64_tr_b16 v[142:143], v196 offset:14344\n"
"v_mfma_f32_16x16x32_bf16 v[48:51], v[152:155], a[160:163], v[48:51]\n"
"s_waitcnt lgkmcnt(4)\n"
"buffer_load_dwordx4 v254, s[60:63], s80 offen lds\n"
"v_mfma_f32_16x16x32_bf16 v[0:3], v[128:131], a[192:195], v[0:3]\n"
"s_add_u32 s80, s87, 65536\n"
"ds_read_b64_tr_b16 v[144:145], v197 offset:8192\n"
"ds_read_b128 a[64:67], v232 offset:16384\n"
"v_mfma_f32_16x16x32_bf16 v[0:3], v[132:135], a[208:211], v[0:3]\n"
"s_add_u32 m0, s46, 65536\n"
"s_mov_b64 s[56:57], s[24:25]\n"
"s_mov_b32 s59, 0x20000\n"
"v_mfma_f32_16x16x32_bf16 v[4:7], v[132:135], a[212:215], v[4:7]\n"
"ds_read_b64_tr_b16 v[146:147], v197 offset:10240\n"
"ds_read_b128 a[68:71], v232 offset:17408\n"
"v_mfma_f32_16x16x32_bf16 v[4:7], v[128:131], a[196:199], v[4:7]\n"
"buffer_load_dwordx4 v254, s[60:63], s80 offen lds\n"
"v_mfma_f32_16x16x32_bf16 v[8:11], v[128:131], a[200:203], v[8:11]\n"
"s_addk_i32 s80, 8192\n"
"ds_read_b64_tr_b16 v[148:149], v197 offset:12288\n"
"ds_read_b128 a[72:75], v232 offset:18432\n"
"v_mfma_f32_16x16x32_bf16 v[8:11], v[132:135], a[216:219], v[8:11]\n"
"s_addk_i32 m0, 1024\n"
"v_accvgpr_write_b32 a128, v160\n"
"v_accvgpr_write_b32 a129, v161\n"
"v_mfma_f32_16x16x32_bf16 v[12:15], v[132:135], a[220:223], v[12:15]\n"
"ds_read_b64_tr_b16 v[150:151], v197 offset:14336\n"
"ds_read_b128 a[76:79], v232 offset:19456\n"
"v_mfma_f32_16x16x32_bf16 v[12:15], v[128:131], a[204:207], v[12:15]\n"
"s_waitcnt lgkmcnt(8)\n"
"buffer_load_dwordx4 v254, s[60:63], s80 offen lds\n"
"v_mfma_f32_16x16x32_bf16 v[28:31], v[136:139], a[204:207], v[28:31]\n"
"s_addk_i32 s80, 8192\n"
"ds_read_b64_tr_b16 v[152:153], v197 offset:8200\n"
"ds_read_b128 a[80:83], v232 offset:20480\n"
"v_mfma_f32_16x16x32_bf16 v[28:31], v[140:143], a[220:223], v[28:31]\n"
"s_addk_i32 m0, 1024\n"
"v_accvgpr_write_b32 a130, v162\n"
"v_accvgpr_write_b32 a131, v163\n"
"v_mfma_f32_16x16x32_bf16 v[24:27], v[140:143], a[216:219], v[24:27]\n"
"ds_read_b64_tr_b16 v[154:155], v197 offset:10248\n"
"ds_read_b128 a[84:87], v232 offset:21504\n"
"v_mfma_f32_16x16x32_bf16 v[24:27], v[136:139], a[200:203], v[24:27]\n"
"buffer_load_dwordx4 v254, s[60:63], s80 offen lds\n"
"v_mfma_f32_16x16x32_bf16 v[20:23], v[136:139], a[196:199], v[20:23]\n"
"s_addk_i32 s80, 8192\n"
"ds_read_b64_tr_b16 v[156:157], v197 offset:12296\n"
"ds_read_b128 a[88:91], v232 offset:22528\n"
"v_mfma_f32_16x16x32_bf16 v[20:23], v[140:143], a[212:215], v[20:23]\n"
"s_addk_i32 m0, 1024\n"
"v_accvgpr_write_b32 a132, v176\n"
"v_accvgpr_write_b32 a133, v177\n"
"v_mfma_f32_16x16x32_bf16 v[16:19], v[140:143], a[208:211], v[16:19]\n"
"ds_read_b64_tr_b16 v[158:159], v197 offset:14344\n"
"ds_read_b128 a[92:95], v232 offset:23552\n"
"v_mfma_f32_16x16x32_bf16 v[16:19], v[136:139], a[192:195], v[16:19]\n"
"s_waitcnt lgkmcnt(9)\n"
"buffer_load_dwordx4 v254, s[60:63], s80 offen lds\n"
"v_mfma_f32_16x16x32_bf16 v[32:35], v[144:147], a[192:195], v[32:35]\n"
"s_waitcnt vmcnt(12)\n"
"ds_read_b64_tr_b16 v[128:129], v198 offset:8192\n"
"v_accvgpr_write_b32 a134, v178\n"
"v_mfma_f32_16x16x32_bf16 v[32:35], v[148:151], a[208:211], v[32:35]\n"
"s_addk_i32 s80, 8192\n"
"v_accvgpr_write_b32 a135, v179\n"
"v_accvgpr_write_b32 a144, v164\n"
"v_mfma_f32_16x16x32_bf16 v[36:39], v[148:151], a[212:215], v[36:39]\n"
"s_addk_i32 m0, 1024\n"
"ds_read_b64_tr_b16 v[130:131], v198 offset:10240\n"
"v_accvgpr_write_b32 a145, v165\n"
"v_mfma_f32_16x16x32_bf16 v[36:39], v[144:147], a[196:199], v[36:39]\n"
"buffer_load_dwordx4 v254, s[60:63], s80 offen lds\n"
"v_mfma_f32_16x16x32_bf16 v[40:43], v[144:147], a[200:203], v[40:43]\n"
"s_addk_i32 s80, 8192\n"
"ds_read_b64_tr_b16 v[132:133], v198 offset:12288\n"
"v_accvgpr_write_b32 a146, v166\n"
"v_mfma_f32_16x16x32_bf16 v[40:43], v[148:151], a[216:219], v[40:43]\n"
"s_addk_i32 m0, 1024\n"
"v_accvgpr_write_b32 a147, v167\n"
"v_accvgpr_write_b32 a148, v180\n"
"v_mfma_f32_16x16x32_bf16 v[44:47], v[148:151], a[220:223], v[44:47]\n"
"ds_read_b64_tr_b16 v[134:135], v198 offset:14336\n"
"v_accvgpr_write_b32 a149, v181\n"
"v_mfma_f32_16x16x32_bf16 v[44:47], v[144:147], a[204:207], v[44:47]\n"
"s_waitcnt lgkmcnt(5)\n"
"buffer_load_dwordx4 v254, s[60:63], s80 offen lds\n"
"v_mfma_f32_16x16x32_bf16 v[60:63], v[152:155], a[204:207], v[60:63]\n"
"s_addk_i32 s80, 8192\n"
"ds_read_b64_tr_b16 v[136:137], v198 offset:8200\n"
"v_accvgpr_write_b32 a150, v182\n"
"v_mfma_f32_16x16x32_bf16 v[60:63], v[156:159], a[220:223], v[60:63]\n"
"s_addk_i32 m0, 1024\n"
"v_accvgpr_write_b32 a151, v183\n"
"v_accvgpr_write_b32 a160, v168\n"
"v_mfma_f32_16x16x32_bf16 v[56:59], v[156:159], a[216:219], v[56:59]\n"
"ds_read_b64_tr_b16 v[138:139], v198 offset:10248\n"
"v_accvgpr_write_b32 a161, v169\n"
"v_mfma_f32_16x16x32_bf16 v[56:59], v[152:155], a[200:203], v[56:59]\n"
"buffer_load_dwordx4 v254, s[60:63], s80 offen lds\n"
"v_mfma_f32_16x16x32_bf16 v[52:55], v[152:155], a[196:199], v[52:55]\n"
"s_addk_i32 s80, 8192\n"
"ds_read_b64_tr_b16 v[140:141], v198 offset:12296\n"
"v_accvgpr_write_b32 a162, v170\n"
"v_mfma_f32_16x16x32_bf16 v[52:55], v[156:159], a[212:215], v[52:55]\n"
"s_addk_i32 m0, 1024\n"
"v_accvgpr_write_b32 a163, v171\n"
"v_accvgpr_write_b32 a164, v184\n"
"v_mfma_f32_16x16x32_bf16 v[48:51], v[156:159], a[208:211], v[48:51]\n"
"ds_read_b64_tr_b16 v[142:143], v198 offset:14344\n"
"v_accvgpr_write_b32 a165, v185\n"
"v_mfma_f32_16x16x32_bf16 v[48:51], v[152:155], a[192:195], v[48:51]\n"
"s_waitcnt lgkmcnt(4)\n"
"buffer_load_dwordx4 v254, s[60:63], s80 offen lds\n"
"v_mfma_f32_16x16x32_bf16 v[0:3], v[128:131], a[224:227], v[0:3]\n"
"s_add_u32 s80, s87, 131072\n"
"ds_read_b64_tr_b16 v[144:145], v199 offset:8192\n"
"ds_read_b128 a[96:99], v232 offset:24576\n"
"v_mfma_f32_16x16x32_bf16 v[0:3], v[132:135], a[240:243], v[0:3]\n"
"s_add_u32 m0, s46, 8192\n"
"v_accvgpr_write_b32 a166, v186\n"
"v_accvgpr_write_b32 a167, v187\n"
"v_mfma_f32_16x16x32_bf16 v[4:7], v[132:135], a[244:247], v[4:7]\n"
"ds_read_b64_tr_b16 v[146:147], v199 offset:10240\n"
"ds_read_b128 a[100:103], v232 offset:25600\n"
"v_mfma_f32_16x16x32_bf16 v[4:7], v[128:131], a[228:231], v[4:7]\n"
"buffer_load_dwordx4 v254, s[60:63], s80 offen lds\n"
"v_mfma_f32_16x16x32_bf16 v[8:11], v[128:131], a[232:235], v[8:11]\n"
"s_addk_i32 s80, 8192\n"
"ds_read_b64_tr_b16 v[148:149], v199 offset:12288\n"
"ds_read_b128 a[104:107], v232 offset:26624\n"
"v_mfma_f32_16x16x32_bf16 v[8:11], v[132:135], a[248:251], v[8:11]\n"
"s_addk_i32 m0, 1024\n"
"v_accvgpr_write_b32 a176, v172\n"
"v_accvgpr_write_b32 a177, v173\n"
"v_mfma_f32_16x16x32_bf16 v[12:15], v[132:135], a[252:255], v[12:15]\n"
"ds_read_b64_tr_b16 v[150:151], v199 offset:14336\n"
"ds_read_b128 a[108:111], v232 offset:27648\n"
"v_mfma_f32_16x16x32_bf16 v[12:15], v[128:131], a[236:239], v[12:15]\n"
"s_waitcnt lgkmcnt(8)\n"
"buffer_load_dwordx4 v254, s[60:63], s80 offen lds\n"
"v_mfma_f32_16x16x32_bf16 v[28:31], v[136:139], a[236:239], v[28:31]\n"
"s_addk_i32 s80, 8192\n"
"ds_read_b64_tr_b16 v[152:153], v199 offset:8200\n"
"ds_read_b128 a[112:115], v232 offset:28672\n"
"v_mfma_f32_16x16x32_bf16 v[28:31], v[140:143], a[252:255], v[28:31]\n"
"s_addk_i32 m0, 1024\n"
"v_accvgpr_write_b32 a178, v174\n"
"v_accvgpr_write_b32 a179, v175\n"
"v_mfma_f32_16x16x32_bf16 v[24:27], v[140:143], a[248:251], v[24:27]\n"
"ds_read_b64_tr_b16 v[154:155], v199 offset:10248\n"
"ds_read_b128 a[116:119], v232 offset:29696\n"
"v_mfma_f32_16x16x32_bf16 v[24:27], v[136:139], a[232:235], v[24:27]\n"
"buffer_load_dwordx4 v254, s[60:63], s80 offen lds\n"
"v_mfma_f32_16x16x32_bf16 v[20:23], v[136:139], a[228:231], v[20:23]\n"
"s_addk_i32 s80, 8192\n"
"ds_read_b64_tr_b16 v[156:157], v199 offset:12296\n"
"ds_read_b128 a[120:123], v232 offset:30720\n"
"v_mfma_f32_16x16x32_bf16 v[20:23], v[140:143], a[244:247], v[20:23]\n"
"s_addk_i32 m0, 1024\n"
"v_accvgpr_write_b32 a180, v188\n"
"v_accvgpr_write_b32 a181, v189\n"
"v_mfma_f32_16x16x32_bf16 v[16:19], v[140:143], a[240:243], v[16:19]\n"
"ds_read_b64_tr_b16 v[158:159], v199 offset:14344\n"
"ds_read_b128 a[124:127], v232 offset:31744\n"
"v_mfma_f32_16x16x32_bf16 v[16:19], v[136:139], a[224:227], v[16:19]\n"
"s_waitcnt lgkmcnt(9)\n"
"buffer_load_dwordx4 v254, s[60:63], s80 offen lds\n"
"v_mfma_f32_16x16x32_bf16 v[32:35], v[144:147], a[224:227], v[32:35]\n"
"s_waitcnt vmcnt(12)\n"
"ds_read_b64_tr_b16 v[128:129], v246 offset:0\n"
"v_accvgpr_write_b32 a182, v190\n"
"v_mfma_f32_16x16x32_bf16 v[32:35], v[148:151], a[240:243], v[32:35]\n"
"s_addk_i32 s80, 8192\n"
"v_accvgpr_write_b32 a183, v191\n"
"v_mfma_f32_16x16x32_bf16 v[36:39], v[148:151], a[244:247], v[36:39]\n"
"s_addk_i32 m0, 1024\n"
"ds_read_b64_tr_b16 v[130:131], v246 offset:2048\n"
"v_mfma_f32_16x16x32_bf16 v[36:39], v[144:147], a[228:231], v[36:39]\n"
"buffer_load_dwordx4 v254, s[60:63], s80 offen lds\n"
"v_mfma_f32_16x16x32_bf16 v[40:43], v[144:147], a[232:235], v[40:43]\n"
"s_addk_i32 s80, 8192\n"
"ds_read_b64_tr_b16 v[132:133], v246 offset:4096\n"
"v_mfma_f32_16x16x32_bf16 v[40:43], v[148:151], a[248:251], v[40:43]\n"
"s_addk_i32 m0, 1024\n"
"v_mfma_f32_16x16x32_bf16 v[44:47], v[148:151], a[252:255], v[44:47]\n"
"ds_read_b64_tr_b16 v[134:135], v246 offset:6144\n"
"v_mfma_f32_16x16x32_bf16 v[44:47], v[144:147], a[236:239], v[44:47]\n"
"s_waitcnt lgkmcnt(5)\n"
"buffer_load_dwordx4 v254, s[60:63], s80 offen lds\n"
"v_mfma_f32_16x16x32_bf16 v[60:63], v[152:155], a[236:239], v[60:63]\n"
"s_addk_i32 s80, 8192\n"
"ds_read_b64_tr_b16 v[136:137], v247 offset:0\n"
"v_mfma_f32_16x16x32_bf16 v[60:63], v[156:159], a[252:255], v[60:63]\n"
"s_addk_i32 m0, 1024\n"
"v_mfma_f32_16x16x32_bf16 v[56:59], v[156:159], a[248:251], v[56:59]\n"
"ds_read_b64_tr_b16 v[138:139], v247 offset:2048\n"
"v_mfma_f32_16x16x32_bf16 v[56:59], v[152:155], a[232:235], v[56:59]\n"
"buffer_load_dwordx4 v254, s[60:63], s80 offen lds\n"
"v_mfma_f32_16x16x32_bf16 v[52:55], v[152:155], a[228:231], v[52:55]\n"
"s_addk_i32 s80, 8192\n"
"ds_read_b64_tr_b16 v[140:141], v247 offset:4096\n"
"v_mfma_f32_16x16x32_bf16 v[52:55], v[156:159], a[244:247], v[52:55]\n"
"s_addk_i32 m0, 1024\n"
"v_mfma_f32_16x16x32_bf16 v[48:51], v[156:159], a[240:243], v[48:51]\n"
"ds_read_b64_tr_b16 v[142:143], v247 offset:6144\n"
"v_mfma_f32_16x16x32_bf16 v[48:51], v[152:155], a[224:227], v[48:51]\n"
"s_waitcnt lgkmcnt(4)\n"
"buffer_load_dwordx4 v254, s[60:63], s80 offen lds\n"
"v_mfma_f32_16x16x32_bf16 v[64:67], v[128:131], a[0:3], 0\n"
"s_add_u32 s80, s87, 196608\n"
"ds_read_b64_tr_b16 v[144:145], v248 offset:0\n"
"v_cvt_pk_bf16_f32 v192, v0, v1\n"
"v_mfma_f32_16x16x32_bf16 v[64:67], v[132:135], a[16:19], v[64:67]\n"
"s_add_u32 m0, s46, 73728\n"
"v_cvt_pk_bf16_f32 v193, v2, v3\n"
"v_cvt_pk_bf16_f32 v194, v16, v17\n"
"v_mfma_f32_16x16x32_bf16 v[68:71], v[132:135], a[20:23], 0\n"
"ds_read_b64_tr_b16 v[146:147], v248 offset:2048\n"
"v_cvt_pk_bf16_f32 v195, v18, v19\n"
"v_mfma_f32_16x16x32_bf16 v[68:71], v[128:131], a[4:7], v[68:71]\n"
"buffer_load_dwordx4 v254, s[60:63], s80 offen lds\n"
"v_mfma_f32_16x16x32_bf16 v[72:75], v[128:131], a[8:11], 0\n"
"s_addk_i32 s80, 8192\n"
"ds_read_b64_tr_b16 v[148:149], v248 offset:4096\n"
"v_cvt_pk_bf16_f32 v196, v4, v5\n"
"v_mfma_f32_16x16x32_bf16 v[72:75], v[132:135], a[24:27], v[72:75]\n"
"s_addk_i32 m0, 1024\n"
"v_cvt_pk_bf16_f32 v197, v6, v7\n"
"v_cvt_pk_bf16_f32 v198, v20, v21\n"
"v_mfma_f32_16x16x32_bf16 v[76:79], v[132:135], a[28:31], 0\n"
"ds_read_b64_tr_b16 v[150:151], v248 offset:6144\n"
"v_cvt_pk_bf16_f32 v199, v22, v23\n"
"v_mfma_f32_16x16x32_bf16 v[76:79], v[128:131], a[12:15], v[76:79]\n"
"s_waitcnt lgkmcnt(4)\n"
"buffer_load_dwordx4 v254, s[60:63], s80 offen lds\n"
"v_mfma_f32_16x16x32_bf16 v[92:95], v[136:139], a[12:15], 0\n"
"s_addk_i32 s80, 8192\n"
"ds_read_b64_tr_b16 v[152:153], v249 offset:0\n"
"v_cvt_pk_bf16_f32 v200, v8, v9\n"
"v_mfma_f32_16x16x32_bf16 v[92:95], v[140:143], a[28:31], v[92:95]\n"
"s_addk_i32 m0, 1024\n"
"v_cvt_pk_bf16_f32 v201, v10, v11\n"
"v_cvt_pk_bf16_f32 v202, v24, v25\n"
"v_mfma_f32_16x16x32_bf16 v[88:91], v[140:143], a[24:27], 0\n"
"ds_read_b64_tr_b16 v[154:155], v249 offset:2048\n"
"v_cvt_pk_bf16_f32 v203, v26, v27\n"
"v_mfma_f32_16x16x32_bf16 v[88:91], v[136:139], a[8:11], v[88:91]\n"
"buffer_load_dwordx4 v254, s[60:63], s80 offen lds\n"
"v_mfma_f32_16x16x32_bf16 v[84:87], v[136:139], a[4:7], 0\n"
"s_addk_i32 s80, 8192\n"
"ds_read_b64_tr_b16 v[156:157], v249 offset:4096\n"
"v_cvt_pk_bf16_f32 v204, v12, v13\n"
"v_mfma_f32_16x16x32_bf16 v[84:87], v[140:143], a[20:23], v[84:87]\n"
"s_addk_i32 m0, 1024\n"
"v_cvt_pk_bf16_f32 v205, v14, v15\n"
"v_cvt_pk_bf16_f32 v206, v28, v29\n"
"v_mfma_f32_16x16x32_bf16 v[80:83], v[140:143], a[16:19], 0\n"
"ds_read_b64_tr_b16 v[158:159], v249 offset:6144\n"
"v_cvt_pk_bf16_f32 v207, v30, v31\n"
"v_mfma_f32_16x16x32_bf16 v[80:83], v[136:139], a[0:3], v[80:83]\n"
"s_waitcnt lgkmcnt(4)\n"
"buffer_load_dwordx4 v254, s[60:63], s80 offen lds\n"
"v_mfma_f32_16x16x32_bf16 v[96:99], v[144:147], a[0:3], 0\n"
"s_waitcnt vmcnt(12)\n"
"ds_read_b64_tr_b16 v[128:129], v250 offset:0\n"
"v_cvt_pk_bf16_f32 v208, v32, v33\n"
"v_mfma_f32_16x16x32_bf16 v[96:99], v[148:151], a[16:19], v[96:99]\n"
"s_addk_i32 s80, 8192\n"
"v_cvt_pk_bf16_f32 v209, v34, v35\n"
"v_cvt_pk_bf16_f32 v210, v48, v49\n"
"v_mfma_f32_16x16x32_bf16 v[100:103], v[148:151], a[20:23], 0\n"
"s_addk_i32 m0, 1024\n"
"ds_read_b64_tr_b16 v[130:131], v250 offset:2048\n"
"v_cvt_pk_bf16_f32 v211, v50, v51\n"
"v_mfma_f32_16x16x32_bf16 v[100:103], v[144:147], a[4:7], v[100:103]\n"
"buffer_load_dwordx4 v254, s[60:63], s80 offen lds\n"
"v_mfma_f32_16x16x32_bf16 v[104:107], v[144:147], a[8:11], 0\n"
".LPG23_%=:\n"
"s_cbranch_execz .LPGR_%=\n"
"s_addk_i32 s80, 8192\n"
"ds_read_b64_tr_b16 v[132:133], v250 offset:4096\n"
"v_cvt_pk_bf16_f32 v212, v36, v37\n"
"v_mfma_f32_16x16x32_bf16 v[104:107], v[148:151], a[24:27], v[104:107]\n"
"s_addk_i32 m0, 1024\n"
"v_cvt_pk_bf16_f32 v213, v38, v39\n"
"v_cvt_pk_bf16_f32 v214, v52, v53\n"
"v_mfma_f32_16x16x32_bf16 v[108:111], v[148:151], a[28:31], 0\n"
"ds_read_b64_tr_b16 v[134:135], v250 offset:6144\n"
"v_cvt_pk_bf16_f32 v215, v54, v55\n"
"v_mfma_f32_16x16x32_bf16 v[108:111], v[144:147], a[12:15], v[108:111]\n"
"s_waitcnt lgkmcnt(4)\n"
"buffer_load_dwordx4 v254, s[60:63], s80 offen lds\n"
"v_mfma_f32_16x16x32_bf16 v[124:127], v[152:155], a[12:15], 0\n"
"s_addk_i32 s80, 8192\n"
"ds_read_b64_tr_b16 v[136:137], v251 offset:0\n"
"v_cvt_pk_bf16_f32 v216, v40, v41\n"
"v_mfma_f32_16x16x32_bf16 v[124:127], v[156:159], a[28:31], v[124:127]\n"
"s_addk_i32 m0, 1024\n"
"v_cvt_pk_bf16_f32 v217, v42, v43\n"
"v_cvt_pk_bf16_f32 v218, v56, v57\n"
"v_mfma_f32_16x16x32_bf16 v[120:123], v[156:159], a[24:27], 0\n"
"ds_read_b64_tr_b16 v[138:139], v251 offset:2048\n"
"v_cvt_pk_bf16_f32 v219, v58, v59\n"
"v_mfma_f32_16x16x32_bf16 v[120:123], v[152:155], a[8:11], v[120:123]\n"
"buffer_load_dwordx4 v254, s[60:63], s80 offen lds\n"
"v_mfma_f32_16x16x32_bf16 v[116:119], v[152:155], a[4:7], 0\n"
"s_addk_i32 s80, 8192\n"
"ds_read_b64_tr_b16 v[140:141], v251 offset:4096\n"
"v_cvt_pk_bf16_f32 v220, v44, v45\n"
"v_mfma_f32_16x16x32_bf16 v[116:119], v[156:159], a[20:23], v[116:119]\n"
"s_addk_i32 m0, 1024\n"
"v_cvt_pk_bf16_f32 v221, v46, v47\n"
"v_cvt_pk_bf16_f32 v222, v60, v61\n"
"v_mfma_f32_16x16x32_bf16 v[112:115], v[156:159], a[16:19], 0\n"
"ds_read_b64_tr_b16 v[142:143], v251 offset:6144\n"
"v_cvt_pk_bf16_f32 v223, v62, v63\n"
"v_mfma_f32_16x16x32_bf16 v[112:115], v[152:155], a[0:3], v[112:115]\n"
"s_waitcnt lgkmcnt(4)\n"
"buffer_load_dwordx4 v254, s[60:63], s80 offen lds\n"
"v_mfma_f32_16x16x32_bf16 v[64:67], v[128:131], a[32:35], v[64:67]\n"
"s_add_u32 s80, s88, 0\n"
"ds_read_b64_tr_b16 v[144:145], v252 offset:0\n"
"v_accvgpr_write_b32 a136, v192\n"
"v_mfma_f32_16x16x32_bf16 v[64:67], v[132:135], a[48:51], v[64:67]\n"
"s_add_u32 m0, s46, 0\n"
"v_accvgpr_write_b32 a137, v193\n"
"v_accvgpr_write_b32 a138, v194\n"
"v_mfma_f32_16x16x32_bf16 v[68:71], v[132:135], a[52:55], v[68:71]\n"
"ds_read_b64_tr_b16 v[146:147], v252 offset:2048\n"
"v_accvgpr_write_b32 a139, v195\n"
"v_mfma_f32_16x16x32_bf16 v[68:71], v[128:131], a[36:39], v[68:71]\n"
"buffer_load_dwordx4 v254, s[60:63], s80 offen lds\n"
"v_mfma_f32_16x16x32_bf16 v[72:75], v[128:131], a[40:43], v[72:75]\n"
"s_addk_i32 s80, 8192\n"
"ds_read_b64_tr_b16 v[148:149], v252 offset:4096\n"
"v_accvgpr_write_b32 a140, v208\n"
"v_mfma_f32_16x16x32_bf16 v[72:75], v[132:135], a[56:59], v[72:75]\n"
"s_addk_i32 m0, 1024\n"
"v_accvgpr_write_b32 a141, v209\n"
"v_accvgpr_write_b32 a142, v210\n"
"v_mfma_f32_16x16x32_bf16 v[76:79], v[132:135], a[60:63], v[76:79]\n"
"ds_read_b64_tr_b16 v[150:151], v252 offset:6144\n"
"v_accvgpr_write_b32 a143, v211\n"
"v_mfma_f32_16x16x32_bf16 v[76:79], v[128:131], a[44:47], v[76:79]\n"
"s_waitcnt lgkmcnt(4)\n"
"buffer_load_dwordx4 v254, s[60:63], s80 offen lds\n"
"v_mfma_f32_16x16x32_bf16 v[92:95], v[136:139], a[44:47], v[92:95]\n"
"s_addk_i32 s80, 8192\n"
"ds_read_b64_tr_b16 v[152:153], v253 offset:0\n"
"v_accvgpr_write_b32 a152, v196\n"
"v_mfma_f32_16x16x32_bf16 v[92:95], v[140:143], a[60:63], v[92:95]\n"
"s_addk_i32 m0, 1024\n"
"v_accvgpr_write_b32 a153, v197\n"
"v_accvgpr_write_b32 a154, v198\n"
"v_mfma_f32_16x16x32_bf16 v[88:91], v[140:143], a[56:59], v[88:91]\n"
"ds_read_b64_tr_b16 v[154:155], v253 offset:2048\n"
"v_accvgpr_write_b32 a155, v199\n"
"v_mfma_f32_16x16x32_bf16 v[88:91], v[136:139], a[40:43], v[88:91]\n"
"buffer_load_dwordx4 v254, s[60:63], s80 offen lds\n"
"v_mfma_f32_16x16x32_bf16 v[84:87], v[136:139], a[36:39], v[84:87]\n"
"s_addk_i32 s80, 8192\n"
"ds_read_b64_tr_b16 v[156:157], v253 offset:4096\n"
"v_accvgpr_write_b32 a156, v212\n"
"v_mfma_f32_16x16x32_bf16 v[84:87], v[140:143], a[52:55], v[84:87]\n"
"s_addk_i32 m0, 1024\n"
"v_accvgpr_write_b32 a157, v213\n"
"v_accvgpr_write_b32 a158, v214\n"
"v_mfma_f32_16x16x32_bf16 v[80:83], v[140:143], a[48:51], v[80:83]\n"
"ds_read_b64_tr_b16 v[158:159], v253 offset:6144\n"
"v_accvgpr_write_b32 a159, v215\n"
"v_mfma_f32_16x16x32_bf16 v[80:83], v[136:139], a[32:35], v[80:83]\n"
"s_waitcnt lgkmcnt(4)\n"
"buffer_load_dwordx4 v254, s[60:63], s80 offen lds\n"
"v_mfma_f32_16x16x32_bf16 v[96:99], v[144:147], a[32:35], v[96:99]\n"
"s_waitcnt vmcnt(12)\n"
"ds_read_b64_tr_b16 v[128:129], v246 offset:8192\n"
"v_accvgpr_write_b32 a168, v200\n"
"v_mfma_f32_16x16x32_bf16 v[96:99], v[148:151], a[48:51], v[96:99]\n"
"s_addk_i32 s80, 8192\n"
"v_accvgpr_write_b32 a169, v201\n"
"v_accvgpr_write_b32 a170, v202\n"
"v_mfma_f32_16x16x32_bf16 v[100:103], v[148:151], a[52:55], v[100:103]\n"
"s_addk_i32 m0, 1024\n"
"ds_read_b64_tr_b16 v[130:131], v246 offset:10240\n"
"v_accvgpr_write_b32 a171, v203\n"
"v_mfma_f32_16x16x32_bf16 v[100:103], v[144:147], a[36:39], v[100:103]\n"
"buffer_load_dwordx4 v254, s[60:63], s80 offen lds\n"
"v_mfma_f32_16x16x32_bf16 v[104:107], v[144:147], a[40:43], v[104:107]\n"
"s_addk_i32 s80, 8192\n"
"ds_read_b64_tr_b16 v[132:133], v246 offset:12288\n"
"v_accvgpr_write_b32 a172, v216\n"
"v_mfma_f32_16x16x32_bf16 v[104:107], v[148:151], a[56:59], v[104:107]\n"
"s_addk_i32 m0, 1024\n"
"v_accvgpr_write_b32 a173, v217\n"
"v_accvgpr_write_b32 a174, v218\n"
"v_mfma_f32_16x16x32_bf16 v[108:111], v[148:151], a[60:63], v[108:111]\n"
"ds_read_b64_tr_b16 v[134:135], v246 offset:14336\n"
"v_accvgpr_write_b32 a175, v219\n"
"v_mfma_f32_16x16x32_bf16 v[108:111], v[144:147], a[44:47], v[108:111]\n"
"s_waitcnt lgkmcnt(4)\n"
"buffer_load_dwordx4 v254, s[60:63], s80 offen lds\n"
"v_mfma_f32_16x16x32_bf16 v[124:127], v[152:155], a[44:47], v[124:127]\n"
"s_addk_i32 s80, 8192\n"
"ds_read_b64_tr_b16 v[136:137], v247 offset:8192\n"
"v_accvgpr_write_b32 a184, v204\n"
"v_mfma_f32_16x16x32_bf16 v[124:127], v[156:159], a[60:63], v[124:127]\n"
"s_addk_i32 m0, 1024\n"
"v_accvgpr_write_b32 a185, v205\n"
"v_accvgpr_write_b32 a186, v206\n"
"v_mfma_f32_16x16x32_bf16 v[120:123], v[156:159], a[56:59], v[120:123]\n"
"ds_read_b64_tr_b16 v[138:139], v247 offset:10240\n"
"v_accvgpr_write_b32 a187, v207\n"
"v_mfma_f32_16x16x32_bf16 v[120:123], v[152:155], a[40:43], v[120:123]\n"
"buffer_load_dwordx4 v254, s[60:63], s80 offen lds\n"
"v_mfma_f32_16x16x32_bf16 v[116:119], v[152:155], a[36:39], v[116:119]\n"
"s_addk_i32 s80, 8192\n"
"ds_read_b64_tr_b16 v[140:141], v247 offset:12288\n"
"v_accvgpr_write_b32 a188, v220\n"
"v_mfma_f32_16x16x32_bf16 v[116:119], v[156:159], a[52:55], v[116:119]\n"
"s_addk_i32 m0, 1024\n"
"v_accvgpr_write_b32 a189, v221\n"
"v_accvgpr_write_b32 a190, v222\n"
"v_mfma_f32_16x16x32_bf16 v[112:115], v[156:159], a[48:51], v[112:115]\n"
"ds_read_b64_tr_b16 v[142:143], v247 offset:14336\n"
"v_accvgpr_write_b32 a191, v223\n"
"v_mfma_f32_16x16x32_bf16 v[112:115], v[152:155], a[32:35], v[112:115]\n"
"s_waitcnt lgkmcnt(4)\n"
"buffer_load_dwordx4 v254, s[60:63], s80 offen lds\n"
"v_mfma_f32_16x16x32_bf16 v[64:67], v[128:131], a[64:67], v[64:67]\n"
"s_add_u32 s80, s88, 65536\n"
"ds_read_b64_tr_b16 v[144:145], v248 offset:8192\n"
"v_xor_b32_e32 v229, 32, v228\n"
"v_mfma_f32_16x16x32_bf16 v[64:67], v[132:135], a[80:83], v[64:67]\n"
"s_add_u32 m0, s46, 65536\n"
"v_xor_b32_e32 v230, 64, v228\n"
"v_xor_b32_e32 v231, 96, v228\n"
"v_mfma_f32_16x16x32_bf16 v[68:71], v[132:135], a[84:87], v[68:71]\n"
"ds_read_b64_tr_b16 v[146:147], v248 offset:10240\n"
"v_xor_b32_e32 v232, 128, v228\n"
"v_mfma_f32_16x16x32_bf16 v[68:71], v[128:131], a[68:71], v[68:71]\n"
"buffer_load_dwordx4 v254, s[60:63], s80 offen lds\n"
"v_mfma_f32_16x16x32_bf16 v[72:75], v[128:131], a[72:75], v[72:75]\n"
"s_addk_i32 s80, 8192\n"
"ds_read_b64_tr_b16 v[148:149], v248 offset:12288\n"
"v_xor_b32_e32 v233, 160, v228\n"
"v_mfma_f32_16x16x32_bf16 v[72:75], v[132:135], a[88:91], v[72:75]\n"
"s_addk_i32 m0, 1024\n"
"v_xor_b32_e32 v234, 192, v228\n"
"v_xor_b32_e32 v235, 224, v228\n"
"v_mfma_f32_16x16x32_bf16 v[76:79], v[132:135], a[92:95], v[76:79]\n"
"ds_read_b64_tr_b16 v[150:151], v248 offset:14336\n"
"v_mfma_f32_16x16x32_bf16 v[76:79], v[128:131], a[76:79], v[76:79]\n"
"s_waitcnt lgkmcnt(4)\n"
"buffer_load_dwordx4 v254, s[60:63], s80 offen lds\n"
"v_mfma_f32_16x16x32_bf16 v[92:95], v[136:139], a[76:79], v[92:95]\n"
"s_addk_i32 s80, 8192\n"
"ds_read_b64_tr_b16 v[152:153], v249 offset:8192\n"
"v_mfma_f32_16x16x32_bf16 v[92:95], v[140:143], a[92:95], v[92:95]\n"
"s_addk_i32 m0, 1024\n"
"v_mfma_f32_16x16x32_bf16 v[88:91], v[140:143], a[88:91], v[88:91]\n"
"ds_read_b64_tr_b16 v[154:155], v249 offset:10240\n"
"v_mfma_f32_16x16x32_bf16 v[88:91], v[136:139], a[72:75], v[88:91]\n"
"buffer_load_dwordx4 v254, s[60:63], s80 offen lds\n"
"v_mfma_f32_16x16x32_bf16 v[84:87], v[136:139], a[68:71], v[84:87]\n"
"s_addk_i32 s80, 8192\n"
"ds_read_b64_tr_b16 v[156:157], v249 offset:12288\n"
"v_mfma_f32_16x16x32_bf16 v[84:87], v[140:143], a[84:87], v[84:87]\n"
"s_addk_i32 m0, 1024\n"
"v_mfma_f32_16x16x32_bf16 v[80:83], v[140:143], a[80:83], v[80:83]\n"
"ds_read_b64_tr_b16 v[158:159], v249 offset:14336\n"
"v_mfma_f32_16x16x32_bf16 v[80:83], v[136:139], a[64:67], v[80:83]\n"
"s_waitcnt lgkmcnt(4)\n"
"buffer_load_dwordx4 v254, s[60:63], s80 offen lds\n"
"v_mfma_f32_16x16x32_bf16 v[96:99], v[144:147], a[64:67], v[96:99]\n"
"s_waitcnt vmcnt(12)\n"
"ds_read_b64_tr_b16 v[128:129], v250 offset:8192\n"
"v_mfma_f32_16x16x32_bf16 v[96:99], v[148:151], a[80:83], v[96:99]\n"
"s_addk_i32 s80, 8192\n"
"v_mfma_f32_16x16x32_bf16 v[100:103], v[148:151], a[84:87], v[100:103]\n"
"s_addk_i32 m0, 1024\n"
"ds_read_b64_tr_b16 v[130:131], v250 offset:10240\n"
"v_mfma_f32_16x16x32_bf16 v[100:103], v[144:147], a[68:71], v[100:103]\n"
"buffer_load_dwordx4 v254, s[60:63], s80 offen lds\n"
"v_mfma_f32_16x16x32_bf16 v[104:107], v[144:147], a[72:75], v[104:107]\n"
"s_addk_i32 s80, 8192\n"
"ds_read_b64_tr_b16 v[132:133], v250 offset:12288\n"
"v_mfma_f32_16x16x32_bf16 v[104:107], v[148:151], a[88:91], v[104:107]\n"
"s_addk_i32 m0, 1024\n"
"v_mfma_f32_16x16x32_bf16 v[108:111], v[148:151], a[92:95], v[108:111]\n"
"ds_read_b64_tr_b16 v[134:135], v250 offset:14336\n"
"v_mfma_f32_16x16x32_bf16 v[108:111], v[144:147], a[76:79], v[108:111]\n"
"s_waitcnt lgkmcnt(4)\n"
"buffer_load_dwordx4 v254, s[60:63], s80 offen lds\n"
"v_mfma_f32_16x16x32_bf16 v[124:127], v[152:155], a[76:79], v[124:127]\n"
"s_addk_i32 s80, 8192\n"
"ds_read_b64_tr_b16 v[136:137], v251 offset:8192\n"
"v_mfma_f32_16x16x32_bf16 v[124:127], v[156:159], a[92:95], v[124:127]\n"
"s_addk_i32 m0, 1024\n"
"v_mfma_f32_16x16x32_bf16 v[120:123], v[156:159], a[88:91], v[120:123]\n"
"ds_read_b64_tr_b16 v[138:139], v251 offset:10240\n"
"v_mfma_f32_16x16x32_bf16 v[120:123], v[152:155], a[72:75], v[120:123]\n"
"buffer_load_dwordx4 v254, s[60:63], s80 offen lds\n"
"v_mfma_f32_16x16x32_bf16 v[116:119], v[152:155], a[68:71], v[116:119]\n"
"s_addk_i32 s80, 8192\n"
"ds_read_b64_tr_b16 v[140:141], v251 offset:12288\n"
"v_mfma_f32_16x16x32_bf16 v[116:119], v[156:159], a[84:87], v[116:119]\n"
"s_addk_i32 m0, 1024\n"
"v_mfma_f32_16x16x32_bf16 v[112:115], v[156:159], a[80:83], v[112:115]\n"
"ds_read_b64_tr_b16 v[142:143], v251 offset:14336\n"
"v_mfma_f32_16x16x32_bf16 v[112:115], v[152:155], a[64:67], v[112:115]\n"
"s_waitcnt lgkmcnt(4)\n"
"buffer_load_dwordx4 v254, s[60:63], s80 offen lds\n"
"v_mfma_f32_16x16x32_bf16 v[64:67], v[128:131], a[96:99], v[64:67]\n"
"s_add_u32 s80, s88, 131072\n"
"ds_read_b64_tr_b16 v[144:145], v252 offset:8192\n"
"v_mfma_f32_16x16x32_bf16 v[64:67], v[132:135], a[112:115], v[64:67]\n"
"s_add_u32 m0, s46, 8192\n"
"v_mfma_f32_16x16x32_bf16 v[68:71], v[132:135], a[116:119], v[68:71]\n"
"ds_read_b64_tr_b16 v[146:147], v252 offset:10240\n"
"v_mfma_f32_16x16x32_bf16 v[68:71], v[128:131], a[100:103], v[68:71]\n"
"buffer_load_dwordx4 v254, s[60:63], s80 offen lds\n"
"v_mfma_f32_16x16x32_bf16 v[72:75], v[128:131], a[104:107], v[72:75]\n"
"s_addk_i32 s80, 8192\n"
"ds_read_b64_tr_b16 v[148:149], v252 offset:12288\n"
"v_mfma_f32_16x16x32_bf16 v[72:75], v[132:135], a[120:123], v[72:75]\n"
"s_addk_i32 m0, 1024\n"
"v_mfma_f32_16x16x32_bf16 v[76:79], v[132:135], a[124:127], v[76:79]\n"
"ds_read_b64_tr_b16 v[150:151], v252 offset:14336\n"
"v_mfma_f32_16x16x32_bf16 v[76:79], v[128:131], a[108:111], v[76:79]\n"
"s_waitcnt lgkmcnt(4)\n"
"buffer_load_dwordx4 v254, s[60:63], s80 offen lds\n"
"v_mfma_f32_16x16x32_bf16 v[92:95], v[136:139], a[108:111], v[92:95]\n"
"s_addk_i32 s80, 8192\n"
"ds_read_b64_tr_b16 v[152:153], v253 offset:8192\n"
"v_mfma_f32_16x16x32_bf16 v[92:95], v[140:143], a[124:127], v[92:95]\n"
"s_addk_i32 m0, 1024\n"
"v_mfma_f32_16x16x32_bf16 v[88:91], v[140:143], a[120:123], v[88:91]\n"
"ds_read_b64_tr_b16 v[154:155], v253 offset:10240\n"
"v_mfma_f32_16x16x32_bf16 v[88:91], v[136:139], a[104:107], v[88:91]\n"
"buffer_load_dwordx4 v254, s[60:63], s80 offen lds\n"
"v_mfma_f32_16x16x32_bf16 v[84:87], v[136:139], a[100:103], v[84:87]\n"
"s_addk_i32 s80, 8192\n"
"ds_read_b64_tr_b16 v[156:157], v253 offset:12288\n"
"v_mfma_f32_16x16x32_bf16 v[84:87], v[140:143], a[116:119], v[84:87]\n"
"s_addk_i32 m0, 1024\n"
"v_mfma_f32_16x16x32_bf16 v[80:83], v[140:143], a[112:115], v[80:83]\n"
"ds_read_b64_tr_b16 v[158:159], v253 offset:14336\n"
"v_mfma_f32_16x16x32_bf16 v[80:83], v[136:139], a[96:99], v[80:83]\n"
"s_waitcnt lgkmcnt(4)\n"
"buffer_load_dwordx4 v254, s[60:63], s80 offen lds\n"
"v_mfma_f32_16x16x32_bf16 v[96:99], v[144:147], a[96:99], v[96:99]\n"
"s_waitcnt vmcnt(12)\n"
"ds_read_b64_tr_b16 v[128:129], v246 offset:0\n"
"v_mfma_f32_16x16x32_bf16 v[96:99], v[148:151], a[112:115], v[96:99]\n"
"s_addk_i32 s80, 8192\n"
"v_mfma_f32_16x16x32_bf16 v[100:103], v[148:151], a[116:119], v[100:103]\n"
"s_addk_i32 m0, 1024\n"
"ds_read_b64_tr_b16 v[130:131], v246 offset:2048\n"
"v_mfma_f32_16x16x32_bf16 v[100:103], v[144:147], a[100:103], v[100:103]\n"
"buffer_load_dwordx4 v254, s[60:63], s80 offen lds\n"
"v_mfma_f32_16x16x32_bf16 v[104:107], v[144:147], a[104:107], v[104:107]\n"
"s_addk_i32 s80, 8192\n"
"ds_read_b64_tr_b16 v[132:133], v246 offset:4096\n"
"v_mfma_f32_16x16x32_bf16 v[104:107], v[148:151], a[120:123], v[104:107]\n"
"s_addk_i32 m0, 1024\n"
"v_mfma_f32_16x16x32_bf16 v[108:111], v[148:151], a[124:127], v[108:111]\n"
"ds_read_b64_tr_b16 v[134:135], v246 offset:6144\n"
"v_mfma_f32_16x16x32_bf16 v[108:111], v[144:147], a[108:111], v[108:111]\n"
"s_waitcnt lgkmcnt(4)\n"
"buffer_load_dwordx4 v254, s[60:63], s80 offen lds\n"
"v_mfma_f32_16x16x32_bf16 v[124:127], v[152:155], a[108:111], v[124:127]\n"
"s_addk_i32 s80, 8192\n"
"ds_read_b64_tr_b16 v[136:137], v247 offset:0\n"
"v_mfma_f32_16x16x32_bf16 v[124:127], v[156:159], a[124:127], v[124:127]\n"
"s_addk_i32 m0, 1024\n"
"v_mfma_f32_16x16x32_bf16 v[120:123], v[156:159], a[120:123], v[120:123]\n"
"ds_read_b64_tr_b16 v[138:139], v247 offset:2048\n"
"v_mfma_f32_16x16x32_bf16 v[120:123], v[152:155], a[104:107], v[120:123]\n"
"buffer_load_dwordx4 v254, s[60:63], s80 offen lds\n"
"v_mfma_f32_16x16x32_bf16 v[116:119], v[152:155], a[100:103], v[116:119]\n"
"s_addk_i32 s80, 8192\n"
"ds_read_b64_tr_b16 v[140:141], v247 offset:4096\n"
"v_mfma_f32_16x16x32_bf16 v[116:119], v[156:159], a[116:119], v[116:119]\n"
"s_addk_i32 m0, 1024\n"
"v_mfma_f32_16x16x32_bf16 v[112:115], v[156:159], a[112:115], v[112:115]\n"
"ds_read_b64_tr_b16 v[142:143], v247 offset:6144\n"
"v_mfma_f32_16x16x32_bf16 v[112:115], v[152:155], a[96:99], v[112:115]\n"
"s_waitcnt lgkmcnt(4)\n"
"buffer_load_dwordx4 v254, s[60:63], s80 offen lds\n"
"v_mfma_f32_16x16x32_bf16 v[0:3], v[128:131], a[0:3], 0\n"
"s_add_u32 s80, s88, 196608\n"
"ds_read_b64_tr_b16 v[144:145], v248 offset:0\n"
"v_cvt_pk_bf16_f32 v64, v64, v65\n"
"v_mfma_f32_16x16x32_bf16 v[0:3], v[132:135], a[16:19], v[0:3]\n"
"s_add_u32 m0, s46, 73728\n"
"v_cvt_pk_bf16_f32 v65, v66, v67\n"
"v_cvt_pk_bf16_f32 v66, v68, v69\n"
"v_mfma_f32_16x16x32_bf16 v[4:7], v[132:135], a[20:23], 0\n"
"ds_read_b64_tr_b16 v[146:147], v248 offset:2048\n"
"v_cvt_pk_bf16_f32 v67, v70, v71\n"
"v_mfma_f32_16x16x32_bf16 v[4:7], v[128:131], a[4:7], v[4:7]\n"
"buffer_load_dwordx4 v254, s[60:63], s80 offen lds\n"
"v_mfma_f32_16x16x32_bf16 v[8:11], v[128:131], a[8:11], 0\n"
"s_addk_i32 s80, 8192\n"
"ds_read_b64_tr_b16 v[148:149], v248 offset:4096\n"
"v_cvt_pk_bf16_f32 v72, v72, v73\n"
"v_mfma_f32_16x16x32_bf16 v[8:11], v[132:135], a[24:27], v[8:11]\n"
"s_addk_i32 m0, 1024\n"
"v_cvt_pk_bf16_f32 v73, v74, v75\n"
"v_cvt_pk_bf16_f32 v74, v76, v77\n"
"v_mfma_f32_16x16x32_bf16 v[12:15], v[132:135], a[28:31], 0\n"
"ds_read_b64_tr_b16 v[150:151], v248 offset:6144\n"
"v_cvt_pk_bf16_f32 v75, v78, v79\n"
"v_mfma_f32_16x16x32_bf16 v[12:15], v[128:131], a[12:15], v[12:15]\n"
"s_waitcnt lgkmcnt(4)\n"
"buffer_load_dwordx4 v254, s[60:63], s80 offen lds\n"
"v_mfma_f32_16x16x32_bf16 v[28:31], v[136:139], a[12:15], 0\n"
"s_addk_i32 s80, 8192\n"
"ds_read_b64_tr_b16 v[152:153], v249 offset:0\n"
"v_cvt_pk_bf16_f32 v80, v80, v81\n"
"v_mfma_f32_16x16x32_bf16 v[28:31], v[140:143], a[28:31], v[28:31]\n"
"s_addk_i32 m0, 1024\n"
"v_cvt_pk_bf16_f32 v81, v82, v83\n"
"v_cvt_pk_bf16_f32 v82, v84, v85\n"
"v_mfma_f32_16x16x32_bf16 v[24:27], v[140:143], a[24:27], 0\n"
"ds_read_b64_tr_b16 v[154:155], v249 offset:2048\n"
"v_cvt_pk_bf16_f32 v83, v86, v87\n"
"v_mfma_f32_16x16x32_bf16 v[24:27], v[136:139], a[8:11], v[24:27]\n"
"buffer_load_dwordx4 v254, s[60:63], s80 offen lds\n"
"v_mfma_f32_16x16x32_bf16 v[20:23], v[136:139], a[4:7], 0\n"
"s_addk_i32 s80, 8192\n"
"ds_read_b64_tr_b16 v[156:157], v249 offset:4096\n"
"v_cvt_pk_bf16_f32 v88, v88, v89\n"
"v_mfma_f32_16x16x32_bf16 v[20:23], v[140:143], a[20:23], v[20:23]\n"
"s_addk_i32 m0, 1024\n"
"v_cvt_pk_bf16_f32 v89, v90, v91\n"
"v_cvt_pk_bf16_f32 v90, v92, v93\n"
"v_mfma_f32_16x16x32_bf16 v[16:19], v[140:143], a[16:19], 0\n"
"ds_read_b64_tr_b16 v[158:159], v249 offset:6144\n"
"v_cvt_pk_bf16_f32 v91, v94, v95\n"
"v_mfma_f32_16x16x32_bf16 v[16:19], v[136:139], a[0:3], v[16:19]\n"
"s_waitcnt lgkmcnt(4)\n"
"buffer_load_dwordx4 v254, s[60:63], s80 offen lds\n"
"v_mfma_f32_16x16x32_bf16 v[32:35], v[144:147], a[0:3], 0\n"
"s_waitcnt vmcnt(12)\n"
"ds_read_b64_tr_b16 v[128:129], v250 offset:0\n"
"v_cvt_pk_bf16_f32 v96, v96, v97\n"
"v_mfma_f32_16x16x32_bf16 v[32:35], v[148:151], a[16:19], v[32:35]\n"
"s_addk_i32 s80, 8192\n"
"v_cvt_pk_bf16_f32 v97, v98, v99\n"
"v_cvt_pk_bf16_f32 v98, v100, v101\n"
"v_mfma_f32_16x16x32_bf16 v[36:39], v[148:151], a[20:23], 0\n"
"s_addk_i32 m0, 1024\n"
"ds_read_b64_tr_b16 v[130:131], v250 offset:2048\n"
"v_cvt_pk_bf16_f32 v99, v102, v103\n"
"v_mfma_f32_16x16x32_bf16 v[36:39], v[144:147], a[4:7], v[36:39]\n"
"buffer_load_dwordx4 v254, s[60:63], s80 offen lds\n"
"v_mfma_f32_16x16x32_bf16 v[40:43], v[144:147], a[8:11], 0\n"
"s_addk_i32 s80, 8192\n"
"ds_read_b64_tr_b16 v[132:133], v250 offset:4096\n"
"v_cvt_pk_bf16_f32 v104, v104, v105\n"
"v_mfma_f32_16x16x32_bf16 v[40:43], v[148:151], a[24:27], v[40:43]\n"
"s_addk_i32 m0, 1024\n"
"v_cvt_pk_bf16_f32 v105, v106, v107\n"
"v_cvt_pk_bf16_f32 v106, v108, v109\n"
"v_mfma_f32_16x16x32_bf16 v[44:47], v[148:151], a[28:31], 0\n"
"ds_read_b64_tr_b16 v[134:135], v250 offset:6144\n"
"v_cvt_pk_bf16_f32 v107, v110, v111\n"
"v_mfma_f32_16x16x32_bf16 v[44:47], v[144:147], a[12:15], v[44:47]\n"
"s_waitcnt lgkmcnt(4)\n"
"buffer_load_dwordx4 v254, s[60:63], s80 offen lds\n"
"v_mfma_f32_16x16x32_bf16 v[60:63], v[152:155], a[12:15], 0\n"
"s_addk_i32 s80, 8192\n"
"ds_read_b64_tr_b16 v[136:137], v251 offset:0\n"
"v_cvt_pk_bf16_f32 v112, v112, v113\n"
"v_mfma_f32_16x16x32_bf16 v[60:63], v[156:159], a[28:31], v[60:63]\n"
"s_addk_i32 m0, 1024\n"
"v_cvt_pk_bf16_f32 v113, v114, v115\n"
"v_cvt_pk_bf16_f32 v114, v116, v117\n"
"v_mfma_f32_16x16x32_bf16 v[56:59], v[156:159], a[24:27], 0\n"
"ds_read_b64_tr_b16 v[138:139], v251 offset:2048\n"
"v_cvt_pk_bf16_f32 v115, v118, v119\n"
"v_mfma_f32_16x16x32_bf16 v[56:59], v[152:155], a[8:11], v[56:59]\n"
"buffer_load_dwordx4 v254, s[60:63], s80 offen lds\n"
"v_mfma_f32_16x16x32_bf16 v[52:55], v[152:155], a[4:7], 0\n"
"s_addk_i32 s80, 8192\n"
"ds_read_b64_tr_b16 v[140:141], v251 offset:4096\n"
"v_cvt_pk_bf16_f32 v120, v120, v121\n"
"v_mfma_f32_16x16x32_bf16 v[52:55], v[156:159], a[20:23], v[52:55]\n"
"s_addk_i32 m0, 1024\n"
"v_cvt_pk_bf16_f32 v121, v122, v123\n"
"v_cvt_pk_bf16_f32 v122, v124, v125\n"
"v_mfma_f32_16x16x32_bf16 v[48:51], v[156:159], a[16:19], 0\n"
"ds_read_b64_tr_b16 v[142:143], v251 offset:6144\n"
"v_cvt_pk_bf16_f32 v123, v126, v127\n"
"v_mfma_f32_16x16x32_bf16 v[48:51], v[152:155], a[0:3], v[48:51]\n"
"s_waitcnt lgkmcnt(4)\n"
"buffer_load_dwordx4 v254, s[60:63], s80 offen lds\n"
"v_mfma_f32_16x16x32_bf16 v[0:3], v[128:131], a[32:35], v[0:3]\n"
"s_add_u32 s44, s44, 16384\n"
"ds_read_b64_tr_b16 v[144:145], v252 offset:0\n"
"v_mfma_f32_16x16x32_bf16 v[0:3], v[132:135], a[48:51], v[0:3]\n"
"s_add_u32 m0, s46, 0\n"
"v_mfma_f32_16x16x32_bf16 v[4:7], v[132:135], a[52:55], v[4:7]\n"
"ds_read_b64_tr_b16 v[146:147], v252 offset:2048\n"
"v_mfma_f32_16x16x32_bf16 v[4:7], v[128:131], a[36:39], v[4:7]\n"
"buffer_load_dwordx4 v236, s[36:39], s44 offen offset:0 lds\n"
"v_mfma_f32_16x16x32_bf16 v[8:11], v[128:131], a[40:43], v[8:11]\n"
"ds_read_b64_tr_b16 v[148:149], v252 offset:4096\n"
"v_mfma_f32_16x16x32_bf16 v[8:11], v[132:135], a[56:59], v[8:11]\n"
"v_mfma_f32_16x16x32_bf16 v[12:15], v[132:135], a[60:63], v[12:15]\n"
"ds_read_b64_tr_b16 v[150:151], v252 offset:6144\n"
"v_mfma_f32_16x16x32_bf16 v[12:15], v[128:131], a[44:47], v[12:15]\n"
"s_waitcnt lgkmcnt(4)\n"
"buffer_load_dwordx4 v237, s[36:39], s44 offen offset:1024 lds\n"
"v_mfma_f32_16x16x32_bf16 v[28:31], v[136:139], a[44:47], v[28:31]\n"
"ds_read_b64_tr_b16 v[152:153], v253 offset:0\n"
"v_mfma_f32_16x16x32_bf16 v[28:31], v[140:143], a[60:63], v[28:31]\n"
"v_mfma_f32_16x16x32_bf16 v[24:27], v[140:143], a[56:59], v[24:27]\n"
"ds_read_b64_tr_b16 v[154:155], v253 offset:2048\n"
"v_mfma_f32_16x16x32_bf16 v[24:27], v[136:139], a[40:43], v[24:27]\n"
"buffer_load_dwordx4 v238, s[36:39], s44 offen offset:2048 lds\n"
"v_mfma_f32_16x16x32_bf16 v[20:23], v[136:139], a[36:39], v[20:23]\n"
"ds_read_b64_tr_b16 v[156:157], v253 offset:4096\n"
"v_mfma_f32_16x16x32_bf16 v[20:23], v[140:143], a[52:55], v[20:23]\n"
"v_mfma_f32_16x16x32_bf16 v[16:19], v[140:143], a[48:51], v[16:19]\n"
"ds_read_b64_tr_b16 v[158:159], v253 offset:6144\n"
"v_mfma_f32_16x16x32_bf16 v[16:19], v[136:139], a[32:35], v[16:19]\n"
"s_waitcnt lgkmcnt(4)\n"
"buffer_load_dwordx4 v239, s[36:39], s44 offen offset:3072 lds\n"
"v_mfma_f32_16x16x32_bf16 v[32:35], v[144:147], a[32:35], v[32:35]\n"
"s_waitcnt vmcnt(12)\n"
"ds_read_b64_tr_b16 v[128:129], v246 offset:8192\n"
"v_mfma_f32_16x16x32_bf16 v[32:35], v[148:151], a[48:51], v[32:35]\n"
"s_add_u32 s44, s44, 16384\n"
"v_mfma_f32_16x16x32_bf16 v[36:39], v[148:151], a[52:55], v[36:39]\n"
"s_add_u32 m0, s46, 4096\n"
"ds_read_b64_tr_b16 v[130:131], v246 offset:10240\n"
"v_mfma_f32_16x16x32_bf16 v[36:39], v[144:147], a[36:39], v[36:39]\n"
"buffer_load_dwordx4 v236, s[36:39], s44 offen offset:0 lds\n"
"v_mfma_f32_16x16x32_bf16 v[40:43], v[144:147], a[40:43], v[40:43]\n"
"ds_read_b64_tr_b16 v[132:133], v246 offset:12288\n"
"v_mfma_f32_16x16x32_bf16 v[40:43], v[148:151], a[56:59], v[40:43]\n"
"v_mfma_f32_16x16x32_bf16 v[44:47], v[148:151], a[60:63], v[44:47]\n"
"ds_read_b64_tr_b16 v[134:135], v246 offset:14336\n"
"v_mfma_f32_16x16x32_bf16 v[44:47], v[144:147], a[44:47], v[44:47]\n"
"s_waitcnt lgkmcnt(4)\n"
"buffer_load_dwordx4 v237, s[36:39], s44 offen offset:1024 lds\n"
"v_mfma_f32_16x16x32_bf16 v[60:63], v[152:155], a[44:47], v[60:63]\n"
"ds_read_b64_tr_b16 v[136:137], v247 offset:8192\n"
"v_mfma_f32_16x16x32_bf16 v[60:63], v[156:159], a[60:63], v[60:63]\n"
"v_mfma_f32_16x16x32_bf16 v[56:59], v[156:159], a[56:59], v[56:59]\n"
"ds_read_b64_tr_b16 v[138:139], v247 offset:10240\n"
"v_mfma_f32_16x16x32_bf16 v[56:59], v[152:155], a[40:43], v[56:59]\n"
"buffer_load_dwordx4 v238, s[36:39], s44 offen offset:2048 lds\n"
"v_mfma_f32_16x16x32_bf16 v[52:55], v[152:155], a[36:39], v[52:55]\n"
"ds_read_b64_tr_b16 v[140:141], v247 offset:12288\n"
"v_mfma_f32_16x16x32_bf16 v[52:55], v[156:159], a[52:55], v[52:55]\n"
"v_mfma_f32_16x16x32_bf16 v[48:51], v[156:159], a[48:51], v[48:51]\n"
"ds_read_b64_tr_b16 v[142:143], v247 offset:14336\n"
"v_mfma_f32_16x16x32_bf16 v[48:51], v[152:155], a[32:35], v[48:51]\n"
"s_waitcnt lgkmcnt(4)\n"
"buffer_load_dwordx4 v239, s[36:39], s44 offen offset:3072 lds\n"
"v_mfma_f32_16x16x32_bf16 v[0:3], v[128:131], a[64:67], v[0:3]\n"
"ds_read_b64_tr_b16 v[144:145], v248 offset:8192\n"
"v_mfma_f32_16x16x32_bf16 v[0:3], v[132:135], a[80:83], v[0:3]\n"
"v_mfma_f32_16x16x32_bf16 v[4:7], v[132:135], a[84:87], v[4:7]\n"
"s_add_u32 s45, s45, 16384\n"
"ds_read_b64_tr_b16 v[146:147], v248 offset:10240\n"
"v_mfma_f32_16x16x32_bf16 v[4:7], v[128:131], a[68:71], v[4:7]\n"
"s_add_u32 m0, s46, 69632\n"
"v_mfma_f32_16x16x32_bf16 v[8:11], v[128:131], a[72:75], v[8:11]\n"
"ds_read_b64_tr_b16 v[148:149], v248 offset:12288\n"
"v_mfma_f32_16x16x32_bf16 v[8:11], v[132:135], a[88:91], v[8:11]\n"
"buffer_load_dwordx4 v240, s[40:43], s45 offen offset:0 lds\n"
"v_mfma_f32_16x16x32_bf16 v[12:15], v[132:135], a[92:95], v[12:15]\n"
"ds_read_b64_tr_b16 v[150:151], v248 offset:14336\n"
"v_mfma_f32_16x16x32_bf16 v[12:15], v[128:131], a[76:79], v[12:15]\n"
"s_waitcnt lgkmcnt(4)\n"
"v_mfma_f32_16x16x32_bf16 v[28:31], v[136:139], a[76:79], v[28:31]\n"
"ds_read_b64_tr_b16 v[152:153], v249 offset:8192\n"
"v_mfma_f32_16x16x32_bf16 v[28:31], v[140:143], a[92:95], v[28:31]\n"
"v_mfma_f32_16x16x32_bf16 v[24:27], v[140:143], a[88:91], v[24:27]\n"
"ds_read_b64_tr_b16 v[154:155], v249 offset:10240\n"
"v_mfma_f32_16x16x32_bf16 v[24:27], v[136:139], a[72:75], v[24:27]\n"
"v_mfma_f32_16x16x32_bf16 v[20:23], v[136:139], a[68:71], v[20:23]\n"
"ds_read_b64_tr_b16 v[156:157], v249 offset:12288\n"
"v_mfma_f32_16x16x32_bf16 v[20:23], v[140:143], a[84:87], v[20:23]\n"
"buffer_load_dwordx4 v241, s[40:43], s45 offen offset:1024 lds\n"
"v_mfma_f32_16x16x32_bf16 v[16:19], v[140:143], a[80:83], v[16:19]\n"
"ds_read_b64_tr_b16 v[158:159], v249 offset:14336\n"
"v_mfma_f32_16x16x32_bf16 v[16:19], v[136:139], a[64:67], v[16:19]\n"
"s_waitcnt lgkmcnt(4)\n"
"v_mfma_f32_16x16x32_bf16 v[32:35], v[144:147], a[64:67], v[32:35]\n"
"s_waitcnt vmcnt(10)\n"
"ds_read_b64_tr_b16 v[128:129], v250 offset:8192\n"
"v_mfma_f32_16x16x32_bf16 v[32:35], v[148:151], a[80:83], v[32:35]\n"
"v_mfma_f32_16x16x32_bf16 v[36:39], v[148:151], a[84:87], v[36:39]\n"
"ds_read_b64_tr_b16 v[130:131], v250 offset:10240\n"
"v_mfma_f32_16x16x32_bf16 v[36:39], v[144:147], a[68:71], v[36:39]\n"
"v_mfma_f32_16x16x32_bf16 v[40:43], v[144:147], a[72:75], v[40:43]\n"
"ds_read_b64_tr_b16 v[132:133], v250 offset:12288\n"
"v_mfma_f32_16x16x32_bf16 v[40:43], v[148:151], a[88:91], v[40:43]\n"
"buffer_load_dwordx4 v240, s[40:43], s45 offen offset:2048 lds\n"
"v_mfma_f32_16x16x32_bf16 v[44:47], v[148:151], a[92:95], v[44:47]\n"
"ds_read_b64_tr_b16 v[134:135], v250 offset:14336\n"
"v_mfma_f32_16x16x32_bf16 v[44:47], v[144:147], a[76:79], v[44:47]\n"
"s_waitcnt lgkmcnt(4)\n"
"v_mfma_f32_16x16x32_bf16 v[60:63], v[152:155], a[76:79], v[60:63]\n"
"ds_read_b64_tr_b16 v[136:137], v251 offset:8192\n"
"v_mfma_f32_16x16x32_bf16 v[60:63], v[156:159], a[92:95], v[60:63]\n"
"v_mfma_f32_16x16x32_bf16 v[56:59], v[156:159], a[88:91], v[56:59]\n"
"ds_read_b64_tr_b16 v[138:139], v251 offset:10240\n"
"v_mfma_f32_16x16x32_bf16 v[56:59], v[152:155], a[72:75], v[56:59]\n"
"v_mfma_f32_16x16x32_bf16 v[52:55], v[152:155], a[68:71], v[52:55]\n"
"ds_read_b64_tr_b16 v[140:141], v251 offset:12288\n"
"v_mfma_f32_16x16x32_bf16 v[52:55], v[156:159], a[84:87], v[52:55]\n"
"buffer_load_dwordx4 v241, s[40:43], s45 offen offset:3072 lds\n"
"v_mfma_f32_16x16x32_bf16 v[48:51], v[156:159], a[80:83], v[48:51]\n"
"ds_read_b64_tr_b16 v[142:143], v251 offset:14336\n"
"v_mfma_f32_16x16x32_bf16 v[48:51], v[152:155], a[64:67], v[48:51]\n"
"s_waitcnt lgkmcnt(4)\n"
"v_mfma_f32_16x16x32_bf16 v[0:3], v[128:131], a[96:99], v[0:3]\n"
"s_add_u32 s44, s44, 16384\n"
"ds_read_b64_tr_b16 v[144:145], v252 offset:8192\n"
"v_mfma_f32_16x16x32_bf16 v[0:3], v[132:135], a[112:115], v[0:3]\n"
"s_add_u32 m0, s46, 8192\n"
"v_mfma_f32_16x16x32_bf16 v[4:7], v[132:135], a[116:119], v[4:7]\n"
"ds_read_b64_tr_b16 v[146:147], v252 offset:10240\n"
"v_mfma_f32_16x16x32_bf16 v[4:7], v[128:131], a[100:103], v[4:7]\n"
"buffer_load_dwordx4 v236, s[36:39], s44 offen offset:0 lds\n"
"v_mfma_f32_16x16x32_bf16 v[8:11], v[128:131], a[104:107], v[8:11]\n"
"ds_read_b64_tr_b16 v[148:149], v252 offset:12288\n"
"v_mfma_f32_16x16x32_bf16 v[8:11], v[132:135], a[120:123], v[8:11]\n"
"v_mfma_f32_16x16x32_bf16 v[12:15], v[132:135], a[124:127], v[12:15]\n"
"ds_read_b64_tr_b16 v[150:151], v252 offset:14336\n"
"v_mfma_f32_16x16x32_bf16 v[12:15], v[128:131], a[108:111], v[12:15]\n"
"s_waitcnt lgkmcnt(4)\n"
"buffer_load_dwordx4 v237, s[36:39], s44 offen offset:1024 lds\n"
"v_mfma_f32_16x16x32_bf16 v[28:31], v[136:139], a[108:111], v[28:31]\n"
".LPG24_%=:\n"
"s_cbranch_execz .LPGR_%=\n"
"ds_read_b64_tr_b16 v[152:153], v253 offset:8192\n"
"v_mfma_f32_16x16x32_bf16 v[28:31], v[140:143], a[124:127], v[28:31]\n"
"v_mfma_f32_16x16x32_bf16 v[24:27], v[140:143], a[120:123], v[24:27]\n"
"ds_read_b64_tr_b16 v[154:155], v253 offset:10240\n"
"v_mfma_f32_16x16x32_bf16 v[24:27], v[136:139], a[104:107], v[24:27]\n"
"buffer_load_dwordx4 v238, s[36:39], s44 offen offset:2048 lds\n"
"v_mfma_f32_16x16x32_bf16 v[20:23], v[136:139], a[100:103], v[20:23]\n"
"ds_read_b64_tr_b16 v[156:157], v253 offset:12288\n"
"v_mfma_f32_16x16x32_bf16 v[20:23], v[140:143], a[116:119], v[20:23]\n"
"v_mfma_f32_16x16x32_bf16 v[16:19], v[140:143], a[112:115], v[16:19]\n"
"ds_read_b64_tr_b16 v[158:159], v253 offset:14336\n"
"v_mfma_f32_16x16x32_bf16 v[16:19], v[136:139], a[96:99], v[16:19]\n"
"s_waitcnt lgkmcnt(4)\n"
"buffer_load_dwordx4 v239, s[36:39], s44 offen offset:3072 lds\n"
"v_mfma_f32_16x16x32_bf16 v[32:35], v[144:147], a[96:99], v[32:35]\n"
"s_add_u32 s44, s44, 16384\n"
"v_mfma_f32_16x16x32_bf16 v[32:35], v[148:151], a[112:115], v[32:35]\n"
"s_add_u32 m0, s46, 12288\n"
"v_mfma_f32_16x16x32_bf16 v[36:39], v[148:151], a[116:119], v[36:39]\n"
"v_mfma_f32_16x16x32_bf16 v[36:39], v[144:147], a[100:103], v[36:39]\n"
"buffer_load_dwordx4 v236, s[36:39], s44 offen offset:0 lds\n"
"v_mfma_f32_16x16x32_bf16 v[40:43], v[144:147], a[104:107], v[40:43]\n"
"v_mfma_f32_16x16x32_bf16 v[40:43], v[148:151], a[120:123], v[40:43]\n"
"v_mfma_f32_16x16x32_bf16 v[44:47], v[148:151], a[124:127], v[44:47]\n"
"v_mfma_f32_16x16x32_bf16 v[44:47], v[144:147], a[108:111], v[44:47]\n"
"s_waitcnt lgkmcnt(0)\n"
"buffer_load_dwordx4 v237, s[36:39], s44 offen offset:1024 lds\n"
"v_mfma_f32_16x16x32_bf16 v[60:63], v[152:155], a[108:111], v[60:63]\n"
"v_mfma_f32_16x16x32_bf16 v[60:63], v[156:159], a[124:127], v[60:63]\n"
"v_mfma_f32_16x16x32_bf16 v[56:59], v[156:159], a[120:123], v[56:59]\n"
"v_mfma_f32_16x16x32_bf16 v[56:59], v[152:155], a[104:107], v[56:59]\n"
"buffer_load_dwordx4 v238, s[36:39], s44 offen offset:2048 lds\n"
"v_mfma_f32_16x16x32_bf16 v[52:55], v[152:155], a[100:103], v[52:55]\n"
"v_mfma_f32_16x16x32_bf16 v[52:55], v[156:159], a[116:119], v[52:55]\n"
"v_mfma_f32_16x16x32_bf16 v[48:51], v[156:159], a[112:115], v[48:51]\n"
"v_mfma_f32_16x16x32_bf16 v[48:51], v[152:155], a[96:99], v[48:51]\n"
"buffer_load_dwordx4 v239, s[36:39], s44 offen offset:3072 lds\n"
"s_waitcnt vmcnt(8)\n"
"s_barrier\n"
"ds_read_b128 a[224:227], v224 offset:0\n"
"ds_read_b128 a[228:231], v225 offset:0\n"
"ds_read_b128 a[232:235], v226 offset:0\n"
"ds_read_b128 a[236:239], v227 offset:0\n"
"s_getreg_b32 s98, hwreg(4, 8, 1)\n"
"s_lshl_b32 s98, s98, 2\n"
"s_add_u32 s98, s98, s77\n"
"s_lshl_b32 s96, s98, 2\n"
"s_add_u32 s96, s96, .LT2_%=-.LTP2_%=\n"
"s_getpc_b64 s[98:99]\n"
".LTP2_%=:\n"
"s_add_u32 s98, s98, s96\n"
"s_addc_u32 s99, s99, 0\n"
"s_mov_b64 exec, 0\n"
"s_swappc_b64 s[96:97], s[98:99]\n"
"s_add_u32 s98, s98, 32\n"
"s_addc_u32 s99, s99, 0\n"
"s_swappc_b64 s[96:97], s[98:99]\n"
"s_branch .LTX2_%=\n"
".LT2_%=:\n"
"s_branch .LJOIN_%=\n"
"s_branch .LPG7_%=\n"
"s_branch .LPG8_%=\n"
"s_branch .LPG9_%=\n"
"s_branch .LPG10_%=\n"
"s_branch .LPG11_%=\n"
"s_branch .LPG12_%=\n"
"s_branch .LPG13_%=\n"
"s_branch .LPG14_%=\n"
"s_branch .LPG15_%=\n"
"s_branch .LPG16_%=\n"
"s_branch .LPG17_%=\n"
"s_branch .LPG18_%=\n"
"s_branch .LPGR_%=\n"
"s_branch .LPGR_%=\n"
"s_branch .LPGR_%=\n"
".LTX2_%=:\n"
"s_mov_b64 exec, -1\n"
"s_waitcnt lgkmcnt(0)\n"
"v_mfma_f32_16x16x32_bf16 v[192:195], a[224:227], a[128:131], 0\n"
"s_add_u32 m0, s46, 73728\n"
"s_add_u32 s45, s45, 16384\n"
"v_cvt_pk_bf16_f32 v0, v0, v1\n"
"v_mfma_f32_16x16x32_bf16 v[192:195], a[228:231], a[132:135], v[192:195]\n"
"buffer_load_dwordx4 v240, s[40:43], s45 offen offset:0 lds\n"
"v_cvt_pk_bf16_f32 v1, v2, v3\n"
"v_mfma_f32_16x16x32_bf16 v[192:195], a[232:235], a[136:139], v[192:195]\n"
"v_cvt_pk_bf16_f32 v2, v4, v5\n"
"v_cvt_pk_bf16_f32 v3, v6, v7\n"
"v_mfma_f32_16x16x32_bf16 v[192:195], a[236:239], a[140:143], v[192:195]\n"
"buffer_load_dwordx4 v241, s[40:43], s45 offen offset:1024 lds\n"
"v_cvt_pk_bf16_f32 v8, v8, v9\n"
"v_mfma_f32_16x16x32_bf16 v[196:199], a[224:227], a[144:147], 0\n"
"v_cvt_pk_bf16_f32 v9, v10, v11\n"
"v_cvt_pk_bf16_f32 v10, v12, v13\n"
"v_mfma_f32_16x16x32_bf16 v[196:199], a[228:231], a[148:151], v[196:199]\n"
"buffer_load_dwordx4 v240, s[40:43], s45 offen offset:2048 lds\n"
"v_cvt_pk_bf16_f32 v11, v14, v15\n"
"v_mfma_f32_16x16x32_bf16 v[196:199], a[232:235], a[152:155], v[196:199]\n"
"v_cvt_pk_bf16_f32 v16, v16, v17\n"
"v_cvt_pk_bf16_f32 v17, v18, v19\n"
"v_mfma_f32_16x16x32_bf16 v[196:199], a[236:239], a[156:159], v[196:199]\n"
"buffer_load_dwordx4 v241, s[40:43], s45 offen offset:3072 lds\n"
"v_cvt_pk_bf16_f32 v18, v20, v21\n"
"v_mfma_f32_16x16x32_bf16 v[200:203], a[224:227], a[160:163], 0\n"
"s_add_u32 m0, s46, 77824\n"
"s_add_u32 s45, s45, 16384\n"
"v_cvt_pk_bf16_f32 v19, v22, v23\n"
"v_mfma_f32_16x16x32_bf16 v[200:203], a[228:231], a[164:167], v[200:203]\n"
"buffer_load_dwordx4 v240, s[40:43], s45 offen offset:0 lds\n"
"v_cvt_pk_bf16_f32 v24, v24, v25\n"
"v_mfma_f32_16x16x32_bf16 v[200:203], a[232:235], a[168:171], v[200:203]\n"
"v_cvt_pk_bf16_f32 v25, v26, v27\n"
"v_cvt_pk_bf16_f32 v26, v28, v29\n"
"v_mfma_f32_16x16x32_bf16 v[200:203], a[236:239], a[172:175], v[200:203]\n"
"buffer_load_dwordx4 v241, s[40:43], s45 offen offset:1024 lds\n"
"v_cvt_pk_bf16_f32 v27, v30, v31\n"
"v_mfma_f32_16x16x32_bf16 v[204:207], a[224:227], a[176:179], 0\n"
"v_cvt_pk_bf16_f32 v32, v32, v33\n"
"v_cvt_pk_bf16_f32 v33, v34, v35\n"
"v_mfma_f32_16x16x32_bf16 v[204:207], a[228:231], a[180:183], v[204:207]\n"
"buffer_load_dwordx4 v240, s[40:43], s45 offen offset:2048 lds\n"
"v_cvt_pk_bf16_f32 v34, v36, v37\n"
"v_mfma_f32_16x16x32_bf16 v[204:207], a[232:235], a[184:187], v[204:207]\n"
"v_cvt_pk_bf16_f32 v35, v38, v39\n"
"v_cvt_pk_bf16_f32 v40, v40, v41\n"
"v_mfma_f32_16x16x32_bf16 v[204:207], a[236:239], a[188:191], v[204:207]\n"
"buffer_load_dwordx4 v241, s[40:43], s45 offen offset:3072 lds\n"
"v_cvt_pk_bf16_f32 v41, v42, v43\n"
"v_cvt_pk_bf16_f32 v42, v44, v45\n"
"v_cvt_pk_bf16_f32 v43, v46, v47\n"
"v_cvt_pk_bf16_f32 v48, v48, v49\n"
"v_cvt_pk_bf16_f32 v49, v50, v51\n"
"v_cvt_pk_bf16_f32 v50, v52, v53\n"
"v_cvt_pk_bf16_f32 v51, v54, v55\n"
"v_cvt_pk_bf16_f32 v56, v56, v57\n"
"v_cvt_pk_bf16_f32 v57, v58, v59\n"
"v_cvt_pk_bf16_f32 v58, v60, v61\n"
"v_cvt_pk_bf16_f32 v59, v62, v63\n"
"s_branch .LJOIN_%=\n"
".LIDLE2_%=:\n"
"s_cbranch_execz .LPGR_%=\n"
"ds_read_b128 a[32:35], v230 offset:2048\n"
"ds_read_b128 a[36:39], v230 offset:18432\n"
"ds_read_b128 a[40:43], v230 offset:34816\n"
"ds_read_b128 a[44:47], v230 offset:51200\n"
"ds_read_b128 a[48:51], v230 offset:3072\n"
"ds_read_b128 a[52:55], v230 offset:19456\n"
"ds_read_b128 a[56:59], v230 offset:35840\n"
"ds_read_b128 a[60:63], v230 offset:52224\n"
"ds_read_b128 a[64:67], v230 offset:4096\n"
"ds_read_b128 a[68:71], v230 offset:20480\n"
"ds_read_b128 a[72:75], v230 offset:36864\n"
"ds_read_b128 a[76:79], v230 offset:53248\n"
"ds_read_b128 a[80:83], v230 offset:5120\n"
"ds_read_b128 a[84:87], v230 offset:21504\n"
"ds_read_b128 a[88:91], v230 offset:37888\n"
"ds_read_b128 a[92:95], v230 offset:54272\n"
"ds_read_b128 a[96:99], v230 offset:6144\n"
"ds_read_b128 a[100:103], v230 offset:22528\n"
"ds_read_b128 a[104:107], v230 offset:38912\n"
"ds_read_b128 a[108:111], v230 offset:55296\n"
"ds_read_b128 a[112:115], v230 offset:7168\n"
"ds_read_b128 a[116:119], v230 offset:23552\n"
"ds_read_b128 a[120:123], v230 offset:39936\n"
"ds_read_b128 a[124:127], v230 offset:56320\n"
"ds_read_b128 a[128:131], v230 offset:8192\n"
"ds_read_b128 a[132:135], v230 offset:24576\n"
"ds_read_b128 a[136:139], v230 offset:40960\n"
"ds_read_b128 a[140:143], v230 offset:57344\n"
"ds_read_b128 a[144:147], v230 offset:9216\n"
"ds_read_b128 a[148:151], v230 offset:25600\n"
"ds_read_b128 a[152:155], v230 offset:41984\n"
"ds_read_b128 a[156:159], v230 offset:58368\n"
"ds_read_b128 a[160:163], v230 offset:10240\n"
"ds_read_b128 a[164:167], v230 offset:26624\n"
"ds_read_b128 a[168:171], v230 offset:43008\n"
"ds_read_b128 a[172:175], v230 offset:59392\n"
"ds_read_b128 a[176:179], v230 offset:11264\n"
"ds_read_b128 a[180:183], v230 offset:27648\n"
"ds_read_b128 a[184:187], v230 offset:44032\n"
"ds_read_b128 a[188:191], v230 offset:60416\n"
"ds_read_b128 a[192:195], v230 offset:12288\n"
"ds_read_b128 a[196:199], v230 offset:28672\n"
"ds_read_b128 a[200:203], v230 offset:45056\n"
"ds_read_b128 a[204:207], v230 offset:61440\n"
"ds_read_b128 a[208:211], v230 offset:13312\n"
"ds_read_b128 a[212:215], v230 offset:29696\n"
"ds_read_b128 a[216:219], v230 offset:46080\n"
"ds_read_b128 a[220:223], v230 offset:62464\n"
"ds_read_b128 a[224:227], v230 offset:14336\n"
"ds_read_b128 a[228:231], v230 offset:30720\n"
"ds_read_b128 a[232:235], v230 offset:47104\n"
"ds_read_b128 a[236:239], v230 offset:63488\n"
"ds_read_b128 a[240:243], v230 offset:15360\n"
"ds_read_b128 a[244:247], v230 offset:31744\n"
"ds_read_b128 a[248:251], v230 offset:48128\n"
"ds_read_b128 a[252:255], v230 offset:64512\n"
"s_getreg_b32 s98, hwreg(4, 8, 1)\n"
"s_waitcnt vmcnt(0)\n"
"s_cmp_lt_u32 s77, 2\n"
"s_cselect_b64 s[72:73], s[6:7], s[8:9]\n"
"s_and_b32 s94, s77, 1\n"
"s_mul_i32 s83, s78, 524288\n"
"s_mul_i32 s95, s35, 4096\n"
"s_add_u32 s83, s83, s95\n"
"s_mul_i32 s95, s94, 2048\n"
"s_add_u32 s83, s83, s95\n"
"s_add_u32 s72, s72, s83\n"
"s_addc_u32 s73, s73, 0\n"
"s_mov_b32 s74, 460800\n"
"s_mov_b32 s75, 0x20000\n"
"s_mov_b32 s76, -65536\n"
"s_mov_b64 s[60:61], s[16:17]\n"
"s_mov_b32 s62, 1835008\n"
"s_mov_b32 s63, 0x20000\n"
"s_mul_i32 s34, s35, 12288\n"
"s_mul_i32 s83, s77, 3072\n"
"s_add_u32 s34, s34, s83\n"
"s_mul_i32 s83, s23, 32768\n"
"s_add_u32 s34, s34, s83\n"
"v_lshlrev_b32_e32 v235, 2, v255\n"
"v_lshlrev_b32_e32 v234, 1, v255\n"
"v_add_u32_e32 v250, 65536, v246\n"
"v_add_u32_e32 v251, 65536, v247\n"
"v_add_u32_e32 v252, 65536, v248\n"
"v_add_u32_e32 v253, 65536, v249\n"
"s_and_b32 s89, s79, 1\n"
"s_mul_i32 s85, s78, 1024\n"
"s_mul_i32 s83, s77, 256\n"
"s_add_u32 s85, s85, s83\n"
"s_mul_i32 s83, s89, 128\n"
"s_add_u32 s85, s85, s83\n"
"s_xor_b32 s86, s85, 128\n"
"s_mov_b64 s[24:25], s[20:21]\n"
"s_mov_b32 s26, 50855936\n"
"s_mov_b32 s27, 0x20000\n"
"s_mul_i32 s28, s79, 8192\n"
"s_mul_i32 s83, s78, 1024\n"
"s_add_u32 s28, s28, s83\n"
"s_mul_i32 s83, s77, 256\n"
"s_add_u32 s28, s28, s83\n"
"s_add_u32 s28, s28, 50331648\n"
"s_mul_i32 s29, s79, 8192\n"
"s_mul_i32 s83, s77, 2048\n"
"s_add_u32 s29, s29, s83\n"
"s_mul_i32 s83, s78, 128\n"
"s_add_u32 s29, s29, s83\n"
"s_add_u32 s29, s29, 50331648\n"
"s_add_u32 s30, s28, 0x40000\n"
"s_add_u32 s31, s29, 0x40000\n"
"v_mov_b32_e32 v243, 1\n"
"v_add_u32_e32 v233, s81, v255\n"
"v_add_u32_e32 v232, 131072, v255\n"
"v_add_u32_e32 v204, 0, v202\n"
"v_xor_b32_e32 v205, v204, v201\n"
"v_lshlrev_b32_e32 v205, 4, v205\n"
"v_lshl_add_u32 v236, v202, 8, v205\n"
"v_lshlrev_b32_e32 v205, 1, v204\n"
"v_xor_b32_e32 v205, v205, v201\n"
"v_lshlrev_b32_e32 v205, 4, v205\n"
"v_lshl_add_u32 v240, v202, 8, v205\n"
"v_add_u32_e32 v204, 4, v202\n"
"v_xor_b32_e32 v205, v204, v201\n"
"v_lshlrev_b32_e32 v205, 4, v205\n"
"v_lshl_add_u32 v237, v202, 8, v205\n"
"v_lshlrev_b32_e32 v205, 1, v204\n"
"v_xor_b32_e32 v205, v205, v201\n"
"v_lshlrev_b32_e32 v205, 4, v205\n"
"v_lshl_add_u32 v241, v202, 8, v205\n"
"v_add_u32_e32 v204, 8, v202\n"
"v_xor_b32_e32 v205, v204, v201\n"
"v_lshlrev_b32_e32 v205, 4, v205\n"
"v_lshl_add_u32 v238, v202, 8, v205\n"
"v_add_u32_e32 v204, 12, v202\n"
"v_xor_b32_e32 v205, v204, v201\n"
"v_lshlrev_b32_e32 v205, 4, v205\n"
"v_lshl_add_u32 v239, v202, 8, v205\n"
"v_bfe_u32 v196, v200, 3, 2\n"
"v_lshlrev_b32_e32 v196, 1, v196\n"
"v_and_b32_e32 v197, 3, v200\n"
"v_xor_b32_e32 v196, v196, v197\n"
"v_lshlrev_b32_e32 v196, 4, v196\n"
"v_lshrrev_b32_e32 v197, 2, v200\n"
"v_lshl_add_u32 v197, v197, 7, v196\n"
"v_add_u32_e32 v196, s46, v197\n"
"v_add_u32_e32 v206, s81, v197\n"
"v_xor_b32_e32 v197, 64, v196\n"
"v_add_u32_e32 v198, 65536, v196\n"
"v_add_u32_e32 v199, 65536, v197\n"
"v_xor_b32_e32 v207, 64, v206\n"
"s_mul_i32 s87, s23, 32768\n"
"s_mul_i32 s83, s77, 256\n"
"s_add_u32 s87, s87, s83\n"
"s_mul_i32 s83, s89, 128\n"
"s_add_u32 s87, s87, s83\n"
"s_xor_b32 s88, s87, 128\n"
"s_mov_b64 s[36:37], s[6:7]\n"
"s_mov_b32 s38, 4194304\n"
"s_mov_b32 s39, 0x20000\n"
"s_mov_b64 s[40:41], s[8:9]\n"
"s_mov_b32 s42, 4194304\n"
"s_mov_b32 s43, 0x20000\n"
"s_mul_i32 s44, s78, 524288\n"
"s_mul_i32 s83, s77, 4096\n"
"s_add_u32 s44, s44, s83\n"
"s_add_u32 s44, s44, -16384\n"
"s_mov_b32 s45, s44\n"
"s_mov_b64 s[64:65], s[12:13]\n"
"s_mov_b32 s66, 4194304\n"
"s_mov_b32 s67, 0x20000\n"
"s_mov_b64 s[68:69], s[20:21]\n"
"s_mov_b32 s70, 41943040\n"
"s_mov_b32 s71, 0x20000\n"
"s_mul_i32 s90, s79, 610304\n"
"s_mul_i32 s83, s77, 151552\n"
"s_add_u32 s90, s90, s83\n"
"s_mul_i32 s83, s78, 2048\n"
"s_add_u32 s90, s90, s83\n"
"s_mul_i32 s83, s89, 75776\n"
"s_add_u32 s90, s90, s83\n"
"s_add_u32 s90, s90, 20971520\n"
"s_mul_i32 s91, s89, -151552\n"
"s_mul_i32 s83, s90, 1\n"
"s_add_u32 s91, s91, s83\n"
"s_add_u32 s91, s91, 75776\n"
"s_mov_b32 s92, 0\n"
"s_waitcnt vmcnt(0) lgkmcnt(0)\n"
"s_barrier\n"
"s_mov_b32 s100, 131072\n"
"s_mov_b32 s101, 98304\n"
"s_add_u32 m0, s81, 0\n"
"s_add_u32 s80, s85, 0\n"
"buffer_load_dwordx4 v229, s[56:59], s80 offen lds\n"
"s_addk_i32 m0, 1024\n"
"s_add_u32 s80, s80, s100\n"
"buffer_load_dwordx4 v229, s[56:59], s80 offen lds\n"
"s_addk_i32 m0, 1024\n"
"s_sub_u32 s80, s80, s101\n"
"buffer_load_dwordx4 v229, s[56:59], s80 offen lds\n"
"s_addk_i32 m0, 1024\n"
"s_add_u32 s80, s80, s100\n"
"buffer_load_dwordx4 v229, s[56:59], s80 offen lds\n"
"s_addk_i32 m0, 1024\n"
"s_add_u32 s80, s80, s101\n"
"buffer_load_dwordx4 v229, s[56:59], s80 offen lds\n"
"s_addk_i32 m0, 1024\n"
"s_add_u32 s80, s80, s100\n"
"buffer_load_dwordx4 v229, s[56:59], s80 offen lds\n"
"s_addk_i32 m0, 1024\n"
"s_sub_u32 s80, s80, s101\n"
"buffer_load_dwordx4 v229, s[56:59], s80 offen lds\n"
"s_addk_i32 m0, 1024\n"
"s_add_u32 s80, s80, s100\n"
"buffer_load_dwordx4 v229, s[56:59], s80 offen lds\n"
"s_mov_b64 exec, 0\n"
"s_cmp_lg_u32 s98, 0\n"
"s_cbranch_scc1 .LW10_%=\n"
"s_call_b64 s[96:97], .LPG5_%=\n"
"s_call_b64 s[96:97], .LPG7_%=\n"
"s_call_b64 s[96:97], .LPG9_%=\n"
"s_call_b64 s[96:97], .LPG11_%=\n"
"s_branch .LW1x_%=\n"
".LW10_%=:\n"
"s_call_b64 s[96:97], .LPG6_%=\n"
"s_call_b64 s[96:97], .LPG8_%=\n"
"s_call_b64 s[96:97], .LPG10_%=\n"
"s_call_b64 s[96:97], .LPG12_%=\n"
".LW1x_%=:\n"
"s_mov_b64 exec, -1\n"
"s_add_u32 m0, s46, 73728\n"
"s_add_u32 s80, s85, 524288\n"
"buffer_load_dwordx4 v229, s[56:59], s80 offen lds\n"
"s_addk_i32 m0, 1024\n"
"s_add_u32 s80, s80, s100\n"
"buffer_load_dwordx4 v229, s[56:59], s80 offen lds\n"
"s_addk_i32 m0, 1024\n"
"s_sub_u32 s80, s80, s101\n"
"buffer_load_dwordx4 v229, s[56:59], s80 offen lds\n"
"s_addk_i32 m0, 1024\n"
"s_add_u32 s80, s80, s100\n"
"buffer_load_dwordx4 v229, s[56:59], s80 offen lds\n"
"s_addk_i32 m0, 1024\n"
"s_add_u32 s80, s80, s101\n"
"buffer_load_dwordx4 v229, s[56:59], s80 offen lds\n"
"s_addk_i32 m0, 1024\n"
"s_add_u32 s80, s80, s100\n"
"buffer_load_dwordx4 v229, s[56:59], s80 offen lds\n"
"s_addk_i32 m0, 1024\n"
"s_sub_u32 s80, s80, s101\n"
"buffer_load_dwordx4 v229, s[56:59], s80 offen lds\n"
"s_addk_i32 m0, 1024\n"
"s_add_u32 s80, s80, s100\n"
"buffer_load_dwordx4 v229, s[56:59], s80 offen lds\n"
"s_mov_b64 exec, 0\n"
"s_cmp_lg_u32 s98, 0\n"
"s_cbranch_scc1 .LW20_%=\n"
"s_call_b64 s[96:97], .LPG13_%=\n"
"s_call_b64 s[96:97], .LPG15_%=\n"
"s_call_b64 s[96:97], .LPG17_%=\n"
"s_call_b64 s[96:97], .LPG6_%=\n"
"s_branch .LW2x_%=\n"
".LW20_%=:\n"
"s_call_b64 s[96:97], .LPG14_%=\n"
"s_call_b64 s[96:97], .LPG16_%=\n"
"s_call_b64 s[96:97], .LPG18_%=\n"
"s_call_b64 s[96:97], .LPG5_%=\n"
".LW2x_%=:\n"
"s_mov_b64 exec, -1\n"
"s_add_u32 m0, s46, 0\n"
"s_add_u32 s80, s85, 1048576\n"
"buffer_load_dwordx4 v229, s[56:59], s80 offen lds\n"
"s_addk_i32 m0, 1024\n"
"s_add_u32 s80, s80, s100\n"
"buffer_load_dwordx4 v229, s[56:59], s80 offen lds\n"
"s_addk_i32 m0, 1024\n"
"s_sub_u32 s80, s80, s101\n"
"buffer_load_dwordx4 v229, s[56:59], s80 offen lds\n"
"s_addk_i32 m0, 1024\n"
"s_add_u32 s80, s80, s100\n"
"buffer_load_dwordx4 v229, s[56:59], s80 offen lds\n"
"s_addk_i32 m0, 1024\n"
"s_add_u32 s80, s80, s101\n"
"buffer_load_dwordx4 v229, s[56:59], s80 offen lds\n"
"s_addk_i32 m0, 1024\n"
"s_add_u32 s80, s80, s100\n"
"buffer_load_dwordx4 v229, s[56:59], s80 offen lds\n"
"s_addk_i32 m0, 1024\n"
"s_sub_u32 s80, s80, s101\n"
"buffer_load_dwordx4 v229, s[56:59], s80 offen lds\n"
"s_addk_i32 m0, 1024\n"
"s_add_u32 s80, s80, s100\n"
"buffer_load_dwordx4 v229, s[56:59], s80 offen lds\n"
"s_mov_b64 exec, 0\n"
"s_cmp_lg_u32 s98, 0\n"
"s_cbranch_scc1 .LW30_%=\n"
"s_call_b64 s[96:97], .LPG8_%=\n"
"s_call_b64 s[96:97], .LPG10_%=\n"
"s_call_b64 s[96:97], .LPG12_%=\n"
"s_call_b64 s[96:97], .LPG14_%=\n"
"s_call_b64 s[96:97], .LPG16_%=\n"
"s_call_b64 s[96:97], .LPG18_%=\n"
"s_branch .LW3x_%=\n"
".LW30_%=:\n"
"s_call_b64 s[96:97], .LPG7_%=\n"
"s_call_b64 s[96:97], .LPG9_%=\n"
"s_call_b64 s[96:97], .LPG11_%=\n"
"s_call_b64 s[96:97], .LPG13_%=\n"
"s_call_b64 s[96:97], .LPG15_%=\n"
"s_call_b64 s[96:97], .LPG17_%=\n"
".LW3x_%=:\n"
"s_mov_b64 exec, -1\n"
"s_waitcnt vmcnt(16)\n"
"ds_read_b64_tr_b16 v[128:129], v206 offset:0\n"
"ds_read_b64_tr_b16 v[130:131], v206 offset:2048\n"
"ds_read_b64_tr_b16 v[132:133], v206 offset:4096\n"
"ds_read_b64_tr_b16 v[134:135], v206 offset:6144\n"
"ds_read_b64_tr_b16 v[136:137], v206 offset:8\n"
"ds_read_b64_tr_b16 v[138:139], v206 offset:2056\n"
"ds_read_b64_tr_b16 v[140:141], v206 offset:4104\n"
"ds_read_b64_tr_b16 v[142:143], v206 offset:6152\n"
"s_waitcnt lgkmcnt(0)\n"
"s_branch .LJ8_%=\n"
".LEND_%=:\n"
  :: "s"(__builtin_amdgcn_kernarg_segment_ptr()), "s"(blockIdx.x), "s"(w + (unsigned)(size_t)lds)
  : "v0","v1","v2","v3","v4","v5","v6","v7","v8","v9","v10","v11","v12","v13","v14","v15","v16","v17","v18","v19","v20","v21","v22","v23","v24","v25","v26","v27","v28","v29","v30","v31","v32","v33","v34","v35","v36","v37","v38","v39","v40","v41","v42","v43","v44","v45","v46","v47","v48","v49","v50","v51","v52","v53","v54","v55","v56","v57","v58","v59","v60","v61","v62","v63","v64","v65","v66","v67","v68","v69","v70","v71","v72","v73","v74","v75","v76","v77","v78","v79","v80","v81","v82","v83","v84","v85","v86","v87","v88","v89","v90","v91","v92","v93","v94","v95","v96","v97","v98","v99","v100","v101","v102","v103","v104","v105","v106","v107","v108","v109","v110","v111","v112","v113","v114","v115","v116","v117","v118","v119","v120","v121","v122","v123","v124","v125","v126","v127","v128","v129","v130","v131","v132","v133","v134","v135","v136","v137","v138","v139","v140","v141","v142","v143","v144","v145","v146","v147","v148","v149","v150","v151","v152","v153","v154","v155","v156","v157","v158","v159","v160","v161","v162","v163","v164","v165","v166","v167","v168","v169","v170","v171","v172","v173","v174","v175","v176","v177","v178","v179","v180","v181","v182","v183","v184","v185","v186","v187","v188","v189","v190","v191","v192","v193","v194","v195","v196","v197","v198","v199","v200","v201","v202","v203","v204","v205","v206","v207","v208","v209","v210","v211","v212","v213","v214","v215","v216","v217","v218","v219","v220","v221","v222","v223","v224","v225","v226","v227","v228","v229","v230","v231","v232","v233","v234","v235","v236","v237","v238","v239","v240","v241","v242","v243","v244","v245","v246","v247","v248","v249","v250","v251","v252","v253","v254","v255",
"a0","a1","a2","a3","a4","a5","a6","a7","a8","a9","a10","a11","a12","a13","a14","a15","a16","a17","a18","a19","a20","a21","a22","a23","a24","a25","a26","a27","a28","a29","a30","a31","a32","a33","a34","a35","a36","a37","a38","a39","a40","a41","a42","a43","a44","a45","a46","a47","a48","a49","a50","a51","a52","a53","a54","a55","a56","a57","a58","a59","a60","a61","a62","a63","a64","a65","a66","a67","a68","a69","a70","a71","a72","a73","a74","a75","a76","a77","a78","a79","a80","a81","a82","a83","a84","a85","a86","a87","a88","a89","a90","a91","a92","a93","a94","a95","a96","a97","a98","a99","a100","a101","a102","a103","a104","a105","a106","a107","a108","a109","a110","a111","a112","a113","a114","a115","a116","a117","a118","a119","a120","a121","a122","a123","a124","a125","a126","a127","a128","a129","a130","a131","a132","a133","a134","a135","a136","a137","a138","a139","a140","a141","a142","a143","a144","a145","a146","a147","a148","a149","a150","a151","a152","a153","a154","a155","a156","a157","a158","a159","a160","a161","a162","a163","a164","a165","a166","a167","a168","a169","a170","a171","a172","a173","a174","a175","a176","a177","a178","a179","a180","a181","a182","a183","a184","a185","a186","a187","a188","a189","a190","a191","a192","a193","a194","a195","a196","a197","a198","a199","a200","a201","a202","a203","a204","a205","a206","a207","a208","a209","a210","a211","a212","a213","a214","a215","a216","a217","a218","a219","a220","a221","a222","a223","a224","a225","a226","a227","a228","a229","a230","a231","a232","a233","a234","a235","a236","a237","a238","a239","a240","a241","a242","a243","a244","a245","a246","a247","a248","a249","a250","a251","a252","a253","a254","a255",
"s4","s5","s6","s7","s8","s9","s10","s11","s12","s13","s14","s15","s16","s17","s18","s19","s20","s21","s22","s23","s24","s25","s26","s27","s28","s29","s30","s31","s32","s33","s34","s35","s36","s37","s38","s39","s40","s41","s42","s43","s44","s45","s46","s47","s48","s49","s50","s51","s52","s53","s54","s55","s56","s57","s58","s59","s60","s61","s62","s63","s64","s65","s66","s67","s68","s69","s70","s71","s72","s73","s74","s75","s76","s77","s78","s79","s80","s81","s82","s83","s84","s85","s86","s87","s88","s89","s90","s91","s92","s93","s94","s95","s96","s97","s98","s99","s100","s101","vcc","memory");
}

extern "C" void kernel_launch(void* const* d_in, const int* in_sizes, int n_in, void* d_out, int out_size, void* d_ws, size_t ws_size, hipStream_t stream) {
  fused_main<<<256, 256, 0, stream>>>(d_in[0], d_in[1], d_in[2], d_in[3], d_in[4], d_in[6], d_in[7], d_out, d_ws);
}
```
